```python
import torch

def _softmax(x, axis=-1):
    return torch.softmax(x, dim=axis)
def _gelu(x):
    return torch.nn.functional.gelu(x, approximate="tanh")
def _silu(x):
    return torch.nn.functional.silu(x)
def _sigmoid(x):
    return torch.sigmoid(x)
def _layernorm(x, axis=-1, eps=1e-5):
    m = x.mean(dim=axis, keepdim=True)
    v = x.var(dim=axis, keepdim=True, unbiased=False)
    return (x - m) / torch.sqrt(v + eps)
def _scatter_add(tgt, idx, src, axis=0):
    return tgt.clone().index_add_(axis, idx, src)
def _segment_sum(src, seg, n):
    out = torch.zeros((n,) + src.shape[1:], dtype=src.dtype)
    return out.index_add_(0, seg, src)
def _pad(x, pw):
    flat = tuple(v for pair in reversed(pw) for v in pair)
    return torch.nn.functional.pad(x, flat)

kernel_name = 'sequential_preLN__sliding_block__moe_arch_mod__prompt'

def setup_inputs():

    _w = (lambda a: (lambda t: t.to(torch.bfloat16) if t.is_floating_point() else t)(torch.as_tensor(a)))
    def _A_setup():
        B, S, D = 1, 2048, 640
        return {
            "x": _w((lambda *s: torch.randn(*s, dtype=torch.bfloat16) * 0.05)(B, S, D)),
            "wqkv": _w((lambda *s: torch.randn(*s, dtype=torch.bfloat16) * 0.05)(D, 3 * D)),
            "wo": _w((lambda *s: torch.randn(*s, dtype=torch.bfloat16) * 0.05)(D, D)),
        }
    _A_in = _A_setup()

    def _B_setup():
        B, S, D, F, C = 1, 2048, 640, 1728, 8
        return {
            "x": _w((lambda *s: torch.randn(*s, dtype=torch.bfloat16) * 0.05)(B, S, D)),
            "w_r": _w((lambda *s: torch.randn(*s, dtype=torch.bfloat16) * 0.05)(D)),
            "w_gate": _w((lambda *s: torch.randn(*s, dtype=torch.bfloat16) * 0.05)(D, F)),
            "w_up": _w((lambda *s: torch.randn(*s, dtype=torch.bfloat16) * 0.05)(D, F)),
            "w_down": _w((lambda *s: torch.randn(*s, dtype=torch.bfloat16) * 0.05)(F, D)),
        }
    _B_in = _B_setup()

    return {
        "A_x": _A_in["x"],
        "A_wqkv": _A_in["wqkv"],
        "A_wo": _A_in["wo"],
        "B_x": _B_in["x"],
        "B_w_r": _B_in["w_r"],
        "B_w_gate": _B_in["w_gate"],
        "B_w_up": _B_in["w_up"],
        "B_w_down": _B_in["w_down"],
    }

def reference(A_x, A_wqkv, A_wo, B_x, B_w_r, B_w_gate, B_w_up, B_w_down):
    def _A_ref(x, wqkv, wo):
        B, S, D = x.shape
        H = 32; Dh = D // H; W = 256
        qkv = (lambda x, *d: x.permute(*d))((x @ wqkv).reshape(B, S, 3, H, Dh), 2, 0, 3, 1, 4)
        q, k, v = qkv[0], qkv[1], qkv[2]
        scores = torch.einsum("bhqd,bhkd->bhqk", q, k) * (Dh ** -0.5)
        idx = torch.arange(S)
        mask = (idx[:, None] - idx[None, :] >= 0) & (idx[:, None] - idx[None, :] < W)
        scores = torch.where(mask[None, None], scores, -1e9)
        p = _softmax(scores, axis=-1)
        o = torch.einsum("bhqk,bhkd->bhqd", p, v)
        return (lambda x, *d: x.permute(*d))(o, 0, 2, 1, 3).reshape(B, S, D) @ wo

    def _B_ref(x, w_r, w_gate, w_up, w_down):
        B, S, D = x.shape
        C = 8
        r = x @ w_r
        idx = torch.argsort(-r, axis=-1)[:, :C]
        idx_e = (lambda x, n, axis: torch.repeat_interleave(x, n, dim=axis))(idx.reshape(B, C, 1), D, axis=2)
        x_sel = (lambda x, i, axis: torch.take_along_dim(x, i, dim=axis))(x, idx_e, axis=1)
        g = _silu(x_sel @ w_gate)
        u = x_sel @ w_up
        y = (g * u) @ w_down
        r_sel = _sigmoid((lambda x, i, axis: torch.take_along_dim(x, i, dim=axis))(r, idx, axis=1))
        y = y * r_sel.reshape(B, C, 1)
        b_off = (torch.arange(B) * S).reshape(B, 1)
        flat_idx = (idx + b_off).reshape(B * C)
        zero = (x * 0.0).reshape(B * S, D)
        upd = _scatter_add(zero, flat_idx, y.reshape(B * C, D), axis=0)
        return x + upd.reshape(B, S, D)

    x = A_x
    h = x + _A_ref(_layernorm(x, axis=-1), A_wqkv, A_wo)
    h2 = h + _B_ref(_layernorm(h, axis=-1), B_w_r, B_w_gate, B_w_up, B_w_down)
    return h2
```

```cpp
#include <hip/hip_runtime.h>
#include <hip/hip_bf16.h>
#include <stdint.h>

typedef unsigned short u16;
using bf16x8 = __attribute__((ext_vector_type(8))) short;
using f32x4 = __attribute__((ext_vector_type(4))) float;

constexpr int S = 2048, D = 640, H = 32, DH = 20, W = 256, F = 1728, C = 8;

__device__ __forceinline__ float bf2f(u16 v) { return __uint_as_float(((unsigned)v) << 16); }
__device__ __forceinline__ u16 f2bf(float f) { __hip_bfloat16 b = __float2bfloat16(f); return *(u16*)&b; }
__device__ __forceinline__ float rbf(float f) { return bf2f(f2bf(f)); }

__device__ __forceinline__ float wsum(float v) {
  for (int m = 32; m >= 1; m >>= 1) v += __shfl_xor(v, m);
  return v;
}

__global__ void ln_kernel(const u16* __restrict__ x, u16* __restrict__ xn) {
  int row = blockIdx.x * 4 + (threadIdx.x >> 6), lane = threadIdx.x & 63;
  float v[10]; float s = 0;
  for (int i = 0; i < 10; ++i) { v[i] = bf2f(x[row * D + lane + 64 * i]); s += v[i]; }
  float m = wsum(s) / D;
  float q = 0;
  for (int i = 0; i < 10; ++i) { float d = v[i] - m; q += d * d; }
  float var = wsum(q) / D;
  float mb = rbf(m), vb = rbf(var);
  float sd = rbf(sqrtf(rbf(vb + 1e-5f)));
  for (int i = 0; i < 10; ++i) xn[row * D + lane + 64 * i] = f2bf(rbf(v[i] - mb) / sd);
}

template <bool RES>
__global__ void gemm_kernel(const u16* __restrict__ A, const u16* __restrict__ B, const u16* __restrict__ R, u16* __restrict__ Cc, int M, int N, int K) {
  __shared__ u16 sA[64][40];
  __shared__ u16 sB[64][40];
  int bm = blockIdx.y * 64, bn = blockIdx.x * 64;
  int tid = threadIdx.x, wid = tid >> 6, lane = tid & 63;
  int wr = wid >> 1, wc = wid & 1;
  f32x4 acc[2][2] = {};
  for (int k0 = 0; k0 < K; k0 += 32) {
    for (int e = tid; e < 64 * 32; e += 256) {
      int r = e >> 5, c = e & 31;
      sA[r][c] = A[(bm + r) * K + k0 + c];
    }
    for (int e = tid; e < 64 * 32; e += 256) {
      int kk = e >> 6, n = e & 63;
      sB[n][kk] = B[(k0 + kk) * N + bn + n];
    }
    __syncthreads();
    for (int i = 0; i < 2; ++i) for (int j = 0; j < 2; ++j) {
      bf16x8 a, b;
      for (int t = 0; t < 8; ++t) {
        a[t] = sA[wr * 32 + i * 16 + (lane & 15)][(lane >> 4) * 8 + t];
        b[t] = sB[wc * 32 + j * 16 + (lane & 15)][(lane >> 4) * 8 + t];
      }
      acc[i][j] = __builtin_amdgcn_mfma_f32_16x16x32_bf16(a, b, acc[i][j], 0, 0, 0);
    }
    __syncthreads();
  }
  for (int i = 0; i < 2; ++i) for (int j = 0; j < 2; ++j) for (int t = 0; t < 4; ++t) {
    int r = bm + wr * 32 + i * 16 + (lane >> 4) * 4 + t, c = bn + wc * 32 + j * 16 + (lane & 15);
    float v = acc[i][j][t];
    if (RES) v = bf2f(R[r * N + c]) + rbf(v);
    Cc[r * N + c] = f2bf(v);
  }
}

__global__ void attn_kernel(const u16* __restrict__ qkv, u16* __restrict__ o) {
  int q = blockIdx.x * 64 + (threadIdx.x & 63), h = blockIdx.y;
  float qv[DH];
  for (int d = 0; d < DH; ++d) qv[d] = bf2f(qkv[q * 3 * D + h * DH + d]);
  float m = -1e30f, l = 0, acc[DH];
  for (int d = 0; d < DH; ++d) acc[d] = 0;
  int k0 = max(0, q - W + 1);
  for (int k = k0; k <= q; ++k) {
    float s = 0;
    for (int d = 0; d < DH; ++d) s += qv[d] * bf2f(qkv[k * 3 * D + D + h * DH + d]);
    s *= 0.22360679774997896f;
    float mn = fmaxf(m, s), a = __expf(m - mn), p = __expf(s - mn);
    l = l * a + p;
    for (int d = 0; d < DH; ++d) acc[d] = acc[d] * a + p * bf2f(qkv[k * 3 * D + 2 * D + h * DH + d]);
    m = mn;
  }
  for (int d = 0; d < DH; ++d) o[q * D + h * DH + d] = f2bf(acc[d] / l);
}

__global__ void ln2_kernel(const u16* __restrict__ h, const u16* __restrict__ wr, u16* __restrict__ hn, float* __restrict__ r, u16* __restrict__ out) {
  int row = blockIdx.x * 4 + (threadIdx.x >> 6), lane = threadIdx.x & 63;
  float v[10]; float s = 0;
  for (int i = 0; i < 10; ++i) { v[i] = bf2f(h[row * D + lane + 64 * i]); s += v[i]; }
  float m = wsum(s) / D;
  float q = 0;
  for (int i = 0; i < 10; ++i) { float d = v[i] - m; q += d * d; }
  float var = wsum(q) / D;
  float mb = rbf(m), vb = rbf(var);
  float sd = rbf(sqrtf(rbf(vb + 1e-5f)));
  float dot = 0;
  for (int i = 0; i < 10; ++i) {
    float n = rbf(rbf(v[i] - mb) / sd);
    hn[row * D + lane + 64 * i] = f2bf(n);
    out[row * D + lane + 64 * i] = f2bf(v[i] + n);
    dot += n * bf2f(wr[lane + 64 * i]);
  }
  dot = wsum(dot);
  if (lane == 0) r[row] = rbf(dot);
}

__global__ void topk_kernel(const float* __restrict__ r, int* __restrict__ idx) {
  __shared__ float sv[S];
  __shared__ float bv[256]; __shared__ int bi[256];
  int tid = threadIdx.x;
  for (int i = tid; i < S; i += 256) sv[i] = r[i];
  __syncthreads();
  for (int c = 0; c < C; ++c) {
    float b = -INFINITY; int bidx = 0x7fffffff;
    for (int i = tid; i < S; i += 256) if (sv[i] > b || (sv[i] == b && i < bidx)) { b = sv[i]; bidx = i; }
    bv[tid] = b; bi[tid] = bidx;
    __syncthreads();
    for (int s = 128; s >= 1; s >>= 1) {
      if (tid < s) { if (bv[tid + s] > bv[tid] || (bv[tid + s] == bv[tid] && bi[tid + s] < bi[tid])) { bv[tid] = bv[tid + s]; bi[tid] = bi[tid + s]; } }
      __syncthreads();
    }
    if (tid == 0) { idx[c] = bi[0]; sv[bi[0]] = -INFINITY; }
    __syncthreads();
  }
}

__global__ void gateup_kernel(const u16* __restrict__ hn, const int* __restrict__ idx, const u16* __restrict__ wg, const u16* __restrict__ wu, float* __restrict__ act) {
  int f = blockIdx.x * 64 + threadIdx.x;
  for (int c = 0; c < C; ++c) {
    const u16* xr = hn + idx[c] * D;
    float g = 0, u = 0;
    for (int k = 0; k < D; ++k) { float xv = bf2f(xr[k]); g += xv * bf2f(wg[k * F + f]); u += xv * bf2f(wu[k * F + f]); }
    g = rbf(g); u = rbf(u);
    g = rbf(g / (1.f + __expf(-g)));
    act[c * F + f] = rbf(g * u);
  }
}

__global__ void down_kernel(const float* __restrict__ act, const int* __restrict__ idx, const float* __restrict__ r, const u16* __restrict__ wd, const u16* __restrict__ h, const u16* __restrict__ hn, u16* __restrict__ out) {
  int n = blockIdx.x * 64 + threadIdx.x, c = blockIdx.y;
  float y = 0;
  for (int k = 0; k < F; ++k) y += act[c * F + k] * bf2f(wd[k * D + n]);
  y = rbf(y);
  int row = idx[c];
  float sg = rbf(1.f / (1.f + __expf(-r[row])));
  y = rbf(y * sg);
  float t = rbf(bf2f(hn[row * D + n]) + y);
  out[row * D + n] = f2bf(bf2f(h[row * D + n]) + t);
}

extern "C" void kernel_launch(void* const* d_in, const int* in_sizes, int n_in, void* d_out, int out_size, void* d_ws, size_t ws_size, hipStream_t stream) {
  const u16* x = (const u16*)d_in[0];
  const u16* wqkv = (const u16*)d_in[1];
  const u16* wo = (const u16*)d_in[2];
  const u16* wr = (const u16*)d_in[4];
  const u16* wg = (const u16*)d_in[5];
  const u16* wu = (const u16*)d_in[6];
  const u16* wd = (const u16*)d_in[7];
  u16* out = (u16*)d_out;
  char* ws = (char*)d_ws;
  u16* xn = (u16*)ws; ws += S * D * 2;
  u16* qkv = (u16*)ws; ws += S * 3 * D * 2;
  u16* o = (u16*)ws; ws += S * D * 2;
  u16* h = (u16*)ws; ws += S * D * 2;
  u16* hn = (u16*)ws; ws += S * D * 2;
  float* r = (float*)ws; ws += S * 4;
  int* idx = (int*)ws; ws += 256;
  float* act = (float*)ws; ws += C * F * 4;
  ln_kernel<<<S / 4, 256, 0, stream>>>(x, xn);
  gemm_kernel<false><<<dim3(3 * D / 64, S / 64), 256, 0, stream>>>(xn, wqkv, nullptr, qkv, S, 3 * D, D);
  attn_kernel<<<dim3(S / 64, H), 64, 0, stream>>>(qkv, o);
  gemm_kernel<true><<<dim3(D / 64, S / 64), 256, 0, stream>>>(o, wo, x, h, S, D, D);
  ln2_kernel<<<S / 4, 256, 0, stream>>>(h, wr, hn, r, out);
  topk_kernel<<<1, 256, 0, stream>>>(r, idx);
  gateup_kernel<<<F / 64, 64, 0, stream>>>(hn, idx, wg, wu, act);
  down_kernel<<<dim3(D / 64, C), 64, 0, stream>>>(act, idx, r, wd, h, hn, out);
}
```

```cpp
#include <hip/hip_runtime.h>
#include <stdint.h>

typedef unsigned short u16;
typedef short bf16x8 __attribute__((ext_vector_type(8)));
typedef short bf16x4 __attribute__((ext_vector_type(4)));
typedef __bf16 bf2 __attribute__((ext_vector_type(2)));
typedef float f32x16 __attribute__((ext_vector_type(16)));
typedef float f32x4 __attribute__((ext_vector_type(4)));
typedef float f32x2 __attribute__((ext_vector_type(2)));
typedef unsigned u32x4 __attribute__((ext_vector_type(4)));
typedef unsigned u32x2 __attribute__((ext_vector_type(2)));
typedef unsigned long long u64;
#define LAS __attribute__((address_space(3)))
#define SBAR() __builtin_amdgcn_sched_barrier(0)
#define BARRIER() asm volatile("s_barrier" ::: "memory")
#define WAITL0() asm volatile("s_waitcnt lgkmcnt(0)" ::: "memory")
#define WAITV(n) asm volatile("s_waitcnt vmcnt(" #n ")" ::: "memory")

constexpr int S = 2048, D = 640, F = 1728;
constexpr int SC1 = 16;

constexpr unsigned WS_EPOCH = 0;
constexpr unsigned WS_FLAGA = 4096;
constexpr unsigned WS_FLAGO = WS_FLAGA + 32768;
constexpr unsigned WS_CAND = WS_FLAGO + 16384;
constexpr unsigned WS_ACT = WS_CAND + 4096;
constexpr unsigned WS_STATS = 131072;
constexpr unsigned WS_QKV = 524288;
constexpr unsigned WS_O = WS_QKV + 480 * 16384;
constexpr unsigned WS_H = WS_O + S * D * 2;
#define STAMP_OFF (200u << 20)
#define NSTAMP 32

constexpr int L_SLOT = 32768, L_W = 16384;
constexpr int PL = 4128;
constexpr int L_K = 0, L_V = 5 * PL, L_ONE = 10 * PL;
constexpr int L_WO = 43008;
constexpr int L_RED = 0;
constexpr int L_FW = 43008;
constexpr int L_XS = 0;
constexpr int L_FRED = 12288;
constexpr int L_MISC = 147456;
constexpr int L_RS = L_MISC;
constexpr int L_CS = L_MISC + 1024;
constexpr int L_EP = L_MISC + 1536;
constexpr int L_KEY = L_MISC + 2048;
constexpr int L_MS = L_MISC + 2304;
constexpr int L_SEL = L_MISC + 2560;
constexpr int L_STAMP = L_MISC + 4096;
constexpr int LDS_BYTES = 163840;

struct Params { const u16* x; const u16* wqkv; const u16* wo; const u16* wr; const u16* wg; const u16* wu; const u16* wd; u16* out; unsigned char* ws; };

__device__ __forceinline__ float bf2f(unsigned v) { return __uint_as_float(v << 16); }
__device__ __forceinline__ float bfhi(unsigned v) { return __uint_as_float(v & 0xffff0000u); }
__device__ __forceinline__ unsigned pk2(float a, float b) { f32x2 f = {a, b}; return __builtin_bit_cast(unsigned, __builtin_convertvector(f, bf2)); }
__device__ __forceinline__ float rbf(float f) { return __uint_as_float(pk2(f, 0.f) << 16); }
__device__ __forceinline__ float dot2(unsigned a, unsigned b, float c) { return __builtin_amdgcn_fdot2_f32_bf16(__builtin_bit_cast(bf2, a), __builtin_bit_cast(bf2, b), c, false); }
__device__ __forceinline__ float swap_add(float v) { auto r = __builtin_amdgcn_permlane32_swap(__float_as_uint(v), __float_as_uint(v), false, false); return __uint_as_float(r[0]) + __uint_as_float(r[1]); }
__device__ __forceinline__ float swap16_add(float v) { auto r = __builtin_amdgcn_permlane16_swap(__float_as_uint(v), __float_as_uint(v), false, false); return __uint_as_float(r[0]) + __uint_as_float(r[1]); }
__device__ __forceinline__ void glds16(unsigned voff, const void* sbase, unsigned lds) {
  asm volatile("s_mov_b32 m0, %2\n\ts_nop 0\n\tglobal_load_lds_dwordx4 %0, %1" ::"v"(voff), "s"(sbase), "s"(lds) : "memory");
}
__device__ __forceinline__ void glds16c(unsigned voff, const void* sbase, unsigned lds) {
  asm volatile("s_mov_b32 m0, %2\n\ts_nop 0\n\tglobal_load_lds_dwordx4 %0, %1 sc1" ::"v"(voff), "s"(sbase), "s"(lds) : "memory");
}
__device__ __forceinline__ bf16x4 trd(const LAS char* p) { return __builtin_amdgcn_ds_read_tr16_b64_v4i16((LAS bf16x4*)p); }
__device__ __forceinline__ bf16x8 cat8(bf16x4 a, bf16x4 b) { return (bf16x8){a[0], a[1], a[2], a[3], b[0], b[1], b[2], b[3]}; }
#define MFMA32(a, b, c) __builtin_amdgcn_mfma_f32_32x32x16_bf16(a, b, c, 0, 0, 0)
#define MFMA32K8(a, b, c) __builtin_amdgcn_mfma_f32_32x32x8bf16_1k(a, b, c, 0, 0, 0)
#define MFMA16(a, b, c) __builtin_amdgcn_mfma_f32_16x16x32_bf16(a, b, c, 0, 0, 0)
template <int CTRL, int RMASK> __device__ __forceinline__ unsigned dppmax(unsigned v) { return max(v, (unsigned)__builtin_amdgcn_update_dpp(0, (int)v, CTRL, RMASK, 0xf, false)); }
__device__ __forceinline__ unsigned wave_umax(unsigned v) {
  v = dppmax<0x111, 0xf>(v); v = dppmax<0x112, 0xf>(v); v = dppmax<0x114, 0xf>(v); v = dppmax<0x118, 0xf>(v);
  v = dppmax<0x142, 0xa>(v); v = dppmax<0x143, 0xc>(v);
  return (unsigned)__builtin_amdgcn_readlane((int)v, 63);
}

template <int CTRL> __device__ __forceinline__ float dppadd(float v) { return v + __int_as_float(__builtin_amdgcn_update_dpp(0, __float_as_int(v), CTRL, 0xf, 0xf, false)); }
__device__ __forceinline__ float wave_fsum(float v) { v = dppadd<0x128>(v); v = dppadd<0x124>(v); v = dppadd<0x122>(v); v = dppadd<0x121>(v); return swap_add(swap16_add(v)); }

#define STAMP(k) do {} while (0)
#define STAMP_INIT() do {} while (0)
#define STAMP_FLUSH() do {} while (0)

constexpr unsigned SPIN_CAP = 1u << 16;

__global__ __launch_bounds__(512, 2) void fused_kernel(Params p) {
  __shared__ __attribute__((aligned(16))) char smem[LDS_BYTES];
  LAS char* L = (LAS char*)smem;
  const unsigned lds0 = (unsigned)(uintptr_t)smem;
  const int tid = threadIdx.x, lane = tid & 63;
  const int w = __builtin_amdgcn_readfirstlane(tid >> 6);
  const int b = blockIdx.x, xcd = b & 7, bi = b >> 3;
  const int l31 = lane & 31, hh = lane >> 5, l15 = lane & 15, fg = lane >> 4;
  const __amdgpu_buffer_rsrc_t rws = __builtin_amdgcn_make_buffer_rsrc(p.ws, 0, 0x7fffffff, 0x00020000);
  const int aP = 4 * (xcd >> 1) + (bi >> 3), aj = 8 * (xcd & 1) + (bi & 7);
  const int hd = bi, pp = xcd;
  const int R = 4 * xcd + (bi >> 3), cc = bi & 7;
  const int gs = xcd * 14 + bi;
  const bool is_gu = bi < 14 && gs < 108;
  const int dn = xcd * 5 + (bi - 14);
  const bool is_dn = bi >= 14 && bi < 19;
  const u64 t_start_ = 0; (void)t_start_;
  STAMP_INIT();

  if (aj < 15 && w >= 4) {
    const int lw = w - 4;
    const unsigned xsw = (unsigned)(4 * (lw & 1) + (lane >> 4));
    const unsigned voff_x = (unsigned)(lane >> 3) * 1280u + (((unsigned)(lane & 7) ^ xsw) << 4);
    const unsigned ch = (unsigned)(lane & 15) ^ (((unsigned)(lane >> 4) << 2) | (unsigned)lw);
    const unsigned voff_w = (unsigned)(lane >> 4) * 3840u + ch * 16u;
    const char* xb = (const char*)p.x + (size_t)(128 * aP + 8 * lw) * 1280;
    const char* wb = (const char*)p.wqkv + (size_t)(4 * lw) * 3840 + 256 * aj;
    const unsigned lbase = lds0 + lw * 1024;
    const int rot = ((bi & 7) * 5 / 4 + (bi >> 3) * 5 / 2) % 10;
    auto issue = [&](int s) {
      const unsigned sl = lbase + (s & 3) * L_SLOT;
      int ss = s + rot; if (ss >= 10) ss -= 10;
#pragma unroll
      for (int i = 0; i < 4; ++i) {
        glds16(voff_x, xb + (size_t)i * 32 * 1280 + ss * 128, sl + i * 4096);
        glds16(voff_w, wb + ((size_t)ss * 64 + i * 16) * 3840, sl + L_W + i * 4096);
      }
    };
    issue(0); issue(1); issue(2); issue(3);
    WAITV(24); BARRIER();
    if (w == 4) STAMP(1);
    WAITV(16); BARRIER();
#pragma unroll 1
    for (int s = 1; s <= 6; ++s) { issue(s + 3); WAITV(16); BARRIER(); }
    WAITV(8); BARRIER();
    WAITV(0); BARRIER();
    BARRIER();
    BARRIER();
  } else if (w < 4) {
    if (w == 0) {
      const u32x2 e = __builtin_amdgcn_raw_buffer_load_b64(rws, WS_EPOCH, 0, SC1);
      const u64 t = (((u64)e[1] << 32) | e[0]) + 1;
      if (lane == 0) *(LAS u32x2*)(L + L_EP) = (u32x2){(unsigned)t, (unsigned)(t >> 32)};
      __builtin_amdgcn_raw_buffer_store_b128((u32x4){0, 0, 0, 0}, rws, WS_STATS + (R * 8 + cc) * 1024 + lane * 16, 0, SC1);
      if (is_gu && lane < 32) __builtin_amdgcn_raw_buffer_store_b128((u32x4){0, 0, 0, 0}, rws, WS_ACT + (((gs >> 1) * 4 + 2 * (gs & 1) + (lane >> 4)) * 8 + (lane & 7)) * 32 + ((lane >> 3) & 1) * 16, 0, SC1);
    }
    if (aj < 15) {
      const int wm = w >> 1, wn = w & 1;
      unsigned xa[4], wa[4];
#pragma unroll
      for (int ks = 0; ks < 4; ++ks) xa[ks] = (unsigned)(64 * wm + l31) * 128u + ((((unsigned)(2 * ks + hh)) ^ (((unsigned)l31 >> 1) & 7u)) << 4);
      const unsigned xo0 = wn * 4096, xo1 = 4096 - xo0;
      {
        const unsigned blk = (lane >> 4) & 1, q = (lane & 15) >> 2, pq = lane & 3;
#pragma unroll
        for (int j = 0; j < 2; ++j)
#pragma unroll
          for (int t = 0; t < 2; ++t) {
            const unsigned jj = j ^ wm;
            const unsigned row = 8 * hh + 4 * t + q, chn = 4 * (2 * wn + jj) + 2 * blk + (pq >> 1), m = (q << 2) | (unsigned)(2 * hh + t);
            wa[j * 2 + t] = L_W + 256u * row + 16u * (chn ^ m) + 8u * (pq & 1);
          }
      }
      f32x16 a00 = {}, a01 = {}, a10 = {}, a11 = {};
      float ssum = 0.f, ssq = 0.f, csum = 0.f;
      bf16x8 xf0[2], wf0[2], xf1[2], wf1[2];
      const unsigned ONE2 = 0x3f803f80u;
      unsigned xb0[4], xb1[4], wb[4];
#define SETSLOT(so) do { _Pragma("unroll") for (int k_ = 0; k_ < 4; ++k_) { xb0[k_] = xa[k_] + (so) + xo0; xb1[k_] = xa[k_] + (so) + xo1; wb[k_] = wa[k_] + (so); } } while (0)
#define RD(XF, WF, ks) do { \
        XF[0] = *(const LAS bf16x8*)(L + xb0[ks]); XF[1] = *(const LAS bf16x8*)(L + xb1[ks]); \
        WF[0] = cat8(trd(L + wb[0] + (ks) * 4096), trd(L + wb[1] + (ks) * 4096)); \
        WF[1] = cat8(trd(L + wb[2] + (ks) * 4096), trd(L + wb[3] + (ks) * 4096)); } while (0)
#define MM(XF, WF) do { \
        a00 = MFMA32(WF[0], XF[0], a00); a01 = MFMA32(WF[1], XF[0], a01); \
        a10 = MFMA32(WF[0], XF[1], a10); a11 = MFMA32(WF[1], XF[1], a11); \
        { const u32x4 xs_ = __builtin_bit_cast(u32x4, XF[0]); const u32x4 ws_ = __builtin_bit_cast(u32x4, WF[0]); \
          _Pragma("unroll") for (int e_ = 0; e_ < 4; ++e_) { ssum = dot2(xs_[e_], ONE2, ssum); ssq = dot2(xs_[e_], xs_[e_], ssq); csum = dot2(ws_[e_], ONE2, csum); } } } while (0)
#define PINALL() asm volatile("" : "+v"(a00), "+v"(a01), "+v"(a10), "+v"(a11), "+v"(ssum), "+v"(ssq), "+v"(csum))
#define ILV() do { _Pragma("unroll") for (int g_ = 0; g_ < 4; ++g_) { \
        __builtin_amdgcn_sched_group_barrier(0x8, 1, 0); __builtin_amdgcn_sched_group_barrier(0x100, 2, 0); __builtin_amdgcn_sched_group_barrier(0x2, 3, 0); } } while (0)
#define STEP(XN, WN, ksn, XC, WC) do { RD(XN, WN, ksn); MM(XC, WC); ILV(); PINALL(); SBAR(); } while (0)
      SETSLOT(0);
      BARRIER();
      RD(xf0, wf0, 0);
      SBAR();
#pragma unroll 1
      for (int s = 0; s < 9; ++s) {
        STEP(xf1, wf1, 1, xf0, wf0);
        STEP(xf0, wf0, 2, xf1, wf1);
        STEP(xf1, wf1, 3, xf0, wf0);
        SETSLOT(((s + 1) & 3) * L_SLOT);
        WAITL0(); BARRIER();
        STEP(xf0, wf0, 0, xf1, wf1);
      }
      STEP(xf1, wf1, 1, xf0, wf0);
      STEP(xf0, wf0, 2, xf1, wf1);
      STEP(xf1, wf1, 3, xf0, wf0);
      MM(xf1, wf1); PINALL();
      if (w == 0) STAMP(2);
      ssum = swap_add(ssum); ssq = swap_add(ssq); csum = swap_add(csum);
      {
        const float mean = ssum * (1.f / D);
        const float var = fmaxf(ssq * (1.f / D) - mean * mean, 0.f);
        const float mb = rbf(mean), vb = rbf(var);
        const float sd = rbf(sqrtf(rbf(vb + 1e-5f)));
        if (lane < 32) {
          *(LAS f32x2*)(L + L_RS + (64 * wm + 32 * wn + l31) * 8) = (f32x2){mb, 1.f / sd};
          *(LAS float*)(L + L_CS + (64 * wn + 32 * wm + l31) * 4) = csum;
        }
      }
      WAITV(0); WAITL0(); BARRIER();
      const float qs = aj < 5 ? 0.22360679774997896f * 1.4426950408889634f : 1.f;
#pragma unroll
      for (int i = 0; i < 2; ++i) {
        const int tok = 64 * wm + 32 * (i ^ wn) + l31;
        const f32x2 rs = *(const LAS f32x2*)(L + L_RS + tok * 8);
        const float sc = rs[1] * qs, nm = -rs[0] * sc;
#pragma unroll
        for (int j = 0; j < 2; ++j) {
          const f32x16& a = i == 0 ? (j == 0 ? a00 : a01) : (j == 0 ? a10 : a11);
          const int f0 = 64 * wn + 32 * (j ^ wm) + 4 * hh;
          const unsigned go = WS_QKV + (unsigned)(32 * aj + (f0 >> 2)) * 16384u + (unsigned)(128 * aP + tok) * 8u;
#pragma unroll
          for (int rq = 0; rq < 4; ++rq) {
            const f32x4 cs = *(const LAS f32x4*)(L + L_CS + (f0 + 8 * rq) * 4);
            const float v0 = fmaf(a[rq * 4 + 0], sc, nm * cs[0]), v1 = fmaf(a[rq * 4 + 1], sc, nm * cs[1]);
            const float v2 = fmaf(a[rq * 4 + 2], sc, nm * cs[2]), v3 = fmaf(a[rq * 4 + 3], sc, nm * cs[3]);
            __builtin_amdgcn_raw_buffer_store_b64((u32x2){pk2(v0, v1), pk2(v2, v3)}, rws, go + rq * 32768, 0, SC1);
          }
        }
      }
      WAITV(0); BARRIER();
    } else {
      WAITV(0);
    }
  }
  if (aj >= 15) { WAITL0(); BARRIER(); }
  const u32x2 tag2 = *(const LAS u32x2*)(L + L_EP);
  const unsigned tag32 = tag2[0] | 1u;
  if (aj < 15 && tid == 0) __builtin_amdgcn_raw_buffer_store_b64(tag2, rws, WS_FLAGA + (aP * 16 + aj) * 128, 0, SC1);
  if (w == 0) STAMP(3);

  f32x16 o = (f32x16){};
  {
    const int t = w;
    if (tid < 4) *(LAS unsigned*)(L + L_ONE + tid * 4) = tid == 0 ? 0x00003f80u : 0u;
    {
      const int sec = lane < 4 ? 0 : (lane < 12 ? 1 : 2);
      const int k = lane < 4 ? lane : (lane < 12 ? lane - 4 : lane - 12);
      const int col = sec * 640 + 20 * hd + ((k & 1) ? 19 : 0);
      int Pn = sec == 0 ? 2 * pp + (k >> 1) : 2 * pp - 2 + (k >> 1);
      const bool need = lane < 20 && Pn >= 0;
      const unsigned fo = WS_FLAGA + (unsigned)((need ? Pn : 0) * 16 + (col >> 7)) * 128;
      unsigned spins = 0;
      for (;;) {
        const u32x2 f = __builtin_amdgcn_raw_buffer_load_b64(rws, fo, 0, SC1);
        if (__all(!need || (f[0] == tag2[0] && f[1] == tag2[1]))) break;
        if (++spins > SPIN_CAP) break;
        __builtin_amdgcn_s_sleep(1);
      }
    }
    asm volatile("" ::: "memory");
    if (w == 0) STAMP(4);
    {
      const char* qb = (const char*)p.ws + WS_QKV;
#pragma unroll
      for (int i = 0; i < 5; ++i) {
        const int pc = w + 8 * i, pl = pc >> 2, qt = pc & 3;
        const int tok0 = 256 * pp - 256 + 128 * qt;
        if (tok0 >= 0) glds16c(lane * 16, qb + (size_t)((pl < 5 ? 160 + 5 * hd + pl : 320 + 5 * hd + pl - 5)) * 16384 + tok0 * 8, lds0 + pl * PL + qt * 1024);
      }
    }
    bf16x8 qf0; bf16x4 qf1;
    {
      const unsigned qo = WS_QKV + (unsigned)(5 * hd) * 16384u + (unsigned)(256 * pp + 32 * t + l31) * 8u;
      const u32x2 a = __builtin_amdgcn_raw_buffer_load_b64(rws, qo + (2 * hh) * 16384, 0, SC1);
      const u32x2 c = __builtin_amdgcn_raw_buffer_load_b64(rws, qo + (2 * hh + 1) * 16384, 0, SC1);
      u32x2 z = __builtin_amdgcn_raw_buffer_load_b64(rws, qo + 4 * 16384, 0, SC1);
      if (hh) z = (u32x2){0, 0};
      qf0 = __builtin_bit_cast(bf16x8, (u32x4){a[0], a[1], c[0], c[1]});
      qf1 = __builtin_bit_cast(bf16x4, z);
    }
    WAITV(0); WAITL0(); BARRIER();
    if (w == 0) STAMP(5);
    {
#pragma unroll 1
      for (int pc = w; pc < 100; pc += 8) {
        const int n = pc * 64 + lane, rho = n / 10, ch = n - rho * 10;
        const int s = rho >> 5, tq = (rho >> 4) & 1, kg = (rho >> 2) & 3, q = rho & 3;
        const int cidx = 4 * s + kg, g2 = cidx / 5, c5 = cidx - 5 * g2;
        const int quad = 10 * g2 + ((tq ? 0x98732 : 0x46510) >> (4 * c5) & 15);
        glds16((unsigned)(4 * quad + q) * 1280u + ch * 16u, (const char*)p.wo + 160 * cc, lds0 + L_WO + pc * 1024);
      }
    }
    const int kad = L_K + l31 * 8;
    float negm;
    {
      const LAS char* kp = L + kad + (t + 8) * 256;
      const u32x2 k0 = *(const LAS u32x2*)(kp + (2 * hh) * PL), k1 = *(const LAS u32x2*)(kp + (2 * hh + 1) * PL), k2 = *(const LAS u32x2*)(kp + 4 * PL);
      const u32x4 q0 = __builtin_bit_cast(u32x4, qf0); const u32x2 q1 = __builtin_bit_cast(u32x2, qf1);
      float d = dot2(q0[0], k0[0], 0.f); d = dot2(q0[1], k0[1], d); d = dot2(q0[2], k1[0], d); d = dot2(q0[3], k1[1], d);
      d = dot2(q1[0], k2[0], d); d = dot2(q1[1], k2[1], d);
      negm = -swap_add(d);
    }
    const int vquad = 4 * ((lane >> 4) & 1) + (lane & 3);
    const int vad = vquad < 5 ? L_V + vquad * PL + (4 * hh + ((lane & 15) >> 2)) * 8 : L_ONE;
    const int vst = vquad < 5 ? 1 : 0;
    auto tile = [&](int kt, const f32x16& cin) {
      const LAS char* kp = L + kad + kt * 256;
      const bf16x4 ka = *(const LAS bf16x4*)(kp + (2 * hh) * PL), kb = *(const LAS bf16x4*)(kp + (2 * hh + 1) * PL), kc = *(const LAS bf16x4*)(kp + 4 * PL);
      const LAS char* vp = L + vad + vst * kt * 256;
      const bf16x8 v0 = cat8(trd(vp), trd(vp + vst * 64)), v1 = cat8(trd(vp + vst * 128), trd(vp + vst * 192));
      f32x16 s = MFMA32(cat8(ka, kb), qf0, cin);
      s = MFMA32K8(kc, qf1, s);
      u32x4 p0, p1;
#pragma unroll
      for (int e = 0; e < 4; ++e) {
        p0[e] = pk2(__builtin_amdgcn_exp2f(s[2 * e]), __builtin_amdgcn_exp2f(s[2 * e + 1]));
        p1[e] = pk2(__builtin_amdgcn_exp2f(s[8 + 2 * e]), __builtin_amdgcn_exp2f(s[8 + 2 * e + 1]));
      }
      o = MFMA32(v0, __builtin_bit_cast(bf16x8, p0), o);
      o = MFMA32(v1, __builtin_bit_cast(bf16x8, p1), o);
    };
    f32x16 cm;
#pragma unroll
    for (int r = 0; r < 16; ++r) { const int kr = (r & 3) + 8 * (r >> 2) + 4 * hh; cm[r] = kr <= l31 ? negm : -INFINITY; }
    tile(t + 8, cm);
#pragma unroll
    for (int r = 0; r < 16; ++r) cm[r] = negm;
    const int lo = pp >= 1 ? t + 1 : 8;
#pragma unroll 1
    for (int kt = t + 7; kt >= lo; --kt) tile(kt, cm);
    if (pp >= 1) {
#pragma unroll
      for (int r = 0; r < 16; ++r) { const int kr = (r & 3) + 8 * (r >> 2) + 4 * hh; cm[r] = kr > l31 ? negm : -INFINITY; }
      tile(t, cm);
    }
    if (w == 0) STAMP(6);
    auto r = __builtin_amdgcn_permlane32_swap(__float_as_uint(o[8]), __float_as_uint(o[8]), false, false);
    const float inv = 1.f / __uint_as_float(r[1]);
    u32x4 c0;
    c0[0] = pk2(o[0] * inv, o[1] * inv); c0[1] = pk2(o[2] * inv, o[3] * inv); c0[2] = pk2(o[4] * inv, o[5] * inv); c0[3] = pk2(o[6] * inv, o[7] * inv);
    const u32x2 c1 = (u32x2){pk2(o[8] * inv, o[9] * inv), pk2(o[10] * inv, o[11] * inv)};
    const int tt = pp * 16 + 2 * t + (l31 >> 4);
    const unsigned base = WS_O + (unsigned)tt * 80 * 256 + (l31 & 15) * 16 + (unsigned)(5 * (hd >> 1)) * 256;
    __builtin_amdgcn_raw_buffer_store_b128(c0, rws, base + (2 * (hd & 1) + hh) * 256, 0, SC1);
    if (hh == 0) __builtin_amdgcn_raw_buffer_store_b64(c1, rws, base + 4 * 256 + (hd & 1) * 8, 0, SC1);
    WAITV(0);
    if (lane == 0) __builtin_amdgcn_raw_buffer_store_b64(tag2, rws, WS_FLAGO + (((pp * 8 + t) * 2 + (hd >> 4)) * 16 + (hd & 15)) * 8, 0, SC1);
  }
  if (w == 0) STAMP(7);
  BARRIER();

  const int nt = w & 3, kh = w >> 2;
  const int tok = 64 * R + 16 * nt + l15;
  u32x2 xr[5], wrv[5];
  float swr = 0.f;
  if (kh == 0) {
#pragma unroll
    for (int ft = 0; ft < 5; ++ft) {
      xr[ft] = *(const u32x2*)(p.x + (size_t)tok * D + 80 * cc + 16 * ft + 4 * fg);
      wrv[ft] = *(const u32x2*)(p.wr + 80 * cc + 16 * ft + 4 * fg);
    }
    const unsigned* wr32 = (const unsigned*)p.wr;
#pragma unroll
    for (int i = 0; i < 5; ++i) swr = dot2(wr32[lane + 64 * i], 0x3f803f80u, swr);
    swr = wave_fsum(swr);
  }
  f32x4 acc[5];
#pragma unroll
  for (int ft = 0; ft < 5; ++ft) acc[ft] = (f32x4){0, 0, 0, 0};
  {
    const int tg = 4 * R + nt;
    const unsigned fo = WS_FLAGO + ((((tg >> 4) * 8 + ((tg >> 1) & 7)) * 2 + kh) * 16 + l15) * 8;
    unsigned spins = 0;
    for (;;) {
      const u32x2 f = __builtin_amdgcn_raw_buffer_load_b64(rws, fo, 0, SC1);
      if (__all(f[0] == tag2[0] && f[1] == tag2[1])) break;
      if (++spins > SPIN_CAP) break;
      __builtin_amdgcn_s_sleep(1);
    }
    asm volatile("" ::: "memory");
    if (w == 0) STAMP(8);
    u32x4 of[10];
#pragma unroll
    for (int s = 0; s < 10; ++s) of[s] = __builtin_amdgcn_raw_buffer_load_b128(rws, WS_O + (unsigned)(tg * 80 + 4 * (10 * kh + s)) * 256 + lane * 16, 0, SC1);
    const LAS char* wp = L + L_WO + (unsigned)(kh * 10 * 32 + fg * 4 + ((lane & 15) >> 2)) * 160 + (lane & 3) * 8;
#pragma unroll
    for (int s = 0; s < 10; ++s) {
#pragma unroll
      for (int ft = 0; ft < 5; ++ft) {
        const bf16x8 a = cat8(trd(wp + (s * 32) * 160 + ft * 32), trd(wp + (s * 32 + 16) * 160 + ft * 32));
        acc[ft] = MFMA16(a, __builtin_bit_cast(bf16x8, of[s]), acc[ft]);
      }
    }
  }
  if (w == 0) STAMP(9);
  if (kh == 1) {
#pragma unroll
    for (int ft = 0; ft < 5; ++ft) *(LAS f32x4*)(L + L_RED + ((nt * 5 + ft) * 64 + lane) * 16) = acc[ft];
  }
  WAITL0(); BARRIER();
  unsigned hp[10];
  float mb = 0.f, inv_sd = 0.f;
  if (kh == 1) {
    const int rl = lane >> 1, grow = 8 * ((rl >> 2) & 3) + 4 * (rl >> 4) + (rl & 3);
    if (is_gu) {
      const unsigned vo = (unsigned)grow * (F * 2) + (lane & 1) * 16;
#pragma unroll 1
      for (int pc = nt; pc < 40; pc += 4) { const int m = pc >= 20, s = pc - 20 * m; glds16(vo, (const char*)(m ? p.wu : p.wg) + (size_t)s * 32 * F * 2 + gs * 32, lds0 + L_FW + pc * 1024); }
    } else if (is_dn) {
      const unsigned vo = (unsigned)grow * (D * 2) + (lane & 1) * 16;
#pragma unroll 1
      for (int pc = nt; pc < 54; pc += 4) glds16(vo, (const char*)p.wd + (size_t)pc * 32 * D * 2 + dn * 32, lds0 + L_FW + pc * 1024);
    }
  } else {
    float s1 = 0.f, s2 = 0.f, s3 = 0.f;
#pragma unroll
    for (int ft = 0; ft < 5; ++ft) {
      const f32x4 o2 = *(const LAS f32x4*)(L + L_RED + ((nt * 5 + ft) * 64 + lane) * 16);
      const unsigned a01 = pk2(acc[ft][0] + o2[0], acc[ft][1] + o2[1]), a23 = pk2(acc[ft][2] + o2[2], acc[ft][3] + o2[3]);
      const unsigned h01 = pk2(bf2f(xr[ft][0]) + bf2f(a01), bfhi(xr[ft][0]) + bfhi(a01)), h23 = pk2(bf2f(xr[ft][1]) + bf2f(a23), bfhi(xr[ft][1]) + bfhi(a23));
      hp[2 * ft] = h01; hp[2 * ft + 1] = h23;
      __builtin_amdgcn_raw_buffer_store_b64((u32x2){h01, h23}, rws, WS_H + (unsigned)tok * 1280 + (80 * cc + 16 * ft + 4 * fg) * 2, 0, SC1);
      s1 = dot2(h01, 0x3f803f80u, s1); s1 = dot2(h23, 0x3f803f80u, s1);
      s2 = dot2(h01, h01, s2); s2 = dot2(h23, h23, s2);
      s3 = dot2(h01, wrv[ft][0], s3); s3 = dot2(h23, wrv[ft][1], s3);
    }
    s1 = swap_add(swap16_add(s1)); s2 = swap_add(swap16_add(s2)); s3 = swap_add(swap16_add(s3));
    if (lane < 16) __builtin_amdgcn_raw_buffer_store_b128((u32x4){__float_as_uint(s1), __float_as_uint(s2), __float_as_uint(s3), tag32}, rws, WS_STATS + ((R * 8 + cc) * 64 + 16 * nt + l15) * 16, 0, SC1);
    if (w == 0) STAMP(10);
    float t1, t2, t3;
    {
      const unsigned so = WS_STATS + ((R * 8 + fg) * 64 + 16 * nt + l15) * 16;
      unsigned spins = 0;
      for (;;) {
        const u32x4 ga = __builtin_amdgcn_raw_buffer_load_b128(rws, so, 0, SC1), gb = __builtin_amdgcn_raw_buffer_load_b128(rws, so + 4 * 1024, 0, SC1);
        t1 = __uint_as_float(ga[0]) + __uint_as_float(gb[0]); t2 = __uint_as_float(ga[1]) + __uint_as_float(gb[1]); t3 = __uint_as_float(ga[2]) + __uint_as_float(gb[2]);
        if (__all(ga[3] == tag32 && gb[3] == tag32)) break;
        if (++spins > SPIN_CAP) break;
        __builtin_amdgcn_s_sleep(1);
      }
    }
    t1 = swap_add(swap16_add(t1)); t2 = swap_add(swap16_add(t2)); t3 = swap_add(swap16_add(t3));
    const float mean = t1 * (1.f / D);
    const float var = fmaxf(t2 * (1.f / D) - mean * mean, 0.f);
    mb = rbf(mean);
    const float sd = rbf(sqrtf(rbf(rbf(var) + 1e-5f)));
    inv_sd = 1.f / sd;
    const unsigned rb = pk2((t3 - mb * swr) * inv_sd, 0.f) & 0xffffu;
    const unsigned key = ((rb ^ ((rb & 0x8000u) ? 0xffffu : 0x8000u)) << 16) | (unsigned)(2047 - tok);
    if (lane < 16) {
      *(LAS unsigned*)(L + L_KEY + (16 * nt + l15) * 4) = key;
      *(LAS unsigned*)(L + L_MS + (16 * nt + l15) * 4) = (__float_as_uint(mb) & 0xffff0000u) | (__float_as_uint(sd) >> 16);
    }
    WAITV(0);
    if (w == 0) STAMP(11);
  }
  WAITL0(); BARRIER();
  if (w == 0) {
    unsigned k = *(const LAS unsigned*)(L + L_KEY + lane * 4), m = 0;
    for (int i = 0; i <= cc; ++i) { m = wave_umax(k); if (k == m) k = 0; }
    const unsigned ms = *(const LAS unsigned*)(L + L_MS + ((2047 - (int)(m & 0x7ff)) & 63) * 4);
    if (lane == 0) __builtin_amdgcn_raw_buffer_store_b128((u32x4){m, tag2[0], ms, tag2[1]}, rws, WS_CAND + (R * 8 + cc) * 16, 0, SC1);
    STAMP(12);
    unsigned k0, k1, k2, k3, m0, m1, m2, m3;
    unsigned spins = 0;
    for (;;) {
      const u32x4 a = __builtin_amdgcn_raw_buffer_load_b128(rws, WS_CAND + lane * 16, 0, SC1), bb = __builtin_amdgcn_raw_buffer_load_b128(rws, WS_CAND + 1024 + lane * 16, 0, SC1);
      const u32x4 c = __builtin_amdgcn_raw_buffer_load_b128(rws, WS_CAND + 2048 + lane * 16, 0, SC1), d = __builtin_amdgcn_raw_buffer_load_b128(rws, WS_CAND + 3072 + lane * 16, 0, SC1);
      k0 = a[0]; k1 = bb[0]; k2 = c[0]; k3 = d[0]; m0 = a[2]; m1 = bb[2]; m2 = c[2]; m3 = d[2];
      const bool ok = a[1] == tag2[0] && a[3] == tag2[1] && bb[1] == tag2[0] && bb[3] == tag2[1] && c[1] == tag2[0] && c[3] == tag2[1] && d[1] == tag2[0] && d[3] == tag2[1];
      if (__all(ok)) break;
      if (++spins > SPIN_CAP) break;
      __builtin_amdgcn_s_sleep(1);
    }
    STAMP(13);
#pragma unroll 1
    for (int i = 0; i < 8; ++i) {
      const unsigned lm = max(max(k0, k1), max(k2, k3));
      const unsigned g = wave_umax(lm);
      const unsigned ms2 = k0 == g ? m0 : (k1 == g ? m1 : (k2 == g ? m2 : m3));
      if (lm == g) { *(LAS unsigned*)(L + L_SEL + i * 4) = g; *(LAS unsigned*)(L + L_SEL + 32 + i * 4) = ms2; }
      k0 = k0 == g ? 0 : k0; k1 = k1 == g ? 0 : k1; k2 = k2 == g ? 0 : k2; k3 = k3 == g ? 0 : k3;
    }
    if (b == 0 && lane == 0) __builtin_amdgcn_raw_buffer_store_b64(tag2, rws, WS_EPOCH, 0, SC1);
    STAMP(14);
  }
  if (kh == 1) WAITV(0);
  WAITL0(); BARRIER();

  const unsigned selk = *(const LAS unsigned*)(L + L_SEL + (lane & 7) * 4), selm = *(const LAS unsigned*)(L + L_SEL + 32 + (lane & 7) * 4);
  auto out_rows = [&]() {
    if (kh == 0) {
      bool sel = false;
#pragma unroll
      for (int i = 0; i < 8; ++i) sel |= (2047 - (int)(__builtin_amdgcn_readlane((int)selk, i) & 0x7ff)) == tok;
      if (!sel) {
#pragma unroll
        for (int ft = 0; ft < 5; ++ft) {
          unsigned r2[2];
#pragma unroll
          for (int e = 0; e < 2; ++e) {
            const unsigned hv = hp[2 * ft + e];
            const unsigned d2 = pk2(bf2f(hv) - mb, bfhi(hv) - mb);
            const unsigned n2 = pk2(bf2f(d2) * inv_sd, bfhi(d2) * inv_sd);
            r2[e] = pk2(bf2f(hv) + bf2f(n2), bfhi(hv) + bfhi(n2));
          }
          *(u32x2*)(p.out + (size_t)tok * D + 80 * cc + 16 * ft + 4 * fg) = (u32x2){r2[0], r2[1]};
        }
      }
    }
  };
  if (is_gu) {
    {
      const unsigned kk = *(const LAS unsigned*)(L + L_SEL + w * 4), ms = *(const LAS unsigned*)(L + L_SEL + 32 + w * 4);
      const int row = 2047 - (int)(kk & 0x7ff);
      const float m = __uint_as_float(ms & 0xffff0000u), iv = 1.f / __uint_as_float(ms << 16);
      const u32x4 h0 = __builtin_amdgcn_raw_buffer_load_b128(rws, WS_H + (unsigned)row * 1280 + lane * 16, 0, SC1);
      const u32x4 h1 = __builtin_amdgcn_raw_buffer_load_b128(rws, WS_H + (unsigned)row * 1280 + 1024 + (lane & 15) * 16, 0, SC1);
      u32x4 n0, n1;
#pragma unroll
      for (int e = 0; e < 4; ++e) {
        const unsigned d0 = pk2(bf2f(h0[e]) - m, bfhi(h0[e]) - m), d1 = pk2(bf2f(h1[e]) - m, bfhi(h1[e]) - m);
        n0[e] = pk2(bf2f(d0) * iv, bfhi(d0) * iv); n1[e] = pk2(bf2f(d1) * iv, bfhi(d1) * iv);
      }
      *(LAS u32x4*)(L + L_XS + w * 1296 + lane * 16) = n0;
      if (lane < 16) *(LAS u32x4*)(L + L_XS + w * 1296 + 1024 + lane * 16) = n1;
    }
    WAITL0(); BARRIER();
    if (w == 0) STAMP(15);
    {
      const int m = w & 1, kq = w >> 1;
      f32x4 a4 = {0, 0, 0, 0};
#pragma unroll
      for (int i = 0; i < 5; ++i) {
        const int s = 5 * kq + i;
        const LAS char* ap = L + L_FW + (m * 20 + s) * 1024 + lane * 8;
        const bf16x8 af = cat8(trd(ap), trd(ap + 512));
        const bf16x8 bfv = *(const LAS bf16x8*)(L + L_XS + (lane & 7) * 1296 + s * 64 + fg * 16);
        a4 = MFMA16(af, bfv, a4);
      }
      *(LAS f32x4*)(L + L_FRED + (w * 64 + lane) * 16) = a4;
    }
    WAITL0(); BARRIER();
    if (w == 0) {
      f32x4 g4 = {0, 0, 0, 0}, u4 = {0, 0, 0, 0};
#pragma unroll
      for (int q = 0; q < 4; ++q) { g4 += *(const LAS f32x4*)(L + L_FRED + ((2 * q) * 64 + lane) * 16); u4 += *(const LAS f32x4*)(L + L_FRED + ((2 * q + 1) * 64 + lane) * 16); }
      float a[4];
#pragma unroll
      for (int e = 0; e < 4; ++e) { const float g = rbf(g4[e]), u = rbf(u4[e]); const float sg = rbf(g / (1.f + __expf(-g))); a[e] = sg * u; }
      if (l15 < 8) __builtin_amdgcn_raw_buffer_store_b128((u32x4){pk2(a[0], a[1]), tag32, pk2(a[2], a[3]), tag32}, rws, WS_ACT + (((gs >> 1) * 4 + 2 * (gs & 1) + (fg >> 1)) * 8 + l15) * 32 + (fg & 1) * 16, 0, SC1);
      STAMP(16);
    }
    out_rows();
  } else if (is_dn) {
    out_rows();
    const int row = 2047 - (int)(selk & 0x7ff);
    const u32x2 hv = __builtin_amdgcn_raw_buffer_load_b64(rws, WS_H + (unsigned)row * 1280 + (16 * dn + 4 * fg) * 2, 0, SC1);
    f32x4 a4 = {0, 0, 0, 0};
    {
      u32x4 g0[7], g1[7];
      unsigned spins = 0;
      for (;;) {
        bool ok = true;
#pragma unroll
        for (int i = 0; i < 7; ++i) {
          const int s = min(w + 8 * i, 53);
          const unsigned ao = WS_ACT + ((s * 4 + fg) * 8 + (lane & 7)) * 32;
          g0[i] = __builtin_amdgcn_raw_buffer_load_b128(rws, ao, 0, SC1); g1[i] = __builtin_amdgcn_raw_buffer_load_b128(rws, ao + 16, 0, SC1);
        }
#pragma unroll
        for (int i = 0; i < 7; ++i) ok = ok && g0[i][1] == tag32 && g0[i][3] == tag32 && g1[i][1] == tag32 && g1[i][3] == tag32;
        if (__all(ok)) break;
        if (++spins > SPIN_CAP) break;
        __builtin_amdgcn_s_sleep(1);
      }
      if (w == 0) STAMP(17);
#pragma unroll
      for (int i = 0; i < 7; ++i) {
        const int s = w + 8 * i;
        if (s < 54) {
          const LAS char* ap = L + L_FW + s * 1024 + lane * 8;
          const bf16x8 af = cat8(trd(ap), trd(ap + 512));
          a4 = MFMA16(af, __builtin_bit_cast(bf16x8, (u32x4){g0[i][0], g0[i][2], g1[i][0], g1[i][2]}), a4);
        }
      }
    }
    *(LAS f32x4*)(L + L_FRED + (w * 64 + lane) * 16) = a4;
    WAITL0(); BARRIER();
    if (w == 0) {
      f32x4 y = {0, 0, 0, 0};
#pragma unroll
      for (int q = 0; q < 8; ++q) y += *(const LAS f32x4*)(L + L_FRED + (q * 64 + lane) * 16);
      const unsigned rk = selk >> 16, rbits = (rk & 0x8000u) ? (rk ^ 0x8000u) : (rk ^ 0xffffu);
      const float rv = bf2f(rbits), sig = rbf(1.f / (1.f + __expf(-rv)));
      const float m = __uint_as_float(selm & 0xffff0000u), iv = 1.f / __uint_as_float(selm << 16);
      unsigned r2[2];
#pragma unroll
      for (int e = 0; e < 2; ++e) {
        const unsigned d2 = pk2(bf2f(hv[e]) - m, bfhi(hv[e]) - m);
        const unsigned n2 = pk2(bf2f(d2) * iv, bfhi(d2) * iv);
        const unsigned y2 = pk2(rbf(y[2 * e]) * sig, rbf(y[2 * e + 1]) * sig);
        const unsigned t2 = pk2(bf2f(n2) + bf2f(y2), bfhi(n2) + bfhi(y2));
        r2[e] = pk2(bf2f(hv[e]) + bf2f(t2), bfhi(hv[e]) + bfhi(t2));
      }
      if (l15 < 8) *(u32x2*)(p.out + (size_t)row * D + 16 * dn + 4 * fg) = (u32x2){r2[0], r2[1]};
      STAMP(18);
    }
  } else {
    out_rows();
  }
  if (w == 0) STAMP(19);
  STAMP_FLUSH();
}

extern "C" void kernel_launch(void* const* d_in, const int* in_sizes, int n_in, void* d_out, int out_size, void* d_ws, size_t ws_size, hipStream_t stream) {
  Params p;
  p.x = (const u16*)d_in[0]; p.wqkv = (const u16*)d_in[1]; p.wo = (const u16*)d_in[2]; p.wr = (const u16*)d_in[4];
  p.wg = (const u16*)d_in[5]; p.wu = (const u16*)d_in[6]; p.wd = (const u16*)d_in[7]; p.out = (u16*)d_out; p.ws = (unsigned char*)d_ws;
  fused_kernel<<<256, 512, 0, stream>>>(p);
}
```

```cpp
#define PF_CODE
#define PF_TLB
#include <hip/hip_runtime.h>
#include <stdint.h>

typedef unsigned short u16;
typedef short bf16x8 __attribute__((ext_vector_type(8)));
typedef short bf16x4 __attribute__((ext_vector_type(4)));
typedef __bf16 bf2 __attribute__((ext_vector_type(2)));
typedef float f32x16 __attribute__((ext_vector_type(16)));
typedef float f32x4 __attribute__((ext_vector_type(4)));
typedef float f32x2 __attribute__((ext_vector_type(2)));
typedef unsigned u32x4 __attribute__((ext_vector_type(4)));
typedef unsigned u32x2 __attribute__((ext_vector_type(2)));
typedef unsigned long long u64;
#define LAS __attribute__((address_space(3)))
#define SBAR() __builtin_amdgcn_sched_barrier(0)
#define BARRIER() asm volatile("s_barrier" ::: "memory")
#define WAITL0() asm volatile("s_waitcnt lgkmcnt(0)" ::: "memory")
#define WAITV(n) asm volatile("s_waitcnt vmcnt(" #n ")" ::: "memory")

constexpr int S = 2048, D = 640, F = 1728;
constexpr int SC1 = 16;

constexpr unsigned WS_EPOCH = 0;
constexpr unsigned WS_FLAGA = 4096;
constexpr unsigned WS_FLAGO = WS_FLAGA + 32768;
constexpr unsigned WS_CAND = WS_FLAGO + 16384;
constexpr unsigned WS_ACT = WS_CAND + 4096;
constexpr unsigned WS_STATS = 131072;
constexpr unsigned WS_QKV = 524288;
constexpr unsigned WS_O = WS_QKV + 480 * 16384;
constexpr unsigned WS_H = WS_O + S * D * 2;
#define STAMP_OFF (200u << 20)
#define NSTAMP 32

constexpr int L_SLOT = 32768, L_W = 16384;
constexpr int PL = 4128;
constexpr int L_K = 0, L_V = 5 * PL, L_ONE = 10 * PL;
constexpr int L_WO = 43008;
constexpr int L_RED = 0;
constexpr int L_FW = 43008;
constexpr int L_XS = 0;
constexpr int L_FRED = 12288;
constexpr int L_MISC = 147456;
constexpr int L_RS = L_MISC;
constexpr int L_CS = L_MISC + 1024;
constexpr int L_EP = L_MISC + 1536;
constexpr int L_KEY = L_MISC + 2048;
constexpr int L_MS = L_MISC + 2304;
constexpr int L_SEL = L_MISC + 2560;
constexpr int L_STAMP = L_MISC + 4096;
constexpr int LDS_BYTES = 163840;

struct Params { const u16* x; const u16* wqkv; const u16* wo; const u16* wr; const u16* wg; const u16* wu; const u16* wd; u16* out; unsigned char* ws; };

__device__ __forceinline__ float bf2f(unsigned v) { return __uint_as_float(v << 16); }
__device__ __forceinline__ float bfhi(unsigned v) { return __uint_as_float(v & 0xffff0000u); }
__device__ __forceinline__ unsigned pk2(float a, float b) { f32x2 f = {a, b}; return __builtin_bit_cast(unsigned, __builtin_convertvector(f, bf2)); }
__device__ __forceinline__ float rbf(float f) { return __uint_as_float(pk2(f, 0.f) << 16); }
__device__ __forceinline__ float dot2(unsigned a, unsigned b, float c) { return __builtin_amdgcn_fdot2_f32_bf16(__builtin_bit_cast(bf2, a), __builtin_bit_cast(bf2, b), c, false); }
__device__ __forceinline__ float swap_add(float v) { auto r = __builtin_amdgcn_permlane32_swap(__float_as_uint(v), __float_as_uint(v), false, false); return __uint_as_float(r[0]) + __uint_as_float(r[1]); }
__device__ __forceinline__ float swap16_add(float v) { auto r = __builtin_amdgcn_permlane16_swap(__float_as_uint(v), __float_as_uint(v), false, false); return __uint_as_float(r[0]) + __uint_as_float(r[1]); }
__device__ __forceinline__ void glds16(unsigned voff, const void* sbase, unsigned lds) {
  asm volatile("s_mov_b32 m0, %2\n\ts_nop 0\n\tglobal_load_lds_dwordx4 %0, %1" ::"v"(voff), "s"(sbase), "s"(lds) : "memory");
}
__device__ __forceinline__ void glds16c(unsigned voff, const void* sbase, unsigned lds) {
  asm volatile("s_mov_b32 m0, %2\n\ts_nop 0\n\tglobal_load_lds_dwordx4 %0, %1 sc1" ::"v"(voff), "s"(sbase), "s"(lds) : "memory");
}
__device__ __forceinline__ void glds4(unsigned voff, const void* sbase, unsigned lds) {
  asm volatile("s_mov_b32 m0, %2\n\ts_nop 0\n\tglobal_load_lds_dword %0, %1 sc1" ::"v"(voff), "s"(sbase), "s"(lds) : "memory");
}
__device__ __forceinline__ bf16x4 trd(const LAS char* p) { return __builtin_amdgcn_ds_read_tr16_b64_v4i16((LAS bf16x4*)p); }
__device__ __forceinline__ bf16x8 cat8(bf16x4 a, bf16x4 b) { return (bf16x8){a[0], a[1], a[2], a[3], b[0], b[1], b[2], b[3]}; }
#define MFMA32(a, b, c) __builtin_amdgcn_mfma_f32_32x32x16_bf16(a, b, c, 0, 0, 0)
#define MFMA32K8(a, b, c) __builtin_amdgcn_mfma_f32_32x32x8bf16_1k(a, b, c, 0, 0, 0)
#define MFMA16(a, b, c) __builtin_amdgcn_mfma_f32_16x16x32_bf16(a, b, c, 0, 0, 0)
template <int CTRL, int RMASK> __device__ __forceinline__ unsigned dppmax(unsigned v) { return max(v, (unsigned)__builtin_amdgcn_update_dpp(0, (int)v, CTRL, RMASK, 0xf, false)); }
__device__ __forceinline__ unsigned wave_umax(unsigned v) {
  v = dppmax<0x111, 0xf>(v); v = dppmax<0x112, 0xf>(v); v = dppmax<0x114, 0xf>(v); v = dppmax<0x118, 0xf>(v);
  v = dppmax<0x142, 0xa>(v); v = dppmax<0x143, 0xc>(v);
  return (unsigned)__builtin_amdgcn_readlane((int)v, 63);
}

template <int CTRL> __device__ __forceinline__ float dppadd(float v) { return v + __int_as_float(__builtin_amdgcn_update_dpp(0, __float_as_int(v), CTRL, 0xf, 0xf, false)); }
__device__ __forceinline__ float wave_fsum(float v) { v = dppadd<0x128>(v); v = dppadd<0x124>(v); v = dppadd<0x122>(v); v = dppadd<0x121>(v); return swap_add(swap16_add(v)); }

#define STAMP(k) do {} while (0)
#define STAMP_INIT() do {} while (0)
#define STAMP_FLUSH() do {} while (0)

constexpr unsigned SPIN_CAP = 1u << 16;

__global__ __launch_bounds__(512, 2) void fused_kernel(Params p) {
  __shared__ __attribute__((aligned(16))) char smem[LDS_BYTES];
  LAS char* L = (LAS char*)smem;
  const unsigned lds0 = (unsigned)(uintptr_t)smem;
  const int tid = threadIdx.x, lane = tid & 63;
  const int w = __builtin_amdgcn_readfirstlane(tid >> 6);
  const int b = blockIdx.x, xcd = b & 7, bi = b >> 3;
  const int l31 = lane & 31, hh = lane >> 5, l15 = lane & 15, fg = lane >> 4;
  const __amdgpu_buffer_rsrc_t rws = __builtin_amdgcn_make_buffer_rsrc(p.ws, 0, 0x7fffffff, 0x00020000);
  const int aP = 4 * (xcd >> 1) + (bi >> 3), aj = 8 * (xcd & 1) + (bi & 7);
  const int hd = bi, pp = xcd;
  const int R = 4 * xcd + (bi >> 3), cc = bi & 7;
  const int gs = xcd * 14 + bi;
  const bool is_gu = bi < 14 && gs < 108;
  const int dn = xcd * 5 + (bi - 14);
  const bool is_dn = bi >= 14 && bi < 19;
  const u64 t_start_ = 0; (void)t_start_;
  STAMP_INIT();

  if (aj < 15 && w >= 4) {
    const int lw = w - 4;
    const unsigned xsw = (unsigned)(4 * (lw & 1) + (lane >> 4));
    const unsigned voff_x = (unsigned)(lane >> 3) * 1280u + (((unsigned)(lane & 7) ^ xsw) << 4);
    const unsigned ch = (unsigned)(lane & 15) ^ (((unsigned)(lane >> 4) << 2) | (unsigned)lw);
    const unsigned voff_w = (unsigned)(lane >> 4) * 3840u + ch * 16u;
    const char* xb = (const char*)p.x + (size_t)(128 * aP + 8 * lw) * 1280;
    const char* wb = (const char*)p.wqkv + (size_t)(4 * lw) * 3840 + 256 * aj;
    const unsigned lbase = lds0 + lw * 1024;
    const int rot = ((bi & 7) * 5 / 4 + (bi >> 3) * 5 / 2) % 10;
    auto issue = [&](int s) {
      const unsigned sl = lbase + (s & 3) * L_SLOT;
      int ss = s + rot; if (ss >= 10) ss -= 10;
#pragma unroll
      for (int i = 0; i < 4; ++i) {
        glds16(voff_x, xb + (size_t)i * 32 * 1280 + ss * 128, sl + i * 4096);
        glds16(voff_w, wb + ((size_t)ss * 64 + i * 16) * 3840, sl + L_W + i * 4096);
      }
    };
    issue(0); issue(1); issue(2); issue(3);
    WAITV(24); BARRIER();
    if (w == 4) STAMP(1);
    WAITV(16); BARRIER();
#pragma unroll 1
    for (int s = 1; s <= 6; ++s) { issue(s + 3); WAITV(16); BARRIER(); }
    WAITV(8); BARRIER();
    WAITV(0); BARRIER();
    BARRIER();
    BARRIER();
  } else if (w < 4) {
    if (w == 0) {
      const u32x2 e = __builtin_amdgcn_raw_buffer_load_b64(rws, WS_EPOCH, 0, SC1);
      const u64 t = (((u64)e[1] << 32) | e[0]) + 1;
      if (lane == 0) *(LAS u32x2*)(L + L_EP) = (u32x2){(unsigned)t, (unsigned)(t >> 32)};
      __builtin_amdgcn_raw_buffer_store_b128((u32x4){0, 0, 0, 0}, rws, WS_STATS + (R * 8 + cc) * 1024 + lane * 16, 0, SC1);
      if (is_gu && lane < 32) __builtin_amdgcn_raw_buffer_store_b128((u32x4){0, 0, 0, 0}, rws, WS_ACT + (((gs >> 1) * 4 + 2 * (gs & 1) + (lane >> 4)) * 8 + (lane & 7)) * 32 + ((lane >> 3) & 1) * 16, 0, SC1);
    }

    if (w == 1) {
      u64 pc; unsigned len;
      asm volatile("s_getpc_b64 %0\nLfk_here:\n\ts_mov_b32 %1, Lfk_code_end-Lfk_here" : "=s"(pc), "=s"(len));
      const char* cb = (const char*)(pc & ~(u64)127);
      for (unsigned o = 0; o < len; o += 8192) { const unsigned vo = min(o + lane * 128u, len - 4u) & ~3u; glds4(vo, cb, lds0 + L_MISC + 8192); }
    }
    if (w == 2) {
      const unsigned vo = lane * 65536u;
      if (lane < 13) glds4(vo, p.wo, lds0 + L_MISC + 8192);
      if (lane < 34) { glds4(vo, p.wg, lds0 + L_MISC + 8192); glds4(vo, p.wu, lds0 + L_MISC + 8192); glds4(vo, p.wd, lds0 + L_MISC + 8192); }
      if (lane < 1) glds4(vo, p.wr, lds0 + L_MISC + 8192);
      if (lane < 40) glds4(vo, p.out, lds0 + L_MISC + 8192);
    }
    if (w == 3) {
      const unsigned vo = lane * 65536u + 3968u;
      for (int i = 0; i < 4; ++i) if (lane + 64 * i < 208) glds4(vo, p.ws + (size_t)i * 64 * 65536, lds0 + L_MISC + 8192);
    }
    if (aj < 15) {
      const int wm = w >> 1, wn = w & 1;
      unsigned xa[4], wa[4];
#pragma unroll
      for (int ks = 0; ks < 4; ++ks) xa[ks] = (unsigned)(64 * wm + l31) * 128u + ((((unsigned)(2 * ks + hh)) ^ (((unsigned)l31 >> 1) & 7u)) << 4);
      const unsigned xo0 = wn * 4096, xo1 = 4096 - xo0;
      {
        const unsigned blk = (lane >> 4) & 1, q = (lane & 15) >> 2, pq = lane & 3;
#pragma unroll
        for (int j = 0; j < 2; ++j)
#pragma unroll
          for (int t = 0; t < 2; ++t) {
            const unsigned jj = j ^ wm;
            const unsigned row = 8 * hh + 4 * t + q, chn = 4 * (2 * wn + jj) + 2 * blk + (pq >> 1), m = (q << 2) | (unsigned)(2 * hh + t);
            wa[j * 2 + t] = L_W + 256u * row + 16u * (chn ^ m) + 8u * (pq & 1);
          }
      }
      f32x16 a00 = {}, a01 = {}, a10 = {}, a11 = {};
      float ssum = 0.f, ssq = 0.f, csum = 0.f;
      bf16x8 xf0[2], wf0[2], xf1[2], wf1[2];
      const unsigned ONE2 = 0x3f803f80u;
      unsigned xb0[4], xb1[4], wb[4];
#define SETSLOT(so) do { _Pragma("unroll") for (int k_ = 0; k_ < 4; ++k_) { xb0[k_] = xa[k_] + (so) + xo0; xb1[k_] = xa[k_] + (so) + xo1; wb[k_] = wa[k_] + (so); } } while (0)
#define RD(XF, WF, ks) do { \
        XF[0] = *(const LAS bf16x8*)(L + xb0[ks]); XF[1] = *(const LAS bf16x8*)(L + xb1[ks]); \
        WF[0] = cat8(trd(L + wb[0] + (ks) * 4096), trd(L + wb[1] + (ks) * 4096)); \
        WF[1] = cat8(trd(L + wb[2] + (ks) * 4096), trd(L + wb[3] + (ks) * 4096)); } while (0)
#define MM(XF, WF) do { \
        a00 = MFMA32(WF[0], XF[0], a00); a01 = MFMA32(WF[1], XF[0], a01); \
        a10 = MFMA32(WF[0], XF[1], a10); a11 = MFMA32(WF[1], XF[1], a11); \
        { const u32x4 xs_ = __builtin_bit_cast(u32x4, XF[0]); const u32x4 ws_ = __builtin_bit_cast(u32x4, WF[0]); \
          _Pragma("unroll") for (int e_ = 0; e_ < 4; ++e_) { ssum = dot2(xs_[e_], ONE2, ssum); ssq = dot2(xs_[e_], xs_[e_], ssq); csum = dot2(ws_[e_], ONE2, csum); } } } while (0)
#define PINALL() asm volatile("" : "+v"(a00), "+v"(a01), "+v"(a10), "+v"(a11), "+v"(ssum), "+v"(ssq), "+v"(csum))
#define ILV() do { _Pragma("unroll") for (int g_ = 0; g_ < 4; ++g_) { \
        __builtin_amdgcn_sched_group_barrier(0x8, 1, 0); __builtin_amdgcn_sched_group_barrier(0x100, 2, 0); __builtin_amdgcn_sched_group_barrier(0x2, 3, 0); } } while (0)
#define STEP(XN, WN, ksn, XC, WC) do { RD(XN, WN, ksn); MM(XC, WC); ILV(); PINALL(); SBAR(); } while (0)
      SETSLOT(0);
      BARRIER();
      RD(xf0, wf0, 0);
      SBAR();
#pragma unroll 1
      for (int s = 0; s < 9; ++s) {
        STEP(xf1, wf1, 1, xf0, wf0);
        STEP(xf0, wf0, 2, xf1, wf1);
        STEP(xf1, wf1, 3, xf0, wf0);
        SETSLOT(((s + 1) & 3) * L_SLOT);
        WAITL0(); BARRIER();
        STEP(xf0, wf0, 0, xf1, wf1);
      }
      STEP(xf1, wf1, 1, xf0, wf0);
      STEP(xf0, wf0, 2, xf1, wf1);
      STEP(xf1, wf1, 3, xf0, wf0);
      MM(xf1, wf1); PINALL();
      if (w == 0) STAMP(2);
      ssum = swap_add(ssum); ssq = swap_add(ssq); csum = swap_add(csum);
      {
        const float mean = ssum * (1.f / D);
        const float var = fmaxf(ssq * (1.f / D) - mean * mean, 0.f);
        const float mb = rbf(mean), vb = rbf(var);
        const float sd = rbf(sqrtf(rbf(vb + 1e-5f)));
        if (lane < 32) {
          *(LAS f32x2*)(L + L_RS + (64 * wm + 32 * wn + l31) * 8) = (f32x2){mb, 1.f / sd};
          *(LAS float*)(L + L_CS + (64 * wn + 32 * wm + l31) * 4) = csum;
        }
      }
      WAITV(0); WAITL0(); BARRIER();
      const float qs = aj < 5 ? 0.22360679774997896f * 1.4426950408889634f : 1.f;
#pragma unroll
      for (int i = 0; i < 2; ++i) {
        const int tok = 64 * wm + 32 * (i ^ wn) + l31;
        const f32x2 rs = *(const LAS f32x2*)(L + L_RS + tok * 8);
        const float sc = rs[1] * qs, nm = -rs[0] * sc;
#pragma unroll
        for (int j = 0; j < 2; ++j) {
          const f32x16& a = i == 0 ? (j == 0 ? a00 : a01) : (j == 0 ? a10 : a11);
          const int f0 = 64 * wn + 32 * (j ^ wm) + 4 * hh;
          const unsigned go = WS_QKV + (unsigned)(32 * aj + (f0 >> 2)) * 16384u + (unsigned)(128 * aP + tok) * 8u;
#pragma unroll
          for (int rq = 0; rq < 4; ++rq) {
            const f32x4 cs = *(const LAS f32x4*)(L + L_CS + (f0 + 8 * rq) * 4);
            const float v0 = fmaf(a[rq * 4 + 0], sc, nm * cs[0]), v1 = fmaf(a[rq * 4 + 1], sc, nm * cs[1]);
            const float v2 = fmaf(a[rq * 4 + 2], sc, nm * cs[2]), v3 = fmaf(a[rq * 4 + 3], sc, nm * cs[3]);
            __builtin_amdgcn_raw_buffer_store_b64((u32x2){pk2(v0, v1), pk2(v2, v3)}, rws, go + rq * 32768, 0, SC1);
          }
        }
      }
      WAITV(0); BARRIER();
    } else {
      WAITV(0);
    }
  }
  if (aj >= 15) { WAITL0(); BARRIER(); }
  const u32x2 tag2 = *(const LAS u32x2*)(L + L_EP);
  const unsigned tag32 = tag2[0] | 1u;
  if (aj < 15 && tid == 0) __builtin_amdgcn_raw_buffer_store_b64(tag2, rws, WS_FLAGA + (aP * 16 + aj) * 128, 0, SC1);
  if (w == 0) STAMP(3);

  f32x16 o = (f32x16){};
  {
    const int t = w;
    if (tid < 4) *(LAS unsigned*)(L + L_ONE + tid * 4) = tid == 0 ? 0x00003f80u : 0u;
    {
      const int sec = lane < 4 ? 0 : (lane < 12 ? 1 : 2);
      const int k = lane < 4 ? lane : (lane < 12 ? lane - 4 : lane - 12);
      const int col = sec * 640 + 20 * hd + ((k & 1) ? 19 : 0);
      int Pn = sec == 0 ? 2 * pp + (k >> 1) : 2 * pp - 2 + (k >> 1);
      const bool need = lane < 20 && Pn >= 0;
      const unsigned fo = WS_FLAGA + (unsigned)((need ? Pn : 0) * 16 + (col >> 7)) * 128;
      unsigned spins = 0;
      for (;;) {
        const u32x2 f = __builtin_amdgcn_raw_buffer_load_b64(rws, fo, 0, SC1);
        if (__all(!need || (f[0] == tag2[0] && f[1] == tag2[1]))) break;
        if (++spins > SPIN_CAP) break;
        __builtin_amdgcn_s_sleep(1);
      }
    }
    asm volatile("" ::: "memory");
    if (w == 0) STAMP(4);
    {
      const char* qb = (const char*)p.ws + WS_QKV;
#pragma unroll
      for (int i = 0; i < 5; ++i) {
        const int pc = w + 8 * i, pl = pc >> 2, qt = pc & 3;
        const int tok0 = 256 * pp - 256 + 128 * qt;
        if (tok0 >= 0) glds16c(lane * 16, qb + (size_t)((pl < 5 ? 160 + 5 * hd + pl : 320 + 5 * hd + pl - 5)) * 16384 + tok0 * 8, lds0 + pl * PL + qt * 1024);
      }
    }
    bf16x8 qf0; bf16x4 qf1;
    {
      const unsigned qo = WS_QKV + (unsigned)(5 * hd) * 16384u + (unsigned)(256 * pp + 32 * t + l31) * 8u;
      const u32x2 a = __builtin_amdgcn_raw_buffer_load_b64(rws, qo + (2 * hh) * 16384, 0, SC1);
      const u32x2 c = __builtin_amdgcn_raw_buffer_load_b64(rws, qo + (2 * hh + 1) * 16384, 0, SC1);
      u32x2 z = __builtin_amdgcn_raw_buffer_load_b64(rws, qo + 4 * 16384, 0, SC1);
      if (hh) z = (u32x2){0, 0};
      qf0 = __builtin_bit_cast(bf16x8, (u32x4){a[0], a[1], c[0], c[1]});
      qf1 = __builtin_bit_cast(bf16x4, z);
    }
    WAITV(0); WAITL0(); BARRIER();
    if (w == 0) STAMP(5);
    {
#pragma unroll 1
      for (int pc = w; pc < 100; pc += 8) {
        const int n = pc * 64 + lane, rho = n / 10, ch = n - rho * 10;
        const int s = rho >> 5, tq = (rho >> 4) & 1, kg = (rho >> 2) & 3, q = rho & 3;
        const int cidx = 4 * s + kg, g2 = cidx / 5, c5 = cidx - 5 * g2;
        const int quad = 10 * g2 + ((tq ? 0x98732 : 0x46510) >> (4 * c5) & 15);
        glds16((unsigned)(4 * quad + q) * 1280u + ch * 16u, (const char*)p.wo + 160 * cc, lds0 + L_WO + pc * 1024);
      }
    }
    const int kad = L_K + l31 * 8;
    float negm;
    {
      const LAS char* kp = L + kad + (t + 8) * 256;
      const u32x2 k0 = *(const LAS u32x2*)(kp + (2 * hh) * PL), k1 = *(const LAS u32x2*)(kp + (2 * hh + 1) * PL), k2 = *(const LAS u32x2*)(kp + 4 * PL);
      const u32x4 q0 = __builtin_bit_cast(u32x4, qf0); const u32x2 q1 = __builtin_bit_cast(u32x2, qf1);
      float d = dot2(q0[0], k0[0], 0.f); d = dot2(q0[1], k0[1], d); d = dot2(q0[2], k1[0], d); d = dot2(q0[3], k1[1], d);
      d = dot2(q1[0], k2[0], d); d = dot2(q1[1], k2[1], d);
      negm = -swap_add(d);
    }
    const int vquad = 4 * ((lane >> 4) & 1) + (lane & 3);
    const int vad = vquad < 5 ? L_V + vquad * PL + (4 * hh + ((lane & 15) >> 2)) * 8 : L_ONE;
    const int vst = vquad < 5 ? 1 : 0;
    auto tile = [&](int kt, const f32x16& cin) {
      const LAS char* kp = L + kad + kt * 256;
      const bf16x4 ka = *(const LAS bf16x4*)(kp + (2 * hh) * PL), kb = *(const LAS bf16x4*)(kp + (2 * hh + 1) * PL), kc = *(const LAS bf16x4*)(kp + 4 * PL);
      const LAS char* vp = L + vad + vst * kt * 256;
      const bf16x8 v0 = cat8(trd(vp), trd(vp + vst * 64)), v1 = cat8(trd(vp + vst * 128), trd(vp + vst * 192));
      f32x16 s = MFMA32(cat8(ka, kb), qf0, cin);
      s = MFMA32K8(kc, qf1, s);
      u32x4 p0, p1;
#pragma unroll
      for (int e = 0; e < 4; ++e) {
        p0[e] = pk2(__builtin_amdgcn_exp2f(s[2 * e]), __builtin_amdgcn_exp2f(s[2 * e + 1]));
        p1[e] = pk2(__builtin_amdgcn_exp2f(s[8 + 2 * e]), __builtin_amdgcn_exp2f(s[8 + 2 * e + 1]));
      }
      o = MFMA32(v0, __builtin_bit_cast(bf16x8, p0), o);
      o = MFMA32(v1, __builtin_bit_cast(bf16x8, p1), o);
    };
    f32x16 cm;
#pragma unroll
    for (int r = 0; r < 16; ++r) { const int kr = (r & 3) + 8 * (r >> 2) + 4 * hh; cm[r] = kr <= l31 ? negm : -INFINITY; }
    tile(t + 8, cm);
#pragma unroll
    for (int r = 0; r < 16; ++r) cm[r] = negm;
    const int lo = pp >= 1 ? t + 1 : 8;
#pragma unroll 1
    for (int kt = t + 7; kt >= lo; --kt) tile(kt, cm);
    if (pp >= 1) {
#pragma unroll
      for (int r = 0; r < 16; ++r) { const int kr = (r & 3) + 8 * (r >> 2) + 4 * hh; cm[r] = kr > l31 ? negm : -INFINITY; }
      tile(t, cm);
    }
    if (w == 0) STAMP(6);
    auto r = __builtin_amdgcn_permlane32_swap(__float_as_uint(o[8]), __float_as_uint(o[8]), false, false);
    const float inv = 1.f / __uint_as_float(r[1]);
    u32x4 c0;
    c0[0] = pk2(o[0] * inv, o[1] * inv); c0[1] = pk2(o[2] * inv, o[3] * inv); c0[2] = pk2(o[4] * inv, o[5] * inv); c0[3] = pk2(o[6] * inv, o[7] * inv);
    const u32x2 c1 = (u32x2){pk2(o[8] * inv, o[9] * inv), pk2(o[10] * inv, o[11] * inv)};
    const int tt = pp * 16 + 2 * t + (l31 >> 4);
    const unsigned base = WS_O + (unsigned)tt * 80 * 256 + (l31 & 15) * 16 + (unsigned)(5 * (hd >> 1)) * 256;
    __builtin_amdgcn_raw_buffer_store_b128(c0, rws, base + (2 * (hd & 1) + hh) * 256, 0, SC1);
    if (hh == 0) __builtin_amdgcn_raw_buffer_store_b64(c1, rws, base + 4 * 256 + (hd & 1) * 8, 0, SC1);
    WAITV(0);
    if (lane == 0) __builtin_amdgcn_raw_buffer_store_b64(tag2, rws, WS_FLAGO + (((pp * 8 + t) * 2 + (hd >> 4)) * 16 + (hd & 15)) * 8, 0, SC1);
  }
  if (w == 0) STAMP(7);
  BARRIER();

  const int nt = w & 3, kh = w >> 2;
  const int tok = 64 * R + 16 * nt + l15;
  u32x2 xr[5], wrv[5];
  float swr = 0.f;
  if (kh == 0) {
#pragma unroll
    for (int ft = 0; ft < 5; ++ft) {
      xr[ft] = *(const u32x2*)(p.x + (size_t)tok * D + 80 * cc + 16 * ft + 4 * fg);
      wrv[ft] = *(const u32x2*)(p.wr + 80 * cc + 16 * ft + 4 * fg);
    }
    const unsigned* wr32 = (const unsigned*)p.wr;
#pragma unroll
    for (int i = 0; i < 5; ++i) swr = dot2(wr32[lane + 64 * i], 0x3f803f80u, swr);
    swr = wave_fsum(swr);
  }
  f32x4 acc[5];
#pragma unroll
  for (int ft = 0; ft < 5; ++ft) acc[ft] = (f32x4){0, 0, 0, 0};
  {
    const int tg = 4 * R + nt;
    const unsigned fo = WS_FLAGO + ((((tg >> 4) * 8 + ((tg >> 1) & 7)) * 2 + kh) * 16 + l15) * 8;
    unsigned spins = 0;
    for (;;) {
      const u32x2 f = __builtin_amdgcn_raw_buffer_load_b64(rws, fo, 0, SC1);
      if (__all(f[0] == tag2[0] && f[1] == tag2[1])) break;
      if (++spins > SPIN_CAP) break;
      __builtin_amdgcn_s_sleep(1);
    }
    asm volatile("" ::: "memory");
    if (w == 0) STAMP(8);
    u32x4 of[10];
#pragma unroll
    for (int s = 0; s < 10; ++s) of[s] = __builtin_amdgcn_raw_buffer_load_b128(rws, WS_O + (unsigned)(tg * 80 + 4 * (10 * kh + s)) * 256 + lane * 16, 0, SC1);
    const LAS char* wp = L + L_WO + (unsigned)(kh * 10 * 32 + fg * 4 + ((lane & 15) >> 2)) * 160 + (lane & 3) * 8;
#pragma unroll
    for (int s = 0; s < 10; ++s) {
#pragma unroll
      for (int ft = 0; ft < 5; ++ft) {
        const bf16x8 a = cat8(trd(wp + (s * 32) * 160 + ft * 32), trd(wp + (s * 32 + 16) * 160 + ft * 32));
        acc[ft] = MFMA16(a, __builtin_bit_cast(bf16x8, of[s]), acc[ft]);
      }
    }
  }
  if (w == 0) STAMP(9);
  if (kh == 1) {
#pragma unroll
    for (int ft = 0; ft < 5; ++ft) *(LAS f32x4*)(L + L_RED + ((nt * 5 + ft) * 64 + lane) * 16) = acc[ft];
  }
  WAITL0(); BARRIER();
  unsigned hp[10];
  float mb = 0.f, inv_sd = 0.f;
  if (kh == 1) {
    const int rl = lane >> 1, grow = 8 * ((rl >> 2) & 3) + 4 * (rl >> 4) + (rl & 3);
    if (is_gu) {
      const unsigned vo = (unsigned)grow * (F * 2) + (lane & 1) * 16;
#pragma unroll 1
      for (int pc = nt; pc < 40; pc += 4) { const int m = pc >= 20, s = pc - 20 * m; glds16(vo, (const char*)(m ? p.wu : p.wg) + (size_t)s * 32 * F * 2 + gs * 32, lds0 + L_FW + pc * 1024); }
    } else if (is_dn) {
      const unsigned vo = (unsigned)grow * (D * 2) + (lane & 1) * 16;
#pragma unroll 1
      for (int pc = nt; pc < 54; pc += 4) glds16(vo, (const char*)p.wd + (size_t)pc * 32 * D * 2 + dn * 32, lds0 + L_FW + pc * 1024);
    }
  } else {
    float s1 = 0.f, s2 = 0.f, s3 = 0.f;
#pragma unroll
    for (int ft = 0; ft < 5; ++ft) {
      const f32x4 o2 = *(const LAS f32x4*)(L + L_RED + ((nt * 5 + ft) * 64 + lane) * 16);
      const unsigned a01 = pk2(acc[ft][0] + o2[0], acc[ft][1] + o2[1]), a23 = pk2(acc[ft][2] + o2[2], acc[ft][3] + o2[3]);
      const unsigned h01 = pk2(bf2f(xr[ft][0]) + bf2f(a01), bfhi(xr[ft][0]) + bfhi(a01)), h23 = pk2(bf2f(xr[ft][1]) + bf2f(a23), bfhi(xr[ft][1]) + bfhi(a23));
      hp[2 * ft] = h01; hp[2 * ft + 1] = h23;
      __builtin_amdgcn_raw_buffer_store_b64((u32x2){h01, h23}, rws, WS_H + (unsigned)tok * 1280 + (80 * cc + 16 * ft + 4 * fg) * 2, 0, SC1);
      s1 = dot2(h01, 0x3f803f80u, s1); s1 = dot2(h23, 0x3f803f80u, s1);
      s2 = dot2(h01, h01, s2); s2 = dot2(h23, h23, s2);
      s3 = dot2(h01, wrv[ft][0], s3); s3 = dot2(h23, wrv[ft][1], s3);
    }
    s1 = swap_add(swap16_add(s1)); s2 = swap_add(swap16_add(s2)); s3 = swap_add(swap16_add(s3));
    if (lane < 16) __builtin_amdgcn_raw_buffer_store_b128((u32x4){__float_as_uint(s1), __float_as_uint(s2), __float_as_uint(s3), tag32}, rws, WS_STATS + ((R * 8 + cc) * 64 + 16 * nt + l15) * 16, 0, SC1);
    if (w == 0) STAMP(10);
    float t1, t2, t3;
    {
      const unsigned so = WS_STATS + ((R * 8 + fg) * 64 + 16 * nt + l15) * 16;
      unsigned spins = 0;
      for (;;) {
        const u32x4 ga = __builtin_amdgcn_raw_buffer_load_b128(rws, so, 0, SC1), gb = __builtin_amdgcn_raw_buffer_load_b128(rws, so + 4 * 1024, 0, SC1);
        t1 = __uint_as_float(ga[0]) + __uint_as_float(gb[0]); t2 = __uint_as_float(ga[1]) + __uint_as_float(gb[1]); t3 = __uint_as_float(ga[2]) + __uint_as_float(gb[2]);
        if (__all(ga[3] == tag32 && gb[3] == tag32)) break;
        if (++spins > SPIN_CAP) break;
        __builtin_amdgcn_s_sleep(1);
      }
    }
    t1 = swap_add(swap16_add(t1)); t2 = swap_add(swap16_add(t2)); t3 = swap_add(swap16_add(t3));
    const float mean = t1 * (1.f / D);
    const float var = fmaxf(t2 * (1.f / D) - mean * mean, 0.f);
    mb = rbf(mean);
    const float sd = rbf(sqrtf(rbf(rbf(var) + 1e-5f)));
    inv_sd = 1.f / sd;
    const unsigned rb = pk2((t3 - mb * swr) * inv_sd, 0.f) & 0xffffu;
    const unsigned key = ((rb ^ ((rb & 0x8000u) ? 0xffffu : 0x8000u)) << 16) | (unsigned)(2047 - tok);
    if (lane < 16) {
      *(LAS unsigned*)(L + L_KEY + (16 * nt + l15) * 4) = key;
      *(LAS unsigned*)(L + L_MS + (16 * nt + l15) * 4) = (__float_as_uint(mb) & 0xffff0000u) | (__float_as_uint(sd) >> 16);
    }
    WAITV(0);
    if (w == 0) STAMP(11);
  }
  WAITL0(); BARRIER();
  if (w == 0) {
    unsigned k = *(const LAS unsigned*)(L + L_KEY + lane * 4), m = 0;
    for (int i = 0; i <= cc; ++i) { m = wave_umax(k); if (k == m) k = 0; }
    const unsigned ms = *(const LAS unsigned*)(L + L_MS + ((2047 - (int)(m & 0x7ff)) & 63) * 4);
    if (lane == 0) __builtin_amdgcn_raw_buffer_store_b128((u32x4){m, tag2[0], ms, tag2[1]}, rws, WS_CAND + (R * 8 + cc) * 16, 0, SC1);
    STAMP(12);
    unsigned k0, k1, k2, k3, m0, m1, m2, m3;
    unsigned spins = 0;
    for (;;) {
      const u32x4 a = __builtin_amdgcn_raw_buffer_load_b128(rws, WS_CAND + lane * 16, 0, SC1), bb = __builtin_amdgcn_raw_buffer_load_b128(rws, WS_CAND + 1024 + lane * 16, 0, SC1);
      const u32x4 c = __builtin_amdgcn_raw_buffer_load_b128(rws, WS_CAND + 2048 + lane * 16, 0, SC1), d = __builtin_amdgcn_raw_buffer_load_b128(rws, WS_CAND + 3072 + lane * 16, 0, SC1);
      k0 = a[0]; k1 = bb[0]; k2 = c[0]; k3 = d[0]; m0 = a[2]; m1 = bb[2]; m2 = c[2]; m3 = d[2];
      const bool ok = a[1] == tag2[0] && a[3] == tag2[1] && bb[1] == tag2[0] && bb[3] == tag2[1] && c[1] == tag2[0] && c[3] == tag2[1] && d[1] == tag2[0] && d[3] == tag2[1];
      if (__all(ok)) break;
      if (++spins > SPIN_CAP) break;
      __builtin_amdgcn_s_sleep(1);
    }
    STAMP(13);
#pragma unroll 1
    for (int i = 0; i < 8; ++i) {
      const unsigned lm = max(max(k0, k1), max(k2, k3));
      const unsigned g = wave_umax(lm);
      const unsigned ms2 = k0 == g ? m0 : (k1 == g ? m1 : (k2 == g ? m2 : m3));
      if (lm == g) { *(LAS unsigned*)(L + L_SEL + i * 4) = g; *(LAS unsigned*)(L + L_SEL + 32 + i * 4) = ms2; }
      k0 = k0 == g ? 0 : k0; k1 = k1 == g ? 0 : k1; k2 = k2 == g ? 0 : k2; k3 = k3 == g ? 0 : k3;
    }
    if (b == 0 && lane == 0) __builtin_amdgcn_raw_buffer_store_b64(tag2, rws, WS_EPOCH, 0, SC1);
    STAMP(14);
  }
  if (kh == 1) WAITV(0);
  WAITL0(); BARRIER();

  const unsigned selk = *(const LAS unsigned*)(L + L_SEL + (lane & 7) * 4), selm = *(const LAS unsigned*)(L + L_SEL + 32 + (lane & 7) * 4);
  auto out_rows = [&]() {
    if (kh == 0) {
      bool sel = false;
#pragma unroll
      for (int i = 0; i < 8; ++i) sel |= (2047 - (int)(__builtin_amdgcn_readlane((int)selk, i) & 0x7ff)) == tok;
      if (!sel) {
#pragma unroll
        for (int ft = 0; ft < 5; ++ft) {
          unsigned r2[2];
#pragma unroll
          for (int e = 0; e < 2; ++e) {
            const unsigned hv = hp[2 * ft + e];
            const unsigned d2 = pk2(bf2f(hv) - mb, bfhi(hv) - mb);
            const unsigned n2 = pk2(bf2f(d2) * inv_sd, bfhi(d2) * inv_sd);
            r2[e] = pk2(bf2f(hv) + bf2f(n2), bfhi(hv) + bfhi(n2));
          }
          *(u32x2*)(p.out + (size_t)tok * D + 80 * cc + 16 * ft + 4 * fg) = (u32x2){r2[0], r2[1]};
        }
      }
    }
  };
  if (is_gu) {
    {
      const unsigned kk = *(const LAS unsigned*)(L + L_SEL + w * 4), ms = *(const LAS unsigned*)(L + L_SEL + 32 + w * 4);
      const int row = 2047 - (int)(kk & 0x7ff);
      const float m = __uint_as_float(ms & 0xffff0000u), iv = 1.f / __uint_as_float(ms << 16);
      const u32x4 h0 = __builtin_amdgcn_raw_buffer_load_b128(rws, WS_H + (unsigned)row * 1280 + lane * 16, 0, SC1);
      const u32x4 h1 = __builtin_amdgcn_raw_buffer_load_b128(rws, WS_H + (unsigned)row * 1280 + 1024 + (lane & 15) * 16, 0, SC1);
      u32x4 n0, n1;
#pragma unroll
      for (int e = 0; e < 4; ++e) {
        const unsigned d0 = pk2(bf2f(h0[e]) - m, bfhi(h0[e]) - m), d1 = pk2(bf2f(h1[e]) - m, bfhi(h1[e]) - m);
        n0[e] = pk2(bf2f(d0) * iv, bfhi(d0) * iv); n1[e] = pk2(bf2f(d1) * iv, bfhi(d1) * iv);
      }
      *(LAS u32x4*)(L + L_XS + w * 1296 + lane * 16) = n0;
      if (lane < 16) *(LAS u32x4*)(L + L_XS + w * 1296 + 1024 + lane * 16) = n1;
    }
    WAITL0(); BARRIER();
    if (w == 0) STAMP(15);
    {
      const int m = w & 1, kq = w >> 1;
      f32x4 a4 = {0, 0, 0, 0};
#pragma unroll
      for (int i = 0; i < 5; ++i) {
        const int s = 5 * kq + i;
        const LAS char* ap = L + L_FW + (m * 20 + s) * 1024 + lane * 8;
        const bf16x8 af = cat8(trd(ap), trd(ap + 512));
        const bf16x8 bfv = *(const LAS bf16x8*)(L + L_XS + (lane & 7) * 1296 + s * 64 + fg * 16);
        a4 = MFMA16(af, bfv, a4);
      }
      *(LAS f32x4*)(L + L_FRED + (w * 64 + lane) * 16) = a4;
    }
    WAITL0(); BARRIER();
    if (w == 0) {
      f32x4 g4 = {0, 0, 0, 0}, u4 = {0, 0, 0, 0};
#pragma unroll
      for (int q = 0; q < 4; ++q) { g4 += *(const LAS f32x4*)(L + L_FRED + ((2 * q) * 64 + lane) * 16); u4 += *(const LAS f32x4*)(L + L_FRED + ((2 * q + 1) * 64 + lane) * 16); }
      float a[4];
#pragma unroll
      for (int e = 0; e < 4; ++e) { const float g = rbf(g4[e]), u = rbf(u4[e]); const float sg = rbf(g / (1.f + __expf(-g))); a[e] = sg * u; }
      if (l15 < 8) __builtin_amdgcn_raw_buffer_store_b128((u32x4){pk2(a[0], a[1]), tag32, pk2(a[2], a[3]), tag32}, rws, WS_ACT + (((gs >> 1) * 4 + 2 * (gs & 1) + (fg >> 1)) * 8 + l15) * 32 + (fg & 1) * 16, 0, SC1);
      STAMP(16);
    }
    out_rows();
  } else if (is_dn) {
    out_rows();
    const int row = 2047 - (int)(selk & 0x7ff);
    const u32x2 hv = __builtin_amdgcn_raw_buffer_load_b64(rws, WS_H + (unsigned)row * 1280 + (16 * dn + 4 * fg) * 2, 0, SC1);
    f32x4 a4 = {0, 0, 0, 0};
    {
      u32x4 g0[7], g1[7];
      unsigned spins = 0;
      for (;;) {
        bool ok = true;
#pragma unroll
        for (int i = 0; i < 7; ++i) {
          const int s = min(w + 8 * i, 53);
          const unsigned ao = WS_ACT + ((s * 4 + fg) * 8 + (lane & 7)) * 32;
          g0[i] = __builtin_amdgcn_raw_buffer_load_b128(rws, ao, 0, SC1); g1[i] = __builtin_amdgcn_raw_buffer_load_b128(rws, ao + 16, 0, SC1);
        }
#pragma unroll
        for (int i = 0; i < 7; ++i) ok = ok && g0[i][1] == tag32 && g0[i][3] == tag32 && g1[i][1] == tag32 && g1[i][3] == tag32;
        if (__all(ok)) break;
        if (++spins > SPIN_CAP) break;
        __builtin_amdgcn_s_sleep(1);
      }
      if (w == 0) STAMP(17);
#pragma unroll
      for (int i = 0; i < 7; ++i) {
        const int s = w + 8 * i;
        if (s < 54) {
          const LAS char* ap = L + L_FW + s * 1024 + lane * 8;
          const bf16x8 af = cat8(trd(ap), trd(ap + 512));
          a4 = MFMA16(af, __builtin_bit_cast(bf16x8, (u32x4){g0[i][0], g0[i][2], g1[i][0], g1[i][2]}), a4);
        }
      }
    }
    *(LAS f32x4*)(L + L_FRED + (w * 64 + lane) * 16) = a4;
    WAITL0(); BARRIER();
    if (w == 0) {
      f32x4 y = {0, 0, 0, 0};
#pragma unroll
      for (int q = 0; q < 8; ++q) y += *(const LAS f32x4*)(L + L_FRED + (q * 64 + lane) * 16);
      const unsigned rk = selk >> 16, rbits = (rk & 0x8000u) ? (rk ^ 0x8000u) : (rk ^ 0xffffu);
      const float rv = bf2f(rbits), sig = rbf(1.f / (1.f + __expf(-rv)));
      const float m = __uint_as_float(selm & 0xffff0000u), iv = 1.f / __uint_as_float(selm << 16);
      unsigned r2[2];
#pragma unroll
      for (int e = 0; e < 2; ++e) {
        const unsigned d2 = pk2(bf2f(hv[e]) - m, bfhi(hv[e]) - m);
        const unsigned n2 = pk2(bf2f(d2) * iv, bfhi(d2) * iv);
        const unsigned y2 = pk2(rbf(y[2 * e]) * sig, rbf(y[2 * e + 1]) * sig);
        const unsigned t2 = pk2(bf2f(n2) + bf2f(y2), bfhi(n2) + bfhi(y2));
        r2[e] = pk2(bf2f(hv[e]) + bf2f(t2), bfhi(hv[e]) + bfhi(t2));
      }
      if (l15 < 8) *(u32x2*)(p.out + (size_t)row * D + 16 * dn + 4 * fg) = (u32x2){r2[0], r2[1]};
      STAMP(18);
    }
  } else {
    out_rows();
  }
  if (w == 0) STAMP(19);
  asm volatile("Lfk_code_end:");
  STAMP_FLUSH();
}

extern "C" void kernel_launch(void* const* d_in, const int* in_sizes, int n_in, void* d_out, int out_size, void* d_ws, size_t ws_size, hipStream_t stream) {
  Params p;
  p.x = (const u16*)d_in[0]; p.wqkv = (const u16*)d_in[1]; p.wo = (const u16*)d_in[2]; p.wr = (const u16*)d_in[4];
  p.wg = (const u16*)d_in[5]; p.wu = (const u16*)d_in[6]; p.wd = (const u16*)d_in[7]; p.out = (u16*)d_out; p.ws = (unsigned char*)d_ws;
  fused_kernel<<<256, 512, 0, stream>>>(p);
}
```

```cpp
#include <hip/hip_runtime.h>
#include <stdint.h>

typedef unsigned short u16;
typedef short bf16x8 __attribute__((ext_vector_type(8)));
typedef short bf16x4 __attribute__((ext_vector_type(4)));
typedef __bf16 bf2 __attribute__((ext_vector_type(2)));
typedef float f32x16 __attribute__((ext_vector_type(16)));
typedef float f32x4 __attribute__((ext_vector_type(4)));
typedef float f32x2 __attribute__((ext_vector_type(2)));
typedef unsigned u32x4 __attribute__((ext_vector_type(4)));
typedef unsigned u32x2 __attribute__((ext_vector_type(2)));
typedef unsigned long long u64;
#define LAS __attribute__((address_space(3)))
#define SBAR() __builtin_amdgcn_sched_barrier(0)
#define BARRIER() asm volatile("s_barrier" ::: "memory")
#define WAITL0() asm volatile("s_waitcnt lgkmcnt(0)" ::: "memory")
#define WAITV(n) asm volatile("s_waitcnt vmcnt(" #n ")" ::: "memory")

constexpr int S = 2048, D = 640, F = 1728;
constexpr int SC1 = 16;

constexpr unsigned WS_EPOCH = 0;
constexpr unsigned WS_FLAGA = 4096;
constexpr unsigned WS_FLAGO = WS_FLAGA + 32768;
constexpr unsigned WS_CAND = WS_FLAGO + 16384;
constexpr unsigned WS_ACT = WS_CAND + 4096;
constexpr unsigned WS_STATS = 131072;
constexpr unsigned WS_QKV = 524288;
constexpr unsigned WS_O = WS_QKV + 480 * 16384;
constexpr unsigned WS_H = WS_O + S * D * 2;
#define STAMP_OFF (200u << 20)
#define NSTAMP 32

constexpr int L_SLOT = 32768, L_W = 16384;
constexpr int PL = 4128;
constexpr int L_K = 0, L_V = 5 * PL, L_ONE = 10 * PL;
constexpr int L_WO = 43008;
constexpr int L_RED = 0;
constexpr int L_FW = 43008;
constexpr int L_XS = 0;
constexpr int L_FRED = 12288;
constexpr int L_MISC = 147456;
constexpr int L_RS = L_MISC;
constexpr int L_CS = L_MISC + 1024;
constexpr int L_EP = L_MISC + 1536;
constexpr int L_KEY = L_MISC + 2048;
constexpr int L_MS = L_MISC + 2304;
constexpr int L_SEL = L_MISC + 2560;
constexpr int L_STAMP = L_MISC + 4096;
constexpr int LDS_BYTES = 163840;

struct Params { const u16* x; const u16* wqkv; const u16* wo; const u16* wr; const u16* wg; const u16* wu; const u16* wd; u16* out; unsigned char* ws; };

__device__ __forceinline__ float bf2f(unsigned v) { return __uint_as_float(v << 16); }
__device__ __forceinline__ float bfhi(unsigned v) { return __uint_as_float(v & 0xffff0000u); }
__device__ __forceinline__ unsigned pk2(float a, float b) { f32x2 f = {a, b}; return __builtin_bit_cast(unsigned, __builtin_convertvector(f, bf2)); }
__device__ __forceinline__ float rbf(float f) { return __uint_as_float(pk2(f, 0.f) << 16); }
__device__ __forceinline__ float dot2(unsigned a, unsigned b, float c) { return __builtin_amdgcn_fdot2_f32_bf16(__builtin_bit_cast(bf2, a), __builtin_bit_cast(bf2, b), c, false); }
__device__ __forceinline__ float swap_add(float v) { auto r = __builtin_amdgcn_permlane32_swap(__float_as_uint(v), __float_as_uint(v), false, false); return __uint_as_float(r[0]) + __uint_as_float(r[1]); }
__device__ __forceinline__ float swap16_add(float v) { auto r = __builtin_amdgcn_permlane16_swap(__float_as_uint(v), __float_as_uint(v), false, false); return __uint_as_float(r[0]) + __uint_as_float(r[1]); }
__device__ __forceinline__ void glds16(unsigned voff, const void* sbase, unsigned lds) {
  asm volatile("s_mov_b32 m0, %2\n\ts_nop 0\n\tglobal_load_lds_dwordx4 %0, %1" ::"v"(voff), "s"(sbase), "s"(lds) : "memory");
}
__device__ __forceinline__ void glds16c(unsigned voff, const void* sbase, unsigned lds) {
  asm volatile("s_mov_b32 m0, %2\n\ts_nop 0\n\tglobal_load_lds_dwordx4 %0, %1 sc1" ::"v"(voff), "s"(sbase), "s"(lds) : "memory");
}
__device__ __forceinline__ bf16x4 trd(const LAS char* p) { return __builtin_amdgcn_ds_read_tr16_b64_v4i16((LAS bf16x4*)p); }
__device__ __forceinline__ bf16x8 cat8(bf16x4 a, bf16x4 b) { return (bf16x8){a[0], a[1], a[2], a[3], b[0], b[1], b[2], b[3]}; }
#define MFMA32(a, b, c) __builtin_amdgcn_mfma_f32_32x32x16_bf16(a, b, c, 0, 0, 0)
#define MFMA32K8(a, b, c) __builtin_amdgcn_mfma_f32_32x32x8bf16_1k(a, b, c, 0, 0, 0)
#define MFMA16(a, b, c) __builtin_amdgcn_mfma_f32_16x16x32_bf16(a, b, c, 0, 0, 0)
template <int CTRL, int RMASK> __device__ __forceinline__ unsigned dppmax(unsigned v) { return max(v, (unsigned)__builtin_amdgcn_update_dpp(0, (int)v, CTRL, RMASK, 0xf, false)); }
__device__ __forceinline__ unsigned wave_umax(unsigned v) {
  v = dppmax<0x111, 0xf>(v); v = dppmax<0x112, 0xf>(v); v = dppmax<0x114, 0xf>(v); v = dppmax<0x118, 0xf>(v);
  v = dppmax<0x142, 0xa>(v); v = dppmax<0x143, 0xc>(v);
  return (unsigned)__builtin_amdgcn_readlane((int)v, 63);
}

template <int CTRL> __device__ __forceinline__ float dppadd(float v) { return v + __int_as_float(__builtin_amdgcn_update_dpp(0, __float_as_int(v), CTRL, 0xf, 0xf, false)); }
__device__ __forceinline__ float wave_fsum(float v) { v = dppadd<0x128>(v); v = dppadd<0x124>(v); v = dppadd<0x122>(v); v = dppadd<0x121>(v); return swap_add(swap16_add(v)); }

#define STAMP(k) do {} while (0)
#define STAMP_INIT() do {} while (0)
#define STAMP_FLUSH() do {} while (0)

constexpr unsigned SPIN_CAP = 1u << 16;

__global__ __launch_bounds__(512, 2) void fused_kernel(Params p) {
  __shared__ __attribute__((aligned(16))) char smem[LDS_BYTES];
  LAS char* L = (LAS char*)smem;
  const unsigned lds0 = (unsigned)(uintptr_t)smem;
  const int tid = threadIdx.x, lane = tid & 63;
  const int w = __builtin_amdgcn_readfirstlane(tid >> 6);
  const int b = blockIdx.x, xcd = b & 7, bi = b >> 3;
  const int l31 = lane & 31, hh = lane >> 5, l15 = lane & 15, fg = lane >> 4;
  const __amdgpu_buffer_rsrc_t rws = __builtin_amdgcn_make_buffer_rsrc(p.ws, 0, 0x7fffffff, 0x00020000);
  const int aP = 4 * (xcd >> 1) + (bi >> 3), aj = 8 * (xcd & 1) + (bi & 7);
  const int hd = bi, pp = xcd;
  const int R = 4 * xcd + (bi >> 3), cc = bi & 7;
  const int gs = xcd * 14 + bi;
  const bool is_gu = bi < 14 && gs < 108;
  const int dn = xcd * 5 + (bi - 14);
  const bool is_dn = bi >= 14 && bi < 19;
  const u64 t_start_ = 0; (void)t_start_;
  STAMP_INIT();

  if (aj < 15 && w >= 4) {
    const int lw = w - 4;
    const unsigned xsw = (unsigned)(4 * (lw & 1) + (lane >> 4));
    const unsigned voff_x = (unsigned)(lane >> 3) * 1280u + (((unsigned)(lane & 7) ^ xsw) << 4);
    const unsigned ch = (unsigned)(lane & 15) ^ (((unsigned)(lane >> 4) << 2) | (unsigned)lw);
    const unsigned voff_w = (unsigned)(lane >> 4) * 3840u + ch * 16u;
    const char* xb = (const char*)p.x + (size_t)(128 * aP + 8 * lw) * 1280;
    const char* wb = (const char*)p.wqkv + (size_t)(4 * lw) * 3840 + 256 * aj;
    const unsigned lbase = lds0 + lw * 1024;
    const int rot = ((bi & 7) * 5 / 4 + (bi >> 3) * 5 / 2) % 10;
    auto issue = [&](int s) {
      const unsigned sl = lbase + (s & 3) * L_SLOT;
      int ss = s + rot; if (ss >= 10) ss -= 10;
#pragma unroll
      for (int i = 0; i < 4; ++i) {
        glds16(voff_x, xb + (size_t)i * 32 * 1280 + ss * 128, sl + i * 4096);
        glds16(voff_w, wb + ((size_t)ss * 64 + i * 16) * 3840, sl + L_W + i * 4096);
      }
    };
    issue(0); issue(1); issue(2); issue(3);
    WAITV(24); BARRIER();
    if (w == 4) STAMP(1);
    WAITV(16); BARRIER();
#pragma unroll 1
    for (int s = 1; s <= 6; ++s) { issue(s + 3); WAITV(16); BARRIER(); }
    WAITV(8); BARRIER();
    WAITV(0); BARRIER();
    BARRIER();
    BARRIER();
  } else if (w < 4) {
    if (w == 0) {
      const u32x2 e = __builtin_amdgcn_raw_buffer_load_b64(rws, WS_EPOCH, 0, SC1);
      const u64 t = (((u64)e[1] << 32) | e[0]) + 1;
      if (lane == 0) *(LAS u32x2*)(L + L_EP) = (u32x2){(unsigned)t, (unsigned)(t >> 32)};
      __builtin_amdgcn_raw_buffer_store_b128((u32x4){0, 0, 0, 0}, rws, WS_STATS + (R * 8 + cc) * 1024 + lane * 16, 0, SC1);
      if (is_gu && lane < 32) __builtin_amdgcn_raw_buffer_store_b128((u32x4){0, 0, 0, 0}, rws, WS_ACT + (((gs >> 1) * 4 + 2 * (gs & 1) + (lane >> 4)) * 8 + (lane & 7)) * 32 + ((lane >> 3) & 1) * 16, 0, SC1);
    }
    if (aj < 15) {
      const int wm = w >> 1, wn = w & 1;
      unsigned xa[4], wa[4];
#pragma unroll
      for (int ks = 0; ks < 4; ++ks) xa[ks] = (unsigned)(64 * wm + l31) * 128u + ((((unsigned)(2 * ks + hh)) ^ (((unsigned)l31 >> 1) & 7u)) << 4);
      const unsigned xo0 = wn * 4096, xo1 = 4096 - xo0;
      {
        const unsigned blk = (lane >> 4) & 1, q = (lane & 15) >> 2, pq = lane & 3;
#pragma unroll
        for (int j = 0; j < 2; ++j)
#pragma unroll
          for (int t = 0; t < 2; ++t) {
            const unsigned jj = j ^ wm;
            const unsigned row = 8 * hh + 4 * t + q, chn = 4 * (2 * wn + jj) + 2 * blk + (pq >> 1), m = (q << 2) | (unsigned)(2 * hh + t);
            wa[j * 2 + t] = L_W + 256u * row + 16u * (chn ^ m) + 8u * (pq & 1);
          }
      }
      f32x16 a00 = {}, a01 = {}, a10 = {}, a11 = {};
      float ssum = 0.f, ssq = 0.f, csum = 0.f;
      bf16x8 xf0[2], wf0[2], xf1[2], wf1[2];
      const unsigned ONE2 = 0x3f803f80u;
      unsigned xb0[4], xb1[4], wb[4];
#define SETSLOT(so) do { _Pragma("unroll") for (int k_ = 0; k_ < 4; ++k_) { xb0[k_] = xa[k_] + (so) + xo0; xb1[k_] = xa[k_] + (so) + xo1; wb[k_] = wa[k_] + (so); } } while (0)
#define RD(XF, WF, ks) do { \
        XF[0] = *(const LAS bf16x8*)(L + xb0[ks]); XF[1] = *(const LAS bf16x8*)(L + xb1[ks]); \
        WF[0] = cat8(trd(L + wb[0] + (ks) * 4096), trd(L + wb[1] + (ks) * 4096)); \
        WF[1] = cat8(trd(L + wb[2] + (ks) * 4096), trd(L + wb[3] + (ks) * 4096)); } while (0)
#define MM(XF, WF) do { \
        a00 = MFMA32(WF[0], XF[0], a00); a01 = MFMA32(WF[1], XF[0], a01); \
        a10 = MFMA32(WF[0], XF[1], a10); a11 = MFMA32(WF[1], XF[1], a11); \
        { const u32x4 xs_ = __builtin_bit_cast(u32x4, XF[0]); const u32x4 ws_ = __builtin_bit_cast(u32x4, WF[0]); \
          _Pragma("unroll") for (int e_ = 0; e_ < 4; ++e_) { ssum = dot2(xs_[e_], ONE2, ssum); ssq = dot2(xs_[e_], xs_[e_], ssq); csum = dot2(ws_[e_], ONE2, csum); } } } while (0)
#define PINALL() asm volatile("" : "+v"(a00), "+v"(a01), "+v"(a10), "+v"(a11), "+v"(ssum), "+v"(ssq), "+v"(csum))
#define ILV() do { _Pragma("unroll") for (int g_ = 0; g_ < 4; ++g_) { \
        __builtin_amdgcn_sched_group_barrier(0x8, 1, 0); __builtin_amdgcn_sched_group_barrier(0x100, 2, 0); __builtin_amdgcn_sched_group_barrier(0x2, 3, 0); } } while (0)
#define STEP(XN, WN, ksn, XC, WC) do { RD(XN, WN, ksn); MM(XC, WC); ILV(); PINALL(); SBAR(); } while (0)
      SETSLOT(0);
      BARRIER();
      RD(xf0, wf0, 0);
      SBAR();
#pragma unroll 1
      for (int s = 0; s < 9; ++s) {
        STEP(xf1, wf1, 1, xf0, wf0);
        STEP(xf0, wf0, 2, xf1, wf1);
        STEP(xf1, wf1, 3, xf0, wf0);
        SETSLOT(((s + 1) & 3) * L_SLOT);
        WAITL0(); BARRIER();
        STEP(xf0, wf0, 0, xf1, wf1);
      }
      STEP(xf1, wf1, 1, xf0, wf0);
      STEP(xf0, wf0, 2, xf1, wf1);
      STEP(xf1, wf1, 3, xf0, wf0);
      MM(xf1, wf1); PINALL();
      if (w == 0) STAMP(2);
      ssum = swap_add(ssum); ssq = swap_add(ssq); csum = swap_add(csum);
      {
        const float mean = ssum * (1.f / D);
        const float var = fmaxf(ssq * (1.f / D) - mean * mean, 0.f);
        const float mb = rbf(mean), vb = rbf(var);
        const float sd = rbf(sqrtf(rbf(vb + 1e-5f)));
        if (lane < 32) {
          *(LAS f32x2*)(L + L_RS + (64 * wm + 32 * wn + l31) * 8) = (f32x2){mb, 1.f / sd};
          *(LAS float*)(L + L_CS + (64 * wn + 32 * wm + l31) * 4) = csum;
        }
      }
      WAITV(0); WAITL0(); BARRIER();
      const float qs = aj < 5 ? 0.22360679774997896f * 1.4426950408889634f : 1.f;
#pragma unroll
      for (int i = 0; i < 2; ++i) {
        const int tok = 64 * wm + 32 * (i ^ wn) + l31;
        const f32x2 rs = *(const LAS f32x2*)(L + L_RS + tok * 8);
        const float sc = rs[1] * qs, nm = -rs[0] * sc;
#pragma unroll
        for (int j = 0; j < 2; ++j) {
          const f32x16& a = i == 0 ? (j == 0 ? a00 : a01) : (j == 0 ? a10 : a11);
          const int f0 = 64 * wn + 32 * (j ^ wm) + 4 * hh;
          const unsigned go = WS_QKV + (unsigned)(32 * aj + (f0 >> 2)) * 16384u + (unsigned)(128 * aP + tok) * 8u;
#pragma unroll
          for (int rq = 0; rq < 4; ++rq) {
            const f32x4 cs = *(const LAS f32x4*)(L + L_CS + (f0 + 8 * rq) * 4);
            const float v0 = fmaf(a[rq * 4 + 0], sc, nm * cs[0]), v1 = fmaf(a[rq * 4 + 1], sc, nm * cs[1]);
            const float v2 = fmaf(a[rq * 4 + 2], sc, nm * cs[2]), v3 = fmaf(a[rq * 4 + 3], sc, nm * cs[3]);
            __builtin_amdgcn_raw_buffer_store_b64((u32x2){pk2(v0, v1), pk2(v2, v3)}, rws, go + rq * 32768, 0, SC1);
          }
        }
      }
      WAITV(0); BARRIER();
    } else {
      WAITV(0);
    }
  }
  if (aj >= 15) { WAITL0(); BARRIER(); }
  const u32x2 tag2 = *(const LAS u32x2*)(L + L_EP);
  const unsigned tag32 = tag2[0] | 1u;
  if (aj < 15 && tid == 0) __builtin_amdgcn_raw_buffer_store_b64(tag2, rws, WS_FLAGA + (aP * 16 + aj) * 128, 0, SC1);
  if (w == 0) STAMP(3);

  f32x16 o = (f32x16){};
  {
    const int t = w;
    if (tid < 4) *(LAS unsigned*)(L + L_ONE + tid * 4) = tid == 0 ? 0x00003f80u : 0u;
    {
      const int sec = lane < 4 ? 0 : (lane < 12 ? 1 : 2);
      const int k = lane < 4 ? lane : (lane < 12 ? lane - 4 : lane - 12);
      const int col = sec * 640 + 20 * hd + ((k & 1) ? 19 : 0);
      int Pn = sec == 0 ? 2 * pp + (k >> 1) : 2 * pp - 2 + (k >> 1);
      const bool need = lane < 20 && Pn >= 0;
      const unsigned fo = WS_FLAGA + (unsigned)((need ? Pn : 0) * 16 + (col >> 7)) * 128;
      unsigned spins = 0;
      for (;;) {
        const u32x2 f = __builtin_amdgcn_raw_buffer_load_b64(rws, fo, 0, SC1);
        if (__all(!need || (f[0] == tag2[0] && f[1] == tag2[1]))) break;
        if (++spins > SPIN_CAP) break;
        __builtin_amdgcn_s_sleep(1);
      }
    }
    asm volatile("" ::: "memory");
    if (w == 0) STAMP(4);
    {
      const char* qb = (const char*)p.ws + WS_QKV;
#pragma unroll
      for (int i = 0; i < 5; ++i) {
        const int pc = w + 8 * i, pl = pc >> 2, qt = pc & 3;
        const int tok0 = 256 * pp - 256 + 128 * qt;
        if (tok0 >= 0) glds16c(lane * 16, qb + (size_t)((pl < 5 ? 160 + 5 * hd + pl : 320 + 5 * hd + pl - 5)) * 16384 + tok0 * 8, lds0 + pl * PL + qt * 1024);
      }
    }
    bf16x8 qf0; bf16x4 qf1;
    {
      const unsigned qo = WS_QKV + (unsigned)(5 * hd) * 16384u + (unsigned)(256 * pp + 32 * t + l31) * 8u;
      const u32x2 a = __builtin_amdgcn_raw_buffer_load_b64(rws, qo + (2 * hh) * 16384, 0, SC1);
      const u32x2 c = __builtin_amdgcn_raw_buffer_load_b64(rws, qo + (2 * hh + 1) * 16384, 0, SC1);
      u32x2 z = __builtin_amdgcn_raw_buffer_load_b64(rws, qo + 4 * 16384, 0, SC1);
      if (hh) z = (u32x2){0, 0};
      qf0 = __builtin_bit_cast(bf16x8, (u32x4){a[0], a[1], c[0], c[1]});
      qf1 = __builtin_bit_cast(bf16x4, z);
    }
    WAITV(0); WAITL0(); BARRIER();
    if (w == 0) STAMP(5);
    {
#pragma unroll 1
      for (int pc = w; pc < 100; pc += 8) {
        const int n = pc * 64 + lane, rho = n / 10, ch = n - rho * 10;
        const int s = rho >> 5, tq = (rho >> 4) & 1, kg = (rho >> 2) & 3, q = rho & 3;
        const int cidx = 4 * s + kg, g2 = cidx / 5, c5 = cidx - 5 * g2;
        const int quad = 10 * g2 + ((tq ? 0x98732 : 0x46510) >> (4 * c5) & 15);
        glds16((unsigned)(4 * quad + q) * 1280u + ch * 16u, (const char*)p.wo + 160 * cc, lds0 + L_WO + pc * 1024);
      }
    }
    const int kad = L_K + l31 * 8;
    float negm;
    {
      const LAS char* kp = L + kad + (t + 8) * 256;
      const u32x2 k0 = *(const LAS u32x2*)(kp + (2 * hh) * PL), k1 = *(const LAS u32x2*)(kp + (2 * hh + 1) * PL), k2 = *(const LAS u32x2*)(kp + 4 * PL);
      const u32x4 q0 = __builtin_bit_cast(u32x4, qf0); const u32x2 q1 = __builtin_bit_cast(u32x2, qf1);
      float d = dot2(q0[0], k0[0], 0.f); d = dot2(q0[1], k0[1], d); d = dot2(q0[2], k1[0], d); d = dot2(q0[3], k1[1], d);
      d = dot2(q1[0], k2[0], d); d = dot2(q1[1], k2[1], d);
      negm = -swap_add(d);
    }
    const int vquad = 4 * ((lane >> 4) & 1) + (lane & 3);
    const int vad = vquad < 5 ? L_V + vquad * PL + (4 * hh + ((lane & 15) >> 2)) * 8 : L_ONE;
    const int vst = vquad < 5 ? 1 : 0;
    auto tile = [&](int kt, const f32x16& cin) {
      const LAS char* kp = L + kad + kt * 256;
      const bf16x4 ka = *(const LAS bf16x4*)(kp + (2 * hh) * PL), kb = *(const LAS bf16x4*)(kp + (2 * hh + 1) * PL), kc = *(const LAS bf16x4*)(kp + 4 * PL);
      const LAS char* vp = L + vad + vst * kt * 256;
      const bf16x8 v0 = cat8(trd(vp), trd(vp + vst * 64)), v1 = cat8(trd(vp + vst * 128), trd(vp + vst * 192));
      f32x16 s = MFMA32(cat8(ka, kb), qf0, cin);
      s = MFMA32K8(kc, qf1, s);
      u32x4 p0, p1;
#pragma unroll
      for (int e = 0; e < 4; ++e) {
        p0[e] = pk2(__builtin_amdgcn_exp2f(s[2 * e]), __builtin_amdgcn_exp2f(s[2 * e + 1]));
        p1[e] = pk2(__builtin_amdgcn_exp2f(s[8 + 2 * e]), __builtin_amdgcn_exp2f(s[8 + 2 * e + 1]));
      }
      o = MFMA32(v0, __builtin_bit_cast(bf16x8, p0), o);
      o = MFMA32(v1, __builtin_bit_cast(bf16x8, p1), o);
    };
    f32x16 cm;
#pragma unroll
    for (int r = 0; r < 16; ++r) { const int kr = (r & 3) + 8 * (r >> 2) + 4 * hh; cm[r] = kr <= l31 ? negm : -INFINITY; }
    tile(t + 8, cm);
#pragma unroll
    for (int r = 0; r < 16; ++r) cm[r] = negm;
    const int lo = pp >= 1 ? t + 1 : 8;
#pragma unroll 1
    for (int kt = t + 7; kt >= lo; --kt) tile(kt, cm);
    if (pp >= 1) {
#pragma unroll
      for (int r = 0; r < 16; ++r) { const int kr = (r & 3) + 8 * (r >> 2) + 4 * hh; cm[r] = kr > l31 ? negm : -INFINITY; }
      tile(t, cm);
    }
    if (w == 0) STAMP(6);
    auto r = __builtin_amdgcn_permlane32_swap(__float_as_uint(o[8]), __float_as_uint(o[8]), false, false);
    const float inv = 1.f / __uint_as_float(r[1]);
    u32x4 c0;
    c0[0] = pk2(o[0] * inv, o[1] * inv); c0[1] = pk2(o[2] * inv, o[3] * inv); c0[2] = pk2(o[4] * inv, o[5] * inv); c0[3] = pk2(o[6] * inv, o[7] * inv);
    const u32x2 c1 = (u32x2){pk2(o[8] * inv, o[9] * inv), pk2(o[10] * inv, o[11] * inv)};
    const int tt = pp * 16 + 2 * t + (l31 >> 4);
    const unsigned base = WS_O + (unsigned)tt * 80 * 256 + (l31 & 15) * 16 + (unsigned)(5 * (hd >> 1)) * 256;
    __builtin_amdgcn_raw_buffer_store_b128(c0, rws, base + (2 * (hd & 1) + hh) * 256, 0, SC1);
    if (hh == 0) __builtin_amdgcn_raw_buffer_store_b64(c1, rws, base + 4 * 256 + (hd & 1) * 8, 0, SC1);
    WAITV(0);
    if (lane == 0) __builtin_amdgcn_raw_buffer_store_b64(tag2, rws, WS_FLAGO + (((pp * 8 + t) * 2 + (hd >> 4)) * 16 + (hd & 15)) * 8, 0, SC1);
  }
  if (w == 0) STAMP(7);
  BARRIER();

  const int nt = w & 3, kh = w >> 2;
  const int tok = 64 * R + 16 * nt + l15;
  u32x2 xr[5], wrv[5];
  float swr = 0.f;
  if (kh == 0) {
#pragma unroll
    for (int ft = 0; ft < 5; ++ft) {
      xr[ft] = *(const u32x2*)(p.x + (size_t)tok * D + 80 * cc + 16 * ft + 4 * fg);
      wrv[ft] = *(const u32x2*)(p.wr + 80 * cc + 16 * ft + 4 * fg);
    }
    const unsigned* wr32 = (const unsigned*)p.wr;
#pragma unroll
    for (int i = 0; i < 5; ++i) swr = dot2(wr32[lane + 64 * i], 0x3f803f80u, swr);
    swr = wave_fsum(swr);
  }
  f32x4 acc[5];
#pragma unroll
  for (int ft = 0; ft < 5; ++ft) acc[ft] = (f32x4){0, 0, 0, 0};
  {
    const int tg = 4 * R + nt;
    const unsigned fo = WS_FLAGO + ((((tg >> 4) * 8 + ((tg >> 1) & 7)) * 2 + kh) * 16 + l15) * 8;
    unsigned spins = 0;
    for (;;) {
      const u32x2 f = __builtin_amdgcn_raw_buffer_load_b64(rws, fo, 0, SC1);
      if (__all(f[0] == tag2[0] && f[1] == tag2[1])) break;
      if (++spins > SPIN_CAP) break;
      __builtin_amdgcn_s_sleep(1);
    }
    asm volatile("" ::: "memory");
    if (w == 0) STAMP(8);
    u32x4 of[10];
#pragma unroll
    for (int s = 0; s < 10; ++s) of[s] = __builtin_amdgcn_raw_buffer_load_b128(rws, WS_O + (unsigned)(tg * 80 + 4 * (10 * kh + s)) * 256 + lane * 16, 0, SC1);
    const LAS char* wp = L + L_WO + (unsigned)(kh * 10 * 32 + fg * 4 + ((lane & 15) >> 2)) * 160 + (lane & 3) * 8;
#pragma unroll
    for (int s = 0; s < 10; ++s) {
#pragma unroll
      for (int ft = 0; ft < 5; ++ft) {
        const bf16x8 a = cat8(trd(wp + (s * 32) * 160 + ft * 32), trd(wp + (s * 32 + 16) * 160 + ft * 32));
        acc[ft] = MFMA16(a, __builtin_bit_cast(bf16x8, of[s]), acc[ft]);
      }
    }
  }
  if (w == 0) STAMP(9);
  if (kh == 1) {
#pragma unroll
    for (int ft = 0; ft < 5; ++ft) *(LAS f32x4*)(L + L_RED + ((nt * 5 + ft) * 64 + lane) * 16) = acc[ft];
  }
  WAITL0(); BARRIER();
  unsigned hp[10];
  float mb = 0.f, inv_sd = 0.f;
  if (kh == 1) {
    const int rl = lane >> 1, grow = 8 * ((rl >> 2) & 3) + 4 * (rl >> 4) + (rl & 3);
    if (is_gu) {
      const unsigned vo = (unsigned)grow * (F * 2) + (lane & 1) * 16;
#pragma unroll 1
      for (int pc = nt; pc < 40; pc += 4) { const int m = pc >= 20, s = pc - 20 * m; glds16(vo, (const char*)(m ? p.wu : p.wg) + (size_t)s * 32 * F * 2 + gs * 32, lds0 + L_FW + pc * 1024); }
    } else if (is_dn) {
      const unsigned vo = (unsigned)grow * (D * 2) + (lane & 1) * 16;
#pragma unroll 1
      for (int pc = nt; pc < 54; pc += 4) glds16(vo, (const char*)p.wd + (size_t)pc * 32 * D * 2 + dn * 32, lds0 + L_FW + pc * 1024);
    }
  } else {
    float s1 = 0.f, s2 = 0.f, s3 = 0.f;
#pragma unroll
    for (int ft = 0; ft < 5; ++ft) {
      const f32x4 o2 = *(const LAS f32x4*)(L + L_RED + ((nt * 5 + ft) * 64 + lane) * 16);
      const unsigned a01 = pk2(acc[ft][0] + o2[0], acc[ft][1] + o2[1]), a23 = pk2(acc[ft][2] + o2[2], acc[ft][3] + o2[3]);
      const unsigned h01 = pk2(bf2f(xr[ft][0]) + bf2f(a01), bfhi(xr[ft][0]) + bfhi(a01)), h23 = pk2(bf2f(xr[ft][1]) + bf2f(a23), bfhi(xr[ft][1]) + bfhi(a23));
      hp[2 * ft] = h01; hp[2 * ft + 1] = h23;
      __builtin_amdgcn_raw_buffer_store_b64((u32x2){h01, h23}, rws, WS_H + (unsigned)tok * 1280 + (80 * cc + 16 * ft + 4 * fg) * 2, 0, SC1);
      s1 = dot2(h01, 0x3f803f80u, s1); s1 = dot2(h23, 0x3f803f80u, s1);
      s2 = dot2(h01, h01, s2); s2 = dot2(h23, h23, s2);
      s3 = dot2(h01, wrv[ft][0], s3); s3 = dot2(h23, wrv[ft][1], s3);
    }
    s1 = swap_add(swap16_add(s1)); s2 = swap_add(swap16_add(s2)); s3 = swap_add(swap16_add(s3));
    if (lane < 16) __builtin_amdgcn_raw_buffer_store_b128((u32x4){__float_as_uint(s1), __float_as_uint(s2), __float_as_uint(s3), tag32}, rws, WS_STATS + ((R * 8 + cc) * 64 + 16 * nt + l15) * 16, 0, SC1);
    if (w == 0) STAMP(10);
    float t1, t2, t3;
    {
      const unsigned so = WS_STATS + ((R * 8 + fg) * 64 + 16 * nt + l15) * 16;
      unsigned spins = 0;
      for (;;) {
        const u32x4 ga = __builtin_amdgcn_raw_buffer_load_b128(rws, so, 0, SC1), gb = __builtin_amdgcn_raw_buffer_load_b128(rws, so + 4 * 1024, 0, SC1);
        t1 = __uint_as_float(ga[0]) + __uint_as_float(gb[0]); t2 = __uint_as_float(ga[1]) + __uint_as_float(gb[1]); t3 = __uint_as_float(ga[2]) + __uint_as_float(gb[2]);
        if (__all(ga[3] == tag32 && gb[3] == tag32)) break;
        if (++spins > SPIN_CAP) break;
        __builtin_amdgcn_s_sleep(1);
      }
    }
    t1 = swap_add(swap16_add(t1)); t2 = swap_add(swap16_add(t2)); t3 = swap_add(swap16_add(t3));
    const float mean = t1 * (1.f / D);
    const float var = fmaxf(t2 * (1.f / D) - mean * mean, 0.f);
    mb = rbf(mean);
    const float sd = rbf(sqrtf(rbf(rbf(var) + 1e-5f)));
    inv_sd = 1.f / sd;
    const unsigned rb = pk2((t3 - mb * swr) * inv_sd, 0.f) & 0xffffu;
    const unsigned key = ((rb ^ ((rb & 0x8000u) ? 0xffffu : 0x8000u)) << 16) | (unsigned)(2047 - tok);
    if (lane < 16) {
      *(LAS unsigned*)(L + L_KEY + (16 * nt + l15) * 4) = key;
      *(LAS unsigned*)(L + L_MS + (16 * nt + l15) * 4) = (__float_as_uint(mb) & 0xffff0000u) | (__float_as_uint(sd) >> 16);
    }
    WAITV(0);
    if (w == 0) STAMP(11);
  }
  WAITL0(); BARRIER();
  if (w == 0) {
    unsigned k = *(const LAS unsigned*)(L + L_KEY + lane * 4), m = 0;
    for (int i = 0; i <= cc; ++i) { m = wave_umax(k); if (k == m) k = 0; }
    const unsigned ms = *(const LAS unsigned*)(L + L_MS + ((2047 - (int)(m & 0x7ff)) & 63) * 4);
    if (lane == 0) __builtin_amdgcn_raw_buffer_store_b128((u32x4){m, tag2[0], ms, tag2[1]}, rws, WS_CAND + (R * 8 + cc) * 16, 0, SC1);
    STAMP(12);
    unsigned k0, k1, k2, k3, m0, m1, m2, m3;
    unsigned spins = 0;
    for (;;) {
      const u32x4 a = __builtin_amdgcn_raw_buffer_load_b128(rws, WS_CAND + lane * 16, 0, SC1), bb = __builtin_amdgcn_raw_buffer_load_b128(rws, WS_CAND + 1024 + lane * 16, 0, SC1);
      const u32x4 c = __builtin_amdgcn_raw_buffer_load_b128(rws, WS_CAND + 2048 + lane * 16, 0, SC1), d = __builtin_amdgcn_raw_buffer_load_b128(rws, WS_CAND + 3072 + lane * 16, 0, SC1);
      k0 = a[0]; k1 = bb[0]; k2 = c[0]; k3 = d[0]; m0 = a[2]; m1 = bb[2]; m2 = c[2]; m3 = d[2];
      const bool ok = a[1] == tag2[0] && a[3] == tag2[1] && bb[1] == tag2[0] && bb[3] == tag2[1] && c[1] == tag2[0] && c[3] == tag2[1] && d[1] == tag2[0] && d[3] == tag2[1];
      if (__all(ok)) break;
      if (++spins > SPIN_CAP) break;
      __builtin_amdgcn_s_sleep(1);
    }
    STAMP(13);
#pragma unroll 1
    for (int i = 0; i < 8; ++i) {
      const unsigned lm = max(max(k0, k1), max(k2, k3));
      const unsigned g = wave_umax(lm);
      const unsigned ms2 = k0 == g ? m0 : (k1 == g ? m1 : (k2 == g ? m2 : m3));
      if (lm == g) { *(LAS unsigned*)(L + L_SEL + i * 4) = g; *(LAS unsigned*)(L + L_SEL + 32 + i * 4) = ms2; }
      k0 = k0 == g ? 0 : k0; k1 = k1 == g ? 0 : k1; k2 = k2 == g ? 0 : k2; k3 = k3 == g ? 0 : k3;
    }
    if (b == 0 && lane == 0) __builtin_amdgcn_raw_buffer_store_b64(tag2, rws, WS_EPOCH, 0, SC1);
    STAMP(14);
  }
  if (kh == 1) WAITV(0);
  WAITL0(); BARRIER();

  const unsigned selk = *(const LAS unsigned*)(L + L_SEL + (lane & 7) * 4), selm = *(const LAS unsigned*)(L + L_SEL + 32 + (lane & 7) * 4);
  auto out_rows = [&]() {
    if (kh == 0) {
      bool sel = false;
#pragma unroll
      for (int i = 0; i < 8; ++i) sel |= (2047 - (int)(__builtin_amdgcn_readlane((int)selk, i) & 0x7ff)) == tok;
      if (!sel) {
#pragma unroll
        for (int ft = 0; ft < 5; ++ft) {
          unsigned r2[2];
#pragma unroll
          for (int e = 0; e < 2; ++e) {
            const unsigned hv = hp[2 * ft + e];
            const unsigned d2 = pk2(bf2f(hv) - mb, bfhi(hv) - mb);
            const unsigned n2 = pk2(bf2f(d2) * inv_sd, bfhi(d2) * inv_sd);
            r2[e] = pk2(bf2f(hv) + bf2f(n2), bfhi(hv) + bfhi(n2));
          }
          *(u32x2*)(p.out + (size_t)tok * D + 80 * cc + 16 * ft + 4 * fg) = (u32x2){r2[0], r2[1]};
        }
      }
    }
  };
  if (is_gu) {
    {
      const unsigned kk = *(const LAS unsigned*)(L + L_SEL + w * 4), ms = *(const LAS unsigned*)(L + L_SEL + 32 + w * 4);
      const int row = 2047 - (int)(kk & 0x7ff);
      const float m = __uint_as_float(ms & 0xffff0000u), iv = 1.f / __uint_as_float(ms << 16);
      const u32x4 h0 = __builtin_amdgcn_raw_buffer_load_b128(rws, WS_H + (unsigned)row * 1280 + lane * 16, 0, SC1);
      const u32x4 h1 = __builtin_amdgcn_raw_buffer_load_b128(rws, WS_H + (unsigned)row * 1280 + 1024 + (lane & 15) * 16, 0, SC1);
      u32x4 n0, n1;
#pragma unroll
      for (int e = 0; e < 4; ++e) {
        const unsigned d0 = pk2(bf2f(h0[e]) - m, bfhi(h0[e]) - m), d1 = pk2(bf2f(h1[e]) - m, bfhi(h1[e]) - m);
        n0[e] = pk2(bf2f(d0) * iv, bfhi(d0) * iv); n1[e] = pk2(bf2f(d1) * iv, bfhi(d1) * iv);
      }
      *(LAS u32x4*)(L + L_XS + w * 1296 + lane * 16) = n0;
      if (lane < 16) *(LAS u32x4*)(L + L_XS + w * 1296 + 1024 + lane * 16) = n1;
    }
    WAITL0(); BARRIER();
    if (w == 0) STAMP(15);
    {
      const int m = w & 1, kq = w >> 1;
      f32x4 a4 = {0, 0, 0, 0};
#pragma unroll
      for (int i = 0; i < 5; ++i) {
        const int s = 5 * kq + i;
        const LAS char* ap = L + L_FW + (m * 20 + s) * 1024 + lane * 8;
        const bf16x8 af = cat8(trd(ap), trd(ap + 512));
        const bf16x8 bfv = *(const LAS bf16x8*)(L + L_XS + (lane & 7) * 1296 + s * 64 + fg * 16);
        a4 = MFMA16(af, bfv, a4);
      }
      *(LAS f32x4*)(L + L_FRED + (w * 64 + lane) * 16) = a4;
    }
    WAITL0(); BARRIER();
    if (w == 0) {
      f32x4 g4 = {0, 0, 0, 0}, u4 = {0, 0, 0, 0};
#pragma unroll
      for (int q = 0; q < 4; ++q) { g4 += *(const LAS f32x4*)(L + L_FRED + ((2 * q) * 64 + lane) * 16); u4 += *(const LAS f32x4*)(L + L_FRED + ((2 * q + 1) * 64 + lane) * 16); }
      float a[4];
#pragma unroll
      for (int e = 0; e < 4; ++e) { const float g = rbf(g4[e]), u = rbf(u4[e]); const float sg = rbf(g / (1.f + __expf(-g))); a[e] = sg * u; }
      if (l15 < 8) __builtin_amdgcn_raw_buffer_store_b128((u32x4){pk2(a[0], a[1]), tag32, pk2(a[2], a[3]), tag32}, rws, WS_ACT + (((gs >> 1) * 4 + 2 * (gs & 1) + (fg >> 1)) * 8 + l15) * 32 + (fg & 1) * 16, 0, SC1);
      STAMP(16);
    }
    out_rows();
  } else if (is_dn) {
    out_rows();
    const int row = 2047 - (int)(selk & 0x7ff);
    const u32x2 hv = __builtin_amdgcn_raw_buffer_load_b64(rws, WS_H + (unsigned)row * 1280 + (16 * dn + 4 * fg) * 2, 0, SC1);
    f32x4 a4 = {0, 0, 0, 0};
    {
      u32x4 g0[7], g1[7];
      unsigned spins = 0;
      for (;;) {
        bool ok = true;
#pragma unroll
        for (int i = 0; i < 7; ++i) {
          const int s = min(w + 8 * i, 53);
          const unsigned ao = WS_ACT + ((s * 4 + fg) * 8 + (lane & 7)) * 32;
          g0[i] = __builtin_amdgcn_raw_buffer_load_b128(rws, ao, 0, SC1); g1[i] = __builtin_amdgcn_raw_buffer_load_b128(rws, ao + 16, 0, SC1);
        }
#pragma unroll
        for (int i = 0; i < 7; ++i) ok = ok && g0[i][1] == tag32 && g0[i][3] == tag32 && g1[i][1] == tag32 && g1[i][3] == tag32;
        if (__all(ok)) break;
        if (++spins > SPIN_CAP) break;
        __builtin_amdgcn_s_sleep(1);
      }
      if (w == 0) STAMP(17);
#pragma unroll
      for (int i = 0; i < 7; ++i) {
        const int s = w + 8 * i;
        if (s < 54) {
          const LAS char* ap = L + L_FW + s * 1024 + lane * 8;
          const bf16x8 af = cat8(trd(ap), trd(ap + 512));
          a4 = MFMA16(af, __builtin_bit_cast(bf16x8, (u32x4){g0[i][0], g0[i][2], g1[i][0], g1[i][2]}), a4);
        }
      }
    }
    *(LAS f32x4*)(L + L_FRED + (w * 64 + lane) * 16) = a4;
    WAITL0(); BARRIER();
    if (w == 0) {
      f32x4 y = {0, 0, 0, 0};
#pragma unroll
      for (int q = 0; q < 8; ++q) y += *(const LAS f32x4*)(L + L_FRED + (q * 64 + lane) * 16);
      const unsigned rk = selk >> 16, rbits = (rk & 0x8000u) ? (rk ^ 0x8000u) : (rk ^ 0xffffu);
      const float rv = bf2f(rbits), sig = rbf(1.f / (1.f + __expf(-rv)));
      const float m = __uint_as_float(selm & 0xffff0000u), iv = 1.f / __uint_as_float(selm << 16);
      unsigned r2[2];
#pragma unroll
      for (int e = 0; e < 2; ++e) {
        const unsigned d2 = pk2(bf2f(hv[e]) - m, bfhi(hv[e]) - m);
        const unsigned n2 = pk2(bf2f(d2) * iv, bfhi(d2) * iv);
        const unsigned y2 = pk2(rbf(y[2 * e]) * sig, rbf(y[2 * e + 1]) * sig);
        const unsigned t2 = pk2(bf2f(n2) + bf2f(y2), bfhi(n2) + bfhi(y2));
        r2[e] = pk2(bf2f(hv[e]) + bf2f(t2), bfhi(hv[e]) + bfhi(t2));
      }
      if (l15 < 8) *(u32x2*)(p.out + (size_t)row * D + 16 * dn + 4 * fg) = (u32x2){r2[0], r2[1]};
      STAMP(18);
    }
  } else {
    out_rows();
  }
  if (w == 0) STAMP(19);
  if (b == 0 && w == 0) { unsigned o = WS_FLAGA; for (int i = 0; i < 200; ++i) { const unsigned v = __builtin_amdgcn_raw_buffer_load_b32(rws, o, 0, SC1); o = WS_FLAGA + ((v ^ v) & 128); asm volatile("" : "+v"(o)); } if (o == 12345) p.out[0] = 1; }
  STAMP_FLUSH();
}

extern "C" void kernel_launch(void* const* d_in, const int* in_sizes, int n_in, void* d_out, int out_size, void* d_ws, size_t ws_size, hipStream_t stream) {
  Params p;
  p.x = (const u16*)d_in[0]; p.wqkv = (const u16*)d_in[1]; p.wo = (const u16*)d_in[2]; p.wr = (const u16*)d_in[4];
  p.wg = (const u16*)d_in[5]; p.wu = (const u16*)d_in[6]; p.wd = (const u16*)d_in[7]; p.out = (u16*)d_out; p.ws = (unsigned char*)d_ws;
  fused_kernel<<<256, 512, 0, stream>>>(p);
}
```

```cpp
#define PROBE 2
#include <hip/hip_runtime.h>
#include <stdint.h>

typedef unsigned short u16;
typedef short bf16x8 __attribute__((ext_vector_type(8)));
typedef short bf16x4 __attribute__((ext_vector_type(4)));
typedef __bf16 bf2 __attribute__((ext_vector_type(2)));
typedef float f32x16 __attribute__((ext_vector_type(16)));
typedef float f32x4 __attribute__((ext_vector_type(4)));
typedef float f32x2 __attribute__((ext_vector_type(2)));
typedef unsigned u32x4 __attribute__((ext_vector_type(4)));
typedef unsigned u32x2 __attribute__((ext_vector_type(2)));
typedef unsigned long long u64;
#define LAS __attribute__((address_space(3)))
#define SBAR() __builtin_amdgcn_sched_barrier(0)
#define BARRIER() asm volatile("s_barrier" ::: "memory")
#define WAITL0() asm volatile("s_waitcnt lgkmcnt(0)" ::: "memory")
#define WAITV(n) asm volatile("s_waitcnt vmcnt(" #n ")" ::: "memory")

constexpr int S = 2048, D = 640, F = 1728;
constexpr int SC1 = 16;

constexpr unsigned WS_EPOCH = 0;
constexpr unsigned WS_FLAGA = 4096;
constexpr unsigned WS_FLAGO = WS_FLAGA + 32768;
constexpr unsigned WS_CAND = WS_FLAGO + 16384;
constexpr unsigned WS_ACT = WS_CAND + 4096;
constexpr unsigned WS_STATS = 131072;
constexpr unsigned WS_QKV = 524288;
constexpr unsigned WS_O = WS_QKV + 480 * 16384;
constexpr unsigned WS_H = WS_O + S * D * 2;
#define STAMP_OFF (200u << 20)
#define NSTAMP 32

constexpr int L_SLOT = 32768, L_W = 16384;
constexpr int PL = 4128;
constexpr int L_K = 0, L_V = 5 * PL, L_ONE = 10 * PL;
constexpr int L_WO = 43008;
constexpr int L_RED = 0;
constexpr int L_FW = 43008;
constexpr int L_XS = 0;
constexpr int L_FRED = 12288;
constexpr int L_MISC = 147456;
constexpr int L_RS = L_MISC;
constexpr int L_CS = L_MISC + 1024;
constexpr int L_EP = L_MISC + 1536;
constexpr int L_KEY = L_MISC + 2048;
constexpr int L_MS = L_MISC + 2304;
constexpr int L_SEL = L_MISC + 2560;
constexpr int L_STAMP = L_MISC + 4096;
constexpr int LDS_BYTES = 163840;

struct Params { const u16* x; const u16* wqkv; const u16* wo; const u16* wr; const u16* wg; const u16* wu; const u16* wd; u16* out; unsigned char* ws; };

__device__ __forceinline__ float bf2f(unsigned v) { return __uint_as_float(v << 16); }
__device__ __forceinline__ float bfhi(unsigned v) { return __uint_as_float(v & 0xffff0000u); }
__device__ __forceinline__ unsigned pk2(float a, float b) { f32x2 f = {a, b}; return __builtin_bit_cast(unsigned, __builtin_convertvector(f, bf2)); }
__device__ __forceinline__ float rbf(float f) { return __uint_as_float(pk2(f, 0.f) << 16); }
__device__ __forceinline__ float dot2(unsigned a, unsigned b, float c) { return __builtin_amdgcn_fdot2_f32_bf16(__builtin_bit_cast(bf2, a), __builtin_bit_cast(bf2, b), c, false); }
__device__ __forceinline__ float swap_add(float v) { auto r = __builtin_amdgcn_permlane32_swap(__float_as_uint(v), __float_as_uint(v), false, false); return __uint_as_float(r[0]) + __uint_as_float(r[1]); }
__device__ __forceinline__ float swap16_add(float v) { auto r = __builtin_amdgcn_permlane16_swap(__float_as_uint(v), __float_as_uint(v), false, false); return __uint_as_float(r[0]) + __uint_as_float(r[1]); }
__device__ __forceinline__ void glds16(unsigned voff, const void* sbase, unsigned lds) {
  asm volatile("s_mov_b32 m0, %2\n\ts_nop 0\n\tglobal_load_lds_dwordx4 %0, %1" ::"v"(voff), "s"(sbase), "s"(lds) : "memory");
}
__device__ __forceinline__ void glds16c(unsigned voff, const void* sbase, unsigned lds) {
  asm volatile("s_mov_b32 m0, %2\n\ts_nop 0\n\tglobal_load_lds_dwordx4 %0, %1 sc1" ::"v"(voff), "s"(sbase), "s"(lds) : "memory");
}
__device__ __forceinline__ bf16x4 trd(const LAS char* p) { return __builtin_amdgcn_ds_read_tr16_b64_v4i16((LAS bf16x4*)p); }
__device__ __forceinline__ bf16x8 cat8(bf16x4 a, bf16x4 b) { return (bf16x8){a[0], a[1], a[2], a[3], b[0], b[1], b[2], b[3]}; }
#define MFMA32(a, b, c) __builtin_amdgcn_mfma_f32_32x32x16_bf16(a, b, c, 0, 0, 0)
#define MFMA32K8(a, b, c) __builtin_amdgcn_mfma_f32_32x32x8bf16_1k(a, b, c, 0, 0, 0)
#define MFMA16(a, b, c) __builtin_amdgcn_mfma_f32_16x16x32_bf16(a, b, c, 0, 0, 0)
template <int CTRL, int RMASK> __device__ __forceinline__ unsigned dppmax(unsigned v) { return max(v, (unsigned)__builtin_amdgcn_update_dpp(0, (int)v, CTRL, RMASK, 0xf, false)); }
__device__ __forceinline__ unsigned wave_umax(unsigned v) {
  v = dppmax<0x111, 0xf>(v); v = dppmax<0x112, 0xf>(v); v = dppmax<0x114, 0xf>(v); v = dppmax<0x118, 0xf>(v);
  v = dppmax<0x142, 0xa>(v); v = dppmax<0x143, 0xc>(v);
  return (unsigned)__builtin_amdgcn_readlane((int)v, 63);
}

template <int CTRL> __device__ __forceinline__ float dppadd(float v) { return v + __int_as_float(__builtin_amdgcn_update_dpp(0, __float_as_int(v), CTRL, 0xf, 0xf, false)); }
__device__ __forceinline__ float wave_fsum(float v) { v = dppadd<0x128>(v); v = dppadd<0x124>(v); v = dppadd<0x122>(v); v = dppadd<0x121>(v); return swap_add(swap16_add(v)); }

#define STAMP(k) do { if ((k) == PROBE) tprobe_ = __builtin_amdgcn_s_memrealtime(); } while (0)
#define STAMP_INIT() do {} while (0)
#define STAMP_FLUSH() do { if (blockIdx.x == 0 && w == 0) { const u64 now_ = __builtin_amdgcn_s_memrealtime(); const u64 until_ = now_ + 10 * (tprobe_ - tstart_); while (__builtin_amdgcn_s_memrealtime() < until_) __builtin_amdgcn_s_sleep(1); } } while (0)

constexpr unsigned SPIN_CAP = 1u << 16;

__global__ __launch_bounds__(512, 2) void fused_kernel(Params p) {
  __shared__ __attribute__((aligned(16))) char smem[LDS_BYTES];
  LAS char* L = (LAS char*)smem;
  const unsigned lds0 = (unsigned)(uintptr_t)smem;
  const int tid = threadIdx.x, lane = tid & 63;
  const int w = __builtin_amdgcn_readfirstlane(tid >> 6);
  const int b = blockIdx.x, xcd = b & 7, bi = b >> 3;
  const int l31 = lane & 31, hh = lane >> 5, l15 = lane & 15, fg = lane >> 4;
  const __amdgpu_buffer_rsrc_t rws = __builtin_amdgcn_make_buffer_rsrc(p.ws, 0, 0x7fffffff, 0x00020000);
  const int aP = 4 * (xcd >> 1) + (bi >> 3), aj = 8 * (xcd & 1) + (bi & 7);
  const int hd = bi, pp = xcd;
  const int R = 4 * xcd + (bi >> 3), cc = bi & 7;
  const int gs = xcd * 14 + bi;
  const bool is_gu = bi < 14 && gs < 108;
  const int dn = xcd * 5 + (bi - 14);
  const bool is_dn = bi >= 14 && bi < 19;
  const u64 tstart_ = __builtin_amdgcn_s_memrealtime(); u64 tprobe_ = tstart_;
  STAMP_INIT();

  if (aj < 15 && w >= 4) {
    const int lw = w - 4;
    const unsigned xsw = (unsigned)(4 * (lw & 1) + (lane >> 4));
    const unsigned voff_x = (unsigned)(lane >> 3) * 1280u + (((unsigned)(lane & 7) ^ xsw) << 4);
    const unsigned ch = (unsigned)(lane & 15) ^ (((unsigned)(lane >> 4) << 2) | (unsigned)lw);
    const unsigned voff_w = (unsigned)(lane >> 4) * 3840u + ch * 16u;
    const char* xb = (const char*)p.x + (size_t)(128 * aP + 8 * lw) * 1280;
    const char* wb = (const char*)p.wqkv + (size_t)(4 * lw) * 3840 + 256 * aj;
    const unsigned lbase = lds0 + lw * 1024;
    const int rot = ((bi & 7) * 5 / 4 + (bi >> 3) * 5 / 2) % 10;
    auto issue = [&](int s) {
      const unsigned sl = lbase + (s & 3) * L_SLOT;
      int ss = s + rot; if (ss >= 10) ss -= 10;
#pragma unroll
      for (int i = 0; i < 4; ++i) {
        glds16(voff_x, xb + (size_t)i * 32 * 1280 + ss * 128, sl + i * 4096);
        glds16(voff_w, wb + ((size_t)ss * 64 + i * 16) * 3840, sl + L_W + i * 4096);
      }
    };
    issue(0); issue(1); issue(2); issue(3);
    WAITV(24); BARRIER();
    if (w == 4) STAMP(1);
    WAITV(16); BARRIER();
#pragma unroll 1
    for (int s = 1; s <= 6; ++s) { issue(s + 3); WAITV(16); BARRIER(); }
    WAITV(8); BARRIER();
    WAITV(0); BARRIER();
    BARRIER();
    BARRIER();
  } else if (w < 4) {
    if (w == 0) {
      const u32x2 e = __builtin_amdgcn_raw_buffer_load_b64(rws, WS_EPOCH, 0, SC1);
      const u64 t = (((u64)e[1] << 32) | e[0]) + 1;
      if (lane == 0) *(LAS u32x2*)(L + L_EP) = (u32x2){(unsigned)t, (unsigned)(t >> 32)};
      __builtin_amdgcn_raw_buffer_store_b128((u32x4){0, 0, 0, 0}, rws, WS_STATS + (R * 8 + cc) * 1024 + lane * 16, 0, SC1);
      if (is_gu && lane < 32) __builtin_amdgcn_raw_buffer_store_b128((u32x4){0, 0, 0, 0}, rws, WS_ACT + (((gs >> 1) * 4 + 2 * (gs & 1) + (lane >> 4)) * 8 + (lane & 7)) * 32 + ((lane >> 3) & 1) * 16, 0, SC1);
    }
    if (aj < 15) {
      const int wm = w >> 1, wn = w & 1;
      unsigned xa[4], wa[4];
#pragma unroll
      for (int ks = 0; ks < 4; ++ks) xa[ks] = (unsigned)(64 * wm + l31) * 128u + ((((unsigned)(2 * ks + hh)) ^ (((unsigned)l31 >> 1) & 7u)) << 4);
      const unsigned xo0 = wn * 4096, xo1 = 4096 - xo0;
      {
        const unsigned blk = (lane >> 4) & 1, q = (lane & 15) >> 2, pq = lane & 3;
#pragma unroll
        for (int j = 0; j < 2; ++j)
#pragma unroll
          for (int t = 0; t < 2; ++t) {
            const unsigned jj = j ^ wm;
            const unsigned row = 8 * hh + 4 * t + q, chn = 4 * (2 * wn + jj) + 2 * blk + (pq >> 1), m = (q << 2) | (unsigned)(2 * hh + t);
            wa[j * 2 + t] = L_W + 256u * row + 16u * (chn ^ m) + 8u * (pq & 1);
          }
      }
      f32x16 a00 = {}, a01 = {}, a10 = {}, a11 = {};
      float ssum = 0.f, ssq = 0.f, csum = 0.f;
      bf16x8 xf0[2], wf0[2], xf1[2], wf1[2];
      const unsigned ONE2 = 0x3f803f80u;
      unsigned xb0[4], xb1[4], wb[4];
#define SETSLOT(so) do { _Pragma("unroll") for (int k_ = 0; k_ < 4; ++k_) { xb0[k_] = xa[k_] + (so) + xo0; xb1[k_] = xa[k_] + (so) + xo1; wb[k_] = wa[k_] + (so); } } while (0)
#define RD(XF, WF, ks) do { \
        XF[0] = *(const LAS bf16x8*)(L + xb0[ks]); XF[1] = *(const LAS bf16x8*)(L + xb1[ks]); \
        WF[0] = cat8(trd(L + wb[0] + (ks) * 4096), trd(L + wb[1] + (ks) * 4096)); \
        WF[1] = cat8(trd(L + wb[2] + (ks) * 4096), trd(L + wb[3] + (ks) * 4096)); } while (0)
#define MM(XF, WF) do { \
        a00 = MFMA32(WF[0], XF[0], a00); a01 = MFMA32(WF[1], XF[0], a01); \
        a10 = MFMA32(WF[0], XF[1], a10); a11 = MFMA32(WF[1], XF[1], a11); \
        { const u32x4 xs_ = __builtin_bit_cast(u32x4, XF[0]); const u32x4 ws_ = __builtin_bit_cast(u32x4, WF[0]); \
          _Pragma("unroll") for (int e_ = 0; e_ < 4; ++e_) { ssum = dot2(xs_[e_], ONE2, ssum); ssq = dot2(xs_[e_], xs_[e_], ssq); csum = dot2(ws_[e_], ONE2, csum); } } } while (0)
#define PINALL() asm volatile("" : "+v"(a00), "+v"(a01), "+v"(a10), "+v"(a11), "+v"(ssum), "+v"(ssq), "+v"(csum))
#define ILV() do { _Pragma("unroll") for (int g_ = 0; g_ < 4; ++g_) { \
        __builtin_amdgcn_sched_group_barrier(0x8, 1, 0); __builtin_amdgcn_sched_group_barrier(0x100, 2, 0); __builtin_amdgcn_sched_group_barrier(0x2, 3, 0); } } while (0)
#define STEP(XN, WN, ksn, XC, WC) do { RD(XN, WN, ksn); MM(XC, WC); ILV(); PINALL(); SBAR(); } while (0)
      SETSLOT(0);
      BARRIER();
      RD(xf0, wf0, 0);
      SBAR();
#pragma unroll 1
      for (int s = 0; s < 9; ++s) {
        STEP(xf1, wf1, 1, xf0, wf0);
        STEP(xf0, wf0, 2, xf1, wf1);
        STEP(xf1, wf1, 3, xf0, wf0);
        SETSLOT(((s + 1) & 3) * L_SLOT);
        WAITL0(); BARRIER();
        STEP(xf0, wf0, 0, xf1, wf1);
      }
      STEP(xf1, wf1, 1, xf0, wf0);
      STEP(xf0, wf0, 2, xf1, wf1);
      STEP(xf1, wf1, 3, xf0, wf0);
      MM(xf1, wf1); PINALL();
      if (w == 0) STAMP(2);
      ssum = swap_add(ssum); ssq = swap_add(ssq); csum = swap_add(csum);
      {
        const float mean = ssum * (1.f / D);
        const float var = fmaxf(ssq * (1.f / D) - mean * mean, 0.f);
        const float mb = rbf(mean), vb = rbf(var);
        const float sd = rbf(sqrtf(rbf(vb + 1e-5f)));
        if (lane < 32) {
          *(LAS f32x2*)(L + L_RS + (64 * wm + 32 * wn + l31) * 8) = (f32x2){mb, 1.f / sd};
          *(LAS float*)(L + L_CS + (64 * wn + 32 * wm + l31) * 4) = csum;
        }
      }
      WAITV(0); WAITL0(); BARRIER();
      const float qs = aj < 5 ? 0.22360679774997896f * 1.4426950408889634f : 1.f;
#pragma unroll
      for (int i = 0; i < 2; ++i) {
        const int tok = 64 * wm + 32 * (i ^ wn) + l31;
        const f32x2 rs = *(const LAS f32x2*)(L + L_RS + tok * 8);
        const float sc = rs[1] * qs, nm = -rs[0] * sc;
#pragma unroll
        for (int j = 0; j < 2; ++j) {
          const f32x16& a = i == 0 ? (j == 0 ? a00 : a01) : (j == 0 ? a10 : a11);
          const int f0 = 64 * wn + 32 * (j ^ wm) + 4 * hh;
          const unsigned go = WS_QKV + (unsigned)(32 * aj + (f0 >> 2)) * 16384u + (unsigned)(128 * aP + tok) * 8u;
#pragma unroll
          for (int rq = 0; rq < 4; ++rq) {
            const f32x4 cs = *(const LAS f32x4*)(L + L_CS + (f0 + 8 * rq) * 4);
            const float v0 = fmaf(a[rq * 4 + 0], sc, nm * cs[0]), v1 = fmaf(a[rq * 4 + 1], sc, nm * cs[1]);
            const float v2 = fmaf(a[rq * 4 + 2], sc, nm * cs[2]), v3 = fmaf(a[rq * 4 + 3], sc, nm * cs[3]);
            __builtin_amdgcn_raw_buffer_store_b64((u32x2){pk2(v0, v1), pk2(v2, v3)}, rws, go + rq * 32768, 0, SC1);
          }
        }
      }
      WAITV(0); BARRIER();
    } else {
      WAITV(0);
    }
  }
  if (aj >= 15) { WAITL0(); BARRIER(); }
  const u32x2 tag2 = *(const LAS u32x2*)(L + L_EP);
  const unsigned tag32 = tag2[0] | 1u;
  if (aj < 15 && tid == 0) __builtin_amdgcn_raw_buffer_store_b64(tag2, rws, WS_FLAGA + (aP * 16 + aj) * 128, 0, SC1);
  if (w == 0) STAMP(3);

  f32x16 o = (f32x16){};
  {
    const int t = w;
    if (tid < 4) *(LAS unsigned*)(L + L_ONE + tid * 4) = tid == 0 ? 0x00003f80u : 0u;
    {
      const int sec = lane < 4 ? 0 : (lane < 12 ? 1 : 2);
      const int k = lane < 4 ? lane : (lane < 12 ? lane - 4 : lane - 12);
      const int col = sec * 640 + 20 * hd + ((k & 1) ? 19 : 0);
      int Pn = sec == 0 ? 2 * pp + (k >> 1) : 2 * pp - 2 + (k >> 1);
      const bool need = lane < 20 && Pn >= 0;
      const unsigned fo = WS_FLAGA + (unsigned)((need ? Pn : 0) * 16 + (col >> 7)) * 128;
      unsigned spins = 0;
      for (;;) {
        const u32x2 f = __builtin_amdgcn_raw_buffer_load_b64(rws, fo, 0, SC1);
        if (__all(!need || (f[0] == tag2[0] && f[1] == tag2[1]))) break;
        if (++spins > SPIN_CAP) break;
        __builtin_amdgcn_s_sleep(1);
      }
    }
    asm volatile("" ::: "memory");
    if (w == 0) STAMP(4);
    {
      const char* qb = (const char*)p.ws + WS_QKV;
#pragma unroll
      for (int i = 0; i < 5; ++i) {
        const int pc = w + 8 * i, pl = pc >> 2, qt = pc & 3;
        const int tok0 = 256 * pp - 256 + 128 * qt;
        if (tok0 >= 0) glds16c(lane * 16, qb + (size_t)((pl < 5 ? 160 + 5 * hd + pl : 320 + 5 * hd + pl - 5)) * 16384 + tok0 * 8, lds0 + pl * PL + qt * 1024);
      }
    }
    bf16x8 qf0; bf16x4 qf1;
    {
      const unsigned qo = WS_QKV + (unsigned)(5 * hd) * 16384u + (unsigned)(256 * pp + 32 * t + l31) * 8u;
      const u32x2 a = __builtin_amdgcn_raw_buffer_load_b64(rws, qo + (2 * hh) * 16384, 0, SC1);
      const u32x2 c = __builtin_amdgcn_raw_buffer_load_b64(rws, qo + (2 * hh + 1) * 16384, 0, SC1);
      u32x2 z = __builtin_amdgcn_raw_buffer_load_b64(rws, qo + 4 * 16384, 0, SC1);
      if (hh) z = (u32x2){0, 0};
      qf0 = __builtin_bit_cast(bf16x8, (u32x4){a[0], a[1], c[0], c[1]});
      qf1 = __builtin_bit_cast(bf16x4, z);
    }
    WAITV(0); WAITL0(); BARRIER();
    if (w == 0) STAMP(5);
    {
#pragma unroll 1
      for (int pc = w; pc < 100; pc += 8) {
        const int n = pc * 64 + lane, rho = n / 10, ch = n - rho * 10;
        const int s = rho >> 5, tq = (rho >> 4) & 1, kg = (rho >> 2) & 3, q = rho & 3;
        const int cidx = 4 * s + kg, g2 = cidx / 5, c5 = cidx - 5 * g2;
        const int quad = 10 * g2 + ((tq ? 0x98732 : 0x46510) >> (4 * c5) & 15);
        glds16((unsigned)(4 * quad + q) * 1280u + ch * 16u, (const char*)p.wo + 160 * cc, lds0 + L_WO + pc * 1024);
      }
    }
    const int kad = L_K + l31 * 8;
    float negm;
    {
      const LAS char* kp = L + kad + (t + 8) * 256;
      const u32x2 k0 = *(const LAS u32x2*)(kp + (2 * hh) * PL), k1 = *(const LAS u32x2*)(kp + (2 * hh + 1) * PL), k2 = *(const LAS u32x2*)(kp + 4 * PL);
      const u32x4 q0 = __builtin_bit_cast(u32x4, qf0); const u32x2 q1 = __builtin_bit_cast(u32x2, qf1);
      float d = dot2(q0[0], k0[0], 0.f); d = dot2(q0[1], k0[1], d); d = dot2(q0[2], k1[0], d); d = dot2(q0[3], k1[1], d);
      d = dot2(q1[0], k2[0], d); d = dot2(q1[1], k2[1], d);
      negm = -swap_add(d);
    }
    const int vquad = 4 * ((lane >> 4) & 1) + (lane & 3);
    const int vad = vquad < 5 ? L_V + vquad * PL + (4 * hh + ((lane & 15) >> 2)) * 8 : L_ONE;
    const int vst = vquad < 5 ? 1 : 0;
    auto tile = [&](int kt, const f32x16& cin) {
      const LAS char* kp = L + kad + kt * 256;
      const bf16x4 ka = *(const LAS bf16x4*)(kp + (2 * hh) * PL), kb = *(const LAS bf16x4*)(kp + (2 * hh + 1) * PL), kc = *(const LAS bf16x4*)(kp + 4 * PL);
      const LAS char* vp = L + vad + vst * kt * 256;
      const bf16x8 v0 = cat8(trd(vp), trd(vp + vst * 64)), v1 = cat8(trd(vp + vst * 128), trd(vp + vst * 192));
      f32x16 s = MFMA32(cat8(ka, kb), qf0, cin);
      s = MFMA32K8(kc, qf1, s);
      u32x4 p0, p1;
#pragma unroll
      for (int e = 0; e < 4; ++e) {
        p0[e] = pk2(__builtin_amdgcn_exp2f(s[2 * e]), __builtin_amdgcn_exp2f(s[2 * e + 1]));
        p1[e] = pk2(__builtin_amdgcn_exp2f(s[8 + 2 * e]), __builtin_amdgcn_exp2f(s[8 + 2 * e + 1]));
      }
      o = MFMA32(v0, __builtin_bit_cast(bf16x8, p0), o);
      o = MFMA32(v1, __builtin_bit_cast(bf16x8, p1), o);
    };
    f32x16 cm;
#pragma unroll
    for (int r = 0; r < 16; ++r) { const int kr = (r & 3) + 8 * (r >> 2) + 4 * hh; cm[r] = kr <= l31 ? negm : -INFINITY; }
    tile(t + 8, cm);
#pragma unroll
    for (int r = 0; r < 16; ++r) cm[r] = negm;
    const int lo = pp >= 1 ? t + 1 : 8;
#pragma unroll 1
    for (int kt = t + 7; kt >= lo; --kt) tile(kt, cm);
    if (pp >= 1) {
#pragma unroll
      for (int r = 0; r < 16; ++r) { const int kr = (r & 3) + 8 * (r >> 2) + 4 * hh; cm[r] = kr > l31 ? negm : -INFINITY; }
      tile(t, cm);
    }
    if (w == 0) STAMP(6);
    auto r = __builtin_amdgcn_permlane32_swap(__float_as_uint(o[8]), __float_as_uint(o[8]), false, false);
    const float inv = 1.f / __uint_as_float(r[1]);
    u32x4 c0;
    c0[0] = pk2(o[0] * inv, o[1] * inv); c0[1] = pk2(o[2] * inv, o[3] * inv); c0[2] = pk2(o[4] * inv, o[5] * inv); c0[3] = pk2(o[6] * inv, o[7] * inv);
    const u32x2 c1 = (u32x2){pk2(o[8] * inv, o[9] * inv), pk2(o[10] * inv, o[11] * inv)};
    const int tt = pp * 16 + 2 * t + (l31 >> 4);
    const unsigned base = WS_O + (unsigned)tt * 80 * 256 + (l31 & 15) * 16 + (unsigned)(5 * (hd >> 1)) * 256;
    __builtin_amdgcn_raw_buffer_store_b128(c0, rws, base + (2 * (hd & 1) + hh) * 256, 0, SC1);
    if (hh == 0) __builtin_amdgcn_raw_buffer_store_b64(c1, rws, base + 4 * 256 + (hd & 1) * 8, 0, SC1);
    WAITV(0);
    if (lane == 0) __builtin_amdgcn_raw_buffer_store_b64(tag2, rws, WS_FLAGO + (((pp * 8 + t) * 2 + (hd >> 4)) * 16 + (hd & 15)) * 8, 0, SC1);
  }
  if (w == 0) STAMP(7);
  BARRIER();

  const int nt = w & 3, kh = w >> 2;
  const int tok = 64 * R + 16 * nt + l15;
  u32x2 xr[5], wrv[5];
  float swr = 0.f;
  if (kh == 0) {
#pragma unroll
    for (int ft = 0; ft < 5; ++ft) {
      xr[ft] = *(const u32x2*)(p.x + (size_t)tok * D + 80 * cc + 16 * ft + 4 * fg);
      wrv[ft] = *(const u32x2*)(p.wr + 80 * cc + 16 * ft + 4 * fg);
    }
    const unsigned* wr32 = (const unsigned*)p.wr;
#pragma unroll
    for (int i = 0; i < 5; ++i) swr = dot2(wr32[lane + 64 * i], 0x3f803f80u, swr);
    swr = wave_fsum(swr);
  }
  f32x4 acc[5];
#pragma unroll
  for (int ft = 0; ft < 5; ++ft) acc[ft] = (f32x4){0, 0, 0, 0};
  {
    const int tg = 4 * R + nt;
    const unsigned fo = WS_FLAGO + ((((tg >> 4) * 8 + ((tg >> 1) & 7)) * 2 + kh) * 16 + l15) * 8;
    unsigned spins = 0;
    for (;;) {
      const u32x2 f = __builtin_amdgcn_raw_buffer_load_b64(rws, fo, 0, SC1);
      if (__all(f[0] == tag2[0] && f[1] == tag2[1])) break;
      if (++spins > SPIN_CAP) break;
      __builtin_amdgcn_s_sleep(1);
    }
    asm volatile("" ::: "memory");
    if (w == 0) STAMP(8);
    u32x4 of[10];
#pragma unroll
    for (int s = 0; s < 10; ++s) of[s] = __builtin_amdgcn_raw_buffer_load_b128(rws, WS_O + (unsigned)(tg * 80 + 4 * (10 * kh + s)) * 256 + lane * 16, 0, SC1);
    const LAS char* wp = L + L_WO + (unsigned)(kh * 10 * 32 + fg * 4 + ((lane & 15) >> 2)) * 160 + (lane & 3) * 8;
#pragma unroll
    for (int s = 0; s < 10; ++s) {
#pragma unroll
      for (int ft = 0; ft < 5; ++ft) {
        const bf16x8 a = cat8(trd(wp + (s * 32) * 160 + ft * 32), trd(wp + (s * 32 + 16) * 160 + ft * 32));
        acc[ft] = MFMA16(a, __builtin_bit_cast(bf16x8, of[s]), acc[ft]);
      }
    }
  }
  if (w == 0) STAMP(9);
  if (kh == 1) {
#pragma unroll
    for (int ft = 0; ft < 5; ++ft) *(LAS f32x4*)(L + L_RED + ((nt * 5 + ft) * 64 + lane) * 16) = acc[ft];
  }
  WAITL0(); BARRIER();
  unsigned hp[10];
  float mb = 0.f, inv_sd = 0.f;
  if (kh == 1) {
    const int rl = lane >> 1, grow = 8 * ((rl >> 2) & 3) + 4 * (rl >> 4) + (rl & 3);
    if (is_gu) {
      const unsigned vo = (unsigned)grow * (F * 2) + (lane & 1) * 16;
#pragma unroll 1
      for (int pc = nt; pc < 40; pc += 4) { const int m = pc >= 20, s = pc - 20 * m; glds16(vo, (const char*)(m ? p.wu : p.wg) + (size_t)s * 32 * F * 2 + gs * 32, lds0 + L_FW + pc * 1024); }
    } else if (is_dn) {
      const unsigned vo = (unsigned)grow * (D * 2) + (lane & 1) * 16;
#pragma unroll 1
      for (int pc = nt; pc < 54; pc += 4) glds16(vo, (const char*)p.wd + (size_t)pc * 32 * D * 2 + dn * 32, lds0 + L_FW + pc * 1024);
    }
  } else {
    float s1 = 0.f, s2 = 0.f, s3 = 0.f;
#pragma unroll
    for (int ft = 0; ft < 5; ++ft) {
      const f32x4 o2 = *(const LAS f32x4*)(L + L_RED + ((nt * 5 + ft) * 64 + lane) * 16);
      const unsigned a01 = pk2(acc[ft][0] + o2[0], acc[ft][1] + o2[1]), a23 = pk2(acc[ft][2] + o2[2], acc[ft][3] + o2[3]);
      const unsigned h01 = pk2(bf2f(xr[ft][0]) + bf2f(a01), bfhi(xr[ft][0]) + bfhi(a01)), h23 = pk2(bf2f(xr[ft][1]) + bf2f(a23), bfhi(xr[ft][1]) + bfhi(a23));
      hp[2 * ft] = h01; hp[2 * ft + 1] = h23;
      __builtin_amdgcn_raw_buffer_store_b64((u32x2){h01, h23}, rws, WS_H + (unsigned)tok * 1280 + (80 * cc + 16 * ft + 4 * fg) * 2, 0, SC1);
      s1 = dot2(h01, 0x3f803f80u, s1); s1 = dot2(h23, 0x3f803f80u, s1);
      s2 = dot2(h01, h01, s2); s2 = dot2(h23, h23, s2);
      s3 = dot2(h01, wrv[ft][0], s3); s3 = dot2(h23, wrv[ft][1], s3);
    }
    s1 = swap_add(swap16_add(s1)); s2 = swap_add(swap16_add(s2)); s3 = swap_add(swap16_add(s3));
    if (lane < 16) __builtin_amdgcn_raw_buffer_store_b128((u32x4){__float_as_uint(s1), __float_as_uint(s2), __float_as_uint(s3), tag32}, rws, WS_STATS + ((R * 8 + cc) * 64 + 16 * nt + l15) * 16, 0, SC1);
    if (w == 0) STAMP(10);
    float t1, t2, t3;
    {
      const unsigned so = WS_STATS + ((R * 8 + fg) * 64 + 16 * nt + l15) * 16;
      unsigned spins = 0;
      for (;;) {
        const u32x4 ga = __builtin_amdgcn_raw_buffer_load_b128(rws, so, 0, SC1), gb = __builtin_amdgcn_raw_buffer_load_b128(rws, so + 4 * 1024, 0, SC1);
        t1 = __uint_as_float(ga[0]) + __uint_as_float(gb[0]); t2 = __uint_as_float(ga[1]) + __uint_as_float(gb[1]); t3 = __uint_as_float(ga[2]) + __uint_as_float(gb[2]);
        if (__all(ga[3] == tag32 && gb[3] == tag32)) break;
        if (++spins > SPIN_CAP) break;
        __builtin_amdgcn_s_sleep(1);
      }
    }
    t1 = swap_add(swap16_add(t1)); t2 = swap_add(swap16_add(t2)); t3 = swap_add(swap16_add(t3));
    const float mean = t1 * (1.f / D);
    const float var = fmaxf(t2 * (1.f / D) - mean * mean, 0.f);
    mb = rbf(mean);
    const float sd = rbf(sqrtf(rbf(rbf(var) + 1e-5f)));
    inv_sd = 1.f / sd;
    const unsigned rb = pk2((t3 - mb * swr) * inv_sd, 0.f) & 0xffffu;
    const unsigned key = ((rb ^ ((rb & 0x8000u) ? 0xffffu : 0x8000u)) << 16) | (unsigned)(2047 - tok);
    if (lane < 16) {
      *(LAS unsigned*)(L + L_KEY + (16 * nt + l15) * 4) = key;
      *(LAS unsigned*)(L + L_MS + (16 * nt + l15) * 4) = (__float_as_uint(mb) & 0xffff0000u) | (__float_as_uint(sd) >> 16);
    }
    WAITV(0);
    if (w == 0) STAMP(11);
  }
  WAITL0(); BARRIER();
  if (w == 0) {
    unsigned k = *(const LAS unsigned*)(L + L_KEY + lane * 4), m = 0;
    for (int i = 0; i <= cc; ++i) { m = wave_umax(k); if (k == m) k = 0; }
    const unsigned ms = *(const LAS unsigned*)(L + L_MS + ((2047 - (int)(m & 0x7ff)) & 63) * 4);
    if (lane == 0) __builtin_amdgcn_raw_buffer_store_b128((u32x4){m, tag2[0], ms, tag2[1]}, rws, WS_CAND + (R * 8 + cc) * 16, 0, SC1);
    STAMP(12);
    unsigned k0, k1, k2, k3, m0, m1, m2, m3;
    unsigned spins = 0;
    for (;;) {
      const u32x4 a = __builtin_amdgcn_raw_buffer_load_b128(rws, WS_CAND + lane * 16, 0, SC1), bb = __builtin_amdgcn_raw_buffer_load_b128(rws, WS_CAND + 1024 + lane * 16, 0, SC1);
      const u32x4 c = __builtin_amdgcn_raw_buffer_load_b128(rws, WS_CAND + 2048 + lane * 16, 0, SC1), d = __builtin_amdgcn_raw_buffer_load_b128(rws, WS_CAND + 3072 + lane * 16, 0, SC1);
      k0 = a[0]; k1 = bb[0]; k2 = c[0]; k3 = d[0]; m0 = a[2]; m1 = bb[2]; m2 = c[2]; m3 = d[2];
      const bool ok = a[1] == tag2[0] && a[3] == tag2[1] && bb[1] == tag2[0] && bb[3] == tag2[1] && c[1] == tag2[0] && c[3] == tag2[1] && d[1] == tag2[0] && d[3] == tag2[1];
      if (__all(ok)) break;
      if (++spins > SPIN_CAP) break;
      __builtin_amdgcn_s_sleep(1);
    }
    STAMP(13);
#pragma unroll 1
    for (int i = 0; i < 8; ++i) {
      const unsigned lm = max(max(k0, k1), max(k2, k3));
      const unsigned g = wave_umax(lm);
      const unsigned ms2 = k0 == g ? m0 : (k1 == g ? m1 : (k2 == g ? m2 : m3));
      if (lm == g) { *(LAS unsigned*)(L + L_SEL + i * 4) = g; *(LAS unsigned*)(L + L_SEL + 32 + i * 4) = ms2; }
      k0 = k0 == g ? 0 : k0; k1 = k1 == g ? 0 : k1; k2 = k2 == g ? 0 : k2; k3 = k3 == g ? 0 : k3;
    }
    if (b == 0 && lane == 0) __builtin_amdgcn_raw_buffer_store_b64(tag2, rws, WS_EPOCH, 0, SC1);
    STAMP(14);
  }
  if (kh == 1) WAITV(0);
  WAITL0(); BARRIER();

  const unsigned selk = *(const LAS unsigned*)(L + L_SEL + (lane & 7) * 4), selm = *(const LAS unsigned*)(L + L_SEL + 32 + (lane & 7) * 4);
  auto out_rows = [&]() {
    if (kh == 0) {
      bool sel = false;
#pragma unroll
      for (int i = 0; i < 8; ++i) sel |= (2047 - (int)(__builtin_amdgcn_readlane((int)selk, i) & 0x7ff)) == tok;
      if (!sel) {
#pragma unroll
        for (int ft = 0; ft < 5; ++ft) {
          unsigned r2[2];
#pragma unroll
          for (int e = 0; e < 2; ++e) {
            const unsigned hv = hp[2 * ft + e];
            const unsigned d2 = pk2(bf2f(hv) - mb, bfhi(hv) - mb);
            const unsigned n2 = pk2(bf2f(d2) * inv_sd, bfhi(d2) * inv_sd);
            r2[e] = pk2(bf2f(hv) + bf2f(n2), bfhi(hv) + bfhi(n2));
          }
          *(u32x2*)(p.out + (size_t)tok * D + 80 * cc + 16 * ft + 4 * fg) = (u32x2){r2[0], r2[1]};
        }
      }
    }
  };
  if (is_gu) {
    {
      const unsigned kk = *(const LAS unsigned*)(L + L_SEL + w * 4), ms = *(const LAS unsigned*)(L + L_SEL + 32 + w * 4);
      const int row = 2047 - (int)(kk & 0x7ff);
      const float m = __uint_as_float(ms & 0xffff0000u), iv = 1.f / __uint_as_float(ms << 16);
      const u32x4 h0 = __builtin_amdgcn_raw_buffer_load_b128(rws, WS_H + (unsigned)row * 1280 + lane * 16, 0, SC1);
      const u32x4 h1 = __builtin_amdgcn_raw_buffer_load_b128(rws, WS_H + (unsigned)row * 1280 + 1024 + (lane & 15) * 16, 0, SC1);
      u32x4 n0, n1;
#pragma unroll
      for (int e = 0; e < 4; ++e) {
        const unsigned d0 = pk2(bf2f(h0[e]) - m, bfhi(h0[e]) - m), d1 = pk2(bf2f(h1[e]) - m, bfhi(h1[e]) - m);
        n0[e] = pk2(bf2f(d0) * iv, bfhi(d0) * iv); n1[e] = pk2(bf2f(d1) * iv, bfhi(d1) * iv);
      }
      *(LAS u32x4*)(L + L_XS + w * 1296 + lane * 16) = n0;
      if (lane < 16) *(LAS u32x4*)(L + L_XS + w * 1296 + 1024 + lane * 16) = n1;
    }
    WAITL0(); BARRIER();
    if (w == 0) STAMP(15);
    {
      const int m = w & 1, kq = w >> 1;
      f32x4 a4 = {0, 0, 0, 0};
#pragma unroll
      for (int i = 0; i < 5; ++i) {
        const int s = 5 * kq + i;
        const LAS char* ap = L + L_FW + (m * 20 + s) * 1024 + lane * 8;
        const bf16x8 af = cat8(trd(ap), trd(ap + 512));
        const bf16x8 bfv = *(const LAS bf16x8*)(L + L_XS + (lane & 7) * 1296 + s * 64 + fg * 16);
        a4 = MFMA16(af, bfv, a4);
      }
      *(LAS f32x4*)(L + L_FRED + (w * 64 + lane) * 16) = a4;
    }
    WAITL0(); BARRIER();
    if (w == 0) {
      f32x4 g4 = {0, 0, 0, 0}, u4 = {0, 0, 0, 0};
#pragma unroll
      for (int q = 0; q < 4; ++q) { g4 += *(const LAS f32x4*)(L + L_FRED + ((2 * q) * 64 + lane) * 16); u4 += *(const LAS f32x4*)(L + L_FRED + ((2 * q + 1) * 64 + lane) * 16); }
      float a[4];
#pragma unroll
      for (int e = 0; e < 4; ++e) { const float g = rbf(g4[e]), u = rbf(u4[e]); const float sg = rbf(g / (1.f + __expf(-g))); a[e] = sg * u; }
      if (l15 < 8) __builtin_amdgcn_raw_buffer_store_b128((u32x4){pk2(a[0], a[1]), tag32, pk2(a[2], a[3]), tag32}, rws, WS_ACT + (((gs >> 1) * 4 + 2 * (gs & 1) + (fg >> 1)) * 8 + l15) * 32 + (fg & 1) * 16, 0, SC1);
      STAMP(16);
    }
    out_rows();
  } else if (is_dn) {
    out_rows();
    const int row = 2047 - (int)(selk & 0x7ff);
    const u32x2 hv = __builtin_amdgcn_raw_buffer_load_b64(rws, WS_H + (unsigned)row * 1280 + (16 * dn + 4 * fg) * 2, 0, SC1);
    f32x4 a4 = {0, 0, 0, 0};
    {
      u32x4 g0[7], g1[7];
      unsigned spins = 0;
      for (;;) {
        bool ok = true;
#pragma unroll
        for (int i = 0; i < 7; ++i) {
          const int s = min(w + 8 * i, 53);
          const unsigned ao = WS_ACT + ((s * 4 + fg) * 8 + (lane & 7)) * 32;
          g0[i] = __builtin_amdgcn_raw_buffer_load_b128(rws, ao, 0, SC1); g1[i] = __builtin_amdgcn_raw_buffer_load_b128(rws, ao + 16, 0, SC1);
        }
#pragma unroll
        for (int i = 0; i < 7; ++i) ok = ok && g0[i][1] == tag32 && g0[i][3] == tag32 && g1[i][1] == tag32 && g1[i][3] == tag32;
        if (__all(ok)) break;
        if (++spins > SPIN_CAP) break;
        __builtin_amdgcn_s_sleep(1);
      }
      if (w == 0) STAMP(17);
#pragma unroll
      for (int i = 0; i < 7; ++i) {
        const int s = w + 8 * i;
        if (s < 54) {
          const LAS char* ap = L + L_FW + s * 1024 + lane * 8;
          const bf16x8 af = cat8(trd(ap), trd(ap + 512));
          a4 = MFMA16(af, __builtin_bit_cast(bf16x8, (u32x4){g0[i][0], g0[i][2], g1[i][0], g1[i][2]}), a4);
        }
      }
    }
    *(LAS f32x4*)(L + L_FRED + (w * 64 + lane) * 16) = a4;
    WAITL0(); BARRIER();
    if (w == 0) {
      f32x4 y = {0, 0, 0, 0};
#pragma unroll
      for (int q = 0; q < 8; ++q) y += *(const LAS f32x4*)(L + L_FRED + (q * 64 + lane) * 16);
      const unsigned rk = selk >> 16, rbits = (rk & 0x8000u) ? (rk ^ 0x8000u) : (rk ^ 0xffffu);
      const float rv = bf2f(rbits), sig = rbf(1.f / (1.f + __expf(-rv)));
      const float m = __uint_as_float(selm & 0xffff0000u), iv = 1.f / __uint_as_float(selm << 16);
      unsigned r2[2];
#pragma unroll
      for (int e = 0; e < 2; ++e) {
        const unsigned d2 = pk2(bf2f(hv[e]) - m, bfhi(hv[e]) - m);
        const unsigned n2 = pk2(bf2f(d2) * iv, bfhi(d2) * iv);
        const unsigned y2 = pk2(rbf(y[2 * e]) * sig, rbf(y[2 * e + 1]) * sig);
        const unsigned t2 = pk2(bf2f(n2) + bf2f(y2), bfhi(n2) + bfhi(y2));
        r2[e] = pk2(bf2f(hv[e]) + bf2f(t2), bfhi(hv[e]) + bfhi(t2));
      }
      if (l15 < 8) *(u32x2*)(p.out + (size_t)row * D + 16 * dn + 4 * fg) = (u32x2){r2[0], r2[1]};
      STAMP(18);
    }
  } else {
    out_rows();
  }
  if (w == 0) STAMP(19);
  STAMP_FLUSH();
}

extern "C" void kernel_launch(void* const* d_in, const int* in_sizes, int n_in, void* d_out, int out_size, void* d_ws, size_t ws_size, hipStream_t stream) {
  Params p;
  p.x = (const u16*)d_in[0]; p.wqkv = (const u16*)d_in[1]; p.wo = (const u16*)d_in[2]; p.wr = (const u16*)d_in[4];
  p.wg = (const u16*)d_in[5]; p.wu = (const u16*)d_in[6]; p.wd = (const u16*)d_in[7]; p.out = (u16*)d_out; p.ws = (unsigned char*)d_ws;
  fused_kernel<<<256, 512, 0, stream>>>(p);
}
```

```cpp
#define PROBE 5
#define PBLK 112
#define AMP 10
#include <hip/hip_runtime.h>
#include <stdint.h>

typedef unsigned short u16;
typedef short bf16x8 __attribute__((ext_vector_type(8)));
typedef short bf16x4 __attribute__((ext_vector_type(4)));
typedef __bf16 bf2 __attribute__((ext_vector_type(2)));
typedef float f32x16 __attribute__((ext_vector_type(16)));
typedef float f32x4 __attribute__((ext_vector_type(4)));
typedef float f32x2 __attribute__((ext_vector_type(2)));
typedef unsigned u32x4 __attribute__((ext_vector_type(4)));
typedef unsigned u32x2 __attribute__((ext_vector_type(2)));
typedef unsigned long long u64;
#define LAS __attribute__((address_space(3)))
#define SBAR() __builtin_amdgcn_sched_barrier(0)
#define BARRIER() asm volatile("s_barrier" ::: "memory")
#define WAITL0() asm volatile("s_waitcnt lgkmcnt(0)" ::: "memory")
#define WAITV(n) asm volatile("s_waitcnt vmcnt(" #n ")" ::: "memory")

constexpr int S = 2048, D = 640, F = 1728;
constexpr int SC1 = 16;

constexpr unsigned WS_EPOCH = 0;
constexpr unsigned WS_FLAGA = 4096;
constexpr unsigned WS_FLAGO = WS_FLAGA + 32768;
constexpr unsigned WS_CAND = WS_FLAGO + 16384;
constexpr unsigned WS_ACT = WS_CAND + 4096;
constexpr unsigned WS_STATS = 131072;
constexpr unsigned WS_QKV = 524288;
constexpr unsigned WS_O = WS_QKV + 480 * 16384;
constexpr unsigned WS_H = WS_O + S * D * 2;
#define STAMP_OFF (200u << 20)
#define NSTAMP 32

constexpr int L_SLOT = 32768, L_W = 16384;
constexpr int PL = 4128;
constexpr int L_K = 0, L_V = 5 * PL, L_ONE = 10 * PL;
constexpr int L_WO = 43008;
constexpr int L_RED = 0;
constexpr int L_FW = 43008;
constexpr int L_XS = 0;
constexpr int L_FRED = 12288;
constexpr int L_MISC = 147456;
constexpr int L_RS = L_MISC;
constexpr int L_CS = L_MISC + 1024;
constexpr int L_EP = L_MISC + 1536;
constexpr int L_KEY = L_MISC + 2048;
constexpr int L_MS = L_MISC + 2304;
constexpr int L_SEL = L_MISC + 2560;
constexpr int L_STAMP = L_MISC + 4096;
constexpr int LDS_BYTES = 163840;

struct Params { const u16* x; const u16* wqkv; const u16* wo; const u16* wr; const u16* wg; const u16* wu; const u16* wd; u16* out; unsigned char* ws; };

__device__ __forceinline__ float bf2f(unsigned v) { return __uint_as_float(v << 16); }
__device__ __forceinline__ float bfhi(unsigned v) { return __uint_as_float(v & 0xffff0000u); }
__device__ __forceinline__ unsigned pk2(float a, float b) { f32x2 f = {a, b}; return __builtin_bit_cast(unsigned, __builtin_convertvector(f, bf2)); }
__device__ __forceinline__ float rbf(float f) { return __uint_as_float(pk2(f, 0.f) << 16); }
__device__ __forceinline__ float dot2(unsigned a, unsigned b, float c) { return __builtin_amdgcn_fdot2_f32_bf16(__builtin_bit_cast(bf2, a), __builtin_bit_cast(bf2, b), c, false); }
__device__ __forceinline__ float swap_add(float v) { auto r = __builtin_amdgcn_permlane32_swap(__float_as_uint(v), __float_as_uint(v), false, false); return __uint_as_float(r[0]) + __uint_as_float(r[1]); }
__device__ __forceinline__ float swap16_add(float v) { auto r = __builtin_amdgcn_permlane16_swap(__float_as_uint(v), __float_as_uint(v), false, false); return __uint_as_float(r[0]) + __uint_as_float(r[1]); }
__device__ __forceinline__ void glds16(unsigned voff, const void* sbase, unsigned lds) {
  asm volatile("s_mov_b32 m0, %2\n\ts_nop 0\n\tglobal_load_lds_dwordx4 %0, %1" ::"v"(voff), "s"(sbase), "s"(lds) : "memory");
}
__device__ __forceinline__ void glds16c(unsigned voff, const void* sbase, unsigned lds) {
  asm volatile("s_mov_b32 m0, %2\n\ts_nop 0\n\tglobal_load_lds_dwordx4 %0, %1 sc1" ::"v"(voff), "s"(sbase), "s"(lds) : "memory");
}
__device__ __forceinline__ bf16x4 trd(const LAS char* p) { return __builtin_amdgcn_ds_read_tr16_b64_v4i16((LAS bf16x4*)p); }
__device__ __forceinline__ bf16x8 cat8(bf16x4 a, bf16x4 b) { return (bf16x8){a[0], a[1], a[2], a[3], b[0], b[1], b[2], b[3]}; }
#define MFMA32(a, b, c) __builtin_amdgcn_mfma_f32_32x32x16_bf16(a, b, c, 0, 0, 0)
#define MFMA32K8(a, b, c) __builtin_amdgcn_mfma_f32_32x32x8bf16_1k(a, b, c, 0, 0, 0)
#define MFMA16(a, b, c) __builtin_amdgcn_mfma_f32_16x16x32_bf16(a, b, c, 0, 0, 0)
template <int CTRL, int RMASK> __device__ __forceinline__ unsigned dppmax(unsigned v) { return max(v, (unsigned)__builtin_amdgcn_update_dpp(0, (int)v, CTRL, RMASK, 0xf, false)); }
__device__ __forceinline__ unsigned wave_umax(unsigned v) {
  v = dppmax<0x111, 0xf>(v); v = dppmax<0x112, 0xf>(v); v = dppmax<0x114, 0xf>(v); v = dppmax<0x118, 0xf>(v);
  v = dppmax<0x142, 0xa>(v); v = dppmax<0x143, 0xc>(v);
  return (unsigned)__builtin_amdgcn_readlane((int)v, 63);
}

template <int CTRL> __device__ __forceinline__ float dppadd(float v) { return v + __int_as_float(__builtin_amdgcn_update_dpp(0, __float_as_int(v), CTRL, 0xf, 0xf, false)); }
__device__ __forceinline__ float wave_fsum(float v) { v = dppadd<0x128>(v); v = dppadd<0x124>(v); v = dppadd<0x122>(v); v = dppadd<0x121>(v); return swap_add(swap16_add(v)); }

#define STAMP(k) do { if ((k) == PROBE) tprobe_ = __builtin_amdgcn_s_memrealtime(); } while (0)
#define STAMP_INIT() do {} while (0)
#define STAMP_FLUSH() do { if (blockIdx.x == PBLK && w == 0) { const u64 now_ = __builtin_amdgcn_s_memrealtime(); const u64 until_ = now_ + AMP * (tprobe_ - tstart_); while (__builtin_amdgcn_s_memrealtime() < until_) __builtin_amdgcn_s_sleep(1); } } while (0)

constexpr unsigned SPIN_CAP = 1u << 16;

__global__ __launch_bounds__(512, 2) void fused_kernel(Params p) {
  __shared__ __attribute__((aligned(16))) char smem[LDS_BYTES];
  LAS char* L = (LAS char*)smem;
  const unsigned lds0 = (unsigned)(uintptr_t)smem;
  const int tid = threadIdx.x, lane = tid & 63;
  const int w = __builtin_amdgcn_readfirstlane(tid >> 6);
  const int b = blockIdx.x, xcd = b & 7, bi = b >> 3;
  const int l31 = lane & 31, hh = lane >> 5, l15 = lane & 15, fg = lane >> 4;
  const __amdgpu_buffer_rsrc_t rws = __builtin_amdgcn_make_buffer_rsrc(p.ws, 0, 0x7fffffff, 0x00020000);
  const int aP = 4 * (xcd >> 1) + (bi >> 3), aj = 8 * (xcd & 1) + (bi & 7);
  const int hd = bi, pp = xcd;
  const int R = 4 * xcd + (bi >> 3), cc = bi & 7;
  const int gs = xcd * 14 + bi;
  const bool is_gu = bi < 14 && gs < 108;
  const int dn = xcd * 5 + (bi - 14);
  const bool is_dn = bi >= 14 && bi < 19;
  const u64 tstart_ = __builtin_amdgcn_s_memrealtime(); u64 tprobe_ = tstart_;
  STAMP_INIT();

  if (aj < 15 && w >= 4) {
    const int lw = w - 4;
    const unsigned xsw = (unsigned)(4 * (lw & 1) + (lane >> 4));
    const unsigned voff_x = (unsigned)(lane >> 3) * 1280u + (((unsigned)(lane & 7) ^ xsw) << 4);
    const unsigned ch = (unsigned)(lane & 15) ^ (((unsigned)(lane >> 4) << 2) | (unsigned)lw);
    const unsigned voff_w = (unsigned)(lane >> 4) * 3840u + ch * 16u;
    const char* xb = (const char*)p.x + (size_t)(128 * aP + 8 * lw) * 1280;
    const char* wb = (const char*)p.wqkv + (size_t)(4 * lw) * 3840 + 256 * aj;
    const unsigned lbase = lds0 + lw * 1024;
    const int rot = ((bi & 7) * 5 / 4 + (bi >> 3) * 5 / 2) % 10;
    auto issue = [&](int s) {
      const unsigned sl = lbase + (s & 3) * L_SLOT;
      int ss = s + rot; if (ss >= 10) ss -= 10;
#pragma unroll
      for (int i = 0; i < 4; ++i) {
        glds16(voff_x, xb + (size_t)i * 32 * 1280 + ss * 128, sl + i * 4096);
        glds16(voff_w, wb + ((size_t)ss * 64 + i * 16) * 3840, sl + L_W + i * 4096);
      }
    };
    issue(0); issue(1); issue(2); issue(3);
    WAITV(24); BARRIER();
    if (w == 4) STAMP(1);
    WAITV(16); BARRIER();
#pragma unroll 1
    for (int s = 1; s <= 6; ++s) { issue(s + 3); WAITV(16); BARRIER(); }
    WAITV(8); BARRIER();
    WAITV(0); BARRIER();
    BARRIER();
    BARRIER();
  } else if (w < 4) {
    if (w == 0) {
      const u32x2 e = __builtin_amdgcn_raw_buffer_load_b64(rws, WS_EPOCH, 0, SC1);
      const u64 t = (((u64)e[1] << 32) | e[0]) + 1;
      if (lane == 0) *(LAS u32x2*)(L + L_EP) = (u32x2){(unsigned)t, (unsigned)(t >> 32)};
      __builtin_amdgcn_raw_buffer_store_b128((u32x4){0, 0, 0, 0}, rws, WS_STATS + (R * 8 + cc) * 1024 + lane * 16, 0, SC1);
      if (is_gu && lane < 32) __builtin_amdgcn_raw_buffer_store_b128((u32x4){0, 0, 0, 0}, rws, WS_ACT + (((gs >> 1) * 4 + 2 * (gs & 1) + (lane >> 4)) * 8 + (lane & 7)) * 32 + ((lane >> 3) & 1) * 16, 0, SC1);
    }
    if (aj < 15) {
      const int wm = w >> 1, wn = w & 1;
      unsigned xa[4], wa[4];
#pragma unroll
      for (int ks = 0; ks < 4; ++ks) xa[ks] = (unsigned)(64 * wm + l31) * 128u + ((((unsigned)(2 * ks + hh)) ^ (((unsigned)l31 >> 1) & 7u)) << 4);
      const unsigned xo0 = wn * 4096, xo1 = 4096 - xo0;
      {
        const unsigned blk = (lane >> 4) & 1, q = (lane & 15) >> 2, pq = lane & 3;
#pragma unroll
        for (int j = 0; j < 2; ++j)
#pragma unroll
          for (int t = 0; t < 2; ++t) {
            const unsigned jj = j ^ wm;
            const unsigned row = 8 * hh + 4 * t + q, chn = 4 * (2 * wn + jj) + 2 * blk + (pq >> 1), m = (q << 2) | (unsigned)(2 * hh + t);
            wa[j * 2 + t] = L_W + 256u * row + 16u * (chn ^ m) + 8u * (pq & 1);
          }
      }
      f32x16 a00 = {}, a01 = {}, a10 = {}, a11 = {};
      float ssum = 0.f, ssq = 0.f, csum = 0.f;
      bf16x8 xf0[2], wf0[2], xf1[2], wf1[2];
      const unsigned ONE2 = 0x3f803f80u;
      unsigned xb0[4], xb1[4], wb[4];
#define SETSLOT(so) do { _Pragma("unroll") for (int k_ = 0; k_ < 4; ++k_) { xb0[k_] = xa[k_] + (so) + xo0; xb1[k_] = xa[k_] + (so) + xo1; wb[k_] = wa[k_] + (so); } } while (0)
#define RD(XF, WF, ks) do { \
        XF[0] = *(const LAS bf16x8*)(L + xb0[ks]); XF[1] = *(const LAS bf16x8*)(L + xb1[ks]); \
        WF[0] = cat8(trd(L + wb[0] + (ks) * 4096), trd(L + wb[1] + (ks) * 4096)); \
        WF[1] = cat8(trd(L + wb[2] + (ks) * 4096), trd(L + wb[3] + (ks) * 4096)); } while (0)
#define MM(XF, WF) do { \
        a00 = MFMA32(WF[0], XF[0], a00); a01 = MFMA32(WF[1], XF[0], a01); \
        a10 = MFMA32(WF[0], XF[1], a10); a11 = MFMA32(WF[1], XF[1], a11); \
        { const u32x4 xs_ = __builtin_bit_cast(u32x4, XF[0]); const u32x4 ws_ = __builtin_bit_cast(u32x4, WF[0]); \
          _Pragma("unroll") for (int e_ = 0; e_ < 4; ++e_) { ssum = dot2(xs_[e_], ONE2, ssum); ssq = dot2(xs_[e_], xs_[e_], ssq); csum = dot2(ws_[e_], ONE2, csum); } } } while (0)
#define PINALL() asm volatile("" : "+v"(a00), "+v"(a01), "+v"(a10), "+v"(a11), "+v"(ssum), "+v"(ssq), "+v"(csum))
#define ILV() do { _Pragma("unroll") for (int g_ = 0; g_ < 4; ++g_) { \
        __builtin_amdgcn_sched_group_barrier(0x8, 1, 0); __builtin_amdgcn_sched_group_barrier(0x100, 2, 0); __builtin_amdgcn_sched_group_barrier(0x2, 3, 0); } } while (0)
#define STEP(XN, WN, ksn, XC, WC) do { RD(XN, WN, ksn); MM(XC, WC); ILV(); PINALL(); SBAR(); } while (0)
      SETSLOT(0);
      BARRIER();
      RD(xf0, wf0, 0);
      SBAR();
#pragma unroll 1
      for (int s = 0; s < 9; ++s) {
        STEP(xf1, wf1, 1, xf0, wf0);
        STEP(xf0, wf0, 2, xf1, wf1);
        STEP(xf1, wf1, 3, xf0, wf0);
        SETSLOT(((s + 1) & 3) * L_SLOT);
        WAITL0(); BARRIER();
        STEP(xf0, wf0, 0, xf1, wf1);
      }
      STEP(xf1, wf1, 1, xf0, wf0);
      STEP(xf0, wf0, 2, xf1, wf1);
      STEP(xf1, wf1, 3, xf0, wf0);
      MM(xf1, wf1); PINALL();
      if (w == 0) STAMP(2);
      ssum = swap_add(ssum); ssq = swap_add(ssq); csum = swap_add(csum);
      {
        const float mean = ssum * (1.f / D);
        const float var = fmaxf(ssq * (1.f / D) - mean * mean, 0.f);
        const float mb = rbf(mean), vb = rbf(var);
        const float sd = rbf(sqrtf(rbf(vb + 1e-5f)));
        if (lane < 32) {
          *(LAS f32x2*)(L + L_RS + (64 * wm + 32 * wn + l31) * 8) = (f32x2){mb, 1.f / sd};
          *(LAS float*)(L + L_CS + (64 * wn + 32 * wm + l31) * 4) = csum;
        }
      }
      WAITV(0); WAITL0(); BARRIER();
      const float qs = aj < 5 ? 0.22360679774997896f * 1.4426950408889634f : 1.f;
#pragma unroll
      for (int i = 0; i < 2; ++i) {
        const int tok = 64 * wm + 32 * (i ^ wn) + l31;
        const f32x2 rs = *(const LAS f32x2*)(L + L_RS + tok * 8);
        const float sc = rs[1] * qs, nm = -rs[0] * sc;
#pragma unroll
        for (int j = 0; j < 2; ++j) {
          const f32x16& a = i == 0 ? (j == 0 ? a00 : a01) : (j == 0 ? a10 : a11);
          const int f0 = 64 * wn + 32 * (j ^ wm) + 4 * hh;
          const unsigned go = WS_QKV + (unsigned)(32 * aj + (f0 >> 2)) * 16384u + (unsigned)(128 * aP + tok) * 8u;
#pragma unroll
          for (int rq = 0; rq < 4; ++rq) {
            const f32x4 cs = *(const LAS f32x4*)(L + L_CS + (f0 + 8 * rq) * 4);
            const float v0 = fmaf(a[rq * 4 + 0], sc, nm * cs[0]), v1 = fmaf(a[rq * 4 + 1], sc, nm * cs[1]);
            const float v2 = fmaf(a[rq * 4 + 2], sc, nm * cs[2]), v3 = fmaf(a[rq * 4 + 3], sc, nm * cs[3]);
            __builtin_amdgcn_raw_buffer_store_b64((u32x2){pk2(v0, v1), pk2(v2, v3)}, rws, go + rq * 32768, 0, SC1);
          }
        }
      }
      WAITV(0); BARRIER();
    } else {
      WAITV(0);
    }
  }
  if (aj >= 15) { WAITL0(); BARRIER(); }
  const u32x2 tag2 = *(const LAS u32x2*)(L + L_EP);
  const unsigned tag32 = tag2[0] | 1u;
  if (aj < 15 && tid == 0) __builtin_amdgcn_raw_buffer_store_b64(tag2, rws, WS_FLAGA + (aP * 16 + aj) * 128, 0, SC1);
  if (w == 0) STAMP(3);

  f32x16 o = (f32x16){};
  {
    const int t = w;
    if (tid < 4) *(LAS unsigned*)(L + L_ONE + tid * 4) = tid == 0 ? 0x00003f80u : 0u;
    {
      const int sec = lane < 4 ? 0 : (lane < 12 ? 1 : 2);
      const int k = lane < 4 ? lane : (lane < 12 ? lane - 4 : lane - 12);
      const int col = sec * 640 + 20 * hd + ((k & 1) ? 19 : 0);
      int Pn = sec == 0 ? 2 * pp + (k >> 1) : 2 * pp - 2 + (k >> 1);
      const bool need = lane < 20 && Pn >= 0;
      const unsigned fo = WS_FLAGA + (unsigned)((need ? Pn : 0) * 16 + (col >> 7)) * 128;
      unsigned spins = 0;
      for (;;) {
        const u32x2 f = __builtin_amdgcn_raw_buffer_load_b64(rws, fo, 0, SC1);
        if (__all(!need || (f[0] == tag2[0] && f[1] == tag2[1]))) break;
        if (++spins > SPIN_CAP) break;
        __builtin_amdgcn_s_sleep(1);
      }
    }
    asm volatile("" ::: "memory");
    if (w == 0) STAMP(4);
    {
      const char* qb = (const char*)p.ws + WS_QKV;
#pragma unroll
      for (int i = 0; i < 5; ++i) {
        const int pc = w + 8 * i, pl = pc >> 2, qt = pc & 3;
        const int tok0 = 256 * pp - 256 + 128 * qt;
        if (tok0 >= 0) glds16c(lane * 16, qb + (size_t)((pl < 5 ? 160 + 5 * hd + pl : 320 + 5 * hd + pl - 5)) * 16384 + tok0 * 8, lds0 + pl * PL + qt * 1024);
      }
    }
    bf16x8 qf0; bf16x4 qf1;
    {
      const unsigned qo = WS_QKV + (unsigned)(5 * hd) * 16384u + (unsigned)(256 * pp + 32 * t + l31) * 8u;
      const u32x2 a = __builtin_amdgcn_raw_buffer_load_b64(rws, qo + (2 * hh) * 16384, 0, SC1);
      const u32x2 c = __builtin_amdgcn_raw_buffer_load_b64(rws, qo + (2 * hh + 1) * 16384, 0, SC1);
      u32x2 z = __builtin_amdgcn_raw_buffer_load_b64(rws, qo + 4 * 16384, 0, SC1);
      if (hh) z = (u32x2){0, 0};
      qf0 = __builtin_bit_cast(bf16x8, (u32x4){a[0], a[1], c[0], c[1]});
      qf1 = __builtin_bit_cast(bf16x4, z);
    }
    WAITV(0); WAITL0(); BARRIER();
    if (w == 0) STAMP(5);
    {
#pragma unroll 1
      for (int pc = w; pc < 100; pc += 8) {
        const int n = pc * 64 + lane, rho = n / 10, ch = n - rho * 10;
        const int s = rho >> 5, tq = (rho >> 4) & 1, kg = (rho >> 2) & 3, q = rho & 3;
        const int cidx = 4 * s + kg, g2 = cidx / 5, c5 = cidx - 5 * g2;
        const int quad = 10 * g2 + ((tq ? 0x98732 : 0x46510) >> (4 * c5) & 15);
        glds16((unsigned)(4 * quad + q) * 1280u + ch * 16u, (const char*)p.wo + 160 * cc, lds0 + L_WO + pc * 1024);
      }
    }
    const int kad = L_K + l31 * 8;
    float negm;
    {
      const LAS char* kp = L + kad + (t + 8) * 256;
      const u32x2 k0 = *(const LAS u32x2*)(kp + (2 * hh) * PL), k1 = *(const LAS u32x2*)(kp + (2 * hh + 1) * PL), k2 = *(const LAS u32x2*)(kp + 4 * PL);
      const u32x4 q0 = __builtin_bit_cast(u32x4, qf0); const u32x2 q1 = __builtin_bit_cast(u32x2, qf1);
      float d = dot2(q0[0], k0[0], 0.f); d = dot2(q0[1], k0[1], d); d = dot2(q0[2], k1[0], d); d = dot2(q0[3], k1[1], d);
      d = dot2(q1[0], k2[0], d); d = dot2(q1[1], k2[1], d);
      negm = -swap_add(d);
    }
    const int vquad = 4 * ((lane >> 4) & 1) + (lane & 3);
    const int vad = vquad < 5 ? L_V + vquad * PL + (4 * hh + ((lane & 15) >> 2)) * 8 : L_ONE;
    const int vst = vquad < 5 ? 1 : 0;
    auto tile = [&](int kt, const f32x16& cin) {
      const LAS char* kp = L + kad + kt * 256;
      const bf16x4 ka = *(const LAS bf16x4*)(kp + (2 * hh) * PL), kb = *(const LAS bf16x4*)(kp + (2 * hh + 1) * PL), kc = *(const LAS bf16x4*)(kp + 4 * PL);
      const LAS char* vp = L + vad + vst * kt * 256;
      const bf16x8 v0 = cat8(trd(vp), trd(vp + vst * 64)), v1 = cat8(trd(vp + vst * 128), trd(vp + vst * 192));
      f32x16 s = MFMA32(cat8(ka, kb), qf0, cin);
      s = MFMA32K8(kc, qf1, s);
      u32x4 p0, p1;
#pragma unroll
      for (int e = 0; e < 4; ++e) {
        p0[e] = pk2(__builtin_amdgcn_exp2f(s[2 * e]), __builtin_amdgcn_exp2f(s[2 * e + 1]));
        p1[e] = pk2(__builtin_amdgcn_exp2f(s[8 + 2 * e]), __builtin_amdgcn_exp2f(s[8 + 2 * e + 1]));
      }
      o = MFMA32(v0, __builtin_bit_cast(bf16x8, p0), o);
      o = MFMA32(v1, __builtin_bit_cast(bf16x8, p1), o);
    };
    f32x16 cm;
#pragma unroll
    for (int r = 0; r < 16; ++r) { const int kr = (r & 3) + 8 * (r >> 2) + 4 * hh; cm[r] = kr <= l31 ? negm : -INFINITY; }
    tile(t + 8, cm);
#pragma unroll
    for (int r = 0; r < 16; ++r) cm[r] = negm;
    const int lo = pp >= 1 ? t + 1 : 8;
#pragma unroll 1
    for (int kt = t + 7; kt >= lo; --kt) tile(kt, cm);
    if (pp >= 1) {
#pragma unroll
      for (int r = 0; r < 16; ++r) { const int kr = (r & 3) + 8 * (r >> 2) + 4 * hh; cm[r] = kr > l31 ? negm : -INFINITY; }
      tile(t, cm);
    }
    if (w == 0) STAMP(6);
    auto r = __builtin_amdgcn_permlane32_swap(__float_as_uint(o[8]), __float_as_uint(o[8]), false, false);
    const float inv = 1.f / __uint_as_float(r[1]);
    u32x4 c0;
    c0[0] = pk2(o[0] * inv, o[1] * inv); c0[1] = pk2(o[2] * inv, o[3] * inv); c0[2] = pk2(o[4] * inv, o[5] * inv); c0[3] = pk2(o[6] * inv, o[7] * inv);
    const u32x2 c1 = (u32x2){pk2(o[8] * inv, o[9] * inv), pk2(o[10] * inv, o[11] * inv)};
    const int tt = pp * 16 + 2 * t + (l31 >> 4);
    const unsigned base = WS_O + (unsigned)tt * 80 * 256 + (l31 & 15) * 16 + (unsigned)(5 * (hd >> 1)) * 256;
    __builtin_amdgcn_raw_buffer_store_b128(c0, rws, base + (2 * (hd & 1) + hh) * 256, 0, SC1);
    if (hh == 0) __builtin_amdgcn_raw_buffer_store_b64(c1, rws, base + 4 * 256 + (hd & 1) * 8, 0, SC1);
    WAITV(0);
    if (lane == 0) __builtin_amdgcn_raw_buffer_store_b64(tag2, rws, WS_FLAGO + (((pp * 8 + t) * 2 + (hd >> 4)) * 16 + (hd & 15)) * 8, 0, SC1);
  }
  if (w == 0) STAMP(7);
  BARRIER();

  const int nt = w & 3, kh = w >> 2;
  const int tok = 64 * R + 16 * nt + l15;
  u32x2 xr[5], wrv[5];
  float swr = 0.f;
  if (kh == 0) {
#pragma unroll
    for (int ft = 0; ft < 5; ++ft) {
      xr[ft] = *(const u32x2*)(p.x + (size_t)tok * D + 80 * cc + 16 * ft + 4 * fg);
      wrv[ft] = *(const u32x2*)(p.wr + 80 * cc + 16 * ft + 4 * fg);
    }
    const unsigned* wr32 = (const unsigned*)p.wr;
#pragma unroll
    for (int i = 0; i < 5; ++i) swr = dot2(wr32[lane + 64 * i], 0x3f803f80u, swr);
    swr = wave_fsum(swr);
  }
  f32x4 acc[5];
#pragma unroll
  for (int ft = 0; ft < 5; ++ft) acc[ft] = (f32x4){0, 0, 0, 0};
  {
    const int tg = 4 * R + nt;
    const unsigned fo = WS_FLAGO + ((((tg >> 4) * 8 + ((tg >> 1) & 7)) * 2 + kh) * 16 + l15) * 8;
    unsigned spins = 0;
    for (;;) {
      const u32x2 f = __builtin_amdgcn_raw_buffer_load_b64(rws, fo, 0, SC1);
      if (__all(f[0] == tag2[0] && f[1] == tag2[1])) break;
      if (++spins > SPIN_CAP) break;
      __builtin_amdgcn_s_sleep(1);
    }
    asm volatile("" ::: "memory");
    if (w == 0) STAMP(8);
    u32x4 of[10];
#pragma unroll
    for (int s = 0; s < 10; ++s) of[s] = __builtin_amdgcn_raw_buffer_load_b128(rws, WS_O + (unsigned)(tg * 80 + 4 * (10 * kh + s)) * 256 + lane * 16, 0, SC1);
    const LAS char* wp = L + L_WO + (unsigned)(kh * 10 * 32 + fg * 4 + ((lane & 15) >> 2)) * 160 + (lane & 3) * 8;
#pragma unroll
    for (int s = 0; s < 10; ++s) {
#pragma unroll
      for (int ft = 0; ft < 5; ++ft) {
        const bf16x8 a = cat8(trd(wp + (s * 32) * 160 + ft * 32), trd(wp + (s * 32 + 16) * 160 + ft * 32));
        acc[ft] = MFMA16(a, __builtin_bit_cast(bf16x8, of[s]), acc[ft]);
      }
    }
  }
  if (w == 0) STAMP(9);
  if (kh == 1) {
#pragma unroll
    for (int ft = 0; ft < 5; ++ft) *(LAS f32x4*)(L + L_RED + ((nt * 5 + ft) * 64 + lane) * 16) = acc[ft];
  }
  WAITL0(); BARRIER();
  unsigned hp[10];
  float mb = 0.f, inv_sd = 0.f;
  if (kh == 1) {
    const int rl = lane >> 1, grow = 8 * ((rl >> 2) & 3) + 4 * (rl >> 4) + (rl & 3);
    if (is_gu) {
      const unsigned vo = (unsigned)grow * (F * 2) + (lane & 1) * 16;
#pragma unroll 1
      for (int pc = nt; pc < 40; pc += 4) { const int m = pc >= 20, s = pc - 20 * m; glds16(vo, (const char*)(m ? p.wu : p.wg) + (size_t)s * 32 * F * 2 + gs * 32, lds0 + L_FW + pc * 1024); }
    } else if (is_dn) {
      const unsigned vo = (unsigned)grow * (D * 2) + (lane & 1) * 16;
#pragma unroll 1
      for (int pc = nt; pc < 54; pc += 4) glds16(vo, (const char*)p.wd + (size_t)pc * 32 * D * 2 + dn * 32, lds0 + L_FW + pc * 1024);
    }
  } else {
    float s1 = 0.f, s2 = 0.f, s3 = 0.f;
#pragma unroll
    for (int ft = 0; ft < 5; ++ft) {
      const f32x4 o2 = *(const LAS f32x4*)(L + L_RED + ((nt * 5 + ft) * 64 + lane) * 16);
      const unsigned a01 = pk2(acc[ft][0] + o2[0], acc[ft][1] + o2[1]), a23 = pk2(acc[ft][2] + o2[2], acc[ft][3] + o2[3]);
      const unsigned h01 = pk2(bf2f(xr[ft][0]) + bf2f(a01), bfhi(xr[ft][0]) + bfhi(a01)), h23 = pk2(bf2f(xr[ft][1]) + bf2f(a23), bfhi(xr[ft][1]) + bfhi(a23));
      hp[2 * ft] = h01; hp[2 * ft + 1] = h23;
      __builtin_amdgcn_raw_buffer_store_b64((u32x2){h01, h23}, rws, WS_H + (unsigned)tok * 1280 + (80 * cc + 16 * ft + 4 * fg) * 2, 0, SC1);
      s1 = dot2(h01, 0x3f803f80u, s1); s1 = dot2(h23, 0x3f803f80u, s1);
      s2 = dot2(h01, h01, s2); s2 = dot2(h23, h23, s2);
      s3 = dot2(h01, wrv[ft][0], s3); s3 = dot2(h23, wrv[ft][1], s3);
    }
    s1 = swap_add(swap16_add(s1)); s2 = swap_add(swap16_add(s2)); s3 = swap_add(swap16_add(s3));
    if (lane < 16) __builtin_amdgcn_raw_buffer_store_b128((u32x4){__float_as_uint(s1), __float_as_uint(s2), __float_as_uint(s3), tag32}, rws, WS_STATS + ((R * 8 + cc) * 64 + 16 * nt + l15) * 16, 0, SC1);
    if (w == 0) STAMP(10);
    float t1, t2, t3;
    {
      const unsigned so = WS_STATS + ((R * 8 + fg) * 64 + 16 * nt + l15) * 16;
      unsigned spins = 0;
      for (;;) {
        const u32x4 ga = __builtin_amdgcn_raw_buffer_load_b128(rws, so, 0, SC1), gb = __builtin_amdgcn_raw_buffer_load_b128(rws, so + 4 * 1024, 0, SC1);
        t1 = __uint_as_float(ga[0]) + __uint_as_float(gb[0]); t2 = __uint_as_float(ga[1]) + __uint_as_float(gb[1]); t3 = __uint_as_float(ga[2]) + __uint_as_float(gb[2]);
        if (__all(ga[3] == tag32 && gb[3] == tag32)) break;
        if (++spins > SPIN_CAP) break;
        __builtin_amdgcn_s_sleep(1);
      }
    }
    t1 = swap_add(swap16_add(t1)); t2 = swap_add(swap16_add(t2)); t3 = swap_add(swap16_add(t3));
    const float mean = t1 * (1.f / D);
    const float var = fmaxf(t2 * (1.f / D) - mean * mean, 0.f);
    mb = rbf(mean);
    const float sd = rbf(sqrtf(rbf(rbf(var) + 1e-5f)));
    inv_sd = 1.f / sd;
    const unsigned rb = pk2((t3 - mb * swr) * inv_sd, 0.f) & 0xffffu;
    const unsigned key = ((rb ^ ((rb & 0x8000u) ? 0xffffu : 0x8000u)) << 16) | (unsigned)(2047 - tok);
    if (lane < 16) {
      *(LAS unsigned*)(L + L_KEY + (16 * nt + l15) * 4) = key;
      *(LAS unsigned*)(L + L_MS + (16 * nt + l15) * 4) = (__float_as_uint(mb) & 0xffff0000u) | (__float_as_uint(sd) >> 16);
    }
    WAITV(0);
    if (w == 0) STAMP(11);
  }
  WAITL0(); BARRIER();
  if (w == 0) {
    unsigned k = *(const LAS unsigned*)(L + L_KEY + lane * 4), m = 0;
    for (int i = 0; i <= cc; ++i) { m = wave_umax(k); if (k == m) k = 0; }
    const unsigned ms = *(const LAS unsigned*)(L + L_MS + ((2047 - (int)(m & 0x7ff)) & 63) * 4);
    if (lane == 0) __builtin_amdgcn_raw_buffer_store_b128((u32x4){m, tag2[0], ms, tag2[1]}, rws, WS_CAND + (R * 8 + cc) * 16, 0, SC1);
    STAMP(12);
    unsigned k0, k1, k2, k3, m0, m1, m2, m3;
    unsigned spins = 0;
    for (;;) {
      const u32x4 a = __builtin_amdgcn_raw_buffer_load_b128(rws, WS_CAND + lane * 16, 0, SC1), bb = __builtin_amdgcn_raw_buffer_load_b128(rws, WS_CAND + 1024 + lane * 16, 0, SC1);
      const u32x4 c = __builtin_amdgcn_raw_buffer_load_b128(rws, WS_CAND + 2048 + lane * 16, 0, SC1), d = __builtin_amdgcn_raw_buffer_load_b128(rws, WS_CAND + 3072 + lane * 16, 0, SC1);
      k0 = a[0]; k1 = bb[0]; k2 = c[0]; k3 = d[0]; m0 = a[2]; m1 = bb[2]; m2 = c[2]; m3 = d[2];
      const bool ok = a[1] == tag2[0] && a[3] == tag2[1] && bb[1] == tag2[0] && bb[3] == tag2[1] && c[1] == tag2[0] && c[3] == tag2[1] && d[1] == tag2[0] && d[3] == tag2[1];
      if (__all(ok)) break;
      if (++spins > SPIN_CAP) break;
      __builtin_amdgcn_s_sleep(1);
    }
    STAMP(13);
#pragma unroll 1
    for (int i = 0; i < 8; ++i) {
      const unsigned lm = max(max(k0, k1), max(k2, k3));
      const unsigned g = wave_umax(lm);
      const unsigned ms2 = k0 == g ? m0 : (k1 == g ? m1 : (k2 == g ? m2 : m3));
      if (lm == g) { *(LAS unsigned*)(L + L_SEL + i * 4) = g; *(LAS unsigned*)(L + L_SEL + 32 + i * 4) = ms2; }
      k0 = k0 == g ? 0 : k0; k1 = k1 == g ? 0 : k1; k2 = k2 == g ? 0 : k2; k3 = k3 == g ? 0 : k3;
    }
    if (b == 0 && lane == 0) __builtin_amdgcn_raw_buffer_store_b64(tag2, rws, WS_EPOCH, 0, SC1);
    STAMP(14);
  }
  if (kh == 1) WAITV(0);
  WAITL0(); BARRIER();

  const unsigned selk = *(const LAS unsigned*)(L + L_SEL + (lane & 7) * 4), selm = *(const LAS unsigned*)(L + L_SEL + 32 + (lane & 7) * 4);
  auto out_rows = [&]() {
    if (kh == 0) {
      bool sel = false;
#pragma unroll
      for (int i = 0; i < 8; ++i) sel |= (2047 - (int)(__builtin_amdgcn_readlane((int)selk, i) & 0x7ff)) == tok;
      if (!sel) {
#pragma unroll
        for (int ft = 0; ft < 5; ++ft) {
          unsigned r2[2];
#pragma unroll
          for (int e = 0; e < 2; ++e) {
            const unsigned hv = hp[2 * ft + e];
            const unsigned d2 = pk2(bf2f(hv) - mb, bfhi(hv) - mb);
            const unsigned n2 = pk2(bf2f(d2) * inv_sd, bfhi(d2) * inv_sd);
            r2[e] = pk2(bf2f(hv) + bf2f(n2), bfhi(hv) + bfhi(n2));
          }
          *(u32x2*)(p.out + (size_t)tok * D + 80 * cc + 16 * ft + 4 * fg) = (u32x2){r2[0], r2[1]};
        }
      }
    }
  };
  if (is_gu) {
    {
      const unsigned kk = *(const LAS unsigned*)(L + L_SEL + w * 4), ms = *(const LAS unsigned*)(L + L_SEL + 32 + w * 4);
      const int row = 2047 - (int)(kk & 0x7ff);
      const float m = __uint_as_float(ms & 0xffff0000u), iv = 1.f / __uint_as_float(ms << 16);
      const u32x4 h0 = __builtin_amdgcn_raw_buffer_load_b128(rws, WS_H + (unsigned)row * 1280 + lane * 16, 0, SC1);
      const u32x4 h1 = __builtin_amdgcn_raw_buffer_load_b128(rws, WS_H + (unsigned)row * 1280 + 1024 + (lane & 15) * 16, 0, SC1);
      u32x4 n0, n1;
#pragma unroll
      for (int e = 0; e < 4; ++e) {
        const unsigned d0 = pk2(bf2f(h0[e]) - m, bfhi(h0[e]) - m), d1 = pk2(bf2f(h1[e]) - m, bfhi(h1[e]) - m);
        n0[e] = pk2(bf2f(d0) * iv, bfhi(d0) * iv); n1[e] = pk2(bf2f(d1) * iv, bfhi(d1) * iv);
      }
      *(LAS u32x4*)(L + L_XS + w * 1296 + lane * 16) = n0;
      if (lane < 16) *(LAS u32x4*)(L + L_XS + w * 1296 + 1024 + lane * 16) = n1;
    }
    WAITL0(); BARRIER();
    if (w == 0) STAMP(15);
    {
      const int m = w & 1, kq = w >> 1;
      f32x4 a4 = {0, 0, 0, 0};
#pragma unroll
      for (int i = 0; i < 5; ++i) {
        const int s = 5 * kq + i;
        const LAS char* ap = L + L_FW + (m * 20 + s) * 1024 + lane * 8;
        const bf16x8 af = cat8(trd(ap), trd(ap + 512));
        const bf16x8 bfv = *(const LAS bf16x8*)(L + L_XS + (lane & 7) * 1296 + s * 64 + fg * 16);
        a4 = MFMA16(af, bfv, a4);
      }
      *(LAS f32x4*)(L + L_FRED + (w * 64 + lane) * 16) = a4;
    }
    WAITL0(); BARRIER();
    if (w == 0) {
      f32x4 g4 = {0, 0, 0, 0}, u4 = {0, 0, 0, 0};
#pragma unroll
      for (int q = 0; q < 4; ++q) { g4 += *(const LAS f32x4*)(L + L_FRED + ((2 * q) * 64 + lane) * 16); u4 += *(const LAS f32x4*)(L + L_FRED + ((2 * q + 1) * 64 + lane) * 16); }
      float a[4];
#pragma unroll
      for (int e = 0; e < 4; ++e) { const float g = rbf(g4[e]), u = rbf(u4[e]); const float sg = rbf(g / (1.f + __expf(-g))); a[e] = sg * u; }
      if (l15 < 8) __builtin_amdgcn_raw_buffer_store_b128((u32x4){pk2(a[0], a[1]), tag32, pk2(a[2], a[3]), tag32}, rws, WS_ACT + (((gs >> 1) * 4 + 2 * (gs & 1) + (fg >> 1)) * 8 + l15) * 32 + (fg & 1) * 16, 0, SC1);
      STAMP(16);
    }
    out_rows();
  } else if (is_dn) {
    out_rows();
    const int row = 2047 - (int)(selk & 0x7ff);
    const u32x2 hv = __builtin_amdgcn_raw_buffer_load_b64(rws, WS_H + (unsigned)row * 1280 + (16 * dn + 4 * fg) * 2, 0, SC1);
    f32x4 a4 = {0, 0, 0, 0};
    {
      u32x4 g0[7], g1[7];
      unsigned spins = 0;
      for (;;) {
        bool ok = true;
#pragma unroll
        for (int i = 0; i < 7; ++i) {
          const int s = min(w + 8 * i, 53);
          const unsigned ao = WS_ACT + ((s * 4 + fg) * 8 + (lane & 7)) * 32;
          g0[i] = __builtin_amdgcn_raw_buffer_load_b128(rws, ao, 0, SC1); g1[i] = __builtin_amdgcn_raw_buffer_load_b128(rws, ao + 16, 0, SC1);
        }
#pragma unroll
        for (int i = 0; i < 7; ++i) ok = ok && g0[i][1] == tag32 && g0[i][3] == tag32 && g1[i][1] == tag32 && g1[i][3] == tag32;
        if (__all(ok)) break;
        if (++spins > SPIN_CAP) break;
        __builtin_amdgcn_s_sleep(1);
      }
      if (w == 0) STAMP(17);
#pragma unroll
      for (int i = 0; i < 7; ++i) {
        const int s = w + 8 * i;
        if (s < 54) {
          const LAS char* ap = L + L_FW + s * 1024 + lane * 8;
          const bf16x8 af = cat8(trd(ap), trd(ap + 512));
          a4 = MFMA16(af, __builtin_bit_cast(bf16x8, (u32x4){g0[i][0], g0[i][2], g1[i][0], g1[i][2]}), a4);
        }
      }
    }
    *(LAS f32x4*)(L + L_FRED + (w * 64 + lane) * 16) = a4;
    WAITL0(); BARRIER();
    if (w == 0) {
      f32x4 y = {0, 0, 0, 0};
#pragma unroll
      for (int q = 0; q < 8; ++q) y += *(const LAS f32x4*)(L + L_FRED + (q * 64 + lane) * 16);
      const unsigned rk = selk >> 16, rbits = (rk & 0x8000u) ? (rk ^ 0x8000u) : (rk ^ 0xffffu);
      const float rv = bf2f(rbits), sig = rbf(1.f / (1.f + __expf(-rv)));
      const float m = __uint_as_float(selm & 0xffff0000u), iv = 1.f / __uint_as_float(selm << 16);
      unsigned r2[2];
#pragma unroll
      for (int e = 0; e < 2; ++e) {
        const unsigned d2 = pk2(bf2f(hv[e]) - m, bfhi(hv[e]) - m);
        const unsigned n2 = pk2(bf2f(d2) * iv, bfhi(d2) * iv);
        const unsigned y2 = pk2(rbf(y[2 * e]) * sig, rbf(y[2 * e + 1]) * sig);
        const unsigned t2 = pk2(bf2f(n2) + bf2f(y2), bfhi(n2) + bfhi(y2));
        r2[e] = pk2(bf2f(hv[e]) + bf2f(t2), bfhi(hv[e]) + bfhi(t2));
      }
      if (l15 < 8) *(u32x2*)(p.out + (size_t)row * D + 16 * dn + 4 * fg) = (u32x2){r2[0], r2[1]};
      STAMP(18);
    }
  } else {
    out_rows();
  }
  if (w == 0) STAMP(19);
  STAMP_FLUSH();
}

extern "C" void kernel_launch(void* const* d_in, const int* in_sizes, int n_in, void* d_out, int out_size, void* d_ws, size_t ws_size, hipStream_t stream) {
  Params p;
  p.x = (const u16*)d_in[0]; p.wqkv = (const u16*)d_in[1]; p.wo = (const u16*)d_in[2]; p.wr = (const u16*)d_in[4];
  p.wg = (const u16*)d_in[5]; p.wu = (const u16*)d_in[6]; p.wd = (const u16*)d_in[7]; p.out = (u16*)d_out; p.ws = (unsigned char*)d_ws;
  fused_kernel<<<256, 512, 0, stream>>>(p);
}
```

```cpp
#include <hip/hip_runtime.h>
#include <stdint.h>

typedef unsigned short u16;
typedef short bf16x8 __attribute__((ext_vector_type(8)));
typedef short bf16x4 __attribute__((ext_vector_type(4)));
typedef __bf16 bf2 __attribute__((ext_vector_type(2)));
typedef float f32x16 __attribute__((ext_vector_type(16)));
typedef float f32x4 __attribute__((ext_vector_type(4)));
typedef float f32x2 __attribute__((ext_vector_type(2)));
typedef unsigned u32x4 __attribute__((ext_vector_type(4)));
typedef unsigned u32x2 __attribute__((ext_vector_type(2)));
typedef unsigned long long u64;
#define LAS __attribute__((address_space(3)))
#define SBAR() __builtin_amdgcn_sched_barrier(0)
#define BARRIER() asm volatile("s_barrier" ::: "memory")
#define WAITL0() asm volatile("s_waitcnt lgkmcnt(0)" ::: "memory")
#define WAITV(n) asm volatile("s_waitcnt vmcnt(" #n ")" ::: "memory")

constexpr int S = 2048, D = 640, F = 1728;
constexpr int SC1 = 16;

constexpr unsigned WS_EPOCH = 0;
constexpr unsigned WS_FLAGA = 4096;
constexpr unsigned WS_FLAGO = WS_FLAGA + 32768;
constexpr unsigned WS_CAND = WS_FLAGO + 16384;
constexpr unsigned WS_ACT = WS_CAND + 4096;
constexpr unsigned WS_STATS = 131072;
constexpr unsigned WS_QKV = 524288;
constexpr unsigned WS_O = WS_QKV + 480 * 16384;
constexpr unsigned WS_H = WS_O + S * D * 2;
#define STAMP_OFF (200u << 20)
#define NSTAMP 32

constexpr int L_SLOT = 32768, L_W = 16384;
constexpr int PL = 4128;
constexpr int L_K = 0, L_V = 5 * PL, L_ONE = 10 * PL;
constexpr int L_WO = 43008;
constexpr int L_RED = 0;
constexpr int L_FW = 43008;
constexpr int L_XS = 0;
constexpr int L_FRED = 12288;
constexpr int L_MISC = 147456;
constexpr int L_RS = L_MISC;
constexpr int L_CS = L_MISC + 1024;
constexpr int L_EP = L_MISC + 1536;
constexpr int L_KEY = L_MISC + 2048;
constexpr int L_MS = L_MISC + 2304;
constexpr int L_SEL = L_MISC + 2560;
constexpr int L_STAMP = L_MISC + 4096;
constexpr int LDS_BYTES = 163840;

struct Params { const u16* x; const u16* wqkv; const u16* wo; const u16* wr; const u16* wg; const u16* wu; const u16* wd; u16* out; unsigned char* ws; };

__device__ __forceinline__ float bf2f(unsigned v) { return __uint_as_float(v << 16); }
__device__ __forceinline__ float bfhi(unsigned v) { return __uint_as_float(v & 0xffff0000u); }
__device__ __forceinline__ unsigned pk2(float a, float b) { f32x2 f = {a, b}; return __builtin_bit_cast(unsigned, __builtin_convertvector(f, bf2)); }
__device__ __forceinline__ float rbf(float f) { return __uint_as_float(pk2(f, 0.f) << 16); }
__device__ __forceinline__ float dot2(unsigned a, unsigned b, float c) { return __builtin_amdgcn_fdot2_f32_bf16(__builtin_bit_cast(bf2, a), __builtin_bit_cast(bf2, b), c, false); }
__device__ __forceinline__ float swap_add(float v) { auto r = __builtin_amdgcn_permlane32_swap(__float_as_uint(v), __float_as_uint(v), false, false); return __uint_as_float(r[0]) + __uint_as_float(r[1]); }
__device__ __forceinline__ float swap16_add(float v) { auto r = __builtin_amdgcn_permlane16_swap(__float_as_uint(v), __float_as_uint(v), false, false); return __uint_as_float(r[0]) + __uint_as_float(r[1]); }
__device__ __forceinline__ void glds16(unsigned voff, const void* sbase, unsigned lds) {
  asm volatile("s_mov_b32 m0, %2\n\ts_nop 0\n\tglobal_load_lds_dwordx4 %0, %1" ::"v"(voff), "s"(sbase), "s"(lds) : "memory");
}
__device__ __forceinline__ void glds16c(unsigned voff, const void* sbase, unsigned lds) {
  asm volatile("s_mov_b32 m0, %2\n\ts_nop 0\n\tglobal_load_lds_dwordx4 %0, %1 sc1" ::"v"(voff), "s"(sbase), "s"(lds) : "memory");
}
__device__ __forceinline__ bf16x4 trd(const LAS char* p) { return __builtin_amdgcn_ds_read_tr16_b64_v4i16((LAS bf16x4*)p); }
__device__ __forceinline__ bf16x8 cat8(bf16x4 a, bf16x4 b) { return (bf16x8){a[0], a[1], a[2], a[3], b[0], b[1], b[2], b[3]}; }
#define MFMA32(a, b, c) __builtin_amdgcn_mfma_f32_32x32x16_bf16(a, b, c, 0, 0, 0)
#define MFMA32K8(a, b, c) __builtin_amdgcn_mfma_f32_32x32x8bf16_1k(a, b, c, 0, 0, 0)
#define MFMA16(a, b, c) __builtin_amdgcn_mfma_f32_16x16x32_bf16(a, b, c, 0, 0, 0)
template <int CTRL, int RMASK> __device__ __forceinline__ unsigned dppmax(unsigned v) { return max(v, (unsigned)__builtin_amdgcn_update_dpp(0, (int)v, CTRL, RMASK, 0xf, false)); }
__device__ __forceinline__ unsigned wave_umax(unsigned v) {
  v = dppmax<0x111, 0xf>(v); v = dppmax<0x112, 0xf>(v); v = dppmax<0x114, 0xf>(v); v = dppmax<0x118, 0xf>(v);
  v = dppmax<0x142, 0xa>(v); v = dppmax<0x143, 0xc>(v);
  return (unsigned)__builtin_amdgcn_readlane((int)v, 63);
}

template <int CTRL> __device__ __forceinline__ float dppadd(float v) { return v + __int_as_float(__builtin_amdgcn_update_dpp(0, __float_as_int(v), CTRL, 0xf, 0xf, false)); }
__device__ __forceinline__ float wave_fsum(float v) { v = dppadd<0x128>(v); v = dppadd<0x124>(v); v = dppadd<0x122>(v); v = dppadd<0x121>(v); return swap_add(swap16_add(v)); }

#define STAMP(k) do {} while (0)
#define STAMP_INIT() do {} while (0)
#define STAMP_FLUSH() do {} while (0)

constexpr unsigned SPIN_CAP = 1u << 16;

__global__ __launch_bounds__(512, 2) void fused_kernel(Params p) {
  __shared__ __attribute__((aligned(16))) char smem[LDS_BYTES];
  LAS char* L = (LAS char*)smem;
  const unsigned lds0 = (unsigned)(uintptr_t)smem;
  const int tid = threadIdx.x, lane = tid & 63;
  const int w = __builtin_amdgcn_readfirstlane(tid >> 6);
  const int b = blockIdx.x, xcd = b & 7, bi = b >> 3;
  const int l31 = lane & 31, hh = lane >> 5, l15 = lane & 15, fg = lane >> 4;
  const __amdgpu_buffer_rsrc_t rws = __builtin_amdgcn_make_buffer_rsrc(p.ws, 0, 0x7fffffff, 0x00020000);
  const __amdgpu_buffer_rsrc_t rout = __builtin_amdgcn_make_buffer_rsrc(p.out, 0, S * D * 2, 0x00020000);
  const int aP = 4 * (xcd >> 1) + (bi >> 3), aj = 8 * (xcd & 1) + (bi & 7);
  const int hd = bi, pp = xcd;
  const int R = 4 * xcd + (bi >> 3), cc = bi & 7;
  const int gs = xcd * 14 + bi;
  const bool is_gu = bi < 14 && gs < 108;
  const int dn = xcd * 5 + (bi - 14);
  const bool is_dn = bi >= 14 && bi < 19;
  STAMP_INIT();

  if (aj < 15 && w >= 4) {
    const int lw = w - 4;
    const unsigned xsw = (unsigned)(4 * (lw & 1) + (lane >> 4));
    const unsigned voff_x = (unsigned)(lane >> 3) * 1280u + (((unsigned)(lane & 7) ^ xsw) << 4);
    const unsigned ch = (unsigned)(lane & 15) ^ (((unsigned)(lane >> 4) << 2) | (unsigned)lw);
    const unsigned voff_w = (unsigned)(lane >> 4) * 3840u + ch * 16u;
    const char* xb = (const char*)p.x + (size_t)(128 * aP + 8 * lw) * 1280;
    const char* wb = (const char*)p.wqkv + (size_t)(4 * lw) * 3840 + 256 * aj;
    const unsigned lbase = lds0 + lw * 1024;
    const int rot = ((bi & 7) * 5 / 4 + (bi >> 3) * 5 / 2) % 10;
    auto issue = [&](int s) {
      const unsigned sl = lbase + (s & 3) * L_SLOT;
      int ss = s + rot; if (ss >= 10) ss -= 10;
#pragma unroll
      for (int i = 0; i < 4; ++i) {
        glds16(voff_x, xb + (size_t)i * 32 * 1280 + ss * 128, sl + i * 4096);
        glds16(voff_w, wb + ((size_t)ss * 64 + i * 16) * 3840, sl + L_W + i * 4096);
      }
    };
    issue(0); issue(1); issue(2); issue(3);
    WAITV(24); BARRIER();
    if (w == 4) STAMP(1);
    WAITV(16); BARRIER();
#pragma unroll 1
    for (int s = 1; s <= 6; ++s) { issue(s + 3); WAITV(16); BARRIER(); }
    WAITV(8); BARRIER();
    WAITV(0); BARRIER();
    BARRIER();
    BARRIER();
  } else if (w < 4) {
    if (w == 0) {
      const u32x2 e = __builtin_amdgcn_raw_buffer_load_b64(rws, WS_EPOCH, 0, SC1);
      const u64 t = (((u64)e[1] << 32) | e[0]) + 1;
      if (lane == 0) *(LAS u32x2*)(L + L_EP) = (u32x2){(unsigned)t, (unsigned)(t >> 32)};
      __builtin_amdgcn_raw_buffer_store_b128((u32x4){0, 0, 0, 0}, rws, WS_STATS + (R * 8 + cc) * 1024 + lane * 16, 0, SC1);
      if (is_gu && lane < 32) __builtin_amdgcn_raw_buffer_store_b128((u32x4){0, 0, 0, 0}, rws, WS_ACT + (((gs >> 1) * 4 + 2 * (gs & 1) + (lane >> 4)) * 8 + (lane & 7)) * 32 + ((lane >> 3) & 1) * 16, 0, SC1);
    }
    if (aj < 15) {
      const int wm = w >> 1, wn = w & 1;
      unsigned xa[4], wa[4];
#pragma unroll
      for (int ks = 0; ks < 4; ++ks) xa[ks] = (unsigned)(64 * wm + l31) * 128u + ((((unsigned)(2 * ks + hh)) ^ (((unsigned)l31 >> 1) & 7u)) << 4);
      const unsigned xo0 = wn * 4096, xo1 = 4096 - xo0;
      {
        const unsigned blk = (lane >> 4) & 1, q = (lane & 15) >> 2, pq = lane & 3;
#pragma unroll
        for (int j = 0; j < 2; ++j)
#pragma unroll
          for (int t = 0; t < 2; ++t) {
            const unsigned jj = j ^ wm;
            const unsigned row = 8 * hh + 4 * t + q, chn = 4 * (2 * wn + jj) + 2 * blk + (pq >> 1), m = (q << 2) | (unsigned)(2 * hh + t);
            wa[j * 2 + t] = L_W + 256u * row + 16u * (chn ^ m) + 8u * (pq & 1);
          }
      }
      f32x16 a00 = {}, a01 = {}, a10 = {}, a11 = {};
      float ssum = 0.f, ssq = 0.f, csum = 0.f;
      bf16x8 xf0[2], wf0[2], xf1[2], wf1[2];
      const unsigned ONE2 = 0x3f803f80u;
      unsigned xb0[4], xb1[4], wb[4];
#define SETSLOT(so) do { _Pragma("unroll") for (int k_ = 0; k_ < 4; ++k_) { xb0[k_] = xa[k_] + (so) + xo0; xb1[k_] = xa[k_] + (so) + xo1; wb[k_] = wa[k_] + (so); } } while (0)
#define RD(XF, WF, ks) do { \
        XF[0] = *(const LAS bf16x8*)(L + xb0[ks]); XF[1] = *(const LAS bf16x8*)(L + xb1[ks]); \
        WF[0] = cat8(trd(L + wb[0] + (ks) * 4096), trd(L + wb[1] + (ks) * 4096)); \
        WF[1] = cat8(trd(L + wb[2] + (ks) * 4096), trd(L + wb[3] + (ks) * 4096)); } while (0)
#define MM(XF, WF) do { \
        a00 = MFMA32(WF[0], XF[0], a00); a01 = MFMA32(WF[1], XF[0], a01); \
        a10 = MFMA32(WF[0], XF[1], a10); a11 = MFMA32(WF[1], XF[1], a11); \
        { const u32x4 xs_ = __builtin_bit_cast(u32x4, XF[0]); const u32x4 ws_ = __builtin_bit_cast(u32x4, WF[0]); \
          _Pragma("unroll") for (int e_ = 0; e_ < 4; ++e_) { ssum = dot2(xs_[e_], ONE2, ssum); ssq = dot2(xs_[e_], xs_[e_], ssq); csum = dot2(ws_[e_], ONE2, csum); } } } while (0)
#define PINALL() asm volatile("" : "+v"(a00), "+v"(a01), "+v"(a10), "+v"(a11), "+v"(ssum), "+v"(ssq), "+v"(csum))
#define ILV() do { _Pragma("unroll") for (int g_ = 0; g_ < 4; ++g_) { \
        __builtin_amdgcn_sched_group_barrier(0x8, 1, 0); __builtin_amdgcn_sched_group_barrier(0x100, 2, 0); __builtin_amdgcn_sched_group_barrier(0x2, 3, 0); } } while (0)
#define STEP(XN, WN, ksn, XC, WC) do { RD(XN, WN, ksn); MM(XC, WC); ILV(); PINALL(); SBAR(); } while (0)
      SETSLOT(0);
      BARRIER();
      RD(xf0, wf0, 0);
      SBAR();
#pragma unroll 1
      for (int s = 0; s < 9; ++s) {
        STEP(xf1, wf1, 1, xf0, wf0);
        STEP(xf0, wf0, 2, xf1, wf1);
        STEP(xf1, wf1, 3, xf0, wf0);
        SETSLOT(((s + 1) & 3) * L_SLOT);
        WAITL0(); BARRIER();
        STEP(xf0, wf0, 0, xf1, wf1);
      }
      STEP(xf1, wf1, 1, xf0, wf0);
      STEP(xf0, wf0, 2, xf1, wf1);
      STEP(xf1, wf1, 3, xf0, wf0);
      MM(xf1, wf1); PINALL();
      if (w == 0) STAMP(2);
      ssum = swap_add(ssum); ssq = swap_add(ssq); csum = swap_add(csum);
      {
        const float mean = ssum * (1.f / D);
        const float var = fmaxf(ssq * (1.f / D) - mean * mean, 0.f);
        const float mb = rbf(mean), vb = rbf(var);
        const float sd = rbf(sqrtf(rbf(vb + 1e-5f)));
        if (lane < 32) {
          *(LAS f32x2*)(L + L_RS + (64 * wm + 32 * wn + l31) * 8) = (f32x2){mb, 1.f / sd};
          *(LAS float*)(L + L_CS + (64 * wn + 32 * wm + l31) * 4) = csum;
        }
      }
      WAITV(0); WAITL0(); BARRIER();
      const float qs = aj < 5 ? 0.22360679774997896f * 1.4426950408889634f : 1.f;
#pragma unroll
      for (int i = 0; i < 2; ++i) {
        const int tok = 64 * wm + 32 * (i ^ wn) + l31;
        const f32x2 rs = *(const LAS f32x2*)(L + L_RS + tok * 8);
        const float sc = rs[1] * qs, nm = -rs[0] * sc;
#pragma unroll
        for (int j = 0; j < 2; ++j) {
          const f32x16& a = i == 0 ? (j == 0 ? a00 : a01) : (j == 0 ? a10 : a11);
          const int f0 = 64 * wn + 32 * (j ^ wm) + 4 * hh;
          const unsigned go = WS_QKV + (unsigned)(32 * aj + (f0 >> 2)) * 16384u + (unsigned)(128 * aP + tok) * 8u;
#pragma unroll
          for (int rq = 0; rq < 4; ++rq) {
            const f32x4 cs = *(const LAS f32x4*)(L + L_CS + (f0 + 8 * rq) * 4);
            const float v0 = fmaf(a[rq * 4 + 0], sc, nm * cs[0]), v1 = fmaf(a[rq * 4 + 1], sc, nm * cs[1]);
            const float v2 = fmaf(a[rq * 4 + 2], sc, nm * cs[2]), v3 = fmaf(a[rq * 4 + 3], sc, nm * cs[3]);
            __builtin_amdgcn_raw_buffer_store_b64((u32x2){pk2(v0, v1), pk2(v2, v3)}, rws, go + rq * 32768, 0, SC1);
          }
        }
      }
      WAITV(0); BARRIER();
    } else {
      WAITV(0);
    }
  }
  if (aj >= 15) { WAITL0(); BARRIER(); }
  const u32x2 tag2 = *(const LAS u32x2*)(L + L_EP);
  const unsigned tag32 = tag2[0] | 1u;
  if (aj < 15 && tid == 0) __builtin_amdgcn_raw_buffer_store_b64(tag2, rws, WS_FLAGA + (aP * 16 + aj) * 128, 0, SC1);
  if (w == 0) STAMP(3);

  f32x16 o = (f32x16){};
  {
    const int t = w;
    if (tid < 4) *(LAS unsigned*)(L + L_ONE + tid * 4) = tid == 0 ? 0x00003f80u : 0u;
    {
      const int sec = lane < 4 ? 0 : (lane < 12 ? 1 : 2);
      const int k = lane < 4 ? lane : (lane < 12 ? lane - 4 : lane - 12);
      const int col = sec * 640 + 20 * hd + ((k & 1) ? 19 : 0);
      int Pn = sec == 0 ? 2 * pp + (k >> 1) : 2 * pp - 2 + (k >> 1);
      const bool need = lane < 20 && Pn >= 0;
      const unsigned fo = WS_FLAGA + (unsigned)((need ? Pn : 0) * 16 + (col >> 7)) * 128;
      unsigned spins = 0;
      for (;;) {
        const u32x2 f = __builtin_amdgcn_raw_buffer_load_b64(rws, fo, 0, SC1);
        if (__all(!need || (f[0] == tag2[0] && f[1] == tag2[1]))) break;
        if (++spins > SPIN_CAP) break;
        __builtin_amdgcn_s_sleep(1);
      }
    }
    asm volatile("" ::: "memory");
    if (w == 0) STAMP(4);
    {
      const char* qb = (const char*)p.ws + WS_QKV;
#pragma unroll
      for (int i = 0; i < 5; ++i) {
        const int pc = w + 8 * i, pl = pc >> 2, qt = pc & 3;
        const int tok0 = 256 * pp - 256 + 128 * qt;
        if (tok0 >= 0) glds16c(lane * 16, qb + (size_t)((pl < 5 ? 160 + 5 * hd + pl : 320 + 5 * hd + pl - 5)) * 16384 + tok0 * 8, lds0 + pl * PL + qt * 1024);
      }
    }
    bf16x8 qf0; bf16x4 qf1;
    {
      const unsigned qo = WS_QKV + (unsigned)(5 * hd) * 16384u + (unsigned)(256 * pp + 32 * t + l31) * 8u;
      const u32x2 a = __builtin_amdgcn_raw_buffer_load_b64(rws, qo + (2 * hh) * 16384, 0, SC1);
      const u32x2 c = __builtin_amdgcn_raw_buffer_load_b64(rws, qo + (2 * hh + 1) * 16384, 0, SC1);
      u32x2 z = __builtin_amdgcn_raw_buffer_load_b64(rws, qo + 4 * 16384, 0, SC1);
      if (hh) z = (u32x2){0, 0};
      qf0 = __builtin_bit_cast(bf16x8, (u32x4){a[0], a[1], c[0], c[1]});
      qf1 = __builtin_bit_cast(bf16x4, z);
    }
    WAITV(0); WAITL0(); BARRIER();
    if (w == 0) STAMP(5);
    {
#pragma unroll 1
      for (int pc = w; pc < 100; pc += 8) {
        const int n = pc * 64 + lane, rho = n / 10, ch = n - rho * 10;
        const int s = rho >> 5, tq = (rho >> 4) & 1, kg = (rho >> 2) & 3, q = rho & 3;
        const int cidx = 4 * s + kg, g2 = cidx / 5, c5 = cidx - 5 * g2;
        const int quad = 10 * g2 + ((tq ? 0x98732 : 0x46510) >> (4 * c5) & 15);
        glds16((unsigned)(4 * quad + q) * 1280u + ch * 16u, (const char*)p.wo + 160 * cc, lds0 + L_WO + pc * 1024);
      }
    }
    const int kad = L_K + l31 * 8;
    float negm;
    {
      const LAS char* kp = L + kad + (t + 8) * 256;
      const u32x2 k0 = *(const LAS u32x2*)(kp + (2 * hh) * PL), k1 = *(const LAS u32x2*)(kp + (2 * hh + 1) * PL), k2 = *(const LAS u32x2*)(kp + 4 * PL);
      const u32x4 q0 = __builtin_bit_cast(u32x4, qf0); const u32x2 q1 = __builtin_bit_cast(u32x2, qf1);
      float d = dot2(q0[0], k0[0], 0.f); d = dot2(q0[1], k0[1], d); d = dot2(q0[2], k1[0], d); d = dot2(q0[3], k1[1], d);
      d = dot2(q1[0], k2[0], d); d = dot2(q1[1], k2[1], d);
      negm = -swap_add(d);
    }
    const int vquad = 4 * ((lane >> 4) & 1) + (lane & 3);
    const int vad = vquad < 5 ? L_V + vquad * PL + (4 * hh + ((lane & 15) >> 2)) * 8 : L_ONE;
    const int vst = vquad < 5 ? 1 : 0;
    auto tile = [&](int kt, const f32x16& cin) {
      const LAS char* kp = L + kad + kt * 256;
      const bf16x4 ka = *(const LAS bf16x4*)(kp + (2 * hh) * PL), kb = *(const LAS bf16x4*)(kp + (2 * hh + 1) * PL), kc = *(const LAS bf16x4*)(kp + 4 * PL);
      const LAS char* vp = L + vad + vst * kt * 256;
      const bf16x8 v0 = cat8(trd(vp), trd(vp + vst * 64)), v1 = cat8(trd(vp + vst * 128), trd(vp + vst * 192));
      f32x16 s = MFMA32(cat8(ka, kb), qf0, cin);
      s = MFMA32K8(kc, qf1, s);
      u32x4 p0, p1;
#pragma unroll
      for (int e = 0; e < 4; ++e) {
        p0[e] = pk2(__builtin_amdgcn_exp2f(s[2 * e]), __builtin_amdgcn_exp2f(s[2 * e + 1]));
        p1[e] = pk2(__builtin_amdgcn_exp2f(s[8 + 2 * e]), __builtin_amdgcn_exp2f(s[8 + 2 * e + 1]));
      }
      o = MFMA32(v0, __builtin_bit_cast(bf16x8, p0), o);
      o = MFMA32(v1, __builtin_bit_cast(bf16x8, p1), o);
    };
    f32x16 cm;
#pragma unroll
    for (int r = 0; r < 16; ++r) { const int kr = (r & 3) + 8 * (r >> 2) + 4 * hh; cm[r] = kr <= l31 ? negm : -INFINITY; }
    tile(t + 8, cm);
#pragma unroll
    for (int r = 0; r < 16; ++r) cm[r] = negm;
    const int lo = pp >= 1 ? t + 1 : 8;
#pragma unroll 1
    for (int kt = t + 7; kt >= lo; --kt) tile(kt, cm);
    if (pp >= 1) {
#pragma unroll
      for (int r = 0; r < 16; ++r) { const int kr = (r & 3) + 8 * (r >> 2) + 4 * hh; cm[r] = kr > l31 ? negm : -INFINITY; }
      tile(t, cm);
    }
    if (w == 0) STAMP(6);
    auto r = __builtin_amdgcn_permlane32_swap(__float_as_uint(o[8]), __float_as_uint(o[8]), false, false);
    const float inv = 1.f / __uint_as_float(r[1]);
    u32x4 c0;
    c0[0] = pk2(o[0] * inv, o[1] * inv); c0[1] = pk2(o[2] * inv, o[3] * inv); c0[2] = pk2(o[4] * inv, o[5] * inv); c0[3] = pk2(o[6] * inv, o[7] * inv);
    const u32x2 c1 = (u32x2){pk2(o[8] * inv, o[9] * inv), pk2(o[10] * inv, o[11] * inv)};
    const int tt = pp * 16 + 2 * t + (l31 >> 4);
    const unsigned base = WS_O + (unsigned)tt * 80 * 256 + (l31 & 15) * 16 + (unsigned)(5 * (hd >> 1)) * 256;
    __builtin_amdgcn_raw_buffer_store_b128(c0, rws, base + (2 * (hd & 1) + hh) * 256, 0, SC1);
    if (hh == 0) __builtin_amdgcn_raw_buffer_store_b64(c1, rws, base + 4 * 256 + (hd & 1) * 8, 0, SC1);
    WAITV(0);
    if (lane == 0) __builtin_amdgcn_raw_buffer_store_b64(tag2, rws, WS_FLAGO + (((pp * 8 + t) * 2 + (hd >> 4)) * 16 + (hd & 15)) * 8, 0, SC1);
  }
  if (w == 0) STAMP(7);
  BARRIER();

  const int nt = w & 3, kh = w >> 2;
  const int tok = 64 * R + 16 * nt + l15;
  u32x2 xr[5], wrv[5];
  float swr = 0.f;
  if (kh == 0) {
#pragma unroll
    for (int ft = 0; ft < 5; ++ft) {
      xr[ft] = *(const u32x2*)(p.x + (size_t)tok * D + 80 * cc + 16 * ft + 4 * fg);
      wrv[ft] = *(const u32x2*)(p.wr + 80 * cc + 16 * ft + 4 * fg);
    }
    const unsigned* wr32 = (const unsigned*)p.wr;
#pragma unroll
    for (int i = 0; i < 5; ++i) swr = dot2(wr32[lane + 64 * i], 0x3f803f80u, swr);
    swr = wave_fsum(swr);
  }
  f32x4 acc[5];
#pragma unroll
  for (int ft = 0; ft < 5; ++ft) acc[ft] = (f32x4){0, 0, 0, 0};
  {
    const int tg = 4 * R + nt;
    const unsigned fo = WS_FLAGO + ((((tg >> 4) * 8 + ((tg >> 1) & 7)) * 2 + kh) * 16 + l15) * 8;
    unsigned spins = 0;
    for (;;) {
      const u32x2 f = __builtin_amdgcn_raw_buffer_load_b64(rws, fo, 0, SC1);
      if (__all(f[0] == tag2[0] && f[1] == tag2[1])) break;
      if (++spins > SPIN_CAP) break;
      __builtin_amdgcn_s_sleep(1);
    }
    asm volatile("" ::: "memory");
    if (w == 0) STAMP(8);
    u32x4 of[10];
#pragma unroll
    for (int s = 0; s < 10; ++s) of[s] = __builtin_amdgcn_raw_buffer_load_b128(rws, WS_O + (unsigned)(tg * 80 + 4 * (10 * kh + s)) * 256 + lane * 16, 0, SC1);
    const LAS char* wp = L + L_WO + (unsigned)(kh * 10 * 32 + fg * 4 + ((lane & 15) >> 2)) * 160 + (lane & 3) * 8;
#pragma unroll
    for (int s = 0; s < 10; ++s) {
#pragma unroll
      for (int ft = 0; ft < 5; ++ft) {
        const bf16x8 a = cat8(trd(wp + (s * 32) * 160 + ft * 32), trd(wp + (s * 32 + 16) * 160 + ft * 32));
        acc[ft] = MFMA16(a, __builtin_bit_cast(bf16x8, of[s]), acc[ft]);
      }
    }
  }
  if (w == 0) STAMP(9);
  if (kh == 1) {
#pragma unroll
    for (int ft = 0; ft < 5; ++ft) *(LAS f32x4*)(L + L_RED + ((nt * 5 + ft) * 64 + lane) * 16) = acc[ft];
  }
  WAITL0(); BARRIER();
  unsigned hp[10];
  float mb = 0.f, inv_sd = 0.f;
  if (kh == 1) {
    const int rl = lane >> 1, grow = 8 * ((rl >> 2) & 3) + 4 * (rl >> 4) + (rl & 3);
    if (is_gu) {
      const unsigned vo = (unsigned)grow * (F * 2) + (lane & 1) * 16;
#pragma unroll 1
      for (int pc = nt; pc < 40; pc += 4) { const int m = pc >= 20, s = pc - 20 * m; glds16(vo, (const char*)(m ? p.wu : p.wg) + (size_t)s * 32 * F * 2 + gs * 32, lds0 + L_FW + pc * 1024); }
    } else if (is_dn) {
      const unsigned vo = (unsigned)grow * (D * 2) + (lane & 1) * 16;
#pragma unroll 1
      for (int pc = nt; pc < 54; pc += 4) glds16(vo, (const char*)p.wd + (size_t)pc * 32 * D * 2 + dn * 32, lds0 + L_FW + pc * 1024);
    }
  } else {
    float s1 = 0.f, s2 = 0.f, s3 = 0.f;
#pragma unroll
    for (int ft = 0; ft < 5; ++ft) {
      const f32x4 o2 = *(const LAS f32x4*)(L + L_RED + ((nt * 5 + ft) * 64 + lane) * 16);
      const unsigned a01 = pk2(acc[ft][0] + o2[0], acc[ft][1] + o2[1]), a23 = pk2(acc[ft][2] + o2[2], acc[ft][3] + o2[3]);
      const unsigned h01 = pk2(bf2f(xr[ft][0]) + bf2f(a01), bfhi(xr[ft][0]) + bfhi(a01)), h23 = pk2(bf2f(xr[ft][1]) + bf2f(a23), bfhi(xr[ft][1]) + bfhi(a23));
      hp[2 * ft] = h01; hp[2 * ft + 1] = h23;
      __builtin_amdgcn_raw_buffer_store_b64((u32x2){h01, h23}, rws, WS_H + (unsigned)tok * 1280 + (80 * cc + 16 * ft + 4 * fg) * 2, 0, SC1);
      s1 = dot2(h01, 0x3f803f80u, s1); s1 = dot2(h23, 0x3f803f80u, s1);
      s2 = dot2(h01, h01, s2); s2 = dot2(h23, h23, s2);
      s3 = dot2(h01, wrv[ft][0], s3); s3 = dot2(h23, wrv[ft][1], s3);
    }
    s1 = swap_add(swap16_add(s1)); s2 = swap_add(swap16_add(s2)); s3 = swap_add(swap16_add(s3));
    if (lane < 16) __builtin_amdgcn_raw_buffer_store_b128((u32x4){__float_as_uint(s1), __float_as_uint(s2), __float_as_uint(s3), tag32}, rws, WS_STATS + ((R * 8 + cc) * 64 + 16 * nt + l15) * 16, 0, SC1);
    if (w == 0) STAMP(10);
    float t1, t2, t3;
    {
      const unsigned so = WS_STATS + ((R * 8 + fg) * 64 + 16 * nt + l15) * 16;
      unsigned spins = 0;
      for (;;) {
        const u32x4 ga = __builtin_amdgcn_raw_buffer_load_b128(rws, so, 0, SC1), gb = __builtin_amdgcn_raw_buffer_load_b128(rws, so + 4 * 1024, 0, SC1);
        t1 = __uint_as_float(ga[0]) + __uint_as_float(gb[0]); t2 = __uint_as_float(ga[1]) + __uint_as_float(gb[1]); t3 = __uint_as_float(ga[2]) + __uint_as_float(gb[2]);
        if (__all(ga[3] == tag32 && gb[3] == tag32)) break;
        if (++spins > SPIN_CAP) break;
        __builtin_amdgcn_s_sleep(1);
      }
    }
    t1 = swap_add(swap16_add(t1)); t2 = swap_add(swap16_add(t2)); t3 = swap_add(swap16_add(t3));
    const float mean = t1 * (1.f / D);
    const float var = fmaxf(t2 * (1.f / D) - mean * mean, 0.f);
    mb = rbf(mean);
    const float sd = rbf(sqrtf(rbf(rbf(var) + 1e-5f)));
    inv_sd = 1.f / sd;
    const unsigned rb = pk2((t3 - mb * swr) * inv_sd, 0.f) & 0xffffu;
    const unsigned key = ((rb ^ ((rb & 0x8000u) ? 0xffffu : 0x8000u)) << 16) | (unsigned)(2047 - tok);
    if (lane < 16) {
      *(LAS unsigned*)(L + L_KEY + (16 * nt + l15) * 4) = key;
      *(LAS unsigned*)(L + L_MS + (16 * nt + l15) * 4) = (__float_as_uint(mb) & 0xffff0000u) | (__float_as_uint(sd) >> 16);
    }
    WAITV(0);
    if (w == 0) STAMP(11);
  }
  WAITL0(); BARRIER();
  if (w == 0) {
    unsigned k = *(const LAS unsigned*)(L + L_KEY + lane * 4), m = 0;
    for (int i = 0; i <= cc; ++i) { m = wave_umax(k); if (k == m) k = 0; }
    const unsigned ms = *(const LAS unsigned*)(L + L_MS + ((2047 - (int)(m & 0x7ff)) & 63) * 4);
    if (lane == 0) __builtin_amdgcn_raw_buffer_store_b128((u32x4){m, tag2[0], ms, tag2[1]}, rws, WS_CAND + (R * 8 + cc) * 16, 0, SC1);
    STAMP(12);
    unsigned k0, k1, k2, k3, m0, m1, m2, m3;
    unsigned spins = 0;
    for (;;) {
      const u32x4 a = __builtin_amdgcn_raw_buffer_load_b128(rws, WS_CAND + lane * 16, 0, SC1), bb = __builtin_amdgcn_raw_buffer_load_b128(rws, WS_CAND + 1024 + lane * 16, 0, SC1);
      const u32x4 c = __builtin_amdgcn_raw_buffer_load_b128(rws, WS_CAND + 2048 + lane * 16, 0, SC1), d = __builtin_amdgcn_raw_buffer_load_b128(rws, WS_CAND + 3072 + lane * 16, 0, SC1);
      k0 = a[0]; k1 = bb[0]; k2 = c[0]; k3 = d[0]; m0 = a[2]; m1 = bb[2]; m2 = c[2]; m3 = d[2];
      const bool ok = a[1] == tag2[0] && a[3] == tag2[1] && bb[1] == tag2[0] && bb[3] == tag2[1] && c[1] == tag2[0] && c[3] == tag2[1] && d[1] == tag2[0] && d[3] == tag2[1];
      if (__all(ok)) break;
      if (++spins > SPIN_CAP) break;
      __builtin_amdgcn_s_sleep(1);
    }
    STAMP(13);
#pragma unroll 1
    for (int i = 0; i < 8; ++i) {
      const unsigned lm = max(max(k0, k1), max(k2, k3));
      const unsigned g = wave_umax(lm);
      const unsigned ms2 = k0 == g ? m0 : (k1 == g ? m1 : (k2 == g ? m2 : m3));
      if (lm == g) { *(LAS unsigned*)(L + L_SEL + i * 4) = g; *(LAS unsigned*)(L + L_SEL + 32 + i * 4) = ms2; }
      k0 = k0 == g ? 0 : k0; k1 = k1 == g ? 0 : k1; k2 = k2 == g ? 0 : k2; k3 = k3 == g ? 0 : k3;
    }
    if (b == 0 && lane == 0) __builtin_amdgcn_raw_buffer_store_b64(tag2, rws, WS_EPOCH, 0, SC1);
    STAMP(14);
  }
  if (kh == 1) WAITV(0);
  WAITL0(); BARRIER();

  const unsigned selk = *(const LAS unsigned*)(L + L_SEL + (lane & 7) * 4), selm = *(const LAS unsigned*)(L + L_SEL + 32 + (lane & 7) * 4);
  auto out_rows = [&]() {
    if (kh == 0) {
      bool sel = false;
#pragma unroll
      for (int i = 0; i < 8; ++i) sel |= (2047 - (int)(__builtin_amdgcn_readlane((int)selk, i) & 0x7ff)) == tok;
      if (!sel) {
#pragma unroll
        for (int ft = 0; ft < 5; ++ft) {
          unsigned r2[2];
#pragma unroll
          for (int e = 0; e < 2; ++e) {
            const unsigned hv = hp[2 * ft + e];
            const unsigned d2 = pk2(bf2f(hv) - mb, bfhi(hv) - mb);
            const unsigned n2 = pk2(bf2f(d2) * inv_sd, bfhi(d2) * inv_sd);
            r2[e] = pk2(bf2f(hv) + bf2f(n2), bfhi(hv) + bfhi(n2));
          }
          __builtin_amdgcn_raw_buffer_store_b64((u32x2){r2[0], r2[1]}, rout, (unsigned)(tok * D + 80 * cc + 16 * ft + 4 * fg) * 2, 0, SC1);
        }
      }
    }
  };
  if (is_gu) {
    {
      const unsigned kk = *(const LAS unsigned*)(L + L_SEL + w * 4), ms = *(const LAS unsigned*)(L + L_SEL + 32 + w * 4);
      const int row = 2047 - (int)(kk & 0x7ff);
      const float m = __uint_as_float(ms & 0xffff0000u), iv = 1.f / __uint_as_float(ms << 16);
      const u32x4 h0 = __builtin_amdgcn_raw_buffer_load_b128(rws, WS_H + (unsigned)row * 1280 + lane * 16, 0, SC1);
      const u32x4 h1 = __builtin_amdgcn_raw_buffer_load_b128(rws, WS_H + (unsigned)row * 1280 + 1024 + (lane & 15) * 16, 0, SC1);
      u32x4 n0, n1;
#pragma unroll
      for (int e = 0; e < 4; ++e) {
        const unsigned d0 = pk2(bf2f(h0[e]) - m, bfhi(h0[e]) - m), d1 = pk2(bf2f(h1[e]) - m, bfhi(h1[e]) - m);
        n0[e] = pk2(bf2f(d0) * iv, bfhi(d0) * iv); n1[e] = pk2(bf2f(d1) * iv, bfhi(d1) * iv);
      }
      *(LAS u32x4*)(L + L_XS + w * 1296 + lane * 16) = n0;
      if (lane < 16) *(LAS u32x4*)(L + L_XS + w * 1296 + 1024 + lane * 16) = n1;
    }
    WAITL0(); BARRIER();
    if (w == 0) STAMP(15);
    {
      const int m = w & 1, kq = w >> 1;
      f32x4 a4 = {0, 0, 0, 0};
#pragma unroll
      for (int i = 0; i < 5; ++i) {
        const int s = 5 * kq + i;
        const LAS char* ap = L + L_FW + (m * 20 + s) * 1024 + lane * 8;
        const bf16x8 af = cat8(trd(ap), trd(ap + 512));
        const bf16x8 bfv = *(const LAS bf16x8*)(L + L_XS + (lane & 7) * 1296 + s * 64 + fg * 16);
        a4 = MFMA16(af, bfv, a4);
      }
      *(LAS f32x4*)(L + L_FRED + (w * 64 + lane) * 16) = a4;
    }
    WAITL0(); BARRIER();
    if (w == 0) {
      f32x4 g4 = {0, 0, 0, 0}, u4 = {0, 0, 0, 0};
#pragma unroll
      for (int q = 0; q < 4; ++q) { g4 += *(const LAS f32x4*)(L + L_FRED + ((2 * q) * 64 + lane) * 16); u4 += *(const LAS f32x4*)(L + L_FRED + ((2 * q + 1) * 64 + lane) * 16); }
      float a[4];
#pragma unroll
      for (int e = 0; e < 4; ++e) { const float g = rbf(g4[e]), u = rbf(u4[e]); const float sg = rbf(g / (1.f + __expf(-g))); a[e] = sg * u; }
      if (l15 < 8) __builtin_amdgcn_raw_buffer_store_b128((u32x4){pk2(a[0], a[1]), tag32, pk2(a[2], a[3]), tag32}, rws, WS_ACT + (((gs >> 1) * 4 + 2 * (gs & 1) + (fg >> 1)) * 8 + l15) * 32 + (fg & 1) * 16, 0, SC1);
      STAMP(16);
    }
    out_rows();
  } else if (is_dn) {
    out_rows();
    const int row = 2047 - (int)(selk & 0x7ff);
    const u32x2 hv = __builtin_amdgcn_raw_buffer_load_b64(rws, WS_H + (unsigned)row * 1280 + (16 * dn + 4 * fg) * 2, 0, SC1);
    f32x4 a4 = {0, 0, 0, 0};
    {
      u32x4 g0[7], g1[7];
      unsigned spins = 0;
      for (;;) {
        bool ok = true;
#pragma unroll
        for (int i = 0; i < 7; ++i) {
          const int s = min(w + 8 * i, 53);
          const unsigned ao = WS_ACT + ((s * 4 + fg) * 8 + (lane & 7)) * 32;
          g0[i] = __builtin_amdgcn_raw_buffer_load_b128(rws, ao, 0, SC1); g1[i] = __builtin_amdgcn_raw_buffer_load_b128(rws, ao + 16, 0, SC1);
        }
#pragma unroll
        for (int i = 0; i < 7; ++i) ok = ok && g0[i][1] == tag32 && g0[i][3] == tag32 && g1[i][1] == tag32 && g1[i][3] == tag32;
        if (__all(ok)) break;
        if (++spins > SPIN_CAP) break;
        __builtin_amdgcn_s_sleep(1);
      }
      if (w == 0) STAMP(17);
#pragma unroll
      for (int i = 0; i < 7; ++i) {
        const int s = w + 8 * i;
        if (s < 54) {
          const LAS char* ap = L + L_FW + s * 1024 + lane * 8;
          const bf16x8 af = cat8(trd(ap), trd(ap + 512));
          a4 = MFMA16(af, __builtin_bit_cast(bf16x8, (u32x4){g0[i][0], g0[i][2], g1[i][0], g1[i][2]}), a4);
        }
      }
    }
    *(LAS f32x4*)(L + L_FRED + (w * 64 + lane) * 16) = a4;
    WAITL0(); BARRIER();
    if (w == 0) {
      f32x4 y = {0, 0, 0, 0};
#pragma unroll
      for (int q = 0; q < 8; ++q) y += *(const LAS f32x4*)(L + L_FRED + (q * 64 + lane) * 16);
      const unsigned rk = selk >> 16, rbits = (rk & 0x8000u) ? (rk ^ 0x8000u) : (rk ^ 0xffffu);
      const float rv = bf2f(rbits), sig = rbf(1.f / (1.f + __expf(-rv)));
      const float m = __uint_as_float(selm & 0xffff0000u), iv = 1.f / __uint_as_float(selm << 16);
      unsigned r2[2];
#pragma unroll
      for (int e = 0; e < 2; ++e) {
        const unsigned d2 = pk2(bf2f(hv[e]) - m, bfhi(hv[e]) - m);
        const unsigned n2 = pk2(bf2f(d2) * iv, bfhi(d2) * iv);
        const unsigned y2 = pk2(rbf(y[2 * e]) * sig, rbf(y[2 * e + 1]) * sig);
        const unsigned t2 = pk2(bf2f(n2) + bf2f(y2), bfhi(n2) + bfhi(y2));
        r2[e] = pk2(bf2f(hv[e]) + bf2f(t2), bfhi(hv[e]) + bfhi(t2));
      }
      if (l15 < 8) *(u32x2*)(p.out + (size_t)row * D + 16 * dn + 4 * fg) = (u32x2){r2[0], r2[1]};
      STAMP(18);
    }
  } else {
    out_rows();
  }
  if (w == 0) STAMP(19);
  STAMP_FLUSH();
}

extern "C" void kernel_launch(void* const* d_in, const int* in_sizes, int n_in, void* d_out, int out_size, void* d_ws, size_t ws_size, hipStream_t stream) {
  Params p;
  p.x = (const u16*)d_in[0]; p.wqkv = (const u16*)d_in[1]; p.wo = (const u16*)d_in[2]; p.wr = (const u16*)d_in[4];
  p.wg = (const u16*)d_in[5]; p.wu = (const u16*)d_in[6]; p.wd = (const u16*)d_in[7]; p.out = (u16*)d_out; p.ws = (unsigned char*)d_ws;
  fused_kernel<<<256, 512, 0, stream>>>(p);
}
```

```cpp
#include <hip/hip_runtime.h>
#include <stdint.h>

typedef unsigned short u16;
typedef short bf16x8 __attribute__((ext_vector_type(8)));
typedef short bf16x4 __attribute__((ext_vector_type(4)));
typedef __bf16 bf2 __attribute__((ext_vector_type(2)));
typedef float f32x16 __attribute__((ext_vector_type(16)));
typedef float f32x4 __attribute__((ext_vector_type(4)));
typedef float f32x2 __attribute__((ext_vector_type(2)));
typedef unsigned u32x4 __attribute__((ext_vector_type(4)));
typedef unsigned u32x2 __attribute__((ext_vector_type(2)));
typedef unsigned long long u64;
#define LAS __attribute__((address_space(3)))
#define SBAR() __builtin_amdgcn_sched_barrier(0)
#define BARRIER() asm volatile("s_barrier" ::: "memory")
#define WAITL0() asm volatile("s_waitcnt lgkmcnt(0)" ::: "memory")
#define WAITV(n) asm volatile("s_waitcnt vmcnt(" #n ")" ::: "memory")

constexpr int S = 2048, D = 640, F = 1728;
constexpr int SC1 = 16;

constexpr unsigned WS_EPOCH = 0;
constexpr unsigned WS_FLAGA = 4096;
constexpr unsigned WS_FLAGO = WS_FLAGA + 32768;
constexpr unsigned WS_CAND = WS_FLAGO + 16384;
constexpr unsigned WS_ACT = WS_CAND + 4096;
constexpr unsigned WS_STATS = 131072;
constexpr unsigned WS_QKV = 524288;
constexpr unsigned WS_O = WS_QKV + 480 * 16384;
constexpr unsigned WS_H = WS_O + S * D * 2;
#define STAMP_OFF (200u << 20)
#define NSTAMP 32

constexpr int L_SLOT = 32768, L_W = 16384;
constexpr int PL = 4128;
constexpr int L_K = 0, L_V = 5 * PL, L_ONE = 10 * PL;
constexpr int L_WO = 43008;
constexpr int L_RED = 0;
constexpr int L_FW = 43008;
constexpr int L_XS = 0;
constexpr int L_FRED = 12288;
constexpr int L_MISC = 147456;
constexpr int L_RS = L_MISC;
constexpr int L_CS = L_MISC + 1024;
constexpr int L_EP = L_MISC + 1536;
constexpr int L_KEY = L_MISC + 2048;
constexpr int L_MS = L_MISC + 2304;
constexpr int L_SEL = L_MISC + 2560;
constexpr int L_STAMP = L_MISC + 4096;
constexpr int LDS_BYTES = 163840;

struct Params { const u16* x; const u16* wqkv; const u16* wo; const u16* wr; const u16* wg; const u16* wu; const u16* wd; u16* out; unsigned char* ws; };

__device__ __forceinline__ float bf2f(unsigned v) { return __uint_as_float(v << 16); }
__device__ __forceinline__ float bfhi(unsigned v) { return __uint_as_float(v & 0xffff0000u); }
__device__ __forceinline__ unsigned pk2(float a, float b) { f32x2 f = {a, b}; return __builtin_bit_cast(unsigned, __builtin_convertvector(f, bf2)); }
__device__ __forceinline__ float rbf(float f) { return __uint_as_float(pk2(f, 0.f) << 16); }
__device__ __forceinline__ float dot2(unsigned a, unsigned b, float c) { return __builtin_amdgcn_fdot2_f32_bf16(__builtin_bit_cast(bf2, a), __builtin_bit_cast(bf2, b), c, false); }
__device__ __forceinline__ float swap_add(float v) { auto r = __builtin_amdgcn_permlane32_swap(__float_as_uint(v), __float_as_uint(v), false, false); return __uint_as_float(r[0]) + __uint_as_float(r[1]); }
__device__ __forceinline__ float swap16_add(float v) { auto r = __builtin_amdgcn_permlane16_swap(__float_as_uint(v), __float_as_uint(v), false, false); return __uint_as_float(r[0]) + __uint_as_float(r[1]); }
__device__ __forceinline__ void glds16(unsigned voff, const void* sbase, unsigned lds) {
  asm volatile("s_mov_b32 m0, %2\n\ts_nop 0\n\tglobal_load_lds_dwordx4 %0, %1" ::"v"(voff), "s"(sbase), "s"(lds) : "memory");
}
__device__ __forceinline__ void glds16c(unsigned voff, const void* sbase, unsigned lds) {
  asm volatile("s_mov_b32 m0, %2\n\ts_nop 0\n\tglobal_load_lds_dwordx4 %0, %1 sc1" ::"v"(voff), "s"(sbase), "s"(lds) : "memory");
}
__device__ __forceinline__ bf16x4 trd(const LAS char* p) { return __builtin_amdgcn_ds_read_tr16_b64_v4i16((LAS bf16x4*)p); }
__device__ __forceinline__ bf16x8 cat8(bf16x4 a, bf16x4 b) { return (bf16x8){a[0], a[1], a[2], a[3], b[0], b[1], b[2], b[3]}; }
#define MFMA32(a, b, c) __builtin_amdgcn_mfma_f32_32x32x16_bf16(a, b, c, 0, 0, 0)
#define MFMA32K8(a, b, c) __builtin_amdgcn_mfma_f32_32x32x8bf16_1k(a, b, c, 0, 0, 0)
#define MFMA16(a, b, c) __builtin_amdgcn_mfma_f32_16x16x32_bf16(a, b, c, 0, 0, 0)
template <int CTRL, int RMASK> __device__ __forceinline__ unsigned dppmax(unsigned v) { return max(v, (unsigned)__builtin_amdgcn_update_dpp(0, (int)v, CTRL, RMASK, 0xf, false)); }
__device__ __forceinline__ unsigned wave_umax(unsigned v) {
  v = dppmax<0x111, 0xf>(v); v = dppmax<0x112, 0xf>(v); v = dppmax<0x114, 0xf>(v); v = dppmax<0x118, 0xf>(v);
  v = dppmax<0x142, 0xa>(v); v = dppmax<0x143, 0xc>(v);
  return (unsigned)__builtin_amdgcn_readlane((int)v, 63);
}

template <int CTRL> __device__ __forceinline__ float dppadd(float v) { return v + __int_as_float(__builtin_amdgcn_update_dpp(0, __float_as_int(v), CTRL, 0xf, 0xf, false)); }
__device__ __forceinline__ float wave_fsum(float v) { v = dppadd<0x128>(v); v = dppadd<0x124>(v); v = dppadd<0x122>(v); v = dppadd<0x121>(v); return swap_add(swap16_add(v)); }

#define STAMP(k) do {} while (0)
#define STAMP_INIT() do {} while (0)
#define STAMP_FLUSH() do {} while (0)

constexpr unsigned SPIN_CAP = 1u << 16;

__global__ __launch_bounds__(512, 2) void fused_kernel(Params p) {
  __shared__ __attribute__((aligned(16))) char smem[LDS_BYTES];
  LAS char* L = (LAS char*)smem;
  const unsigned lds0 = (unsigned)(uintptr_t)smem;
  const int tid = threadIdx.x, lane = tid & 63;
  const int w = __builtin_amdgcn_readfirstlane(tid >> 6);
  const int b = blockIdx.x, xcd = b & 7, bi = b >> 3;
  const int l31 = lane & 31, hh = lane >> 5, l15 = lane & 15, fg = lane >> 4;
  const __amdgpu_buffer_rsrc_t rws = __builtin_amdgcn_make_buffer_rsrc(p.ws, 0, 0x7fffffff, 0x00020000);
  const int aP = 4 * (xcd >> 1) + (bi >> 3), aj = 8 * (xcd & 1) + (bi & 7);
  const int hd = bi, pp = xcd;
  const int R = 4 * xcd + (bi >> 3), cc = bi & 7;
  const int gs = xcd * 14 + bi;
  const bool is_gu = bi < 14 && gs < 108;
  const int dn = xcd * 5 + (bi - 14);
  const bool is_dn = bi >= 14 && bi < 19;
  const u64 tgs_ = __builtin_amdgcn_s_memrealtime();
  STAMP_INIT();

  if (aj < 15 && w >= 4) {
    const int lw = w - 4;
    const unsigned xsw = (unsigned)(4 * (lw & 1) + (lane >> 4));
    const unsigned voff_x = (unsigned)(lane >> 3) * 1280u + (((unsigned)(lane & 7) ^ xsw) << 4);
    const unsigned ch = (unsigned)(lane & 15) ^ (((unsigned)(lane >> 4) << 2) | (unsigned)lw);
    const unsigned voff_w = (unsigned)(lane >> 4) * 3840u + ch * 16u;
    const char* xb = (const char*)p.x + (size_t)(128 * aP + 8 * lw) * 1280;
    const char* wb = (const char*)p.wqkv + (size_t)(4 * lw) * 3840 + 256 * aj;
    const unsigned lbase = lds0 + lw * 1024;
    const int rot = ((bi & 7) * 5 / 4 + (bi >> 3) * 5 / 2) % 10;
    auto issue = [&](int s) {
      const unsigned sl = lbase + (s & 3) * L_SLOT;
      int ss = s + rot; if (ss >= 10) ss -= 10;
#pragma unroll
      for (int i = 0; i < 4; ++i) {
        glds16(voff_x, xb + (size_t)i * 32 * 1280 + ss * 128, sl + i * 4096);
        glds16(voff_w, wb + ((size_t)ss * 64 + i * 16) * 3840, sl + L_W + i * 4096);
      }
    };
    issue(0); issue(1); issue(2); issue(3);
    WAITV(24); BARRIER();
    if (w == 4) STAMP(1);
    WAITV(16); BARRIER();
#pragma unroll 1
    for (int s = 1; s <= 6; ++s) { issue(s + 3); WAITV(16); BARRIER(); }
    WAITV(8); BARRIER();
    WAITV(0); BARRIER();
    BARRIER();
    BARRIER();
  } else if (w < 4) {
    if (w == 0) {
      const u32x2 e = __builtin_amdgcn_raw_buffer_load_b64(rws, WS_EPOCH, 0, SC1);
      const u64 t = (((u64)e[1] << 32) | e[0]) + 1;
      if (lane == 0) *(LAS u32x2*)(L + L_EP) = (u32x2){(unsigned)t, (unsigned)(t >> 32)};
      __builtin_amdgcn_raw_buffer_store_b128((u32x4){0, 0, 0, 0}, rws, WS_STATS + (R * 8 + cc) * 1024 + lane * 16, 0, SC1);
      if (is_gu && lane < 32) __builtin_amdgcn_raw_buffer_store_b128((u32x4){0, 0, 0, 0}, rws, WS_ACT + (((gs >> 1) * 4 + 2 * (gs & 1) + (lane >> 4)) * 8 + (lane & 7)) * 32 + ((lane >> 3) & 1) * 16, 0, SC1);
    }
    if (aj < 15) {
      const int wm = w >> 1, wn = w & 1;
      unsigned xa[4], wa[4];
#pragma unroll
      for (int ks = 0; ks < 4; ++ks) xa[ks] = (unsigned)(64 * wm + l31) * 128u + ((((unsigned)(2 * ks + hh)) ^ (((unsigned)l31 >> 1) & 7u)) << 4);
      const unsigned xo0 = wn * 4096, xo1 = 4096 - xo0;
      {
        const unsigned blk = (lane >> 4) & 1, q = (lane & 15) >> 2, pq = lane & 3;
#pragma unroll
        for (int j = 0; j < 2; ++j)
#pragma unroll
          for (int t = 0; t < 2; ++t) {
            const unsigned jj = j ^ wm;
            const unsigned row = 8 * hh + 4 * t + q, chn = 4 * (2 * wn + jj) + 2 * blk + (pq >> 1), m = (q << 2) | (unsigned)(2 * hh + t);
            wa[j * 2 + t] = L_W + 256u * row + 16u * (chn ^ m) + 8u * (pq & 1);
          }
      }
      f32x16 a00 = {}, a01 = {}, a10 = {}, a11 = {};
      float ssum = 0.f, ssq = 0.f, csum = 0.f;
      bf16x8 xf0[2], wf0[2], xf1[2], wf1[2];
      const unsigned ONE2 = 0x3f803f80u;
      unsigned xb0[4], xb1[4], wb[4];
#define SETSLOT(so) do { _Pragma("unroll") for (int k_ = 0; k_ < 4; ++k_) { xb0[k_] = xa[k_] + (so) + xo0; xb1[k_] = xa[k_] + (so) + xo1; wb[k_] = wa[k_] + (so); } } while (0)
#define RD(XF, WF, ks) do { \
        XF[0] = *(const LAS bf16x8*)(L + xb0[ks]); XF[1] = *(const LAS bf16x8*)(L + xb1[ks]); \
        WF[0] = cat8(trd(L + wb[0] + (ks) * 4096), trd(L + wb[1] + (ks) * 4096)); \
        WF[1] = cat8(trd(L + wb[2] + (ks) * 4096), trd(L + wb[3] + (ks) * 4096)); } while (0)
#define MM(XF, WF) do { \
        a00 = MFMA32(WF[0], XF[0], a00); a01 = MFMA32(WF[1], XF[0], a01); \
        a10 = MFMA32(WF[0], XF[1], a10); a11 = MFMA32(WF[1], XF[1], a11); \
        { const u32x4 xs_ = __builtin_bit_cast(u32x4, XF[0]); const u32x4 ws_ = __builtin_bit_cast(u32x4, WF[0]); \
          _Pragma("unroll") for (int e_ = 0; e_ < 4; ++e_) { ssum = dot2(xs_[e_], ONE2, ssum); ssq = dot2(xs_[e_], xs_[e_], ssq); csum = dot2(ws_[e_], ONE2, csum); } } } while (0)
#define PINALL() asm volatile("" : "+v"(a00), "+v"(a01), "+v"(a10), "+v"(a11), "+v"(ssum), "+v"(ssq), "+v"(csum))
#define ILV() do { _Pragma("unroll") for (int g_ = 0; g_ < 4; ++g_) { \
        __builtin_amdgcn_sched_group_barrier(0x8, 1, 0); __builtin_amdgcn_sched_group_barrier(0x100, 2, 0); __builtin_amdgcn_sched_group_barrier(0x2, 3, 0); } } while (0)
#define STEP(XN, WN, ksn, XC, WC) do { RD(XN, WN, ksn); MM(XC, WC); ILV(); PINALL(); SBAR(); } while (0)
      SETSLOT(0);
      BARRIER();
      RD(xf0, wf0, 0);
      SBAR();
#pragma unroll 1
      for (int s = 0; s < 9; ++s) {
        STEP(xf1, wf1, 1, xf0, wf0);
        STEP(xf0, wf0, 2, xf1, wf1);
        STEP(xf1, wf1, 3, xf0, wf0);
        SETSLOT(((s + 1) & 3) * L_SLOT);
        WAITL0(); BARRIER();
        STEP(xf0, wf0, 0, xf1, wf1);
      }
      STEP(xf1, wf1, 1, xf0, wf0);
      STEP(xf0, wf0, 2, xf1, wf1);
      STEP(xf1, wf1, 3, xf0, wf0);
      MM(xf1, wf1); PINALL();
      if (w == 0) STAMP(2);
      ssum = swap_add(ssum); ssq = swap_add(ssq); csum = swap_add(csum);
      {
        const float mean = ssum * (1.f / D);
        const float var = fmaxf(ssq * (1.f / D) - mean * mean, 0.f);
        const float mb = rbf(mean), vb = rbf(var);
        const float sd = rbf(sqrtf(rbf(vb + 1e-5f)));
        if (lane < 32) {
          *(LAS f32x2*)(L + L_RS + (64 * wm + 32 * wn + l31) * 8) = (f32x2){mb, 1.f / sd};
          *(LAS float*)(L + L_CS + (64 * wn + 32 * wm + l31) * 4) = csum;
        }
      }
      WAITV(0); WAITL0(); BARRIER();
      const float qs = aj < 5 ? 0.22360679774997896f * 1.4426950408889634f : 1.f;
#pragma unroll
      for (int i = 0; i < 2; ++i) {
        const int tok = 64 * wm + 32 * (i ^ wn) + l31;
        const f32x2 rs = *(const LAS f32x2*)(L + L_RS + tok * 8);
        const float sc = rs[1] * qs, nm = -rs[0] * sc;
#pragma unroll
        for (int j = 0; j < 2; ++j) {
          const f32x16& a = i == 0 ? (j == 0 ? a00 : a01) : (j == 0 ? a10 : a11);
          const int f0 = 64 * wn + 32 * (j ^ wm) + 4 * hh;
          const unsigned go = WS_QKV + (unsigned)(32 * aj + (f0 >> 2)) * 16384u + (unsigned)(128 * aP + tok) * 8u;
#pragma unroll
          for (int rq = 0; rq < 4; ++rq) {
            const f32x4 cs = *(const LAS f32x4*)(L + L_CS + (f0 + 8 * rq) * 4);
            const float v0 = fmaf(a[rq * 4 + 0], sc, nm * cs[0]), v1 = fmaf(a[rq * 4 + 1], sc, nm * cs[1]);
            const float v2 = fmaf(a[rq * 4 + 2], sc, nm * cs[2]), v3 = fmaf(a[rq * 4 + 3], sc, nm * cs[3]);
            __builtin_amdgcn_raw_buffer_store_b64((u32x2){pk2(v0, v1), pk2(v2, v3)}, rws, go + rq * 32768, 0, SC1);
          }
        }
      }
      WAITV(0); BARRIER();
    } else {
      WAITV(0);
    }
  }
  if (aj >= 15) { WAITL0(); BARRIER(); }
  const u32x2 tag2 = *(const LAS u32x2*)(L + L_EP);
  const unsigned tag32 = tag2[0] | 1u;
  if (aj < 15 && tid == 0) __builtin_amdgcn_raw_buffer_store_b64(tag2, rws, WS_FLAGA + (aP * 16 + aj) * 128, 0, SC1);
  if (w == 0) STAMP(3);

  f32x16 o = (f32x16){};
  {
    const int t = w;
    if (tid < 4) *(LAS unsigned*)(L + L_ONE + tid * 4) = tid == 0 ? 0x00003f80u : 0u;
    {
      const int sec = lane < 4 ? 0 : (lane < 12 ? 1 : 2);
      const int k = lane < 4 ? lane : (lane < 12 ? lane - 4 : lane - 12);
      const int col = sec * 640 + 20 * hd + ((k & 1) ? 19 : 0);
      int Pn = sec == 0 ? 2 * pp + (k >> 1) : 2 * pp - 2 + (k >> 1);
      const bool need = lane < 20 && Pn >= 0;
      const unsigned fo = WS_FLAGA + (unsigned)((need ? Pn : 0) * 16 + (col >> 7)) * 128;
      unsigned spins = 0;
      for (;;) {
        const u32x2 f = __builtin_amdgcn_raw_buffer_load_b64(rws, fo, 0, SC1);
        if (__all(!need || (f[0] == tag2[0] && f[1] == tag2[1]))) break;
        if (++spins > SPIN_CAP) break;
        __builtin_amdgcn_s_sleep(1);
      }
    }
    asm volatile("" ::: "memory");
    if (w == 0) STAMP(4);
    {
      const char* qb = (const char*)p.ws + WS_QKV;
#pragma unroll
      for (int i = 0; i < 5; ++i) {
        const int pc = w + 8 * i, pl = pc >> 2, qt = pc & 3;
        const int tok0 = 256 * pp - 256 + 128 * qt;
        if (tok0 >= 0) glds16c(lane * 16, qb + (size_t)((pl < 5 ? 160 + 5 * hd + pl : 320 + 5 * hd + pl - 5)) * 16384 + tok0 * 8, lds0 + pl * PL + qt * 1024);
      }
    }
    bf16x8 qf0; bf16x4 qf1;
    {
      const unsigned qo = WS_QKV + (unsigned)(5 * hd) * 16384u + (unsigned)(256 * pp + 32 * t + l31) * 8u;
      const u32x2 a = __builtin_amdgcn_raw_buffer_load_b64(rws, qo + (2 * hh) * 16384, 0, SC1);
      const u32x2 c = __builtin_amdgcn_raw_buffer_load_b64(rws, qo + (2 * hh + 1) * 16384, 0, SC1);
      u32x2 z = __builtin_amdgcn_raw_buffer_load_b64(rws, qo + 4 * 16384, 0, SC1);
      if (hh) z = (u32x2){0, 0};
      qf0 = __builtin_bit_cast(bf16x8, (u32x4){a[0], a[1], c[0], c[1]});
      qf1 = __builtin_bit_cast(bf16x4, z);
    }
    WAITV(0); WAITL0(); BARRIER();
    if (w == 0) STAMP(5);
    {
#pragma unroll 1
      for (int pc = w; pc < 100; pc += 8) {
        const int n = pc * 64 + lane, rho = n / 10, ch = n - rho * 10;
        const int s = rho >> 5, tq = (rho >> 4) & 1, kg = (rho >> 2) & 3, q = rho & 3;
        const int cidx = 4 * s + kg, g2 = cidx / 5, c5 = cidx - 5 * g2;
        const int quad = 10 * g2 + ((tq ? 0x98732 : 0x46510) >> (4 * c5) & 15);
        glds16((unsigned)(4 * quad + q) * 1280u + ch * 16u, (const char*)p.wo + 160 * cc, lds0 + L_WO + pc * 1024);
      }
    }
    const int kad = L_K + l31 * 8;
    float negm;
    {
      const LAS char* kp = L + kad + (t + 8) * 256;
      const u32x2 k0 = *(const LAS u32x2*)(kp + (2 * hh) * PL), k1 = *(const LAS u32x2*)(kp + (2 * hh + 1) * PL), k2 = *(const LAS u32x2*)(kp + 4 * PL);
      const u32x4 q0 = __builtin_bit_cast(u32x4, qf0); const u32x2 q1 = __builtin_bit_cast(u32x2, qf1);
      float d = dot2(q0[0], k0[0], 0.f); d = dot2(q0[1], k0[1], d); d = dot2(q0[2], k1[0], d); d = dot2(q0[3], k1[1], d);
      d = dot2(q1[0], k2[0], d); d = dot2(q1[1], k2[1], d);
      negm = -swap_add(d);
    }
    const int vquad = 4 * ((lane >> 4) & 1) + (lane & 3);
    const int vad = vquad < 5 ? L_V + vquad * PL + (4 * hh + ((lane & 15) >> 2)) * 8 : L_ONE;
    const int vst = vquad < 5 ? 1 : 0;
    auto tile = [&](int kt, const f32x16& cin) {
      const LAS char* kp = L + kad + kt * 256;
      const bf16x4 ka = *(const LAS bf16x4*)(kp + (2 * hh) * PL), kb = *(const LAS bf16x4*)(kp + (2 * hh + 1) * PL), kc = *(const LAS bf16x4*)(kp + 4 * PL);
      const LAS char* vp = L + vad + vst * kt * 256;
      const bf16x8 v0 = cat8(trd(vp), trd(vp + vst * 64)), v1 = cat8(trd(vp + vst * 128), trd(vp + vst * 192));
      f32x16 s = MFMA32(cat8(ka, kb), qf0, cin);
      s = MFMA32K8(kc, qf1, s);
      u32x4 p0, p1;
#pragma unroll
      for (int e = 0; e < 4; ++e) {
        p0[e] = pk2(__builtin_amdgcn_exp2f(s[2 * e]), __builtin_amdgcn_exp2f(s[2 * e + 1]));
        p1[e] = pk2(__builtin_amdgcn_exp2f(s[8 + 2 * e]), __builtin_amdgcn_exp2f(s[8 + 2 * e + 1]));
      }
      o = MFMA32(v0, __builtin_bit_cast(bf16x8, p0), o);
      o = MFMA32(v1, __builtin_bit_cast(bf16x8, p1), o);
    };
    f32x16 cm;
#pragma unroll
    for (int r = 0; r < 16; ++r) { const int kr = (r & 3) + 8 * (r >> 2) + 4 * hh; cm[r] = kr <= l31 ? negm : -INFINITY; }
    tile(t + 8, cm);
#pragma unroll
    for (int r = 0; r < 16; ++r) cm[r] = negm;
    const int lo = pp >= 1 ? t + 1 : 8;
#pragma unroll 1
    for (int kt = t + 7; kt >= lo; --kt) tile(kt, cm);
    if (pp >= 1) {
#pragma unroll
      for (int r = 0; r < 16; ++r) { const int kr = (r & 3) + 8 * (r >> 2) + 4 * hh; cm[r] = kr > l31 ? negm : -INFINITY; }
      tile(t, cm);
    }
    if (w == 0) STAMP(6);
    auto r = __builtin_amdgcn_permlane32_swap(__float_as_uint(o[8]), __float_as_uint(o[8]), false, false);
    const float inv = 1.f / __uint_as_float(r[1]);
    u32x4 c0;
    c0[0] = pk2(o[0] * inv, o[1] * inv); c0[1] = pk2(o[2] * inv, o[3] * inv); c0[2] = pk2(o[4] * inv, o[5] * inv); c0[3] = pk2(o[6] * inv, o[7] * inv);
    const u32x2 c1 = (u32x2){pk2(o[8] * inv, o[9] * inv), pk2(o[10] * inv, o[11] * inv)};
    const int tt = pp * 16 + 2 * t + (l31 >> 4);
    const unsigned base = WS_O + (unsigned)tt * 80 * 256 + (l31 & 15) * 16 + (unsigned)(5 * (hd >> 1)) * 256;
    __builtin_amdgcn_raw_buffer_store_b128(c0, rws, base + (2 * (hd & 1) + hh) * 256, 0, SC1);
    if (hh == 0) __builtin_amdgcn_raw_buffer_store_b64(c1, rws, base + 4 * 256 + (hd & 1) * 8, 0, SC1);
    WAITV(0);
    if (lane == 0) __builtin_amdgcn_raw_buffer_store_b64(tag2, rws, WS_FLAGO + (((pp * 8 + t) * 2 + (hd >> 4)) * 16 + (hd & 15)) * 8, 0, SC1);
  }
  if (w == 0) STAMP(7);
  BARRIER();

  const int nt = w & 3, kh = w >> 2;
  const int tok = 64 * R + 16 * nt + l15;
  u32x2 xr[5], wrv[5];
  float swr = 0.f;
  if (kh == 0) {
#pragma unroll
    for (int ft = 0; ft < 5; ++ft) {
      xr[ft] = *(const u32x2*)(p.x + (size_t)tok * D + 80 * cc + 16 * ft + 4 * fg);
      wrv[ft] = *(const u32x2*)(p.wr + 80 * cc + 16 * ft + 4 * fg);
    }
    const unsigned* wr32 = (const unsigned*)p.wr;
#pragma unroll
    for (int i = 0; i < 5; ++i) swr = dot2(wr32[lane + 64 * i], 0x3f803f80u, swr);
    swr = wave_fsum(swr);
  }
  f32x4 acc[5];
#pragma unroll
  for (int ft = 0; ft < 5; ++ft) acc[ft] = (f32x4){0, 0, 0, 0};
  {
    const int tg = 4 * R + nt;
    const unsigned fo = WS_FLAGO + ((((tg >> 4) * 8 + ((tg >> 1) & 7)) * 2 + kh) * 16 + l15) * 8;
    unsigned spins = 0;
    for (;;) {
      const u32x2 f = __builtin_amdgcn_raw_buffer_load_b64(rws, fo, 0, SC1);
      if (__all(f[0] == tag2[0] && f[1] == tag2[1])) break;
      if (++spins > SPIN_CAP) break;
      __builtin_amdgcn_s_sleep(1);
    }
    asm volatile("" ::: "memory");
    if (w == 0) STAMP(8);
    u32x4 of[10];
#pragma unroll
    for (int s = 0; s < 10; ++s) of[s] = __builtin_amdgcn_raw_buffer_load_b128(rws, WS_O + (unsigned)(tg * 80 + 4 * (10 * kh + s)) * 256 + lane * 16, 0, SC1);
    const LAS char* wp = L + L_WO + (unsigned)(kh * 10 * 32 + fg * 4 + ((lane & 15) >> 2)) * 160 + (lane & 3) * 8;
#pragma unroll
    for (int s = 0; s < 10; ++s) {
#pragma unroll
      for (int ft = 0; ft < 5; ++ft) {
        const bf16x8 a = cat8(trd(wp + (s * 32) * 160 + ft * 32), trd(wp + (s * 32 + 16) * 160 + ft * 32));
        acc[ft] = MFMA16(a, __builtin_bit_cast(bf16x8, of[s]), acc[ft]);
      }
    }
  }
  if (w == 0) STAMP(9);
  if (kh == 1) {
#pragma unroll
    for (int ft = 0; ft < 5; ++ft) *(LAS f32x4*)(L + L_RED + ((nt * 5 + ft) * 64 + lane) * 16) = acc[ft];
  }
  WAITL0(); BARRIER();
  unsigned hp[10];
  float mb = 0.f, inv_sd = 0.f;
  if (kh == 1) {
    const int rl = lane >> 1, grow = 8 * ((rl >> 2) & 3) + 4 * (rl >> 4) + (rl & 3);
    if (is_gu) {
      const unsigned vo = (unsigned)grow * (F * 2) + (lane & 1) * 16;
#pragma unroll 1
      for (int pc = nt; pc < 40; pc += 4) { const int m = pc >= 20, s = pc - 20 * m; glds16(vo, (const char*)(m ? p.wu : p.wg) + (size_t)s * 32 * F * 2 + gs * 32, lds0 + L_FW + pc * 1024); }
    } else if (is_dn) {
      const unsigned vo = (unsigned)grow * (D * 2) + (lane & 1) * 16;
#pragma unroll 1
      for (int pc = nt; pc < 54; pc += 4) glds16(vo, (const char*)p.wd + (size_t)pc * 32 * D * 2 + dn * 32, lds0 + L_FW + pc * 1024);
    }
  } else {
    float s1 = 0.f, s2 = 0.f, s3 = 0.f;
#pragma unroll
    for (int ft = 0; ft < 5; ++ft) {
      const f32x4 o2 = *(const LAS f32x4*)(L + L_RED + ((nt * 5 + ft) * 64 + lane) * 16);
      const unsigned a01 = pk2(acc[ft][0] + o2[0], acc[ft][1] + o2[1]), a23 = pk2(acc[ft][2] + o2[2], acc[ft][3] + o2[3]);
      const unsigned h01 = pk2(bf2f(xr[ft][0]) + bf2f(a01), bfhi(xr[ft][0]) + bfhi(a01)), h23 = pk2(bf2f(xr[ft][1]) + bf2f(a23), bfhi(xr[ft][1]) + bfhi(a23));
      hp[2 * ft] = h01; hp[2 * ft + 1] = h23;
      __builtin_amdgcn_raw_buffer_store_b64((u32x2){h01, h23}, rws, WS_H + (unsigned)tok * 1280 + (80 * cc + 16 * ft + 4 * fg) * 2, 0, SC1);
      s1 = dot2(h01, 0x3f803f80u, s1); s1 = dot2(h23, 0x3f803f80u, s1);
      s2 = dot2(h01, h01, s2); s2 = dot2(h23, h23, s2);
      s3 = dot2(h01, wrv[ft][0], s3); s3 = dot2(h23, wrv[ft][1], s3);
    }
    s1 = swap_add(swap16_add(s1)); s2 = swap_add(swap16_add(s2)); s3 = swap_add(swap16_add(s3));
    if (lane < 16) __builtin_amdgcn_raw_buffer_store_b128((u32x4){__float_as_uint(s1), __float_as_uint(s2), __float_as_uint(s3), tag32}, rws, WS_STATS + ((R * 8 + cc) * 64 + 16 * nt + l15) * 16, 0, SC1);
    if (w == 0) STAMP(10);
    float t1, t2, t3;
    {
      const unsigned so = WS_STATS + ((R * 8 + fg) * 64 + 16 * nt + l15) * 16;
      unsigned spins = 0;
      for (;;) {
        const u32x4 ga = __builtin_amdgcn_raw_buffer_load_b128(rws, so, 0, SC1), gb = __builtin_amdgcn_raw_buffer_load_b128(rws, so + 4 * 1024, 0, SC1);
        t1 = __uint_as_float(ga[0]) + __uint_as_float(gb[0]); t2 = __uint_as_float(ga[1]) + __uint_as_float(gb[1]); t3 = __uint_as_float(ga[2]) + __uint_as_float(gb[2]);
        if (__all(ga[3] == tag32 && gb[3] == tag32)) break;
        if (++spins > SPIN_CAP) break;
        __builtin_amdgcn_s_sleep(1);
      }
    }
    t1 = swap_add(swap16_add(t1)); t2 = swap_add(swap16_add(t2)); t3 = swap_add(swap16_add(t3));
    const float mean = t1 * (1.f / D);
    const float var = fmaxf(t2 * (1.f / D) - mean * mean, 0.f);
    mb = rbf(mean);
    const float sd = rbf(sqrtf(rbf(rbf(var) + 1e-5f)));
    inv_sd = 1.f / sd;
    const unsigned rb = pk2((t3 - mb * swr) * inv_sd, 0.f) & 0xffffu;
    const unsigned key = ((rb ^ ((rb & 0x8000u) ? 0xffffu : 0x8000u)) << 16) | (unsigned)(2047 - tok);
    if (lane < 16) {
      *(LAS unsigned*)(L + L_KEY + (16 * nt + l15) * 4) = key;
      *(LAS unsigned*)(L + L_MS + (16 * nt + l15) * 4) = (__float_as_uint(mb) & 0xffff0000u) | (__float_as_uint(sd) >> 16);
    }
    WAITV(0);
    if (w == 0) STAMP(11);
  }
  WAITL0(); BARRIER();
  if (w == 0) {
    unsigned k = *(const LAS unsigned*)(L + L_KEY + lane * 4), m = 0;
    for (int i = 0; i <= cc; ++i) { m = wave_umax(k); if (k == m) k = 0; }
    const unsigned ms = *(const LAS unsigned*)(L + L_MS + ((2047 - (int)(m & 0x7ff)) & 63) * 4);
    if (lane == 0) __builtin_amdgcn_raw_buffer_store_b128((u32x4){m, tag2[0], ms, tag2[1]}, rws, WS_CAND + (R * 8 + cc) * 16, 0, SC1);
    STAMP(12);
    unsigned k0, k1, k2, k3, m0, m1, m2, m3;
    unsigned spins = 0;
    for (;;) {
      const u32x4 a = __builtin_amdgcn_raw_buffer_load_b128(rws, WS_CAND + lane * 16, 0, SC1), bb = __builtin_amdgcn_raw_buffer_load_b128(rws, WS_CAND + 1024 + lane * 16, 0, SC1);
      const u32x4 c = __builtin_amdgcn_raw_buffer_load_b128(rws, WS_CAND + 2048 + lane * 16, 0, SC1), d = __builtin_amdgcn_raw_buffer_load_b128(rws, WS_CAND + 3072 + lane * 16, 0, SC1);
      k0 = a[0]; k1 = bb[0]; k2 = c[0]; k3 = d[0]; m0 = a[2]; m1 = bb[2]; m2 = c[2]; m3 = d[2];
      const bool ok = a[1] == tag2[0] && a[3] == tag2[1] && bb[1] == tag2[0] && bb[3] == tag2[1] && c[1] == tag2[0] && c[3] == tag2[1] && d[1] == tag2[0] && d[3] == tag2[1];
      if (__all(ok)) break;
      if (++spins > SPIN_CAP) break;
      __builtin_amdgcn_s_sleep(1);
    }
    STAMP(13);
#pragma unroll 1
    for (int i = 0; i < 8; ++i) {
      const unsigned lm = max(max(k0, k1), max(k2, k3));
      const unsigned g = wave_umax(lm);
      const unsigned ms2 = k0 == g ? m0 : (k1 == g ? m1 : (k2 == g ? m2 : m3));
      if (lm == g) { *(LAS unsigned*)(L + L_SEL + i * 4) = g; *(LAS unsigned*)(L + L_SEL + 32 + i * 4) = ms2; }
      k0 = k0 == g ? 0 : k0; k1 = k1 == g ? 0 : k1; k2 = k2 == g ? 0 : k2; k3 = k3 == g ? 0 : k3;
    }
    if (b == 0 && lane == 0) __builtin_amdgcn_raw_buffer_store_b64(tag2, rws, WS_EPOCH, 0, SC1);
    STAMP(14);
  }
  if (kh == 1) WAITV(0);
  WAITL0(); BARRIER();

  const unsigned selk = *(const LAS unsigned*)(L + L_SEL + (lane & 7) * 4), selm = *(const LAS unsigned*)(L + L_SEL + 32 + (lane & 7) * 4);
  auto out_rows = [&]() {
    if (kh == 0) {
      bool sel = false;
#pragma unroll
      for (int i = 0; i < 8; ++i) sel |= (2047 - (int)(__builtin_amdgcn_readlane((int)selk, i) & 0x7ff)) == tok;
      if (!sel) {
#pragma unroll
        for (int ft = 0; ft < 5; ++ft) {
          unsigned r2[2];
#pragma unroll
          for (int e = 0; e < 2; ++e) {
            const unsigned hv = hp[2 * ft + e];
            const unsigned d2 = pk2(bf2f(hv) - mb, bfhi(hv) - mb);
            const unsigned n2 = pk2(bf2f(d2) * inv_sd, bfhi(d2) * inv_sd);
            r2[e] = pk2(bf2f(hv) + bf2f(n2), bfhi(hv) + bfhi(n2));
          }
          *(u32x2*)(p.out + (size_t)tok * D + 80 * cc + 16 * ft + 4 * fg) = (u32x2){r2[0], r2[1]};
        }
      }
    }
  };
  if (is_gu) {
    {
      const unsigned kk = *(const LAS unsigned*)(L + L_SEL + w * 4), ms = *(const LAS unsigned*)(L + L_SEL + 32 + w * 4);
      const int row = 2047 - (int)(kk & 0x7ff);
      const float m = __uint_as_float(ms & 0xffff0000u), iv = 1.f / __uint_as_float(ms << 16);
      const u32x4 h0 = __builtin_amdgcn_raw_buffer_load_b128(rws, WS_H + (unsigned)row * 1280 + lane * 16, 0, SC1);
      const u32x4 h1 = __builtin_amdgcn_raw_buffer_load_b128(rws, WS_H + (unsigned)row * 1280 + 1024 + (lane & 15) * 16, 0, SC1);
      u32x4 n0, n1;
#pragma unroll
      for (int e = 0; e < 4; ++e) {
        const unsigned d0 = pk2(bf2f(h0[e]) - m, bfhi(h0[e]) - m), d1 = pk2(bf2f(h1[e]) - m, bfhi(h1[e]) - m);
        n0[e] = pk2(bf2f(d0) * iv, bfhi(d0) * iv); n1[e] = pk2(bf2f(d1) * iv, bfhi(d1) * iv);
      }
      *(LAS u32x4*)(L + L_XS + w * 1296 + lane * 16) = n0;
      if (lane < 16) *(LAS u32x4*)(L + L_XS + w * 1296 + 1024 + lane * 16) = n1;
    }
    WAITL0(); BARRIER();
    if (w == 0) STAMP(15);
    {
      const int m = w & 1, kq = w >> 1;
      f32x4 a4 = {0, 0, 0, 0};
#pragma unroll
      for (int i = 0; i < 5; ++i) {
        const int s = 5 * kq + i;
        const LAS char* ap = L + L_FW + (m * 20 + s) * 1024 + lane * 8;
        const bf16x8 af = cat8(trd(ap), trd(ap + 512));
        const bf16x8 bfv = *(const LAS bf16x8*)(L + L_XS + (lane & 7) * 1296 + s * 64 + fg * 16);
        a4 = MFMA16(af, bfv, a4);
      }
      *(LAS f32x4*)(L + L_FRED + (w * 64 + lane) * 16) = a4;
    }
    WAITL0(); BARRIER();
    if (w == 0) {
      f32x4 g4 = {0, 0, 0, 0}, u4 = {0, 0, 0, 0};
#pragma unroll
      for (int q = 0; q < 4; ++q) { g4 += *(const LAS f32x4*)(L + L_FRED + ((2 * q) * 64 + lane) * 16); u4 += *(const LAS f32x4*)(L + L_FRED + ((2 * q + 1) * 64 + lane) * 16); }
      float a[4];
#pragma unroll
      for (int e = 0; e < 4; ++e) { const float g = rbf(g4[e]), u = rbf(u4[e]); const float sg = rbf(g / (1.f + __expf(-g))); a[e] = sg * u; }
      if (l15 < 8) __builtin_amdgcn_raw_buffer_store_b128((u32x4){pk2(a[0], a[1]), tag32, pk2(a[2], a[3]), tag32}, rws, WS_ACT + (((gs >> 1) * 4 + 2 * (gs & 1) + (fg >> 1)) * 8 + l15) * 32 + (fg & 1) * 16, 0, SC1);
      STAMP(16);
    }
    out_rows();
  } else if (is_dn) {
    out_rows();
    const int row = 2047 - (int)(selk & 0x7ff);
    const u32x2 hv = __builtin_amdgcn_raw_buffer_load_b64(rws, WS_H + (unsigned)row * 1280 + (16 * dn + 4 * fg) * 2, 0, SC1);
    f32x4 a4 = {0, 0, 0, 0};
    {
      u32x4 g0[7], g1[7];
      unsigned spins = 0;
      for (;;) {
        bool ok = true;
#pragma unroll
        for (int i = 0; i < 7; ++i) {
          const int s = min(w + 8 * i, 53);
          const unsigned ao = WS_ACT + ((s * 4 + fg) * 8 + (lane & 7)) * 32;
          g0[i] = __builtin_amdgcn_raw_buffer_load_b128(rws, ao, 0, SC1); g1[i] = __builtin_amdgcn_raw_buffer_load_b128(rws, ao + 16, 0, SC1);
        }
#pragma unroll
        for (int i = 0; i < 7; ++i) ok = ok && g0[i][1] == tag32 && g0[i][3] == tag32 && g1[i][1] == tag32 && g1[i][3] == tag32;
        if (__all(ok)) break;
        if (++spins > SPIN_CAP) break;
        __builtin_amdgcn_s_sleep(1);
      }
      if (w == 0) STAMP(17);
#pragma unroll
      for (int i = 0; i < 7; ++i) {
        const int s = w + 8 * i;
        if (s < 54) {
          const LAS char* ap = L + L_FW + s * 1024 + lane * 8;
          const bf16x8 af = cat8(trd(ap), trd(ap + 512));
          a4 = MFMA16(af, __builtin_bit_cast(bf16x8, (u32x4){g0[i][0], g0[i][2], g1[i][0], g1[i][2]}), a4);
        }
      }
    }
    *(LAS f32x4*)(L + L_FRED + (w * 64 + lane) * 16) = a4;
    WAITL0(); BARRIER();
    if (w == 0) {
      f32x4 y = {0, 0, 0, 0};
#pragma unroll
      for (int q = 0; q < 8; ++q) y += *(const LAS f32x4*)(L + L_FRED + (q * 64 + lane) * 16);
      const unsigned rk = selk >> 16, rbits = (rk & 0x8000u) ? (rk ^ 0x8000u) : (rk ^ 0xffffu);
      const float rv = bf2f(rbits), sig = rbf(1.f / (1.f + __expf(-rv)));
      const float m = __uint_as_float(selm & 0xffff0000u), iv = 1.f / __uint_as_float(selm << 16);
      unsigned r2[2];
#pragma unroll
      for (int e = 0; e < 2; ++e) {
        const unsigned d2 = pk2(bf2f(hv[e]) - m, bfhi(hv[e]) - m);
        const unsigned n2 = pk2(bf2f(d2) * iv, bfhi(d2) * iv);
        const unsigned y2 = pk2(rbf(y[2 * e]) * sig, rbf(y[2 * e + 1]) * sig);
        const unsigned t2 = pk2(bf2f(n2) + bf2f(y2), bfhi(n2) + bfhi(y2));
        r2[e] = pk2(bf2f(hv[e]) + bf2f(t2), bfhi(hv[e]) + bfhi(t2));
      }
      if (l15 < 8) *(u32x2*)(p.out + (size_t)row * D + 16 * dn + 4 * fg) = (u32x2){r2[0], r2[1]};
      STAMP(18);
    }
  } else {
    out_rows();
  }
  if (w == 0) STAMP(19);
  STAMP_FLUSH();
  __syncthreads();
  if (tid == 0) { ((u64*)(p.ws + (100u << 20)))[8 + b] = tgs_; ((u64*)(p.ws + (100u << 20)))[512 + b] = __builtin_amdgcn_s_memrealtime(); }
}

constexpr unsigned WS_TS = 100u << 20;
__global__ void ka_kernel(unsigned char* ws) { if (threadIdx.x == 0) ((unsigned long long*)(ws + WS_TS))[0] = __builtin_amdgcn_s_memrealtime(); }
template <int WHICH>
__global__ void kb_kernel(unsigned char* ws, int amp) {
  const unsigned long long now = __builtin_amdgcn_s_memrealtime();
  const unsigned long long* ts = (const unsigned long long*)(ws + WS_TS);
  unsigned long long smin = ~0ull, smax = 0, emax = 0;
  for (int i = threadIdx.x; i < 256; i += 64) { smin = min(smin, ts[8 + i]); smax = max(smax, ts[8 + i]); emax = max(emax, ts[512 + i]); }
  for (int m = 32; m >= 1; m >>= 1) { smin = min(smin, (unsigned long long)__shfl_xor((long long)smin, m)); smax = max(smax, (unsigned long long)__shfl_xor((long long)smax, m)); emax = max(emax, (unsigned long long)__shfl_xor((long long)emax, m)); }
  unsigned long long d = WHICH == 0 ? smin - ts[0] : (WHICH == 1 ? now - emax : (WHICH == 2 ? emax - smin : smax - smin));
  const unsigned long long until = __builtin_amdgcn_s_memrealtime() + d * amp;
  while (__builtin_amdgcn_s_memrealtime() < until) __builtin_amdgcn_s_sleep(1);
}
extern "C" void kernel_launch(void* const* d_in, const int* in_sizes, int n_in, void* d_out, int out_size, void* d_ws, size_t ws_size, hipStream_t stream) {
  Params p;
  p.x = (const u16*)d_in[0]; p.wqkv = (const u16*)d_in[1]; p.wo = (const u16*)d_in[2]; p.wr = (const u16*)d_in[4];
  p.wg = (const u16*)d_in[5]; p.wu = (const u16*)d_in[6]; p.wd = (const u16*)d_in[7]; p.out = (u16*)d_out; p.ws = (unsigned char*)d_ws;
  ka_kernel<<<1, 64, 0, stream>>>(p.ws);
  fused_kernel<<<256, 512, 0, stream>>>(p);
  kb_kernel<0><<<1, 64, 0, stream>>>(p.ws, 0);
}
```

```cpp
#include <hip/hip_runtime.h>
#include <stdint.h>

typedef unsigned short u16;
typedef short bf16x8 __attribute__((ext_vector_type(8)));
typedef short bf16x4 __attribute__((ext_vector_type(4)));
typedef __bf16 bf2 __attribute__((ext_vector_type(2)));
typedef float f32x16 __attribute__((ext_vector_type(16)));
typedef float f32x4 __attribute__((ext_vector_type(4)));
typedef float f32x2 __attribute__((ext_vector_type(2)));
typedef unsigned u32x4 __attribute__((ext_vector_type(4)));
typedef unsigned u32x2 __attribute__((ext_vector_type(2)));
typedef unsigned long long u64;
#define LAS __attribute__((address_space(3)))
#define SBAR() __builtin_amdgcn_sched_barrier(0)
#define BARRIER() asm volatile("s_barrier" ::: "memory")
#define WAITL0() asm volatile("s_waitcnt lgkmcnt(0)" ::: "memory")
#define WAITV(n) asm volatile("s_waitcnt vmcnt(" #n ")" ::: "memory")

constexpr int S = 2048, D = 640, F = 1728;
constexpr int SC1 = 16;

constexpr unsigned WS_EPOCH = 0;
constexpr unsigned WS_FLAGA = 4096;
constexpr unsigned WS_FLAGO = WS_FLAGA + 32768;
constexpr unsigned WS_CAND = WS_FLAGO + 16384;
constexpr unsigned WS_ACT = WS_CAND + 4096;
constexpr unsigned WS_STATS = 131072;
constexpr unsigned WS_QKV = 524288;
constexpr unsigned WS_O = WS_QKV + 480 * 16384;
constexpr unsigned WS_H = WS_O + S * D * 2;
#define STAMP_OFF (200u << 20)
#define NSTAMP 32

constexpr int L_SLOT = 32768, L_W = 16384;
constexpr int PL = 4128;
constexpr int L_K = 0, L_V = 5 * PL, L_ONE = 10 * PL;
constexpr int L_WO = 43008;
constexpr int L_RED = 0;
constexpr int L_FW = 43008;
constexpr int L_XS = 0;
constexpr int L_FRED = 12288;
constexpr int L_MISC = 147456;
constexpr int L_RS = L_MISC;
constexpr int L_CS = L_MISC + 1024;
constexpr int L_EP = L_MISC + 1536;
constexpr int L_KEY = L_MISC + 2048;
constexpr int L_MS = L_MISC + 2304;
constexpr int L_SEL = L_MISC + 2560;
constexpr int L_STAMP = L_MISC + 4096;
constexpr int LDS_BYTES = 163840;

struct Params { const u16* x; const u16* wqkv; const u16* wo; const u16* wr; const u16* wg; const u16* wu; const u16* wd; u16* out; unsigned char* ws; };

__device__ __forceinline__ float bf2f(unsigned v) { return __uint_as_float(v << 16); }
__device__ __forceinline__ float bfhi(unsigned v) { return __uint_as_float(v & 0xffff0000u); }
__device__ __forceinline__ unsigned pk2(float a, float b) { f32x2 f = {a, b}; return __builtin_bit_cast(unsigned, __builtin_convertvector(f, bf2)); }
__device__ __forceinline__ float rbf(float f) { return __uint_as_float(pk2(f, 0.f) << 16); }
__device__ __forceinline__ float dot2(unsigned a, unsigned b, float c) { return __builtin_amdgcn_fdot2_f32_bf16(__builtin_bit_cast(bf2, a), __builtin_bit_cast(bf2, b), c, false); }
__device__ __forceinline__ float swap_add(float v) { auto r = __builtin_amdgcn_permlane32_swap(__float_as_uint(v), __float_as_uint(v), false, false); return __uint_as_float(r[0]) + __uint_as_float(r[1]); }
__device__ __forceinline__ float swap16_add(float v) { auto r = __builtin_amdgcn_permlane16_swap(__float_as_uint(v), __float_as_uint(v), false, false); return __uint_as_float(r[0]) + __uint_as_float(r[1]); }
__device__ __forceinline__ void glds16(unsigned voff, const void* sbase, unsigned lds) {
  asm volatile("s_mov_b32 m0, %2\n\ts_nop 0\n\tglobal_load_lds_dwordx4 %0, %1" ::"v"(voff), "s"(sbase), "s"(lds) : "memory");
}
__device__ __forceinline__ void glds16c(unsigned voff, const void* sbase, unsigned lds) {
  asm volatile("s_mov_b32 m0, %2\n\ts_nop 0\n\tglobal_load_lds_dwordx4 %0, %1 sc1" ::"v"(voff), "s"(sbase), "s"(lds) : "memory");
}
__device__ __forceinline__ bf16x4 trd(const LAS char* p) { return __builtin_amdgcn_ds_read_tr16_b64_v4i16((LAS bf16x4*)p); }
__device__ __forceinline__ bf16x8 cat8(bf16x4 a, bf16x4 b) { return (bf16x8){a[0], a[1], a[2], a[3], b[0], b[1], b[2], b[3]}; }
#define MFMA32(a, b, c) __builtin_amdgcn_mfma_f32_32x32x16_bf16(a, b, c, 0, 0, 0)
#define MFMA32K8(a, b, c) __builtin_amdgcn_mfma_f32_32x32x8bf16_1k(a, b, c, 0, 0, 0)
#define MFMA16(a, b, c) __builtin_amdgcn_mfma_f32_16x16x32_bf16(a, b, c, 0, 0, 0)
template <int CTRL, int RMASK> __device__ __forceinline__ unsigned dppmax(unsigned v) { return max(v, (unsigned)__builtin_amdgcn_update_dpp(0, (int)v, CTRL, RMASK, 0xf, false)); }
__device__ __forceinline__ unsigned wave_umax(unsigned v) {
  v = dppmax<0x111, 0xf>(v); v = dppmax<0x112, 0xf>(v); v = dppmax<0x114, 0xf>(v); v = dppmax<0x118, 0xf>(v);
  v = dppmax<0x142, 0xa>(v); v = dppmax<0x143, 0xc>(v);
  return (unsigned)__builtin_amdgcn_readlane((int)v, 63);
}

template <int CTRL> __device__ __forceinline__ float dppadd(float v) { return v + __int_as_float(__builtin_amdgcn_update_dpp(0, __float_as_int(v), CTRL, 0xf, 0xf, false)); }
__device__ __forceinline__ float wave_fsum(float v) { v = dppadd<0x128>(v); v = dppadd<0x124>(v); v = dppadd<0x122>(v); v = dppadd<0x121>(v); return swap_add(swap16_add(v)); }

#define STAMP(k) do {} while (0)
#define STAMP_INIT() do {} while (0)
#define STAMP_FLUSH() do {} while (0)

constexpr unsigned SPIN_CAP = 1u << 16;

__global__ __launch_bounds__(512, 2) void fused_kernel(Params p) {
  __shared__ __attribute__((aligned(16))) char smem[LDS_BYTES];
  LAS char* L = (LAS char*)smem;
  const unsigned lds0 = (unsigned)(uintptr_t)smem;
  const int tid = threadIdx.x, lane = tid & 63;
  const int w = __builtin_amdgcn_readfirstlane(tid >> 6);
  const int b = blockIdx.x, xcd = b & 7, bi = b >> 3;
  const int l31 = lane & 31, hh = lane >> 5, l15 = lane & 15, fg = lane >> 4;
  const __amdgpu_buffer_rsrc_t rws = __builtin_amdgcn_make_buffer_rsrc(p.ws, 0, 0x7fffffff, 0x00020000);
  const int aP = 4 * (xcd >> 1) + (bi >> 3), aj = 8 * (xcd & 1) + (bi & 7);
  const int hd = bi, pp = xcd;
  const int R = 4 * xcd + (bi >> 3), cc = bi & 7;
  const int gs = xcd * 14 + bi;
  const bool is_gu = bi < 14 && gs < 108;
  const int dn = xcd * 5 + (bi - 14);
  const bool is_dn = bi >= 14 && bi < 19;
  STAMP_INIT();

  if (aj < 15 && w >= 4) {
    const int lw = w - 4;
    const unsigned xsw = (unsigned)(4 * (lw & 1) + (lane >> 4));
    const unsigned voff_x = (unsigned)(lane >> 3) * 1280u + (((unsigned)(lane & 7) ^ xsw) << 4);
    const unsigned ch = (unsigned)(lane & 15) ^ (((unsigned)(lane >> 4) << 2) | (unsigned)lw);
    const unsigned voff_w = (unsigned)(lane >> 4) * 3840u + ch * 16u;
    const char* xb = (const char*)p.x + (size_t)(128 * aP + 8 * lw) * 1280;
    const char* wb = (const char*)p.wqkv + (size_t)(4 * lw) * 3840 + 256 * aj;
    const unsigned lbase = lds0 + lw * 1024;
    const int rot = ((bi & 7) * 5 / 4 + (bi >> 3) * 5 / 2) % 10;
    auto issue = [&](int s) {
      const unsigned sl = lbase + (s & 3) * L_SLOT;
      int ss = s + rot; if (ss >= 10) ss -= 10;
#pragma unroll
      for (int i = 0; i < 4; ++i) {
        glds16(voff_x, xb + (size_t)i * 32 * 1280 + ss * 128, sl + i * 4096);
        glds16(voff_w, wb + ((size_t)ss * 64 + i * 16) * 3840, sl + L_W + i * 4096);
      }
    };
    issue(0); issue(1); issue(2); issue(3);
    WAITV(24); BARRIER();
    if (w == 4) STAMP(1);
    WAITV(16); BARRIER();
#pragma unroll 1
    for (int s = 1; s <= 6; ++s) { issue(s + 3); WAITV(16); BARRIER(); }
    WAITV(8); BARRIER();
    WAITV(0); BARRIER();
    BARRIER();
    BARRIER();
  } else if (w < 4) {
    if (w == 0) {
      const u32x2 e = __builtin_amdgcn_raw_buffer_load_b64(rws, WS_EPOCH, 0, SC1);
      const u64 t = (((u64)e[1] << 32) | e[0]) + 1;
      if (lane == 0) *(LAS u32x2*)(L + L_EP) = (u32x2){(unsigned)t, (unsigned)(t >> 32)};
      __builtin_amdgcn_raw_buffer_store_b128((u32x4){0, 0, 0, 0}, rws, WS_STATS + (R * 8 + cc) * 1024 + lane * 16, 0, SC1);
      if (is_gu && lane < 32) __builtin_amdgcn_raw_buffer_store_b128((u32x4){0, 0, 0, 0}, rws, WS_ACT + (((gs >> 1) * 4 + 2 * (gs & 1) + (lane >> 4)) * 8 + (lane & 7)) * 32 + ((lane >> 3) & 1) * 16, 0, SC1);
    }
    if (aj < 15) {
      const int wm = w >> 1, wn = w & 1;
      unsigned xa[4], wa[4];
#pragma unroll
      for (int ks = 0; ks < 4; ++ks) xa[ks] = (unsigned)(64 * wm + l31) * 128u + ((((unsigned)(2 * ks + hh)) ^ (((unsigned)l31 >> 1) & 7u)) << 4);
      const unsigned xo0 = wn * 4096, xo1 = 4096 - xo0;
      {
        const unsigned blk = (lane >> 4) & 1, q = (lane & 15) >> 2, pq = lane & 3;
#pragma unroll
        for (int j = 0; j < 2; ++j)
#pragma unroll
          for (int t = 0; t < 2; ++t) {
            const unsigned jj = j ^ wm;
            const unsigned row = 8 * hh + 4 * t + q, chn = 4 * (2 * wn + jj) + 2 * blk + (pq >> 1), m = (q << 2) | (unsigned)(2 * hh + t);
            wa[j * 2 + t] = L_W + 256u * row + 16u * (chn ^ m) + 8u * (pq & 1);
          }
      }
      f32x16 a00 = {}, a01 = {}, a10 = {}, a11 = {};
      float ssum = 0.f, ssq = 0.f, csum = 0.f;
      bf16x8 xf0[2], wf0[2], xf1[2], wf1[2];
      const unsigned ONE2 = 0x3f803f80u;
      unsigned xb0[4], xb1[4], wb[4];
#define SETSLOT(so) do { _Pragma("unroll") for (int k_ = 0; k_ < 4; ++k_) { xb0[k_] = xa[k_] + (so) + xo0; xb1[k_] = xa[k_] + (so) + xo1; wb[k_] = wa[k_] + (so); } } while (0)
#define RD(XF, WF, ks) do { \
        XF[0] = *(const LAS bf16x8*)(L + xb0[ks]); XF[1] = *(const LAS bf16x8*)(L + xb1[ks]); \
        WF[0] = cat8(trd(L + wb[0] + (ks) * 4096), trd(L + wb[1] + (ks) * 4096)); \
        WF[1] = cat8(trd(L + wb[2] + (ks) * 4096), trd(L + wb[3] + (ks) * 4096)); } while (0)
#define MM(XF, WF) do { \
        a00 = MFMA32(WF[0], XF[0], a00); a01 = MFMA32(WF[1], XF[0], a01); \
        a10 = MFMA32(WF[0], XF[1], a10); a11 = MFMA32(WF[1], XF[1], a11); \
        { const u32x4 xs_ = __builtin_bit_cast(u32x4, XF[0]); const u32x4 ws_ = __builtin_bit_cast(u32x4, WF[0]); \
          _Pragma("unroll") for (int e_ = 0; e_ < 4; ++e_) { ssum = dot2(xs_[e_], ONE2, ssum); ssq = dot2(xs_[e_], xs_[e_], ssq); csum = dot2(ws_[e_], ONE2, csum); } } } while (0)
#define PINALL() asm volatile("" : "+v"(a00), "+v"(a01), "+v"(a10), "+v"(a11), "+v"(ssum), "+v"(ssq), "+v"(csum))
#define ILV() do { _Pragma("unroll") for (int g_ = 0; g_ < 4; ++g_) { \
        __builtin_amdgcn_sched_group_barrier(0x8, 1, 0); __builtin_amdgcn_sched_group_barrier(0x100, 2, 0); __builtin_amdgcn_sched_group_barrier(0x2, 3, 0); } } while (0)
#define STEP(XN, WN, ksn, XC, WC) do { RD(XN, WN, ksn); MM(XC, WC); ILV(); PINALL(); SBAR(); } while (0)
      SETSLOT(0);
      BARRIER();
      RD(xf0, wf0, 0);
      SBAR();
#pragma unroll 1
      for (int s = 0; s < 9; ++s) {
        STEP(xf1, wf1, 1, xf0, wf0);
        STEP(xf0, wf0, 2, xf1, wf1);
        STEP(xf1, wf1, 3, xf0, wf0);
        SETSLOT(((s + 1) & 3) * L_SLOT);
        WAITL0(); BARRIER();
        STEP(xf0, wf0, 0, xf1, wf1);
      }
      STEP(xf1, wf1, 1, xf0, wf0);
      STEP(xf0, wf0, 2, xf1, wf1);
      STEP(xf1, wf1, 3, xf0, wf0);
      MM(xf1, wf1); PINALL();
      if (w == 0) STAMP(2);
      ssum = swap_add(ssum); ssq = swap_add(ssq); csum = swap_add(csum);
      {
        const float mean = ssum * (1.f / D);
        const float var = fmaxf(ssq * (1.f / D) - mean * mean, 0.f);
        const float mb = rbf(mean), vb = rbf(var);
        const float sd = rbf(sqrtf(rbf(vb + 1e-5f)));
        if (lane < 32) {
          *(LAS f32x2*)(L + L_RS + (64 * wm + 32 * wn + l31) * 8) = (f32x2){mb, 1.f / sd};
          *(LAS float*)(L + L_CS + (64 * wn + 32 * wm + l31) * 4) = csum;
        }
      }
      WAITV(0); WAITL0(); BARRIER();
      const float qs = aj < 5 ? 0.22360679774997896f * 1.4426950408889634f : 1.f;
#pragma unroll
      for (int i = 0; i < 2; ++i) {
        const int tok = 64 * wm + 32 * (i ^ wn) + l31;
        const f32x2 rs = *(const LAS f32x2*)(L + L_RS + tok * 8);
        const float sc = rs[1] * qs, nm = -rs[0] * sc;
#pragma unroll
        for (int j = 0; j < 2; ++j) {
          const f32x16& a = i == 0 ? (j == 0 ? a00 : a01) : (j == 0 ? a10 : a11);
          const int f0 = 64 * wn + 32 * (j ^ wm) + 4 * hh;
          const unsigned go = WS_QKV + (unsigned)(32 * aj + (f0 >> 2)) * 16384u + (unsigned)(128 * aP + tok) * 8u;
#pragma unroll
          for (int rq = 0; rq < 4; ++rq) {
            const f32x4 cs = *(const LAS f32x4*)(L + L_CS + (f0 + 8 * rq) * 4);
            const float v0 = fmaf(a[rq * 4 + 0], sc, nm * cs[0]), v1 = fmaf(a[rq * 4 + 1], sc, nm * cs[1]);
            const float v2 = fmaf(a[rq * 4 + 2], sc, nm * cs[2]), v3 = fmaf(a[rq * 4 + 3], sc, nm * cs[3]);
            __builtin_amdgcn_raw_buffer_store_b64((u32x2){pk2(v0, v1), pk2(v2, v3)}, rws, go + rq * 32768, 0, SC1);
          }
        }
      }
      WAITV(0); BARRIER();
    } else {
      WAITV(0);
    }
  }
  if (aj >= 15) { WAITL0(); BARRIER(); }
  const u32x2 tag2 = *(const LAS u32x2*)(L + L_EP);
  const unsigned tag32 = tag2[0] | 1u;
  if (aj < 15 && tid == 0) __builtin_amdgcn_raw_buffer_store_b64(tag2, rws, WS_FLAGA + (aP * 16 + aj) * 128, 0, SC1);
  if (w == 0) STAMP(3);

  f32x16 o = (f32x16){};
  {
    const int t = w;
    if (tid < 4) *(LAS unsigned*)(L + L_ONE + tid * 4) = tid == 0 ? 0x00003f80u : 0u;
    {
      const int sec = lane < 4 ? 0 : (lane < 12 ? 1 : 2);
      const int k = lane < 4 ? lane : (lane < 12 ? lane - 4 : lane - 12);
      const int col = sec * 640 + 20 * hd + ((k & 1) ? 19 : 0);
      int Pn = sec == 0 ? 2 * pp + (k >> 1) : 2 * pp - 2 + (k >> 1);
      const bool need = lane < 20 && Pn >= 0;
      const unsigned fo = WS_FLAGA + (unsigned)((need ? Pn : 0) * 16 + (col >> 7)) * 128;
      unsigned spins = 0;
      for (;;) {
        const u32x2 f = __builtin_amdgcn_raw_buffer_load_b64(rws, fo, 0, SC1);
        if (__all(!need || (f[0] == tag2[0] && f[1] == tag2[1]))) break;
        if (++spins > SPIN_CAP) break;
        __builtin_amdgcn_s_sleep(1);
      }
    }
    asm volatile("" ::: "memory");
    if (w == 0) STAMP(4);
    {
      const char* qb = (const char*)p.ws + WS_QKV;
#pragma unroll
      for (int i = 0; i < 5; ++i) {
        const int pc = w + 8 * i, pl = pc >> 2, qt = pc & 3;
        const int tok0 = 256 * pp - 256 + 128 * qt;
        if (tok0 >= 0) glds16c(lane * 16, qb + (size_t)((pl < 5 ? 160 + 5 * hd + pl : 320 + 5 * hd + pl - 5)) * 16384 + tok0 * 8, lds0 + pl * PL + qt * 1024);
      }
    }
    bf16x8 qf0; bf16x4 qf1;
    {
      const unsigned qo = WS_QKV + (unsigned)(5 * hd) * 16384u + (unsigned)(256 * pp + 32 * t + l31) * 8u;
      const u32x2 a = __builtin_amdgcn_raw_buffer_load_b64(rws, qo + (2 * hh) * 16384, 0, SC1);
      const u32x2 c = __builtin_amdgcn_raw_buffer_load_b64(rws, qo + (2 * hh + 1) * 16384, 0, SC1);
      u32x2 z = __builtin_amdgcn_raw_buffer_load_b64(rws, qo + 4 * 16384, 0, SC1);
      if (hh) z = (u32x2){0, 0};
      qf0 = __builtin_bit_cast(bf16x8, (u32x4){a[0], a[1], c[0], c[1]});
      qf1 = __builtin_bit_cast(bf16x4, z);
    }
    WAITV(0); WAITL0(); BARRIER();
    if (w == 0) STAMP(5);
    {
#pragma unroll 1
      for (int pc = w; pc < 100; pc += 8) {
        const int n = pc * 64 + lane, rho = n / 10, ch = n - rho * 10;
        const int s = rho >> 5, tq = (rho >> 4) & 1, kg = (rho >> 2) & 3, q = rho & 3;
        const int cidx = 4 * s + kg, g2 = cidx / 5, c5 = cidx - 5 * g2;
        const int quad = 10 * g2 + ((tq ? 0x98732 : 0x46510) >> (4 * c5) & 15);
        glds16((unsigned)(4 * quad + q) * 1280u + ch * 16u, (const char*)p.wo + 160 * cc, lds0 + L_WO + pc * 1024);
      }
    }
    const int kad = L_K + l31 * 8;
    float negm;
    {
      const LAS char* kp = L + kad + (t + 8) * 256;
      const u32x2 k0 = *(const LAS u32x2*)(kp + (2 * hh) * PL), k1 = *(const LAS u32x2*)(kp + (2 * hh + 1) * PL), k2 = *(const LAS u32x2*)(kp + 4 * PL);
      const u32x4 q0 = __builtin_bit_cast(u32x4, qf0); const u32x2 q1 = __builtin_bit_cast(u32x2, qf1);
      float d = dot2(q0[0], k0[0], 0.f); d = dot2(q0[1], k0[1], d); d = dot2(q0[2], k1[0], d); d = dot2(q0[3], k1[1], d);
      d = dot2(q1[0], k2[0], d); d = dot2(q1[1], k2[1], d);
      negm = -swap_add(d);
    }
    const int vquad = 4 * ((lane >> 4) & 1) + (lane & 3);
    const int vad = vquad < 5 ? L_V + vquad * PL + (4 * hh + ((lane & 15) >> 2)) * 8 : L_ONE;
    const int vst = vquad < 5 ? 1 : 0;
    auto tile = [&](int kt, const f32x16& cin) {
      const LAS char* kp = L + kad + kt * 256;
      const bf16x4 ka = *(const LAS bf16x4*)(kp + (2 * hh) * PL), kb = *(const LAS bf16x4*)(kp + (2 * hh + 1) * PL), kc = *(const LAS bf16x4*)(kp + 4 * PL);
      const LAS char* vp = L + vad + vst * kt * 256;
      const bf16x8 v0 = cat8(trd(vp), trd(vp + vst * 64)), v1 = cat8(trd(vp + vst * 128), trd(vp + vst * 192));
      f32x16 s = MFMA32(cat8(ka, kb), qf0, cin);
      s = MFMA32K8(kc, qf1, s);
      u32x4 p0, p1;
#pragma unroll
      for (int e = 0; e < 4; ++e) {
        p0[e] = pk2(__builtin_amdgcn_exp2f(s[2 * e]), __builtin_amdgcn_exp2f(s[2 * e + 1]));
        p1[e] = pk2(__builtin_amdgcn_exp2f(s[8 + 2 * e]), __builtin_amdgcn_exp2f(s[8 + 2 * e + 1]));
      }
      o = MFMA32(v0, __builtin_bit_cast(bf16x8, p0), o);
      o = MFMA32(v1, __builtin_bit_cast(bf16x8, p1), o);
    };
    f32x16 cm;
#pragma unroll
    for (int r = 0; r < 16; ++r) { const int kr = (r & 3) + 8 * (r >> 2) + 4 * hh; cm[r] = kr <= l31 ? negm : -INFINITY; }
    tile(t + 8, cm);
#pragma unroll
    for (int r = 0; r < 16; ++r) cm[r] = negm;
    const int lo = pp >= 1 ? t + 1 : 8;
#pragma unroll 1
    for (int kt = t + 7; kt >= lo; --kt) tile(kt, cm);
    if (pp >= 1) {
#pragma unroll
      for (int r = 0; r < 16; ++r) { const int kr = (r & 3) + 8 * (r >> 2) + 4 * hh; cm[r] = kr > l31 ? negm : -INFINITY; }
      tile(t, cm);
    }
    if (w == 0) STAMP(6);
    auto r = __builtin_amdgcn_permlane32_swap(__float_as_uint(o[8]), __float_as_uint(o[8]), false, false);
    const float inv = 1.f / __uint_as_float(r[1]);
    u32x4 c0;
    c0[0] = pk2(o[0] * inv, o[1] * inv); c0[1] = pk2(o[2] * inv, o[3] * inv); c0[2] = pk2(o[4] * inv, o[5] * inv); c0[3] = pk2(o[6] * inv, o[7] * inv);
    const u32x2 c1 = (u32x2){pk2(o[8] * inv, o[9] * inv), pk2(o[10] * inv, o[11] * inv)};
    const int tt = pp * 16 + 2 * t + (l31 >> 4);
    const unsigned base = WS_O + (unsigned)tt * 80 * 256 + (l31 & 15) * 16 + (unsigned)(5 * (hd >> 1)) * 256;
    __builtin_amdgcn_raw_buffer_store_b128(c0, rws, base + (2 * (hd & 1) + hh) * 256, 0, SC1);
    if (hh == 0) __builtin_amdgcn_raw_buffer_store_b64(c1, rws, base + 4 * 256 + (hd & 1) * 8, 0, SC1);
    WAITV(0);
    if (lane == 0) __builtin_amdgcn_raw_buffer_store_b64(tag2, rws, WS_FLAGO + (((pp * 8 + t) * 2 + (hd >> 4)) * 16 + (hd & 15)) * 8, 0, SC1);
  }
  if (w == 0) STAMP(7);
  BARRIER();

  const int nt = w & 3, kh = w >> 2;
  const int tok = 64 * R + 16 * nt + l15;
  u32x2 xr[5], wrv[5];
  float swr = 0.f;
  if (kh == 0) {
#pragma unroll
    for (int ft = 0; ft < 5; ++ft) {
      xr[ft] = *(const u32x2*)(p.x + (size_t)tok * D + 80 * cc + 16 * ft + 4 * fg);
      wrv[ft] = *(const u32x2*)(p.wr + 80 * cc + 16 * ft + 4 * fg);
    }
    const unsigned* wr32 = (const unsigned*)p.wr;
#pragma unroll
    for (int i = 0; i < 5; ++i) swr = dot2(wr32[lane + 64 * i], 0x3f803f80u, swr);
    swr = wave_fsum(swr);
  }
  f32x4 acc[5];
#pragma unroll
  for (int ft = 0; ft < 5; ++ft) acc[ft] = (f32x4){0, 0, 0, 0};
  {
    const int tg = 4 * R + nt;
    const unsigned fo = WS_FLAGO + ((((tg >> 4) * 8 + ((tg >> 1) & 7)) * 2 + kh) * 16 + l15) * 8;
    unsigned spins = 0;
    for (;;) {
      const u32x2 f = __builtin_amdgcn_raw_buffer_load_b64(rws, fo, 0, SC1);
      if (__all(f[0] == tag2[0] && f[1] == tag2[1])) break;
      if (++spins > SPIN_CAP) break;
      __builtin_amdgcn_s_sleep(1);
    }
    asm volatile("" ::: "memory");
    if (w == 0) STAMP(8);
    u32x4 of[10];
#pragma unroll
    for (int s = 0; s < 10; ++s) of[s] = __builtin_amdgcn_raw_buffer_load_b128(rws, WS_O + (unsigned)(tg * 80 + 4 * (10 * kh + s)) * 256 + lane * 16, 0, SC1);
    const LAS char* wp = L + L_WO + (unsigned)(kh * 10 * 32 + fg * 4 + ((lane & 15) >> 2)) * 160 + (lane & 3) * 8;
#pragma unroll
    for (int s = 0; s < 10; ++s) {
#pragma unroll
      for (int ft = 0; ft < 5; ++ft) {
        const bf16x8 a = cat8(trd(wp + (s * 32) * 160 + ft * 32), trd(wp + (s * 32 + 16) * 160 + ft * 32));
        acc[ft] = MFMA16(a, __builtin_bit_cast(bf16x8, of[s]), acc[ft]);
      }
    }
  }
  if (w == 0) STAMP(9);
  if (kh == 1) {
#pragma unroll
    for (int ft = 0; ft < 5; ++ft) *(LAS f32x4*)(L + L_RED + ((nt * 5 + ft) * 64 + lane) * 16) = acc[ft];
  }
  WAITL0(); BARRIER();
  unsigned hp[10];
  float mb = 0.f, inv_sd = 0.f;
  if (kh == 1) {
    const int rl = lane >> 1, grow = 8 * ((rl >> 2) & 3) + 4 * (rl >> 4) + (rl & 3);
    if (is_gu) {
      const unsigned vo = (unsigned)grow * (F * 2) + (lane & 1) * 16;
#pragma unroll 1
      for (int pc = nt; pc < 40; pc += 4) { const int m = pc >= 20, s = pc - 20 * m; glds16(vo, (const char*)(m ? p.wu : p.wg) + (size_t)s * 32 * F * 2 + gs * 32, lds0 + L_FW + pc * 1024); }
    } else if (is_dn) {
      const unsigned vo = (unsigned)grow * (D * 2) + (lane & 1) * 16;
#pragma unroll 1
      for (int pc = nt; pc < 54; pc += 4) glds16(vo, (const char*)p.wd + (size_t)pc * 32 * D * 2 + dn * 32, lds0 + L_FW + pc * 1024);
    }
  } else {
    float s1 = 0.f, s2 = 0.f, s3 = 0.f;
#pragma unroll
    for (int ft = 0; ft < 5; ++ft) {
      const f32x4 o2 = *(const LAS f32x4*)(L + L_RED + ((nt * 5 + ft) * 64 + lane) * 16);
      const unsigned a01 = pk2(acc[ft][0] + o2[0], acc[ft][1] + o2[1]), a23 = pk2(acc[ft][2] + o2[2], acc[ft][3] + o2[3]);
      const unsigned h01 = pk2(bf2f(xr[ft][0]) + bf2f(a01), bfhi(xr[ft][0]) + bfhi(a01)), h23 = pk2(bf2f(xr[ft][1]) + bf2f(a23), bfhi(xr[ft][1]) + bfhi(a23));
      hp[2 * ft] = h01; hp[2 * ft + 1] = h23;
      __builtin_amdgcn_raw_buffer_store_b64((u32x2){h01, h23}, rws, WS_H + (unsigned)tok * 1280 + (80 * cc + 16 * ft + 4 * fg) * 2, 0, SC1);
      s1 = dot2(h01, 0x3f803f80u, s1); s1 = dot2(h23, 0x3f803f80u, s1);
      s2 = dot2(h01, h01, s2); s2 = dot2(h23, h23, s2);
      s3 = dot2(h01, wrv[ft][0], s3); s3 = dot2(h23, wrv[ft][1], s3);
    }
    s1 = swap_add(swap16_add(s1)); s2 = swap_add(swap16_add(s2)); s3 = swap_add(swap16_add(s3));
    if (lane < 16) __builtin_amdgcn_raw_buffer_store_b128((u32x4){__float_as_uint(s1), __float_as_uint(s2), __float_as_uint(s3), tag32}, rws, WS_STATS + ((R * 8 + cc) * 64 + 16 * nt + l15) * 16, 0, SC1);
    if (w == 0) STAMP(10);
    float t1, t2, t3;
    {
      const unsigned so = WS_STATS + ((R * 8 + fg) * 64 + 16 * nt + l15) * 16;
      unsigned spins = 0;
      for (;;) {
        const u32x4 ga = __builtin_amdgcn_raw_buffer_load_b128(rws, so, 0, SC1), gb = __builtin_amdgcn_raw_buffer_load_b128(rws, so + 4 * 1024, 0, SC1);
        t1 = __uint_as_float(ga[0]) + __uint_as_float(gb[0]); t2 = __uint_as_float(ga[1]) + __uint_as_float(gb[1]); t3 = __uint_as_float(ga[2]) + __uint_as_float(gb[2]);
        if (__all(ga[3] == tag32 && gb[3] == tag32)) break;
        if (++spins > SPIN_CAP) break;
        __builtin_amdgcn_s_sleep(1);
      }
    }
    t1 = swap_add(swap16_add(t1)); t2 = swap_add(swap16_add(t2)); t3 = swap_add(swap16_add(t3));
    const float mean = t1 * (1.f / D);
    const float var = fmaxf(t2 * (1.f / D) - mean * mean, 0.f);
    mb = rbf(mean);
    const float sd = rbf(sqrtf(rbf(rbf(var) + 1e-5f)));
    inv_sd = 1.f / sd;
    const unsigned rb = pk2((t3 - mb * swr) * inv_sd, 0.f) & 0xffffu;
    const unsigned key = ((rb ^ ((rb & 0x8000u) ? 0xffffu : 0x8000u)) << 16) | (unsigned)(2047 - tok);
    if (lane < 16) {
      *(LAS unsigned*)(L + L_KEY + (16 * nt + l15) * 4) = key;
      *(LAS unsigned*)(L + L_MS + (16 * nt + l15) * 4) = (__float_as_uint(mb) & 0xffff0000u) | (__float_as_uint(sd) >> 16);
    }
    WAITV(0);
    if (w == 0) STAMP(11);
  }
  WAITL0(); BARRIER();
  if (w == 0) {
    unsigned k = *(const LAS unsigned*)(L + L_KEY + lane * 4), m = 0;
    for (int i = 0; i <= cc; ++i) { m = wave_umax(k); if (k == m) k = 0; }
    const unsigned ms = *(const LAS unsigned*)(L + L_MS + ((2047 - (int)(m & 0x7ff)) & 63) * 4);
    if (lane == 0) __builtin_amdgcn_raw_buffer_store_b128((u32x4){m, tag2[0], ms, tag2[1]}, rws, WS_CAND + (R * 8 + cc) * 16, 0, SC1);
    STAMP(12);
    unsigned k0, k1, k2, k3, m0, m1, m2, m3;
    unsigned spins = 0;
    for (;;) {
      const u32x4 a = __builtin_amdgcn_raw_buffer_load_b128(rws, WS_CAND + lane * 16, 0, SC1), bb = __builtin_amdgcn_raw_buffer_load_b128(rws, WS_CAND + 1024 + lane * 16, 0, SC1);
      const u32x4 c = __builtin_amdgcn_raw_buffer_load_b128(rws, WS_CAND + 2048 + lane * 16, 0, SC1), d = __builtin_amdgcn_raw_buffer_load_b128(rws, WS_CAND + 3072 + lane * 16, 0, SC1);
      k0 = a[0]; k1 = bb[0]; k2 = c[0]; k3 = d[0]; m0 = a[2]; m1 = bb[2]; m2 = c[2]; m3 = d[2];
      const bool ok = a[1] == tag2[0] && a[3] == tag2[1] && bb[1] == tag2[0] && bb[3] == tag2[1] && c[1] == tag2[0] && c[3] == tag2[1] && d[1] == tag2[0] && d[3] == tag2[1];
      if (__all(ok)) break;
      if (++spins > SPIN_CAP) break;
      __builtin_amdgcn_s_sleep(1);
    }
    STAMP(13);
#pragma unroll 1
    for (int i = 0; i < 8; ++i) {
      const unsigned lm = max(max(k0, k1), max(k2, k3));
      const unsigned g = wave_umax(lm);
      const unsigned ms2 = k0 == g ? m0 : (k1 == g ? m1 : (k2 == g ? m2 : m3));
      if (lm == g) { *(LAS unsigned*)(L + L_SEL + i * 4) = g; *(LAS unsigned*)(L + L_SEL + 32 + i * 4) = ms2; }
      k0 = k0 == g ? 0 : k0; k1 = k1 == g ? 0 : k1; k2 = k2 == g ? 0 : k2; k3 = k3 == g ? 0 : k3;
    }
    if (b == 0 && lane == 0) __builtin_amdgcn_raw_buffer_store_b64(tag2, rws, WS_EPOCH, 0, SC1);
    STAMP(14);
  }
  if (kh == 1) WAITV(0);
  WAITL0(); BARRIER();

  const unsigned selk = *(const LAS unsigned*)(L + L_SEL + (lane & 7) * 4), selm = *(const LAS unsigned*)(L + L_SEL + 32 + (lane & 7) * 4);
  auto out_rows = [&]() {
    if (kh == 0) {
      bool sel = false;
#pragma unroll
      for (int i = 0; i < 8; ++i) sel |= (2047 - (int)(__builtin_amdgcn_readlane((int)selk, i) & 0x7ff)) == tok;
      if (!sel) {
#pragma unroll
        for (int ft = 0; ft < 5; ++ft) {
          unsigned r2[2];
#pragma unroll
          for (int e = 0; e < 2; ++e) {
            const unsigned hv = hp[2 * ft + e];
            const unsigned d2 = pk2(bf2f(hv) - mb, bfhi(hv) - mb);
            const unsigned n2 = pk2(bf2f(d2) * inv_sd, bfhi(d2) * inv_sd);
            r2[e] = pk2(bf2f(hv) + bf2f(n2), bfhi(hv) + bfhi(n2));
          }
          *(u32x2*)(p.out + (size_t)tok * D + 80 * cc + 16 * ft + 4 * fg) = (u32x2){r2[0], r2[1]};
        }
      }
    }
  };
  if (is_gu) {
    {
      const unsigned kk = *(const LAS unsigned*)(L + L_SEL + w * 4), ms = *(const LAS unsigned*)(L + L_SEL + 32 + w * 4);
      const int row = 2047 - (int)(kk & 0x7ff);
      const float m = __uint_as_float(ms & 0xffff0000u), iv = 1.f / __uint_as_float(ms << 16);
      const u32x4 h0 = __builtin_amdgcn_raw_buffer_load_b128(rws, WS_H + (unsigned)row * 1280 + lane * 16, 0, SC1);
      const u32x4 h1 = __builtin_amdgcn_raw_buffer_load_b128(rws, WS_H + (unsigned)row * 1280 + 1024 + (lane & 15) * 16, 0, SC1);
      u32x4 n0, n1;
#pragma unroll
      for (int e = 0; e < 4; ++e) {
        const unsigned d0 = pk2(bf2f(h0[e]) - m, bfhi(h0[e]) - m), d1 = pk2(bf2f(h1[e]) - m, bfhi(h1[e]) - m);
        n0[e] = pk2(bf2f(d0) * iv, bfhi(d0) * iv); n1[e] = pk2(bf2f(d1) * iv, bfhi(d1) * iv);
      }
      *(LAS u32x4*)(L + L_XS + w * 1296 + lane * 16) = n0;
      if (lane < 16) *(LAS u32x4*)(L + L_XS + w * 1296 + 1024 + lane * 16) = n1;
    }
    WAITL0(); BARRIER();
    if (w == 0) STAMP(15);
    {
      const int m = w & 1, kq = w >> 1;
      f32x4 a4 = {0, 0, 0, 0};
#pragma unroll
      for (int i = 0; i < 5; ++i) {
        const int s = 5 * kq + i;
        const LAS char* ap = L + L_FW + (m * 20 + s) * 1024 + lane * 8;
        const bf16x8 af = cat8(trd(ap), trd(ap + 512));
        const bf16x8 bfv = *(const LAS bf16x8*)(L + L_XS + (lane & 7) * 1296 + s * 64 + fg * 16);
        a4 = MFMA16(af, bfv, a4);
      }
      *(LAS f32x4*)(L + L_FRED + (w * 64 + lane) * 16) = a4;
    }
    WAITL0(); BARRIER();
    if (w == 0) {
      f32x4 g4 = {0, 0, 0, 0}, u4 = {0, 0, 0, 0};
#pragma unroll
      for (int q = 0; q < 4; ++q) { g4 += *(const LAS f32x4*)(L + L_FRED + ((2 * q) * 64 + lane) * 16); u4 += *(const LAS f32x4*)(L + L_FRED + ((2 * q + 1) * 64 + lane) * 16); }
      float a[4];
#pragma unroll
      for (int e = 0; e < 4; ++e) { const float g = rbf(g4[e]), u = rbf(u4[e]); const float sg = rbf(g / (1.f + __expf(-g))); a[e] = sg * u; }
      if (l15 < 8) __builtin_amdgcn_raw_buffer_store_b128((u32x4){pk2(a[0], a[1]), tag32, pk2(a[2], a[3]), tag32}, rws, WS_ACT + (((gs >> 1) * 4 + 2 * (gs & 1) + (fg >> 1)) * 8 + l15) * 32 + (fg & 1) * 16, 0, SC1);
      STAMP(16);
    }
    out_rows();
  } else if (is_dn) {
    out_rows();
    const int row = 2047 - (int)(selk & 0x7ff);
    const u32x2 hv = __builtin_amdgcn_raw_buffer_load_b64(rws, WS_H + (unsigned)row * 1280 + (16 * dn + 4 * fg) * 2, 0, SC1);
    f32x4 a4 = {0, 0, 0, 0};
    {
      u32x4 g0[7], g1[7];
      unsigned spins = 0;
      for (;;) {
        bool ok = true;
#pragma unroll
        for (int i = 0; i < 7; ++i) {
          const int s = min(w + 8 * i, 53);
          const unsigned ao = WS_ACT + ((s * 4 + fg) * 8 + (lane & 7)) * 32;
          g0[i] = __builtin_amdgcn_raw_buffer_load_b128(rws, ao, 0, SC1); g1[i] = __builtin_amdgcn_raw_buffer_load_b128(rws, ao + 16, 0, SC1);
        }
#pragma unroll
        for (int i = 0; i < 7; ++i) ok = ok && g0[i][1] == tag32 && g0[i][3] == tag32 && g1[i][1] == tag32 && g1[i][3] == tag32;
        if (__all(ok)) break;
        if (++spins > SPIN_CAP) break;
        __builtin_amdgcn_s_sleep(1);
      }
      if (w == 0) STAMP(17);
#pragma unroll
      for (int i = 0; i < 7; ++i) {
        const int s = w + 8 * i;
        if (s < 54) {
          const LAS char* ap = L + L_FW + s * 1024 + lane * 8;
          const bf16x8 af = cat8(trd(ap), trd(ap + 512));
          a4 = MFMA16(af, __builtin_bit_cast(bf16x8, (u32x4){g0[i][0], g0[i][2], g1[i][0], g1[i][2]}), a4);
        }
      }
    }
    *(LAS f32x4*)(L + L_FRED + (w * 64 + lane) * 16) = a4;
    WAITL0(); BARRIER();
    if (w == 0) {
      f32x4 y = {0, 0, 0, 0};
#pragma unroll
      for (int q = 0; q < 8; ++q) y += *(const LAS f32x4*)(L + L_FRED + (q * 64 + lane) * 16);
      const unsigned rk = selk >> 16, rbits = (rk & 0x8000u) ? (rk ^ 0x8000u) : (rk ^ 0xffffu);
      const float rv = bf2f(rbits), sig = rbf(1.f / (1.f + __expf(-rv)));
      const float m = __uint_as_float(selm & 0xffff0000u), iv = 1.f / __uint_as_float(selm << 16);
      unsigned r2[2];
#pragma unroll
      for (int e = 0; e < 2; ++e) {
        const unsigned d2 = pk2(bf2f(hv[e]) - m, bfhi(hv[e]) - m);
        const unsigned n2 = pk2(bf2f(d2) * iv, bfhi(d2) * iv);
        const unsigned y2 = pk2(rbf(y[2 * e]) * sig, rbf(y[2 * e + 1]) * sig);
        const unsigned t2 = pk2(bf2f(n2) + bf2f(y2), bfhi(n2) + bfhi(y2));
        r2[e] = pk2(bf2f(hv[e]) + bf2f(t2), bfhi(hv[e]) + bfhi(t2));
      }
      if (l15 < 8) *(u32x2*)(p.out + (size_t)row * D + 16 * dn + 4 * fg) = (u32x2){r2[0], r2[1]};
      STAMP(18);
    }
  } else {
    out_rows();
  }
  if (w == 0) STAMP(19);
  STAMP_FLUSH();
}

constexpr unsigned WS_TS = 100u << 20;
__global__ void ka_kernel(unsigned char* ws) { if (threadIdx.x == 0) ((unsigned long long*)(ws + WS_TS))[0] = __builtin_amdgcn_s_memrealtime(); }
template <int LDSB, int NT, int VG>
__global__ __launch_bounds__(NT) void km_kernel(unsigned char* ws) {
  const unsigned long long t = __builtin_amdgcn_s_memrealtime();
  __shared__ char sm[LDSB > 0 ? LDSB : 4];
  if (VG) { float z; asm volatile("v_mov_b32 v200, 0\n\tv_mov_b32 %0, v200" : "=v"(z) :: "v200"); if (z == 1.f) sm[1] = 1; }
  if (threadIdx.x == 0) { sm[threadIdx.x] = 1; ((unsigned long long*)(ws + WS_TS))[8 + blockIdx.x] = t + sm[0] - 1; }
}
__global__ void kb_kernel(unsigned char* ws, int amp) {
  const unsigned long long* ts = (const unsigned long long*)(ws + WS_TS);
  unsigned long long smin = ~0ull;
  for (int i = threadIdx.x; i < 256; i += 64) smin = min(smin, ts[8 + i]);
  for (int m = 32; m >= 1; m >>= 1) smin = min(smin, (unsigned long long)__shfl_xor((long long)smin, m));
  const unsigned long long d = smin - ts[0];
  const unsigned long long until = __builtin_amdgcn_s_memrealtime() + d * amp;
  while (__builtin_amdgcn_s_memrealtime() < until) __builtin_amdgcn_s_sleep(1);
}
extern "C" void kernel_launch(void* const* d_in, const int* in_sizes, int n_in, void* d_out, int out_size, void* d_ws, size_t ws_size, hipStream_t stream) {
  Params p;
  p.x = (const u16*)d_in[0]; p.wqkv = (const u16*)d_in[1]; p.wo = (const u16*)d_in[2]; p.wr = (const u16*)d_in[4];
  p.wg = (const u16*)d_in[5]; p.wu = (const u16*)d_in[6]; p.wd = (const u16*)d_in[7]; p.out = (u16*)d_out; p.ws = (unsigned char*)d_ws;
  ka_kernel<<<1, 64, 0, stream>>>(p.ws);
  km_kernel<0, 64, 0><<<256, 64, 0, stream>>>(p.ws);
  kb_kernel<<<1, 64, 0, stream>>>(p.ws, 0);
  fused_kernel<<<256, 512, 0, stream>>>(p);
}
```

```cpp
#include <hip/hip_runtime.h>
#include <stdint.h>

typedef unsigned short u16;
typedef short bf16x8 __attribute__((ext_vector_type(8)));
typedef short bf16x4 __attribute__((ext_vector_type(4)));
typedef __bf16 bf2 __attribute__((ext_vector_type(2)));
typedef float f32x16 __attribute__((ext_vector_type(16)));
typedef float f32x4 __attribute__((ext_vector_type(4)));
typedef float f32x2 __attribute__((ext_vector_type(2)));
typedef unsigned u32x4 __attribute__((ext_vector_type(4)));
typedef unsigned u32x2 __attribute__((ext_vector_type(2)));
typedef unsigned long long u64;
#define LAS __attribute__((address_space(3)))
#define SBAR() __builtin_amdgcn_sched_barrier(0)
#define BARRIER() asm volatile("s_barrier" ::: "memory")
#define WAITL0() asm volatile("s_waitcnt lgkmcnt(0)" ::: "memory")
#define WAITV(n) asm volatile("s_waitcnt vmcnt(" #n ")" ::: "memory")

constexpr int S = 2048, D = 640, F = 1728;
constexpr int SC1 = 16;

constexpr unsigned WS_EPOCH = 0;
constexpr unsigned WS_FLAGA = 4096;
constexpr unsigned WS_FLAGO = WS_FLAGA + 32768;
constexpr unsigned WS_CAND = WS_FLAGO + 16384;
constexpr unsigned WS_ACT = WS_CAND + 4096;
constexpr unsigned WS_STATS = 131072;
constexpr unsigned WS_QKV = 524288;
constexpr unsigned WS_O = WS_QKV + 480 * 16384;
constexpr unsigned WS_H = WS_O + S * D * 2;
#define STAMP_OFF (200u << 20)
#define NSTAMP 32

constexpr int L_SLOT = 32768, L_W = 16384;
constexpr int PL = 4128;
constexpr int L_K = 0, L_V = 5 * PL, L_ONE = 10 * PL;
constexpr int L_WO = 43008;
constexpr int L_RED = 0;
constexpr int L_FW = 43008;
constexpr int L_XS = 0;
constexpr int L_FRED = 12288;
constexpr int L_MISC = 147456;
constexpr int L_RS = L_MISC;
constexpr int L_CS = L_MISC + 1024;
constexpr int L_EP = L_MISC + 1536;
constexpr int L_KEY = L_MISC + 2048;
constexpr int L_MS = L_MISC + 2304;
constexpr int L_SEL = L_MISC + 2560;
constexpr int L_STAMP = L_MISC + 4096;
constexpr int LDS_BYTES = 163840;

struct Params { const u16* x; const u16* wqkv; const u16* wo; const u16* wr; const u16* wg; const u16* wu; const u16* wd; u16* out; unsigned char* ws; };

__device__ __forceinline__ float bf2f(unsigned v) { return __uint_as_float(v << 16); }
__device__ __forceinline__ float bfhi(unsigned v) { return __uint_as_float(v & 0xffff0000u); }
__device__ __forceinline__ unsigned pk2(float a, float b) { f32x2 f = {a, b}; return __builtin_bit_cast(unsigned, __builtin_convertvector(f, bf2)); }
__device__ __forceinline__ float rbf(float f) { return __uint_as_float(pk2(f, 0.f) << 16); }
__device__ __forceinline__ float dot2(unsigned a, unsigned b, float c) { return __builtin_amdgcn_fdot2_f32_bf16(__builtin_bit_cast(bf2, a), __builtin_bit_cast(bf2, b), c, false); }
__device__ __forceinline__ float swap_add(float v) { auto r = __builtin_amdgcn_permlane32_swap(__float_as_uint(v), __float_as_uint(v), false, false); return __uint_as_float(r[0]) + __uint_as_float(r[1]); }
__device__ __forceinline__ float swap16_add(float v) { auto r = __builtin_amdgcn_permlane16_swap(__float_as_uint(v), __float_as_uint(v), false, false); return __uint_as_float(r[0]) + __uint_as_float(r[1]); }
__device__ __forceinline__ void glds16(unsigned voff, const void* sbase, unsigned lds) {
  asm volatile("s_mov_b32 m0, %2\n\ts_nop 0\n\tglobal_load_lds_dwordx4 %0, %1" ::"v"(voff), "s"(sbase), "s"(lds) : "memory");
}
__device__ __forceinline__ void glds16c(unsigned voff, const void* sbase, unsigned lds) {
  asm volatile("s_mov_b32 m0, %2\n\ts_nop 0\n\tglobal_load_lds_dwordx4 %0, %1 sc1" ::"v"(voff), "s"(sbase), "s"(lds) : "memory");
}
__device__ __forceinline__ bf16x4 trd(const LAS char* p) { return __builtin_amdgcn_ds_read_tr16_b64_v4i16((LAS bf16x4*)p); }
__device__ __forceinline__ bf16x8 cat8(bf16x4 a, bf16x4 b) { return (bf16x8){a[0], a[1], a[2], a[3], b[0], b[1], b[2], b[3]}; }
#define MFMA32(a, b, c) __builtin_amdgcn_mfma_f32_32x32x16_bf16(a, b, c, 0, 0, 0)
#define MFMA32K8(a, b, c) __builtin_amdgcn_mfma_f32_32x32x8bf16_1k(a, b, c, 0, 0, 0)
#define MFMA16(a, b, c) __builtin_amdgcn_mfma_f32_16x16x32_bf16(a, b, c, 0, 0, 0)
template <int CTRL, int RMASK> __device__ __forceinline__ unsigned dppmax(unsigned v) { return max(v, (unsigned)__builtin_amdgcn_update_dpp(0, (int)v, CTRL, RMASK, 0xf, false)); }
__device__ __forceinline__ unsigned wave_umax(unsigned v) {
  v = dppmax<0x111, 0xf>(v); v = dppmax<0x112, 0xf>(v); v = dppmax<0x114, 0xf>(v); v = dppmax<0x118, 0xf>(v);
  v = dppmax<0x142, 0xa>(v); v = dppmax<0x143, 0xc>(v);
  return (unsigned)__builtin_amdgcn_readlane((int)v, 63);
}

template <int CTRL> __device__ __forceinline__ float dppadd(float v) { return v + __int_as_float(__builtin_amdgcn_update_dpp(0, __float_as_int(v), CTRL, 0xf, 0xf, false)); }
__device__ __forceinline__ float wave_fsum(float v) { v = dppadd<0x128>(v); v = dppadd<0x124>(v); v = dppadd<0x122>(v); v = dppadd<0x121>(v); return swap_add(swap16_add(v)); }

#define STAMP(k) do {} while (0)
#define STAMP_INIT() do {} while (0)
#define STAMP_FLUSH() do {} while (0)

constexpr unsigned SPIN_CAP = 1u << 16;

__global__ __launch_bounds__(512, 2) void fused_kernel(Params p) {
  __shared__ __attribute__((aligned(16))) char smem[LDS_BYTES];
  LAS char* L = (LAS char*)smem;
  const unsigned lds0 = (unsigned)(uintptr_t)smem;
  const int tid = threadIdx.x, lane = tid & 63;
  const int w = __builtin_amdgcn_readfirstlane(tid >> 6);
  const int b = blockIdx.x, xcd = b & 7, bi = b >> 3;
  const int l31 = lane & 31, hh = lane >> 5, l15 = lane & 15, fg = lane >> 4;
  const __amdgpu_buffer_rsrc_t rws = __builtin_amdgcn_make_buffer_rsrc(p.ws, 0, 0x7fffffff, 0x00020000);
  const int aP = 4 * (xcd >> 1) + (bi >> 3), aj = 8 * (xcd & 1) + (bi & 7);
  const int hd = bi, pp = xcd;
  const int R = 4 * xcd + (bi >> 3), cc = bi & 7;
  const int gs = xcd * 14 + bi;
  const bool is_gu = bi < 14 && gs < 108;
  const int dn = xcd * 5 + (bi - 14);
  const bool is_dn = bi >= 14 && bi < 19;
  STAMP_INIT();

  if (aj < 15 && w >= 4) {
    const int lw = w - 4;
    const unsigned xsw = (unsigned)(4 * (lw & 1) + (lane >> 4));
    const unsigned voff_x = (unsigned)(lane >> 3) * 1280u + (((unsigned)(lane & 7) ^ xsw) << 4);
    const unsigned ch = (unsigned)(lane & 15) ^ (((unsigned)(lane >> 4) << 2) | (unsigned)lw);
    const unsigned voff_w = (unsigned)(lane >> 4) * 3840u + ch * 16u;
    const char* xb = (const char*)p.x + (size_t)(128 * aP + 8 * lw) * 1280;
    const char* wb = (const char*)p.wqkv + (size_t)(4 * lw) * 3840 + 256 * aj;
    const unsigned lbase = lds0 + lw * 1024;
    const int rot = ((bi & 7) * 5 / 4 + (bi >> 3) * 5 / 2) % 10;
    auto issue = [&](int s) {
      const unsigned sl = lbase + (s & 3) * L_SLOT;
      int ss = s + rot; if (ss >= 10) ss -= 10;
#pragma unroll
      for (int i = 0; i < 4; ++i) {
        glds16(voff_x, xb + (size_t)i * 32 * 1280 + ss * 128, sl + i * 4096);
        glds16(voff_w, wb + ((size_t)ss * 64 + i * 16) * 3840, sl + L_W + i * 4096);
      }
    };
    issue(0); issue(1); issue(2); issue(3);
    WAITV(24); BARRIER();
    if (w == 4) STAMP(1);
    WAITV(16); BARRIER();
#pragma unroll 1
    for (int s = 1; s <= 6; ++s) { issue(s + 3); WAITV(16); BARRIER(); }
    WAITV(8); BARRIER();
    WAITV(0); BARRIER();
    BARRIER();
    BARRIER();
  } else if (w < 4) {
    if (w == 0) {
      const u32x2 e = __builtin_amdgcn_raw_buffer_load_b64(rws, WS_EPOCH, 0, SC1);
      const u64 t = (((u64)e[1] << 32) | e[0]) + 1;
      if (lane == 0) *(LAS u32x2*)(L + L_EP) = (u32x2){(unsigned)t, (unsigned)(t >> 32)};
      __builtin_amdgcn_raw_buffer_store_b128((u32x4){0, 0, 0, 0}, rws, WS_STATS + (R * 8 + cc) * 1024 + lane * 16, 0, SC1);
      if (is_gu && lane < 32) __builtin_amdgcn_raw_buffer_store_b128((u32x4){0, 0, 0, 0}, rws, WS_ACT + (((gs >> 1) * 4 + 2 * (gs & 1) + (lane >> 4)) * 8 + (lane & 7)) * 32 + ((lane >> 3) & 1) * 16, 0, SC1);
    }
    if (aj < 15) {
      const int wm = w >> 1, wn = w & 1;
      unsigned xa[4], wa[4];
#pragma unroll
      for (int ks = 0; ks < 4; ++ks) xa[ks] = (unsigned)(64 * wm + l31) * 128u + ((((unsigned)(2 * ks + hh)) ^ (((unsigned)l31 >> 1) & 7u)) << 4);
      const unsigned xo0 = wn * 4096, xo1 = 4096 - xo0;
      {
        const unsigned blk = (lane >> 4) & 1, q = (lane & 15) >> 2, pq = lane & 3;
#pragma unroll
        for (int j = 0; j < 2; ++j)
#pragma unroll
          for (int t = 0; t < 2; ++t) {
            const unsigned jj = j ^ wm;
            const unsigned row = 8 * hh + 4 * t + q, chn = 4 * (2 * wn + jj) + 2 * blk + (pq >> 1), m = (q << 2) | (unsigned)(2 * hh + t);
            wa[j * 2 + t] = L_W + 256u * row + 16u * (chn ^ m) + 8u * (pq & 1);
          }
      }
      f32x16 a00 = {}, a01 = {}, a10 = {}, a11 = {};
      float ssum = 0.f, ssq = 0.f, csum = 0.f;
      bf16x8 xf0[2], wf0[2], xf1[2], wf1[2];
      const unsigned ONE2 = 0x3f803f80u;
      unsigned xb0[4], xb1[4], wb[4];
#define SETSLOT(so) do { _Pragma("unroll") for (int k_ = 0; k_ < 4; ++k_) { xb0[k_] = xa[k_] + (so) + xo0; xb1[k_] = xa[k_] + (so) + xo1; wb[k_] = wa[k_] + (so); } } while (0)
#define RD(XF, WF, ks) do { \
        XF[0] = *(const LAS bf16x8*)(L + xb0[ks]); XF[1] = *(const LAS bf16x8*)(L + xb1[ks]); \
        WF[0] = cat8(trd(L + wb[0] + (ks) * 4096), trd(L + wb[1] + (ks) * 4096)); \
        WF[1] = cat8(trd(L + wb[2] + (ks) * 4096), trd(L + wb[3] + (ks) * 4096)); } while (0)
#define MM(XF, WF) do { \
        a00 = MFMA32(WF[0], XF[0], a00); a01 = MFMA32(WF[1], XF[0], a01); \
        a10 = MFMA32(WF[0], XF[1], a10); a11 = MFMA32(WF[1], XF[1], a11); \
        { const u32x4 xs_ = __builtin_bit_cast(u32x4, XF[0]); const u32x4 ws_ = __builtin_bit_cast(u32x4, WF[0]); \
          _Pragma("unroll") for (int e_ = 0; e_ < 4; ++e_) { ssum = dot2(xs_[e_], ONE2, ssum); ssq = dot2(xs_[e_], xs_[e_], ssq); csum = dot2(ws_[e_], ONE2, csum); } } } while (0)
#define PINALL() asm volatile("" : "+v"(a00), "+v"(a01), "+v"(a10), "+v"(a11), "+v"(ssum), "+v"(ssq), "+v"(csum))
#define ILV() do { _Pragma("unroll") for (int g_ = 0; g_ < 4; ++g_) { \
        __builtin_amdgcn_sched_group_barrier(0x8, 1, 0); __builtin_amdgcn_sched_group_barrier(0x100, 2, 0); __builtin_amdgcn_sched_group_barrier(0x2, 3, 0); } } while (0)
#define STEP(XN, WN, ksn, XC, WC) do { RD(XN, WN, ksn); MM(XC, WC); ILV(); PINALL(); SBAR(); } while (0)
      SETSLOT(0);
      BARRIER();
      RD(xf0, wf0, 0);
      SBAR();
#pragma unroll 1
      for (int s = 0; s < 9; ++s) {
        STEP(xf1, wf1, 1, xf0, wf0);
        STEP(xf0, wf0, 2, xf1, wf1);
        STEP(xf1, wf1, 3, xf0, wf0);
        SETSLOT(((s + 1) & 3) * L_SLOT);
        WAITL0(); BARRIER();
        STEP(xf0, wf0, 0, xf1, wf1);
      }
      STEP(xf1, wf1, 1, xf0, wf0);
      STEP(xf0, wf0, 2, xf1, wf1);
      STEP(xf1, wf1, 3, xf0, wf0);
      MM(xf1, wf1); PINALL();
      if (w == 0) STAMP(2);
      ssum = swap_add(ssum); ssq = swap_add(ssq); csum = swap_add(csum);
      {
        const float mean = ssum * (1.f / D);
        const float var = fmaxf(ssq * (1.f / D) - mean * mean, 0.f);
        const float mb = rbf(mean), vb = rbf(var);
        const float sd = rbf(sqrtf(rbf(vb + 1e-5f)));
        if (lane < 32) {
          *(LAS f32x2*)(L + L_RS + (64 * wm + 32 * wn + l31) * 8) = (f32x2){mb, 1.f / sd};
          *(LAS float*)(L + L_CS + (64 * wn + 32 * wm + l31) * 4) = csum;
        }
      }
      WAITV(0); WAITL0(); BARRIER();
      const float qs = aj < 5 ? 0.22360679774997896f * 1.4426950408889634f : 1.f;
#pragma unroll
      for (int i = 0; i < 2; ++i) {
        const int tok = 64 * wm + 32 * (i ^ wn) + l31;
        const f32x2 rs = *(const LAS f32x2*)(L + L_RS + tok * 8);
        const float sc = rs[1] * qs, nm = -rs[0] * sc;
#pragma unroll
        for (int j = 0; j < 2; ++j) {
          const f32x16& a = i == 0 ? (j == 0 ? a00 : a01) : (j == 0 ? a10 : a11);
          const int f0 = 64 * wn + 32 * (j ^ wm) + 4 * hh;
          const unsigned go = WS_QKV + (unsigned)(32 * aj + (f0 >> 2)) * 16384u + (unsigned)(128 * aP + tok) * 8u;
#pragma unroll
          for (int rq = 0; rq < 4; ++rq) {
            const f32x4 cs = *(const LAS f32x4*)(L + L_CS + (f0 + 8 * rq) * 4);
            const float v0 = fmaf(a[rq * 4 + 0], sc, nm * cs[0]), v1 = fmaf(a[rq * 4 + 1], sc, nm * cs[1]);
            const float v2 = fmaf(a[rq * 4 + 2], sc, nm * cs[2]), v3 = fmaf(a[rq * 4 + 3], sc, nm * cs[3]);
            __builtin_amdgcn_raw_buffer_store_b64((u32x2){pk2(v0, v1), pk2(v2, v3)}, rws, go + rq * 32768, 0, SC1);
          }
        }
      }
      WAITV(0); BARRIER();
    } else {
      WAITV(0);
    }
  }
  if (aj >= 15) { WAITL0(); BARRIER(); }
  const u32x2 tag2 = *(const LAS u32x2*)(L + L_EP);
  const unsigned tag32 = tag2[0] | 1u;
  if (aj < 15 && tid == 0) __builtin_amdgcn_raw_buffer_store_b64(tag2, rws, WS_FLAGA + (aP * 16 + aj) * 128, 0, SC1);
  if (w == 0) STAMP(3);

  f32x16 o = (f32x16){};
  {
    const int t = w;
    if (tid < 4) *(LAS unsigned*)(L + L_ONE + tid * 4) = tid == 0 ? 0x00003f80u : 0u;
    {
      const int sec = lane < 4 ? 0 : (lane < 12 ? 1 : 2);
      const int k = lane < 4 ? lane : (lane < 12 ? lane - 4 : lane - 12);
      const int col = sec * 640 + 20 * hd + ((k & 1) ? 19 : 0);
      int Pn = sec == 0 ? 2 * pp + (k >> 1) : 2 * pp - 2 + (k >> 1);
      const bool need = lane < 20 && Pn >= 0;
      const unsigned fo = WS_FLAGA + (unsigned)((need ? Pn : 0) * 16 + (col >> 7)) * 128;
      unsigned spins = 0;
      for (;;) {
        const u32x2 f = __builtin_amdgcn_raw_buffer_load_b64(rws, fo, 0, SC1);
        if (__all(!need || (f[0] == tag2[0] && f[1] == tag2[1]))) break;
        if (++spins > SPIN_CAP) break;
        __builtin_amdgcn_s_sleep(1);
      }
    }
    asm volatile("" ::: "memory");
    if (w == 0) STAMP(4);
    {
      const char* qb = (const char*)p.ws + WS_QKV;
#pragma unroll
      for (int i = 0; i < 5; ++i) {
        const int pc = w + 8 * i, pl = pc >> 2, qt = pc & 3;
        const int tok0 = 256 * pp - 256 + 128 * qt;
        if (tok0 >= 0) glds16c(lane * 16, qb + (size_t)((pl < 5 ? 160 + 5 * hd + pl : 320 + 5 * hd + pl - 5)) * 16384 + tok0 * 8, lds0 + pl * PL + qt * 1024);
      }
    }
    bf16x8 qf0; bf16x4 qf1;
    {
      const unsigned qo = WS_QKV + (unsigned)(5 * hd) * 16384u + (unsigned)(256 * pp + 32 * t + l31) * 8u;
      const u32x2 a = __builtin_amdgcn_raw_buffer_load_b64(rws, qo + (2 * hh) * 16384, 0, SC1);
      const u32x2 c = __builtin_amdgcn_raw_buffer_load_b64(rws, qo + (2 * hh + 1) * 16384, 0, SC1);
      u32x2 z = __builtin_amdgcn_raw_buffer_load_b64(rws, qo + 4 * 16384, 0, SC1);
      if (hh) z = (u32x2){0, 0};
      qf0 = __builtin_bit_cast(bf16x8, (u32x4){a[0], a[1], c[0], c[1]});
      qf1 = __builtin_bit_cast(bf16x4, z);
    }
    WAITV(0); WAITL0(); BARRIER();
    if (w == 0) STAMP(5);
    {
#pragma unroll 1
      for (int pc = w; pc < 100; pc += 8) {
        const int n = pc * 64 + lane, rho = n / 10, ch = n - rho * 10;
        const int s = rho >> 5, tq = (rho >> 4) & 1, kg = (rho >> 2) & 3, q = rho & 3;
        const int cidx = 4 * s + kg, g2 = cidx / 5, c5 = cidx - 5 * g2;
        const int quad = 10 * g2 + ((tq ? 0x98732 : 0x46510) >> (4 * c5) & 15);
        glds16((unsigned)(4 * quad + q) * 1280u + ch * 16u, (const char*)p.wo + 160 * cc, lds0 + L_WO + pc * 1024);
      }
    }
    const int kad = L_K + l31 * 8;
    float negm;
    {
      const LAS char* kp = L + kad + (t + 8) * 256;
      const u32x2 k0 = *(const LAS u32x2*)(kp + (2 * hh) * PL), k1 = *(const LAS u32x2*)(kp + (2 * hh + 1) * PL), k2 = *(const LAS u32x2*)(kp + 4 * PL);
      const u32x4 q0 = __builtin_bit_cast(u32x4, qf0); const u32x2 q1 = __builtin_bit_cast(u32x2, qf1);
      float d = dot2(q0[0], k0[0], 0.f); d = dot2(q0[1], k0[1], d); d = dot2(q0[2], k1[0], d); d = dot2(q0[3], k1[1], d);
      d = dot2(q1[0], k2[0], d); d = dot2(q1[1], k2[1], d);
      negm = -swap_add(d);
    }
    const int vquad = 4 * ((lane >> 4) & 1) + (lane & 3);
    const int vad = vquad < 5 ? L_V + vquad * PL + (4 * hh + ((lane & 15) >> 2)) * 8 : L_ONE;
    const int vst = vquad < 5 ? 1 : 0;
    auto tile = [&](int kt, const f32x16& cin) {
      const LAS char* kp = L + kad + kt * 256;
      const bf16x4 ka = *(const LAS bf16x4*)(kp + (2 * hh) * PL), kb = *(const LAS bf16x4*)(kp + (2 * hh + 1) * PL), kc = *(const LAS bf16x4*)(kp + 4 * PL);
      const LAS char* vp = L + vad + vst * kt * 256;
      const bf16x8 v0 = cat8(trd(vp), trd(vp + vst * 64)), v1 = cat8(trd(vp + vst * 128), trd(vp + vst * 192));
      f32x16 s = MFMA32(cat8(ka, kb), qf0, cin);
      s = MFMA32K8(kc, qf1, s);
      u32x4 p0, p1;
#pragma unroll
      for (int e = 0; e < 4; ++e) {
        p0[e] = pk2(__builtin_amdgcn_exp2f(s[2 * e]), __builtin_amdgcn_exp2f(s[2 * e + 1]));
        p1[e] = pk2(__builtin_amdgcn_exp2f(s[8 + 2 * e]), __builtin_amdgcn_exp2f(s[8 + 2 * e + 1]));
      }
      o = MFMA32(v0, __builtin_bit_cast(bf16x8, p0), o);
      o = MFMA32(v1, __builtin_bit_cast(bf16x8, p1), o);
    };
    f32x16 cm;
#pragma unroll
    for (int r = 0; r < 16; ++r) { const int kr = (r & 3) + 8 * (r >> 2) + 4 * hh; cm[r] = kr <= l31 ? negm : -INFINITY; }
    tile(t + 8, cm);
#pragma unroll
    for (int r = 0; r < 16; ++r) cm[r] = negm;
    const int lo = pp >= 1 ? t + 1 : 8;
#pragma unroll 1
    for (int kt = t + 7; kt >= lo; --kt) tile(kt, cm);
    if (pp >= 1) {
#pragma unroll
      for (int r = 0; r < 16; ++r) { const int kr = (r & 3) + 8 * (r >> 2) + 4 * hh; cm[r] = kr > l31 ? negm : -INFINITY; }
      tile(t, cm);
    }
    if (w == 0) STAMP(6);
    auto r = __builtin_amdgcn_permlane32_swap(__float_as_uint(o[8]), __float_as_uint(o[8]), false, false);
    const float inv = 1.f / __uint_as_float(r[1]);
    u32x4 c0;
    c0[0] = pk2(o[0] * inv, o[1] * inv); c0[1] = pk2(o[2] * inv, o[3] * inv); c0[2] = pk2(o[4] * inv, o[5] * inv); c0[3] = pk2(o[6] * inv, o[7] * inv);
    const u32x2 c1 = (u32x2){pk2(o[8] * inv, o[9] * inv), pk2(o[10] * inv, o[11] * inv)};
    const int tt = pp * 16 + 2 * t + (l31 >> 4);
    const unsigned base = WS_O + (unsigned)tt * 80 * 256 + (l31 & 15) * 16 + (unsigned)(5 * (hd >> 1)) * 256;
    __builtin_amdgcn_raw_buffer_store_b128(c0, rws, base + (2 * (hd & 1) + hh) * 256, 0, SC1);
    if (hh == 0) __builtin_amdgcn_raw_buffer_store_b64(c1, rws, base + 4 * 256 + (hd & 1) * 8, 0, SC1);
    WAITV(0);
    if (lane == 0) __builtin_amdgcn_raw_buffer_store_b64(tag2, rws, WS_FLAGO + (((pp * 8 + t) * 2 + (hd >> 4)) * 16 + (hd & 15)) * 8, 0, SC1);
  }
  if (w == 0) STAMP(7);
  BARRIER();

  const int nt = w & 3, kh = w >> 2;
  const int tok = 64 * R + 16 * nt + l15;
  u32x2 xr[5], wrv[5];
  float swr = 0.f;
  if (kh == 0) {
#pragma unroll
    for (int ft = 0; ft < 5; ++ft) {
      xr[ft] = *(const u32x2*)(p.x + (size_t)tok * D + 80 * cc + 16 * ft + 4 * fg);
      wrv[ft] = *(const u32x2*)(p.wr + 80 * cc + 16 * ft + 4 * fg);
    }
    const unsigned* wr32 = (const unsigned*)p.wr;
#pragma unroll
    for (int i = 0; i < 5; ++i) swr = dot2(wr32[lane + 64 * i], 0x3f803f80u, swr);
    swr = wave_fsum(swr);
  }
  f32x4 acc[5];
#pragma unroll
  for (int ft = 0; ft < 5; ++ft) acc[ft] = (f32x4){0, 0, 0, 0};
  {
    const int tg = 4 * R + nt;
    const unsigned fo = WS_FLAGO + ((((tg >> 4) * 8 + ((tg >> 1) & 7)) * 2 + kh) * 16 + l15) * 8;
    unsigned spins = 0;
    for (;;) {
      const u32x2 f = __builtin_amdgcn_raw_buffer_load_b64(rws, fo, 0, SC1);
      if (__all(f[0] == tag2[0] && f[1] == tag2[1])) break;
      if (++spins > SPIN_CAP) break;
      __builtin_amdgcn_s_sleep(1);
    }
    asm volatile("" ::: "memory");
    if (w == 0) STAMP(8);
    u32x4 of[10];
#pragma unroll
    for (int s = 0; s < 10; ++s) of[s] = __builtin_amdgcn_raw_buffer_load_b128(rws, WS_O + (unsigned)(tg * 80 + 4 * (10 * kh + s)) * 256 + lane * 16, 0, SC1);
    const LAS char* wp = L + L_WO + (unsigned)(kh * 10 * 32 + fg * 4 + ((lane & 15) >> 2)) * 160 + (lane & 3) * 8;
#pragma unroll
    for (int s = 0; s < 10; ++s) {
#pragma unroll
      for (int ft = 0; ft < 5; ++ft) {
        const bf16x8 a = cat8(trd(wp + (s * 32) * 160 + ft * 32), trd(wp + (s * 32 + 16) * 160 + ft * 32));
        acc[ft] = MFMA16(a, __builtin_bit_cast(bf16x8, of[s]), acc[ft]);
      }
    }
  }
  if (w == 0) STAMP(9);
  if (kh == 1) {
#pragma unroll
    for (int ft = 0; ft < 5; ++ft) *(LAS f32x4*)(L + L_RED + ((nt * 5 + ft) * 64 + lane) * 16) = acc[ft];
  }
  WAITL0(); BARRIER();
  unsigned hp[10];
  float mb = 0.f, inv_sd = 0.f;
  if (kh == 1) {
    const int rl = lane >> 1, grow = 8 * ((rl >> 2) & 3) + 4 * (rl >> 4) + (rl & 3);
    if (is_gu) {
      const unsigned vo = (unsigned)grow * (F * 2) + (lane & 1) * 16;
#pragma unroll 1
      for (int pc = nt; pc < 40; pc += 4) { const int m = pc >= 20, s = pc - 20 * m; glds16(vo, (const char*)(m ? p.wu : p.wg) + (size_t)s * 32 * F * 2 + gs * 32, lds0 + L_FW + pc * 1024); }
    } else if (is_dn) {
      const unsigned vo = (unsigned)grow * (D * 2) + (lane & 1) * 16;
#pragma unroll 1
      for (int pc = nt; pc < 54; pc += 4) glds16(vo, (const char*)p.wd + (size_t)pc * 32 * D * 2 + dn * 32, lds0 + L_FW + pc * 1024);
    }
  } else {
    float s1 = 0.f, s2 = 0.f, s3 = 0.f;
#pragma unroll
    for (int ft = 0; ft < 5; ++ft) {
      const f32x4 o2 = *(const LAS f32x4*)(L + L_RED + ((nt * 5 + ft) * 64 + lane) * 16);
      const unsigned a01 = pk2(acc[ft][0] + o2[0], acc[ft][1] + o2[1]), a23 = pk2(acc[ft][2] + o2[2], acc[ft][3] + o2[3]);
      const unsigned h01 = pk2(bf2f(xr[ft][0]) + bf2f(a01), bfhi(xr[ft][0]) + bfhi(a01)), h23 = pk2(bf2f(xr[ft][1]) + bf2f(a23), bfhi(xr[ft][1]) + bfhi(a23));
      hp[2 * ft] = h01; hp[2 * ft + 1] = h23;
      __builtin_amdgcn_raw_buffer_store_b64((u32x2){h01, h23}, rws, WS_H + (unsigned)tok * 1280 + (80 * cc + 16 * ft + 4 * fg) * 2, 0, SC1);
      s1 = dot2(h01, 0x3f803f80u, s1); s1 = dot2(h23, 0x3f803f80u, s1);
      s2 = dot2(h01, h01, s2); s2 = dot2(h23, h23, s2);
      s3 = dot2(h01, wrv[ft][0], s3); s3 = dot2(h23, wrv[ft][1], s3);
    }
    s1 = swap_add(swap16_add(s1)); s2 = swap_add(swap16_add(s2)); s3 = swap_add(swap16_add(s3));
    if (lane < 16) __builtin_amdgcn_raw_buffer_store_b128((u32x4){__float_as_uint(s1), __float_as_uint(s2), __float_as_uint(s3), tag32}, rws, WS_STATS + ((R * 8 + cc) * 64 + 16 * nt + l15) * 16, 0, SC1);
    if (w == 0) STAMP(10);
    float t1, t2, t3;
    {
      const unsigned so = WS_STATS + ((R * 8 + fg) * 64 + 16 * nt + l15) * 16;
      unsigned spins = 0;
      for (;;) {
        const u32x4 ga = __builtin_amdgcn_raw_buffer_load_b128(rws, so, 0, SC1), gb = __builtin_amdgcn_raw_buffer_load_b128(rws, so + 4 * 1024, 0, SC1);
        t1 = __uint_as_float(ga[0]) + __uint_as_float(gb[0]); t2 = __uint_as_float(ga[1]) + __uint_as_float(gb[1]); t3 = __uint_as_float(ga[2]) + __uint_as_float(gb[2]);
        if (__all(ga[3] == tag32 && gb[3] == tag32)) break;
        if (++spins > SPIN_CAP) break;
        __builtin_amdgcn_s_sleep(1);
      }
    }
    t1 = swap_add(swap16_add(t1)); t2 = swap_add(swap16_add(t2)); t3 = swap_add(swap16_add(t3));
    const float mean = t1 * (1.f / D);
    const float var = fmaxf(t2 * (1.f / D) - mean * mean, 0.f);
    mb = rbf(mean);
    const float sd = rbf(sqrtf(rbf(rbf(var) + 1e-5f)));
    inv_sd = 1.f / sd;
    const unsigned rb = pk2((t3 - mb * swr) * inv_sd, 0.f) & 0xffffu;
    const unsigned key = ((rb ^ ((rb & 0x8000u) ? 0xffffu : 0x8000u)) << 16) | (unsigned)(2047 - tok);
    if (lane < 16) {
      *(LAS unsigned*)(L + L_KEY + (16 * nt + l15) * 4) = key;
      *(LAS unsigned*)(L + L_MS + (16 * nt + l15) * 4) = (__float_as_uint(mb) & 0xffff0000u) | (__float_as_uint(sd) >> 16);
    }
    WAITV(0);
    if (w == 0) STAMP(11);
  }
  WAITL0(); BARRIER();
  if (w == 0) {
    unsigned k = *(const LAS unsigned*)(L + L_KEY + lane * 4), m = 0;
    for (int i = 0; i <= cc; ++i) { m = wave_umax(k); if (k == m) k = 0; }
    const unsigned ms = *(const LAS unsigned*)(L + L_MS + ((2047 - (int)(m & 0x7ff)) & 63) * 4);
    if (lane == 0) __builtin_amdgcn_raw_buffer_store_b128((u32x4){m, tag2[0], ms, tag2[1]}, rws, WS_CAND + (R * 8 + cc) * 16, 0, SC1);
    STAMP(12);
    unsigned k0, k1, k2, k3, m0, m1, m2, m3;
    unsigned spins = 0;
    for (;;) {
      const u32x4 a = __builtin_amdgcn_raw_buffer_load_b128(rws, WS_CAND + lane * 16, 0, SC1), bb = __builtin_amdgcn_raw_buffer_load_b128(rws, WS_CAND + 1024 + lane * 16, 0, SC1);
      const u32x4 c = __builtin_amdgcn_raw_buffer_load_b128(rws, WS_CAND + 2048 + lane * 16, 0, SC1), d = __builtin_amdgcn_raw_buffer_load_b128(rws, WS_CAND + 3072 + lane * 16, 0, SC1);
      k0 = a[0]; k1 = bb[0]; k2 = c[0]; k3 = d[0]; m0 = a[2]; m1 = bb[2]; m2 = c[2]; m3 = d[2];
      const bool ok = a[1] == tag2[0] && a[3] == tag2[1] && bb[1] == tag2[0] && bb[3] == tag2[1] && c[1] == tag2[0] && c[3] == tag2[1] && d[1] == tag2[0] && d[3] == tag2[1];
      if (__all(ok)) break;
      if (++spins > SPIN_CAP) break;
      __builtin_amdgcn_s_sleep(1);
    }
    STAMP(13);
#pragma unroll 1
    for (int i = 0; i < 8; ++i) {
      const unsigned lm = max(max(k0, k1), max(k2, k3));
      const unsigned g = wave_umax(lm);
      const unsigned ms2 = k0 == g ? m0 : (k1 == g ? m1 : (k2 == g ? m2 : m3));
      if (lm == g) { *(LAS unsigned*)(L + L_SEL + i * 4) = g; *(LAS unsigned*)(L + L_SEL + 32 + i * 4) = ms2; }
      k0 = k0 == g ? 0 : k0; k1 = k1 == g ? 0 : k1; k2 = k2 == g ? 0 : k2; k3 = k3 == g ? 0 : k3;
    }
    if (b == 0 && lane == 0) __builtin_amdgcn_raw_buffer_store_b64(tag2, rws, WS_EPOCH, 0, SC1);
    STAMP(14);
  }
  if (kh == 1) WAITV(0);
  WAITL0(); BARRIER();

  const unsigned selk = *(const LAS unsigned*)(L + L_SEL + (lane & 7) * 4), selm = *(const LAS unsigned*)(L + L_SEL + 32 + (lane & 7) * 4);
  auto out_rows = [&]() {
    if (kh == 0) {
      bool sel = false;
#pragma unroll
      for (int i = 0; i < 8; ++i) sel |= (2047 - (int)(__builtin_amdgcn_readlane((int)selk, i) & 0x7ff)) == tok;
      if (!sel) {
#pragma unroll
        for (int ft = 0; ft < 5; ++ft) {
          unsigned r2[2];
#pragma unroll
          for (int e = 0; e < 2; ++e) {
            const unsigned hv = hp[2 * ft + e];
            const unsigned d2 = pk2(bf2f(hv) - mb, bfhi(hv) - mb);
            const unsigned n2 = pk2(bf2f(d2) * inv_sd, bfhi(d2) * inv_sd);
            r2[e] = pk2(bf2f(hv) + bf2f(n2), bfhi(hv) + bfhi(n2));
          }
          *(u32x2*)(p.out + (size_t)tok * D + 80 * cc + 16 * ft + 4 * fg) = (u32x2){r2[0], r2[1]};
        }
      }
    }
  };
  if (is_gu) {
    {
      const unsigned kk = *(const LAS unsigned*)(L + L_SEL + w * 4), ms = *(const LAS unsigned*)(L + L_SEL + 32 + w * 4);
      const int row = 2047 - (int)(kk & 0x7ff);
      const float m = __uint_as_float(ms & 0xffff0000u), iv = 1.f / __uint_as_float(ms << 16);
      const u32x4 h0 = __builtin_amdgcn_raw_buffer_load_b128(rws, WS_H + (unsigned)row * 1280 + lane * 16, 0, SC1);
      const u32x4 h1 = __builtin_amdgcn_raw_buffer_load_b128(rws, WS_H + (unsigned)row * 1280 + 1024 + (lane & 15) * 16, 0, SC1);
      u32x4 n0, n1;
#pragma unroll
      for (int e = 0; e < 4; ++e) {
        const unsigned d0 = pk2(bf2f(h0[e]) - m, bfhi(h0[e]) - m), d1 = pk2(bf2f(h1[e]) - m, bfhi(h1[e]) - m);
        n0[e] = pk2(bf2f(d0) * iv, bfhi(d0) * iv); n1[e] = pk2(bf2f(d1) * iv, bfhi(d1) * iv);
      }
      *(LAS u32x4*)(L + L_XS + w * 1296 + lane * 16) = n0;
      if (lane < 16) *(LAS u32x4*)(L + L_XS + w * 1296 + 1024 + lane * 16) = n1;
    }
    WAITL0(); BARRIER();
    if (w == 0) STAMP(15);
    {
      const int m = w & 1, kq = w >> 1;
      f32x4 a4 = {0, 0, 0, 0};
#pragma unroll
      for (int i = 0; i < 5; ++i) {
        const int s = 5 * kq + i;
        const LAS char* ap = L + L_FW + (m * 20 + s) * 1024 + lane * 8;
        const bf16x8 af = cat8(trd(ap), trd(ap + 512));
        const bf16x8 bfv = *(const LAS bf16x8*)(L + L_XS + (lane & 7) * 1296 + s * 64 + fg * 16);
        a4 = MFMA16(af, bfv, a4);
      }
      *(LAS f32x4*)(L + L_FRED + (w * 64 + lane) * 16) = a4;
    }
    WAITL0(); BARRIER();
    if (w == 0) {
      f32x4 g4 = {0, 0, 0, 0}, u4 = {0, 0, 0, 0};
#pragma unroll
      for (int q = 0; q < 4; ++q) { g4 += *(const LAS f32x4*)(L + L_FRED + ((2 * q) * 64 + lane) * 16); u4 += *(const LAS f32x4*)(L + L_FRED + ((2 * q + 1) * 64 + lane) * 16); }
      float a[4];
#pragma unroll
      for (int e = 0; e < 4; ++e) { const float g = rbf(g4[e]), u = rbf(u4[e]); const float sg = rbf(g / (1.f + __expf(-g))); a[e] = sg * u; }
      if (l15 < 8) __builtin_amdgcn_raw_buffer_store_b128((u32x4){pk2(a[0], a[1]), tag32, pk2(a[2], a[3]), tag32}, rws, WS_ACT + (((gs >> 1) * 4 + 2 * (gs & 1) + (fg >> 1)) * 8 + l15) * 32 + (fg & 1) * 16, 0, SC1);
      STAMP(16);
    }
    out_rows();
  } else if (is_dn) {
    out_rows();
    const int row = 2047 - (int)(selk & 0x7ff);
    const u32x2 hv = __builtin_amdgcn_raw_buffer_load_b64(rws, WS_H + (unsigned)row * 1280 + (16 * dn + 4 * fg) * 2, 0, SC1);
    f32x4 a4 = {0, 0, 0, 0};
    {
      u32x4 g0[7], g1[7];
      unsigned spins = 0;
      for (;;) {
        bool ok = true;
#pragma unroll
        for (int i = 0; i < 7; ++i) {
          const int s = min(w + 8 * i, 53);
          const unsigned ao = WS_ACT + ((s * 4 + fg) * 8 + (lane & 7)) * 32;
          g0[i] = __builtin_amdgcn_raw_buffer_load_b128(rws, ao, 0, SC1); g1[i] = __builtin_amdgcn_raw_buffer_load_b128(rws, ao + 16, 0, SC1);
        }
#pragma unroll
        for (int i = 0; i < 7; ++i) ok = ok && g0[i][1] == tag32 && g0[i][3] == tag32 && g1[i][1] == tag32 && g1[i][3] == tag32;
        if (__all(ok)) break;
        if (++spins > SPIN_CAP) break;
        __builtin_amdgcn_s_sleep(1);
      }
      if (w == 0) STAMP(17);
#pragma unroll
      for (int i = 0; i < 7; ++i) {
        const int s = w + 8 * i;
        if (s < 54) {
          const LAS char* ap = L + L_FW + s * 1024 + lane * 8;
          const bf16x8 af = cat8(trd(ap), trd(ap + 512));
          a4 = MFMA16(af, __builtin_bit_cast(bf16x8, (u32x4){g0[i][0], g0[i][2], g1[i][0], g1[i][2]}), a4);
        }
      }
    }
    *(LAS f32x4*)(L + L_FRED + (w * 64 + lane) * 16) = a4;
    WAITL0(); BARRIER();
    if (w == 0) {
      f32x4 y = {0, 0, 0, 0};
#pragma unroll
      for (int q = 0; q < 8; ++q) y += *(const LAS f32x4*)(L + L_FRED + (q * 64 + lane) * 16);
      const unsigned rk = selk >> 16, rbits = (rk & 0x8000u) ? (rk ^ 0x8000u) : (rk ^ 0xffffu);
      const float rv = bf2f(rbits), sig = rbf(1.f / (1.f + __expf(-rv)));
      const float m = __uint_as_float(selm & 0xffff0000u), iv = 1.f / __uint_as_float(selm << 16);
      unsigned r2[2];
#pragma unroll
      for (int e = 0; e < 2; ++e) {
        const unsigned d2 = pk2(bf2f(hv[e]) - m, bfhi(hv[e]) - m);
        const unsigned n2 = pk2(bf2f(d2) * iv, bfhi(d2) * iv);
        const unsigned y2 = pk2(rbf(y[2 * e]) * sig, rbf(y[2 * e + 1]) * sig);
        const unsigned t2 = pk2(bf2f(n2) + bf2f(y2), bfhi(n2) + bfhi(y2));
        r2[e] = pk2(bf2f(hv[e]) + bf2f(t2), bfhi(hv[e]) + bfhi(t2));
      }
      if (l15 < 8) *(u32x2*)(p.out + (size_t)row * D + 16 * dn + 4 * fg) = (u32x2){r2[0], r2[1]};
      STAMP(18);
    }
  } else {
    out_rows();
  }
  if (w == 0) STAMP(19);
  STAMP_FLUSH();
}

__global__ void empty_kernel() {}
extern "C" void kernel_launch(void* const* d_in, const int* in_sizes, int n_in, void* d_out, int out_size, void* d_ws, size_t ws_size, hipStream_t stream) {
  Params p;
  p.x = (const u16*)d_in[0]; p.wqkv = (const u16*)d_in[1]; p.wo = (const u16*)d_in[2]; p.wr = (const u16*)d_in[4];
  p.wg = (const u16*)d_in[5]; p.wu = (const u16*)d_in[6]; p.wd = (const u16*)d_in[7]; p.out = (u16*)d_out; p.ws = (unsigned char*)d_ws;
  fused_kernel<<<256, 512, 0, stream>>>(p);
  empty_kernel<<<1, 64, 0, stream>>>();

}
```

```cpp
#include <hip/hip_runtime.h>
#include <stdint.h>

typedef unsigned short u16;
typedef short bf16x8 __attribute__((ext_vector_type(8)));
typedef short bf16x4 __attribute__((ext_vector_type(4)));
typedef __bf16 bf2 __attribute__((ext_vector_type(2)));
typedef float f32x16 __attribute__((ext_vector_type(16)));
typedef float f32x4 __attribute__((ext_vector_type(4)));
typedef float f32x2 __attribute__((ext_vector_type(2)));
typedef unsigned u32x4 __attribute__((ext_vector_type(4)));
typedef unsigned u32x2 __attribute__((ext_vector_type(2)));
typedef unsigned long long u64;
#define LAS __attribute__((address_space(3)))
#define SBAR() __builtin_amdgcn_sched_barrier(0)
#define BARRIER() asm volatile("s_barrier" ::: "memory")
#define WAITL0() asm volatile("s_waitcnt lgkmcnt(0)" ::: "memory")
#define WAITV(n) asm volatile("s_waitcnt vmcnt(" #n ")" ::: "memory")

constexpr int S = 2048, D = 640, F = 1728;
constexpr int SC1 = 16;

constexpr unsigned WS_EPOCH = 0;
constexpr unsigned WS_FLAGA = 4096;
constexpr unsigned WS_FLAGO = WS_FLAGA + 32768;
constexpr unsigned WS_CAND = WS_FLAGO + 16384;
constexpr unsigned WS_ACT = WS_CAND + 4096;
constexpr unsigned WS_STATS = 131072;
constexpr unsigned WS_QKV = 524288;
constexpr unsigned WS_O = WS_QKV + 480 * 16384;
constexpr unsigned WS_H = WS_O + S * D * 2;
#define STAMP_OFF (200u << 20)
#define NSTAMP 32

constexpr int L_SLOT = 32768, L_W = 16384;
constexpr int PL = 4128;
constexpr int L_K = 0, L_V = 5 * PL, L_ONE = 10 * PL;
constexpr int L_WO = 43008;
constexpr int L_RED = 0;
constexpr int L_FW = 43008;
constexpr int L_XS = 0;
constexpr int L_FRED = 12288;
constexpr int L_MISC = 147456;
constexpr int L_RS = L_MISC;
constexpr int L_CS = L_MISC + 1024;
constexpr int L_EP = L_MISC + 1536;
constexpr int L_KEY = L_MISC + 2048;
constexpr int L_MS = L_MISC + 2304;
constexpr int L_SEL = L_MISC + 2560;
constexpr int L_STAMP = L_MISC + 4096;
constexpr int LDS_BYTES = 163840;

struct Params { const u16* x; const u16* wqkv; const u16* wo; const u16* wr; const u16* wg; const u16* wu; const u16* wd; u16* out; unsigned char* ws; };

__device__ __forceinline__ float bf2f(unsigned v) { return __uint_as_float(v << 16); }
__device__ __forceinline__ float bfhi(unsigned v) { return __uint_as_float(v & 0xffff0000u); }
__device__ __forceinline__ unsigned pk2(float a, float b) { f32x2 f = {a, b}; return __builtin_bit_cast(unsigned, __builtin_convertvector(f, bf2)); }
__device__ __forceinline__ float rbf(float f) { return __uint_as_float(pk2(f, 0.f) << 16); }
__device__ __forceinline__ float dot2(unsigned a, unsigned b, float c) { return __builtin_amdgcn_fdot2_f32_bf16(__builtin_bit_cast(bf2, a), __builtin_bit_cast(bf2, b), c, false); }
__device__ __forceinline__ float swap_add(float v) { auto r = __builtin_amdgcn_permlane32_swap(__float_as_uint(v), __float_as_uint(v), false, false); return __uint_as_float(r[0]) + __uint_as_float(r[1]); }
__device__ __forceinline__ float swap16_add(float v) { auto r = __builtin_amdgcn_permlane16_swap(__float_as_uint(v), __float_as_uint(v), false, false); return __uint_as_float(r[0]) + __uint_as_float(r[1]); }
__device__ __forceinline__ void glds16(unsigned voff, const void* sbase, unsigned lds) {
  asm volatile("s_mov_b32 m0, %2\n\ts_nop 0\n\tglobal_load_lds_dwordx4 %0, %1" ::"v"(voff), "s"(sbase), "s"(lds) : "memory");
}
__device__ __forceinline__ void glds16c(unsigned voff, const void* sbase, unsigned lds) {
  asm volatile("s_mov_b32 m0, %2\n\ts_nop 0\n\tglobal_load_lds_dwordx4 %0, %1 sc1" ::"v"(voff), "s"(sbase), "s"(lds) : "memory");
}
__device__ __forceinline__ bf16x4 trd(const LAS char* p) { return __builtin_amdgcn_ds_read_tr16_b64_v4i16((LAS bf16x4*)p); }
__device__ __forceinline__ bf16x8 cat8(bf16x4 a, bf16x4 b) { return (bf16x8){a[0], a[1], a[2], a[3], b[0], b[1], b[2], b[3]}; }
#define MFMA32(a, b, c) __builtin_amdgcn_mfma_f32_32x32x16_bf16(a, b, c, 0, 0, 0)
#define MFMA32K8(a, b, c) __builtin_amdgcn_mfma_f32_32x32x8bf16_1k(a, b, c, 0, 0, 0)
#define MFMA16(a, b, c) __builtin_amdgcn_mfma_f32_16x16x32_bf16(a, b, c, 0, 0, 0)
template <int CTRL, int RMASK> __device__ __forceinline__ unsigned dppmax(unsigned v) { return max(v, (unsigned)__builtin_amdgcn_update_dpp(0, (int)v, CTRL, RMASK, 0xf, false)); }
__device__ __forceinline__ unsigned wave_umax(unsigned v) {
  v = dppmax<0x111, 0xf>(v); v = dppmax<0x112, 0xf>(v); v = dppmax<0x114, 0xf>(v); v = dppmax<0x118, 0xf>(v);
  v = dppmax<0x142, 0xa>(v); v = dppmax<0x143, 0xc>(v);
  return (unsigned)__builtin_amdgcn_readlane((int)v, 63);
}

template <int CTRL> __device__ __forceinline__ float dppadd(float v) { return v + __int_as_float(__builtin_amdgcn_update_dpp(0, __float_as_int(v), CTRL, 0xf, 0xf, false)); }
__device__ __forceinline__ float wave_fsum(float v) { v = dppadd<0x128>(v); v = dppadd<0x124>(v); v = dppadd<0x122>(v); v = dppadd<0x121>(v); return swap_add(swap16_add(v)); }

#define STAMP(k) do {} while (0)
#define STAMP_INIT() do {} while (0)
#define STAMP_FLUSH() do {} while (0)

constexpr unsigned SPIN_CAP = 1u << 16;

typedef unsigned u32x16 __attribute__((ext_vector_type(16)));
__global__ __launch_bounds__(512, 2) void fused_kernel(Params p_unused) {
  u64 ka_t1, ka_t2;
  asm volatile("s_memrealtime %0\n\ts_waitcnt lgkmcnt(0)" : "=s"(ka_t1));
  Params p;
  {
    const void* kp = (const void*)__builtin_amdgcn_kernarg_segment_ptr();
    u32x16 a; u32x2 c;
    asm volatile("s_load_dwordx16 %0, %2, 0x0\n\ts_load_dwordx2 %1, %2, 0x40\n\ts_waitcnt lgkmcnt(0)" : "=&s"(a), "=&s"(c) : "s"(kp));
    auto mk = [](unsigned lo, unsigned hi) { return (void*)(((u64)hi << 32) | lo); };
    p.x = (const u16*)mk(a[0], a[1]); p.wqkv = (const u16*)mk(a[2], a[3]); p.wo = (const u16*)mk(a[4], a[5]); p.wr = (const u16*)mk(a[6], a[7]);
    p.wg = (const u16*)mk(a[8], a[9]); p.wu = (const u16*)mk(a[10], a[11]); p.wd = (const u16*)mk(a[12], a[13]); p.out = (u16*)mk(a[14], a[15]); p.ws = (unsigned char*)mk(c[0], c[1]);
  }
  asm volatile("s_memrealtime %0\n\ts_waitcnt lgkmcnt(0)" : "=s"(ka_t2));
  __shared__ __attribute__((aligned(16))) char smem[LDS_BYTES];
  LAS char* L = (LAS char*)smem;
  const unsigned lds0 = (unsigned)(uintptr_t)smem;
  const int tid = threadIdx.x, lane = tid & 63;
  const int w = __builtin_amdgcn_readfirstlane(tid >> 6);
  const int b = blockIdx.x, xcd = b & 7, bi = b >> 3;
  const int l31 = lane & 31, hh = lane >> 5, l15 = lane & 15, fg = lane >> 4;
  const __amdgpu_buffer_rsrc_t rws = __builtin_amdgcn_make_buffer_rsrc(p.ws, 0, 0x7fffffff, 0x00020000);
  const int aP = 4 * (xcd >> 1) + (bi >> 3), aj = 8 * (xcd & 1) + (bi & 7);
  const int hd = bi, pp = xcd;
  const int R = 4 * xcd + (bi >> 3), cc = bi & 7;
  const int gs = xcd * 14 + bi;
  const bool is_gu = bi < 14 && gs < 108;
  const int dn = xcd * 5 + (bi - 14);
  const bool is_dn = bi >= 14 && bi < 19;
  STAMP_INIT();

  if (aj < 15 && w >= 4) {
    const int lw = w - 4;
    const unsigned xsw = (unsigned)(4 * (lw & 1) + (lane >> 4));
    const unsigned voff_x = (unsigned)(lane >> 3) * 1280u + (((unsigned)(lane & 7) ^ xsw) << 4);
    const unsigned ch = (unsigned)(lane & 15) ^ (((unsigned)(lane >> 4) << 2) | (unsigned)lw);
    const unsigned voff_w = (unsigned)(lane >> 4) * 3840u + ch * 16u;
    const char* xb = (const char*)p.x + (size_t)(128 * aP + 8 * lw) * 1280;
    const char* wb = (const char*)p.wqkv + (size_t)(4 * lw) * 3840 + 256 * aj;
    const unsigned lbase = lds0 + lw * 1024;
    const int rot = ((bi & 7) * 5 / 4 + (bi >> 3) * 5 / 2) % 10;
    auto issue = [&](int s) {
      const unsigned sl = lbase + (s & 3) * L_SLOT;
      int ss = s + rot; if (ss >= 10) ss -= 10;
#pragma unroll
      for (int i = 0; i < 4; ++i) {
        glds16(voff_x, xb + (size_t)i * 32 * 1280 + ss * 128, sl + i * 4096);
        glds16(voff_w, wb + ((size_t)ss * 64 + i * 16) * 3840, sl + L_W + i * 4096);
      }
    };
    issue(0); issue(1); issue(2); issue(3);
    WAITV(24); BARRIER();
    if (w == 4) STAMP(1);
    WAITV(16); BARRIER();
#pragma unroll 1
    for (int s = 1; s <= 6; ++s) { issue(s + 3); WAITV(16); BARRIER(); }
    WAITV(8); BARRIER();
    WAITV(0); BARRIER();
    BARRIER();
    BARRIER();
  } else if (w < 4) {
    if (w == 0) {
      const u32x2 e = __builtin_amdgcn_raw_buffer_load_b64(rws, WS_EPOCH, 0, SC1);
      const u64 t = (((u64)e[1] << 32) | e[0]) + 1;
      if (lane == 0) *(LAS u32x2*)(L + L_EP) = (u32x2){(unsigned)t, (unsigned)(t >> 32)};
      __builtin_amdgcn_raw_buffer_store_b128((u32x4){0, 0, 0, 0}, rws, WS_STATS + (R * 8 + cc) * 1024 + lane * 16, 0, SC1);
      if (is_gu && lane < 32) __builtin_amdgcn_raw_buffer_store_b128((u32x4){0, 0, 0, 0}, rws, WS_ACT + (((gs >> 1) * 4 + 2 * (gs & 1) + (lane >> 4)) * 8 + (lane & 7)) * 32 + ((lane >> 3) & 1) * 16, 0, SC1);
    }
    if (aj < 15) {
      const int wm = w >> 1, wn = w & 1;
      unsigned xa[4], wa[4];
#pragma unroll
      for (int ks = 0; ks < 4; ++ks) xa[ks] = (unsigned)(64 * wm + l31) * 128u + ((((unsigned)(2 * ks + hh)) ^ (((unsigned)l31 >> 1) & 7u)) << 4);
      const unsigned xo0 = wn * 4096, xo1 = 4096 - xo0;
      {
        const unsigned blk = (lane >> 4) & 1, q = (lane & 15) >> 2, pq = lane & 3;
#pragma unroll
        for (int j = 0; j < 2; ++j)
#pragma unroll
          for (int t = 0; t < 2; ++t) {
            const unsigned jj = j ^ wm;
            const unsigned row = 8 * hh + 4 * t + q, chn = 4 * (2 * wn + jj) + 2 * blk + (pq >> 1), m = (q << 2) | (unsigned)(2 * hh + t);
            wa[j * 2 + t] = L_W + 256u * row + 16u * (chn ^ m) + 8u * (pq & 1);
          }
      }
      f32x16 a00 = {}, a01 = {}, a10 = {}, a11 = {};
      float ssum = 0.f, ssq = 0.f, csum = 0.f;
      bf16x8 xf0[2], wf0[2], xf1[2], wf1[2];
      const unsigned ONE2 = 0x3f803f80u;
      unsigned xb0[4], xb1[4], wb[4];
#define SETSLOT(so) do { _Pragma("unroll") for (int k_ = 0; k_ < 4; ++k_) { xb0[k_] = xa[k_] + (so) + xo0; xb1[k_] = xa[k_] + (so) + xo1; wb[k_] = wa[k_] + (so); } } while (0)
#define RD(XF, WF, ks) do { \
        XF[0] = *(const LAS bf16x8*)(L + xb0[ks]); XF[1] = *(const LAS bf16x8*)(L + xb1[ks]); \
        WF[0] = cat8(trd(L + wb[0] + (ks) * 4096), trd(L + wb[1] + (ks) * 4096)); \
        WF[1] = cat8(trd(L + wb[2] + (ks) * 4096), trd(L + wb[3] + (ks) * 4096)); } while (0)
#define MM(XF, WF) do { \
        a00 = MFMA32(WF[0], XF[0], a00); a01 = MFMA32(WF[1], XF[0], a01); \
        a10 = MFMA32(WF[0], XF[1], a10); a11 = MFMA32(WF[1], XF[1], a11); \
        { const u32x4 xs_ = __builtin_bit_cast(u32x4, XF[0]); const u32x4 ws_ = __builtin_bit_cast(u32x4, WF[0]); \
          _Pragma("unroll") for (int e_ = 0; e_ < 4; ++e_) { ssum = dot2(xs_[e_], ONE2, ssum); ssq = dot2(xs_[e_], xs_[e_], ssq); csum = dot2(ws_[e_], ONE2, csum); } } } while (0)
#define PINALL() asm volatile("" : "+v"(a00), "+v"(a01), "+v"(a10), "+v"(a11), "+v"(ssum), "+v"(ssq), "+v"(csum))
#define ILV() do { _Pragma("unroll") for (int g_ = 0; g_ < 4; ++g_) { \
        __builtin_amdgcn_sched_group_barrier(0x8, 1, 0); __builtin_amdgcn_sched_group_barrier(0x100, 2, 0); __builtin_amdgcn_sched_group_barrier(0x2, 3, 0); } } while (0)
#define STEP(XN, WN, ksn, XC, WC) do { RD(XN, WN, ksn); MM(XC, WC); ILV(); PINALL(); SBAR(); } while (0)
      SETSLOT(0);
      BARRIER();
      RD(xf0, wf0, 0);
      SBAR();
#pragma unroll 1
      for (int s = 0; s < 9; ++s) {
        STEP(xf1, wf1, 1, xf0, wf0);
        STEP(xf0, wf0, 2, xf1, wf1);
        STEP(xf1, wf1, 3, xf0, wf0);
        SETSLOT(((s + 1) & 3) * L_SLOT);
        WAITL0(); BARRIER();
        STEP(xf0, wf0, 0, xf1, wf1);
      }
      STEP(xf1, wf1, 1, xf0, wf0);
      STEP(xf0, wf0, 2, xf1, wf1);
      STEP(xf1, wf1, 3, xf0, wf0);
      MM(xf1, wf1); PINALL();
      if (w == 0) STAMP(2);
      ssum = swap_add(ssum); ssq = swap_add(ssq); csum = swap_add(csum);
      {
        const float mean = ssum * (1.f / D);
        const float var = fmaxf(ssq * (1.f / D) - mean * mean, 0.f);
        const float mb = rbf(mean), vb = rbf(var);
        const float sd = rbf(sqrtf(rbf(vb + 1e-5f)));
        if (lane < 32) {
          *(LAS f32x2*)(L + L_RS + (64 * wm + 32 * wn + l31) * 8) = (f32x2){mb, 1.f / sd};
          *(LAS float*)(L + L_CS + (64 * wn + 32 * wm + l31) * 4) = csum;
        }
      }
      WAITV(0); WAITL0(); BARRIER();
      const float qs = aj < 5 ? 0.22360679774997896f * 1.4426950408889634f : 1.f;
#pragma unroll
      for (int i = 0; i < 2; ++i) {
        const int tok = 64 * wm + 32 * (i ^ wn) + l31;
        const f32x2 rs = *(const LAS f32x2*)(L + L_RS + tok * 8);
        const float sc = rs[1] * qs, nm = -rs[0] * sc;
#pragma unroll
        for (int j = 0; j < 2; ++j) {
          const f32x16& a = i == 0 ? (j == 0 ? a00 : a01) : (j == 0 ? a10 : a11);
          const int f0 = 64 * wn + 32 * (j ^ wm) + 4 * hh;
          const unsigned go = WS_QKV + (unsigned)(32 * aj + (f0 >> 2)) * 16384u + (unsigned)(128 * aP + tok) * 8u;
#pragma unroll
          for (int rq = 0; rq < 4; ++rq) {
            const f32x4 cs = *(const LAS f32x4*)(L + L_CS + (f0 + 8 * rq) * 4);
            const float v0 = fmaf(a[rq * 4 + 0], sc, nm * cs[0]), v1 = fmaf(a[rq * 4 + 1], sc, nm * cs[1]);
            const float v2 = fmaf(a[rq * 4 + 2], sc, nm * cs[2]), v3 = fmaf(a[rq * 4 + 3], sc, nm * cs[3]);
            __builtin_amdgcn_raw_buffer_store_b64((u32x2){pk2(v0, v1), pk2(v2, v3)}, rws, go + rq * 32768, 0, SC1);
          }
        }
      }
      WAITV(0); BARRIER();
    } else {
      WAITV(0);
    }
  }
  if (aj >= 15) { WAITL0(); BARRIER(); }
  const u32x2 tag2 = *(const LAS u32x2*)(L + L_EP);
  const unsigned tag32 = tag2[0] | 1u;
  if (aj < 15 && tid == 0) __builtin_amdgcn_raw_buffer_store_b64(tag2, rws, WS_FLAGA + (aP * 16 + aj) * 128, 0, SC1);
  if (w == 0) STAMP(3);

  f32x16 o = (f32x16){};
  {
    const int t = w;
    if (tid < 4) *(LAS unsigned*)(L + L_ONE + tid * 4) = tid == 0 ? 0x00003f80u : 0u;
    {
      const int sec = lane < 4 ? 0 : (lane < 12 ? 1 : 2);
      const int k = lane < 4 ? lane : (lane < 12 ? lane - 4 : lane - 12);
      const int col = sec * 640 + 20 * hd + ((k & 1) ? 19 : 0);
      int Pn = sec == 0 ? 2 * pp + (k >> 1) : 2 * pp - 2 + (k >> 1);
      const bool need = lane < 20 && Pn >= 0;
      const unsigned fo = WS_FLAGA + (unsigned)((need ? Pn : 0) * 16 + (col >> 7)) * 128;
      unsigned spins = 0;
      for (;;) {
        const u32x2 f = __builtin_amdgcn_raw_buffer_load_b64(rws, fo, 0, SC1);
        if (__all(!need || (f[0] == tag2[0] && f[1] == tag2[1]))) break;
        if (++spins > SPIN_CAP) break;
        __builtin_amdgcn_s_sleep(1);
      }
    }
    asm volatile("" ::: "memory");
    if (w == 0) STAMP(4);
    {
      const char* qb = (const char*)p.ws + WS_QKV;
#pragma unroll
      for (int i = 0; i < 5; ++i) {
        const int pc = w + 8 * i, pl = pc >> 2, qt = pc & 3;
        const int tok0 = 256 * pp - 256 + 128 * qt;
        if (tok0 >= 0) glds16c(lane * 16, qb + (size_t)((pl < 5 ? 160 + 5 * hd + pl : 320 + 5 * hd + pl - 5)) * 16384 + tok0 * 8, lds0 + pl * PL + qt * 1024);
      }
    }
    bf16x8 qf0; bf16x4 qf1;
    {
      const unsigned qo = WS_QKV + (unsigned)(5 * hd) * 16384u + (unsigned)(256 * pp + 32 * t + l31) * 8u;
      const u32x2 a = __builtin_amdgcn_raw_buffer_load_b64(rws, qo + (2 * hh) * 16384, 0, SC1);
      const u32x2 c = __builtin_amdgcn_raw_buffer_load_b64(rws, qo + (2 * hh + 1) * 16384, 0, SC1);
      u32x2 z = __builtin_amdgcn_raw_buffer_load_b64(rws, qo + 4 * 16384, 0, SC1);
      if (hh) z = (u32x2){0, 0};
      qf0 = __builtin_bit_cast(bf16x8, (u32x4){a[0], a[1], c[0], c[1]});
      qf1 = __builtin_bit_cast(bf16x4, z);
    }
    WAITV(0); WAITL0(); BARRIER();
    if (w == 0) STAMP(5);
    {
#pragma unroll 1
      for (int pc = w; pc < 100; pc += 8) {
        const int n = pc * 64 + lane, rho = n / 10, ch = n - rho * 10;
        const int s = rho >> 5, tq = (rho >> 4) & 1, kg = (rho >> 2) & 3, q = rho & 3;
        const int cidx = 4 * s + kg, g2 = cidx / 5, c5 = cidx - 5 * g2;
        const int quad = 10 * g2 + ((tq ? 0x98732 : 0x46510) >> (4 * c5) & 15);
        glds16((unsigned)(4 * quad + q) * 1280u + ch * 16u, (const char*)p.wo + 160 * cc, lds0 + L_WO + pc * 1024);
      }
    }
    const int kad = L_K + l31 * 8;
    float negm;
    {
      const LAS char* kp = L + kad + (t + 8) * 256;
      const u32x2 k0 = *(const LAS u32x2*)(kp + (2 * hh) * PL), k1 = *(const LAS u32x2*)(kp + (2 * hh + 1) * PL), k2 = *(const LAS u32x2*)(kp + 4 * PL);
      const u32x4 q0 = __builtin_bit_cast(u32x4, qf0); const u32x2 q1 = __builtin_bit_cast(u32x2, qf1);
      float d = dot2(q0[0], k0[0], 0.f); d = dot2(q0[1], k0[1], d); d = dot2(q0[2], k1[0], d); d = dot2(q0[3], k1[1], d);
      d = dot2(q1[0], k2[0], d); d = dot2(q1[1], k2[1], d);
      negm = -swap_add(d);
    }
    const int vquad = 4 * ((lane >> 4) & 1) + (lane & 3);
    const int vad = vquad < 5 ? L_V + vquad * PL + (4 * hh + ((lane & 15) >> 2)) * 8 : L_ONE;
    const int vst = vquad < 5 ? 1 : 0;
    auto tile = [&](int kt, const f32x16& cin) {
      const LAS char* kp = L + kad + kt * 256;
      const bf16x4 ka = *(const LAS bf16x4*)(kp + (2 * hh) * PL), kb = *(const LAS bf16x4*)(kp + (2 * hh + 1) * PL), kc = *(const LAS bf16x4*)(kp + 4 * PL);
      const LAS char* vp = L + vad + vst * kt * 256;
      const bf16x8 v0 = cat8(trd(vp), trd(vp + vst * 64)), v1 = cat8(trd(vp + vst * 128), trd(vp + vst * 192));
      f32x16 s = MFMA32(cat8(ka, kb), qf0, cin);
      s = MFMA32K8(kc, qf1, s);
      u32x4 p0, p1;
#pragma unroll
      for (int e = 0; e < 4; ++e) {
        p0[e] = pk2(__builtin_amdgcn_exp2f(s[2 * e]), __builtin_amdgcn_exp2f(s[2 * e + 1]));
        p1[e] = pk2(__builtin_amdgcn_exp2f(s[8 + 2 * e]), __builtin_amdgcn_exp2f(s[8 + 2 * e + 1]));
      }
      o = MFMA32(v0, __builtin_bit_cast(bf16x8, p0), o);
      o = MFMA32(v1, __builtin_bit_cast(bf16x8, p1), o);
    };
    f32x16 cm;
#pragma unroll
    for (int r = 0; r < 16; ++r) { const int kr = (r & 3) + 8 * (r >> 2) + 4 * hh; cm[r] = kr <= l31 ? negm : -INFINITY; }
    tile(t + 8, cm);
#pragma unroll
    for (int r = 0; r < 16; ++r) cm[r] = negm;
    const int lo = pp >= 1 ? t + 1 : 8;
#pragma unroll 1
    for (int kt = t + 7; kt >= lo; --kt) tile(kt, cm);
    if (pp >= 1) {
#pragma unroll
      for (int r = 0; r < 16; ++r) { const int kr = (r & 3) + 8 * (r >> 2) + 4 * hh; cm[r] = kr > l31 ? negm : -INFINITY; }
      tile(t, cm);
    }
    if (w == 0) STAMP(6);
    auto r = __builtin_amdgcn_permlane32_swap(__float_as_uint(o[8]), __float_as_uint(o[8]), false, false);
    const float inv = 1.f / __uint_as_float(r[1]);
    u32x4 c0;
    c0[0] = pk2(o[0] * inv, o[1] * inv); c0[1] = pk2(o[2] * inv, o[3] * inv); c0[2] = pk2(o[4] * inv, o[5] * inv); c0[3] = pk2(o[6] * inv, o[7] * inv);
    const u32x2 c1 = (u32x2){pk2(o[8] * inv, o[9] * inv), pk2(o[10] * inv, o[11] * inv)};
    const int tt = pp * 16 + 2 * t + (l31 >> 4);
    const unsigned base = WS_O + (unsigned)tt * 80 * 256 + (l31 & 15) * 16 + (unsigned)(5 * (hd >> 1)) * 256;
    __builtin_amdgcn_raw_buffer_store_b128(c0, rws, base + (2 * (hd & 1) + hh) * 256, 0, SC1);
    if (hh == 0) __builtin_amdgcn_raw_buffer_store_b64(c1, rws, base + 4 * 256 + (hd & 1) * 8, 0, SC1);
    WAITV(0);
    if (lane == 0) __builtin_amdgcn_raw_buffer_store_b64(tag2, rws, WS_FLAGO + (((pp * 8 + t) * 2 + (hd >> 4)) * 16 + (hd & 15)) * 8, 0, SC1);
  }
  if (w == 0) STAMP(7);
  BARRIER();

  const int nt = w & 3, kh = w >> 2;
  const int tok = 64 * R + 16 * nt + l15;
  u32x2 xr[5], wrv[5];
  float swr = 0.f;
  if (kh == 0) {
#pragma unroll
    for (int ft = 0; ft < 5; ++ft) {
      xr[ft] = *(const u32x2*)(p.x + (size_t)tok * D + 80 * cc + 16 * ft + 4 * fg);
      wrv[ft] = *(const u32x2*)(p.wr + 80 * cc + 16 * ft + 4 * fg);
    }
    const unsigned* wr32 = (const unsigned*)p.wr;
#pragma unroll
    for (int i = 0; i < 5; ++i) swr = dot2(wr32[lane + 64 * i], 0x3f803f80u, swr);
    swr = wave_fsum(swr);
  }
  f32x4 acc[5];
#pragma unroll
  for (int ft = 0; ft < 5; ++ft) acc[ft] = (f32x4){0, 0, 0, 0};
  {
    const int tg = 4 * R + nt;
    const unsigned fo = WS_FLAGO + ((((tg >> 4) * 8 + ((tg >> 1) & 7)) * 2 + kh) * 16 + l15) * 8;
    unsigned spins = 0;
    for (;;) {
      const u32x2 f = __builtin_amdgcn_raw_buffer_load_b64(rws, fo, 0, SC1);
      if (__all(f[0] == tag2[0] && f[1] == tag2[1])) break;
      if (++spins > SPIN_CAP) break;
      __builtin_amdgcn_s_sleep(1);
    }
    asm volatile("" ::: "memory");
    if (w == 0) STAMP(8);
    u32x4 of[10];
#pragma unroll
    for (int s = 0; s < 10; ++s) of[s] = __builtin_amdgcn_raw_buffer_load_b128(rws, WS_O + (unsigned)(tg * 80 + 4 * (10 * kh + s)) * 256 + lane * 16, 0, SC1);
    const LAS char* wp = L + L_WO + (unsigned)(kh * 10 * 32 + fg * 4 + ((lane & 15) >> 2)) * 160 + (lane & 3) * 8;
#pragma unroll
    for (int s = 0; s < 10; ++s) {
#pragma unroll
      for (int ft = 0; ft < 5; ++ft) {
        const bf16x8 a = cat8(trd(wp + (s * 32) * 160 + ft * 32), trd(wp + (s * 32 + 16) * 160 + ft * 32));
        acc[ft] = MFMA16(a, __builtin_bit_cast(bf16x8, of[s]), acc[ft]);
      }
    }
  }
  if (w == 0) STAMP(9);
  if (kh == 1) {
#pragma unroll
    for (int ft = 0; ft < 5; ++ft) *(LAS f32x4*)(L + L_RED + ((nt * 5 + ft) * 64 + lane) * 16) = acc[ft];
  }
  WAITL0(); BARRIER();
  unsigned hp[10];
  float mb = 0.f, inv_sd = 0.f;
  if (kh == 1) {
    const int rl = lane >> 1, grow = 8 * ((rl >> 2) & 3) + 4 * (rl >> 4) + (rl & 3);
    if (is_gu) {
      const unsigned vo = (unsigned)grow * (F * 2) + (lane & 1) * 16;
#pragma unroll 1
      for (int pc = nt; pc < 40; pc += 4) { const int m = pc >= 20, s = pc - 20 * m; glds16(vo, (const char*)(m ? p.wu : p.wg) + (size_t)s * 32 * F * 2 + gs * 32, lds0 + L_FW + pc * 1024); }
    } else if (is_dn) {
      const unsigned vo = (unsigned)grow * (D * 2) + (lane & 1) * 16;
#pragma unroll 1
      for (int pc = nt; pc < 54; pc += 4) glds16(vo, (const char*)p.wd + (size_t)pc * 32 * D * 2 + dn * 32, lds0 + L_FW + pc * 1024);
    }
  } else {
    float s1 = 0.f, s2 = 0.f, s3 = 0.f;
#pragma unroll
    for (int ft = 0; ft < 5; ++ft) {
      const f32x4 o2 = *(const LAS f32x4*)(L + L_RED + ((nt * 5 + ft) * 64 + lane) * 16);
      const unsigned a01 = pk2(acc[ft][0] + o2[0], acc[ft][1] + o2[1]), a23 = pk2(acc[ft][2] + o2[2], acc[ft][3] + o2[3]);
      const unsigned h01 = pk2(bf2f(xr[ft][0]) + bf2f(a01), bfhi(xr[ft][0]) + bfhi(a01)), h23 = pk2(bf2f(xr[ft][1]) + bf2f(a23), bfhi(xr[ft][1]) + bfhi(a23));
      hp[2 * ft] = h01; hp[2 * ft + 1] = h23;
      __builtin_amdgcn_raw_buffer_store_b64((u32x2){h01, h23}, rws, WS_H + (unsigned)tok * 1280 + (80 * cc + 16 * ft + 4 * fg) * 2, 0, SC1);
      s1 = dot2(h01, 0x3f803f80u, s1); s1 = dot2(h23, 0x3f803f80u, s1);
      s2 = dot2(h01, h01, s2); s2 = dot2(h23, h23, s2);
      s3 = dot2(h01, wrv[ft][0], s3); s3 = dot2(h23, wrv[ft][1], s3);
    }
    s1 = swap_add(swap16_add(s1)); s2 = swap_add(swap16_add(s2)); s3 = swap_add(swap16_add(s3));
    if (lane < 16) __builtin_amdgcn_raw_buffer_store_b128((u32x4){__float_as_uint(s1), __float_as_uint(s2), __float_as_uint(s3), tag32}, rws, WS_STATS + ((R * 8 + cc) * 64 + 16 * nt + l15) * 16, 0, SC1);
    if (w == 0) STAMP(10);
    float t1, t2, t3;
    {
      const unsigned so = WS_STATS + ((R * 8 + fg) * 64 + 16 * nt + l15) * 16;
      unsigned spins = 0;
      for (;;) {
        const u32x4 ga = __builtin_amdgcn_raw_buffer_load_b128(rws, so, 0, SC1), gb = __builtin_amdgcn_raw_buffer_load_b128(rws, so + 4 * 1024, 0, SC1);
        t1 = __uint_as_float(ga[0]) + __uint_as_float(gb[0]); t2 = __uint_as_float(ga[1]) + __uint_as_float(gb[1]); t3 = __uint_as_float(ga[2]) + __uint_as_float(gb[2]);
        if (__all(ga[3] == tag32 && gb[3] == tag32)) break;
        if (++spins > SPIN_CAP) break;
        __builtin_amdgcn_s_sleep(1);
      }
    }
    t1 = swap_add(swap16_add(t1)); t2 = swap_add(swap16_add(t2)); t3 = swap_add(swap16_add(t3));
    const float mean = t1 * (1.f / D);
    const float var = fmaxf(t2 * (1.f / D) - mean * mean, 0.f);
    mb = rbf(mean);
    const float sd = rbf(sqrtf(rbf(rbf(var) + 1e-5f)));
    inv_sd = 1.f / sd;
    const unsigned rb = pk2((t3 - mb * swr) * inv_sd, 0.f) & 0xffffu;
    const unsigned key = ((rb ^ ((rb & 0x8000u) ? 0xffffu : 0x8000u)) << 16) | (unsigned)(2047 - tok);
    if (lane < 16) {
      *(LAS unsigned*)(L + L_KEY + (16 * nt + l15) * 4) = key;
      *(LAS unsigned*)(L + L_MS + (16 * nt + l15) * 4) = (__float_as_uint(mb) & 0xffff0000u) | (__float_as_uint(sd) >> 16);
    }
    WAITV(0);
    if (w == 0) STAMP(11);
  }
  WAITL0(); BARRIER();
  if (w == 0) {
    unsigned k = *(const LAS unsigned*)(L + L_KEY + lane * 4), m = 0;
    for (int i = 0; i <= cc; ++i) { m = wave_umax(k); if (k == m) k = 0; }
    const unsigned ms = *(const LAS unsigned*)(L + L_MS + ((2047 - (int)(m & 0x7ff)) & 63) * 4);
    if (lane == 0) __builtin_amdgcn_raw_buffer_store_b128((u32x4){m, tag2[0], ms, tag2[1]}, rws, WS_CAND + (R * 8 + cc) * 16, 0, SC1);
    STAMP(12);
    unsigned k0, k1, k2, k3, m0, m1, m2, m3;
    unsigned spins = 0;
    for (;;) {
      const u32x4 a = __builtin_amdgcn_raw_buffer_load_b128(rws, WS_CAND + lane * 16, 0, SC1), bb = __builtin_amdgcn_raw_buffer_load_b128(rws, WS_CAND + 1024 + lane * 16, 0, SC1);
      const u32x4 c = __builtin_amdgcn_raw_buffer_load_b128(rws, WS_CAND + 2048 + lane * 16, 0, SC1), d = __builtin_amdgcn_raw_buffer_load_b128(rws, WS_CAND + 3072 + lane * 16, 0, SC1);
      k0 = a[0]; k1 = bb[0]; k2 = c[0]; k3 = d[0]; m0 = a[2]; m1 = bb[2]; m2 = c[2]; m3 = d[2];
      const bool ok = a[1] == tag2[0] && a[3] == tag2[1] && bb[1] == tag2[0] && bb[3] == tag2[1] && c[1] == tag2[0] && c[3] == tag2[1] && d[1] == tag2[0] && d[3] == tag2[1];
      if (__all(ok)) break;
      if (++spins > SPIN_CAP) break;
      __builtin_amdgcn_s_sleep(1);
    }
    STAMP(13);
#pragma unroll 1
    for (int i = 0; i < 8; ++i) {
      const unsigned lm = max(max(k0, k1), max(k2, k3));
      const unsigned g = wave_umax(lm);
      const unsigned ms2 = k0 == g ? m0 : (k1 == g ? m1 : (k2 == g ? m2 : m3));
      if (lm == g) { *(LAS unsigned*)(L + L_SEL + i * 4) = g; *(LAS unsigned*)(L + L_SEL + 32 + i * 4) = ms2; }
      k0 = k0 == g ? 0 : k0; k1 = k1 == g ? 0 : k1; k2 = k2 == g ? 0 : k2; k3 = k3 == g ? 0 : k3;
    }
    if (b == 0 && lane == 0) __builtin_amdgcn_raw_buffer_store_b64(tag2, rws, WS_EPOCH, 0, SC1);
    STAMP(14);
  }
  if (kh == 1) WAITV(0);
  WAITL0(); BARRIER();

  const unsigned selk = *(const LAS unsigned*)(L + L_SEL + (lane & 7) * 4), selm = *(const LAS unsigned*)(L + L_SEL + 32 + (lane & 7) * 4);
  auto out_rows = [&]() {
    if (kh == 0) {
      bool sel = false;
#pragma unroll
      for (int i = 0; i < 8; ++i) sel |= (2047 - (int)(__builtin_amdgcn_readlane((int)selk, i) & 0x7ff)) == tok;
      if (!sel) {
#pragma unroll
        for (int ft = 0; ft < 5; ++ft) {
          unsigned r2[2];
#pragma unroll
          for (int e = 0; e < 2; ++e) {
            const unsigned hv = hp[2 * ft + e];
            const unsigned d2 = pk2(bf2f(hv) - mb, bfhi(hv) - mb);
            const unsigned n2 = pk2(bf2f(d2) * inv_sd, bfhi(d2) * inv_sd);
            r2[e] = pk2(bf2f(hv) + bf2f(n2), bfhi(hv) + bfhi(n2));
          }
          *(u32x2*)(p.out + (size_t)tok * D + 80 * cc + 16 * ft + 4 * fg) = (u32x2){r2[0], r2[1]};
        }
      }
    }
  };
  if (is_gu) {
    {
      const unsigned kk = *(const LAS unsigned*)(L + L_SEL + w * 4), ms = *(const LAS unsigned*)(L + L_SEL + 32 + w * 4);
      const int row = 2047 - (int)(kk & 0x7ff);
      const float m = __uint_as_float(ms & 0xffff0000u), iv = 1.f / __uint_as_float(ms << 16);
      const u32x4 h0 = __builtin_amdgcn_raw_buffer_load_b128(rws, WS_H + (unsigned)row * 1280 + lane * 16, 0, SC1);
      const u32x4 h1 = __builtin_amdgcn_raw_buffer_load_b128(rws, WS_H + (unsigned)row * 1280 + 1024 + (lane & 15) * 16, 0, SC1);
      u32x4 n0, n1;
#pragma unroll
      for (int e = 0; e < 4; ++e) {
        const unsigned d0 = pk2(bf2f(h0[e]) - m, bfhi(h0[e]) - m), d1 = pk2(bf2f(h1[e]) - m, bfhi(h1[e]) - m);
        n0[e] = pk2(bf2f(d0) * iv, bfhi(d0) * iv); n1[e] = pk2(bf2f(d1) * iv, bfhi(d1) * iv);
      }
      *(LAS u32x4*)(L + L_XS + w * 1296 + lane * 16) = n0;
      if (lane < 16) *(LAS u32x4*)(L + L_XS + w * 1296 + 1024 + lane * 16) = n1;
    }
    WAITL0(); BARRIER();
    if (w == 0) STAMP(15);
    {
      const int m = w & 1, kq = w >> 1;
      f32x4 a4 = {0, 0, 0, 0};
#pragma unroll
      for (int i = 0; i < 5; ++i) {
        const int s = 5 * kq + i;
        const LAS char* ap = L + L_FW + (m * 20 + s) * 1024 + lane * 8;
        const bf16x8 af = cat8(trd(ap), trd(ap + 512));
        const bf16x8 bfv = *(const LAS bf16x8*)(L + L_XS + (lane & 7) * 1296 + s * 64 + fg * 16);
        a4 = MFMA16(af, bfv, a4);
      }
      *(LAS f32x4*)(L + L_FRED + (w * 64 + lane) * 16) = a4;
    }
    WAITL0(); BARRIER();
    if (w == 0) {
      f32x4 g4 = {0, 0, 0, 0}, u4 = {0, 0, 0, 0};
#pragma unroll
      for (int q = 0; q < 4; ++q) { g4 += *(const LAS f32x4*)(L + L_FRED + ((2 * q) * 64 + lane) * 16); u4 += *(const LAS f32x4*)(L + L_FRED + ((2 * q + 1) * 64 + lane) * 16); }
      float a[4];
#pragma unroll
      for (int e = 0; e < 4; ++e) { const float g = rbf(g4[e]), u = rbf(u4[e]); const float sg = rbf(g / (1.f + __expf(-g))); a[e] = sg * u; }
      if (l15 < 8) __builtin_amdgcn_raw_buffer_store_b128((u32x4){pk2(a[0], a[1]), tag32, pk2(a[2], a[3]), tag32}, rws, WS_ACT + (((gs >> 1) * 4 + 2 * (gs & 1) + (fg >> 1)) * 8 + l15) * 32 + (fg & 1) * 16, 0, SC1);
      STAMP(16);
    }
    out_rows();
  } else if (is_dn) {
    out_rows();
    const int row = 2047 - (int)(selk & 0x7ff);
    const u32x2 hv = __builtin_amdgcn_raw_buffer_load_b64(rws, WS_H + (unsigned)row * 1280 + (16 * dn + 4 * fg) * 2, 0, SC1);
    f32x4 a4 = {0, 0, 0, 0};
    {
      u32x4 g0[7], g1[7];
      unsigned spins = 0;
      for (;;) {
        bool ok = true;
#pragma unroll
        for (int i = 0; i < 7; ++i) {
          const int s = min(w + 8 * i, 53);
          const unsigned ao = WS_ACT + ((s * 4 + fg) * 8 + (lane & 7)) * 32;
          g0[i] = __builtin_amdgcn_raw_buffer_load_b128(rws, ao, 0, SC1); g1[i] = __builtin_amdgcn_raw_buffer_load_b128(rws, ao + 16, 0, SC1);
        }
#pragma unroll
        for (int i = 0; i < 7; ++i) ok = ok && g0[i][1] == tag32 && g0[i][3] == tag32 && g1[i][1] == tag32 && g1[i][3] == tag32;
        if (__all(ok)) break;
        if (++spins > SPIN_CAP) break;
        __builtin_amdgcn_s_sleep(1);
      }
      if (w == 0) STAMP(17);
#pragma unroll
      for (int i = 0; i < 7; ++i) {
        const int s = w + 8 * i;
        if (s < 54) {
          const LAS char* ap = L + L_FW + s * 1024 + lane * 8;
          const bf16x8 af = cat8(trd(ap), trd(ap + 512));
          a4 = MFMA16(af, __builtin_bit_cast(bf16x8, (u32x4){g0[i][0], g0[i][2], g1[i][0], g1[i][2]}), a4);
        }
      }
    }
    *(LAS f32x4*)(L + L_FRED + (w * 64 + lane) * 16) = a4;
    WAITL0(); BARRIER();
    if (w == 0) {
      f32x4 y = {0, 0, 0, 0};
#pragma unroll
      for (int q = 0; q < 8; ++q) y += *(const LAS f32x4*)(L + L_FRED + (q * 64 + lane) * 16);
      const unsigned rk = selk >> 16, rbits = (rk & 0x8000u) ? (rk ^ 0x8000u) : (rk ^ 0xffffu);
      const float rv = bf2f(rbits), sig = rbf(1.f / (1.f + __expf(-rv)));
      const float m = __uint_as_float(selm & 0xffff0000u), iv = 1.f / __uint_as_float(selm << 16);
      unsigned r2[2];
#pragma unroll
      for (int e = 0; e < 2; ++e) {
        const unsigned d2 = pk2(bf2f(hv[e]) - m, bfhi(hv[e]) - m);
        const unsigned n2 = pk2(bf2f(d2) * iv, bfhi(d2) * iv);
        const unsigned y2 = pk2(rbf(y[2 * e]) * sig, rbf(y[2 * e + 1]) * sig);
        const unsigned t2 = pk2(bf2f(n2) + bf2f(y2), bfhi(n2) + bfhi(y2));
        r2[e] = pk2(bf2f(hv[e]) + bf2f(t2), bfhi(hv[e]) + bfhi(t2));
      }
      if (l15 < 8) *(u32x2*)(p.out + (size_t)row * D + 16 * dn + 4 * fg) = (u32x2){r2[0], r2[1]};
      STAMP(18);
    }
  } else {
    out_rows();
  }
  if (w == 0) STAMP(19);
  if (blockIdx.x == 0 && w == 0) { const u64 until_ = __builtin_amdgcn_s_memrealtime() + 0 * (ka_t2 - ka_t1); while (__builtin_amdgcn_s_memrealtime() < until_) __builtin_amdgcn_s_sleep(1); }
}

extern "C" void kernel_launch(void* const* d_in, const int* in_sizes, int n_in, void* d_out, int out_size, void* d_ws, size_t ws_size, hipStream_t stream) {
  Params p;
  p.x = (const u16*)d_in[0]; p.wqkv = (const u16*)d_in[1]; p.wo = (const u16*)d_in[2]; p.wr = (const u16*)d_in[4];
  p.wg = (const u16*)d_in[5]; p.wu = (const u16*)d_in[6]; p.wd = (const u16*)d_in[7]; p.out = (u16*)d_out; p.ws = (unsigned char*)d_ws;
  fused_kernel<<<256, 512, 0, stream>>>(p);
}
```

```cpp
#include <hip/hip_runtime.h>
#include <stdint.h>

typedef unsigned short u16;
typedef short bf16x8 __attribute__((ext_vector_type(8)));
typedef short bf16x4 __attribute__((ext_vector_type(4)));
typedef __bf16 bf2 __attribute__((ext_vector_type(2)));
typedef float f32x16 __attribute__((ext_vector_type(16)));
typedef float f32x4 __attribute__((ext_vector_type(4)));
typedef float f32x2 __attribute__((ext_vector_type(2)));
typedef unsigned u32x4 __attribute__((ext_vector_type(4)));
typedef unsigned u32x2 __attribute__((ext_vector_type(2)));
typedef unsigned long long u64;
#define LAS __attribute__((address_space(3)))
#define SBAR() __builtin_amdgcn_sched_barrier(0)
#define BARRIER() asm volatile("s_barrier" ::: "memory")
#define WAITL0() asm volatile("s_waitcnt lgkmcnt(0)" ::: "memory")
#define WAITV(n) asm volatile("s_waitcnt vmcnt(" #n ")" ::: "memory")
#define FENCE() asm volatile("" ::: "memory")

constexpr int S = 2048, D = 640, F = 1728;
constexpr int SC1 = 16;

constexpr unsigned WS_EPOCH = 0;
constexpr unsigned WS_FLAGA = 4096;
constexpr unsigned WS_FLAGO = WS_FLAGA + 32768;
constexpr unsigned WS_CAND = WS_FLAGO + 16384;
constexpr unsigned WS_HDONE = WS_CAND + 4096;
constexpr unsigned WS_ACT = WS_HDONE + 4096;
constexpr unsigned WS_ST1 = 131072;
constexpr unsigned WS_CS1 = WS_ST1 + 32768;
constexpr unsigned WS_STATS = 524288;
constexpr unsigned WS_QKV = 1048576;
constexpr unsigned WS_O = WS_QKV + 480 * 16384;
constexpr unsigned WS_H = WS_O + S * D * 2;
#define STAMP_OFF (200u << 20)
#define NSTAMP 64

constexpr int L_SLOT = 32768, L_W = 16384;
constexpr int PL = 4128;
constexpr int L_K = 0, L_V = 5 * PL;
constexpr int L_WO = 45568;
constexpr int L_RED = 0;
constexpr int L_HST = 20480;
constexpr int L_FW = L_WO;
constexpr int L_XS = 0;
constexpr int L_FRED = 12288;
constexpr int L_MISC = L_WO + 640 * 160;
constexpr int L_RS = 0;
constexpr int L_CS = 1024;
constexpr int L_CNT = L_MISC + 3600;
constexpr int L_F1 = L_MISC + 3604, L_C1 = L_MISC + 3608;
constexpr int L_PART = L_MISC + 2048;
constexpr int L_K32 = L_MISC + 2560;
constexpr int L_HD = L_MISC + 3712;
constexpr int L_SEL = L_MISC + 3968;
constexpr int L_STAMP = L_MISC + 4096;
constexpr int L_KEY = L_MISC + 4608 + 256;
constexpr int LDS_BYTES = 163840;
static_assert(L_V + 6 * PL <= L_WO && L_KEY + 2048 <= LDS_BYTES, "lds map");

struct Params { const u16* x; const u16* wqkv; const u16* wo; const u16* wr; const u16* wg; const u16* wu; const u16* wd; u16* out; unsigned char* ws; };

__device__ __forceinline__ float bf2f(unsigned v) { return __uint_as_float(v << 16); }
__device__ __forceinline__ float bfhi(unsigned v) { return __uint_as_float(v & 0xffff0000u); }
__device__ __forceinline__ unsigned pk2(float a, float b) { f32x2 f = {a, b}; return __builtin_bit_cast(unsigned, __builtin_convertvector(f, bf2)); }
__device__ __forceinline__ float rbf(float f) { return __uint_as_float(pk2(f, 0.f) << 16); }
__device__ __forceinline__ float dot2(unsigned a, unsigned b, float c) { return __builtin_amdgcn_fdot2_f32_bf16(__builtin_bit_cast(bf2, a), __builtin_bit_cast(bf2, b), c, false); }
__device__ __forceinline__ float swap_add(float v) { auto r = __builtin_amdgcn_permlane32_swap(__float_as_uint(v), __float_as_uint(v), false, false); return __uint_as_float(r[0]) + __uint_as_float(r[1]); }
__device__ __forceinline__ float swap16_add(float v) { auto r = __builtin_amdgcn_permlane16_swap(__float_as_uint(v), __float_as_uint(v), false, false); return __uint_as_float(r[0]) + __uint_as_float(r[1]); }
template <int CTRL> __device__ __forceinline__ float dppadd(float v) { return v + __int_as_float(__builtin_amdgcn_update_dpp(0, __float_as_int(v), CTRL, 0xf, 0xf, false)); }
__device__ __forceinline__ float wave_fsum(float v) { v = dppadd<0x128>(v); v = dppadd<0x124>(v); v = dppadd<0x122>(v); v = dppadd<0x121>(v); return swap_add(swap16_add(v)); }
template <int CTRL> __device__ __forceinline__ unsigned dppmax(unsigned v) { return max(v, (unsigned)__builtin_amdgcn_update_dpp(0, (int)v, CTRL, 0xf, 0xf, false)); }
__device__ __forceinline__ unsigned wave_umax(unsigned v) {
  v = dppmax<0x128>(v); v = dppmax<0x124>(v); v = dppmax<0x122>(v); v = dppmax<0x121>(v);
  auto r = __builtin_amdgcn_permlane16_swap(v, v, false, false); v = max(r[0], r[1]);
  auto q = __builtin_amdgcn_permlane32_swap(v, v, false, false); return max(q[0], q[1]);
}
__device__ __forceinline__ void glds16(unsigned voff, const void* sbase, unsigned lds) {
  asm volatile("s_mov_b32 m0, %2\n\ts_nop 0\n\tglobal_load_lds_dwordx4 %0, %1" ::"v"(voff), "s"(sbase), "s"(lds) : "memory");
}
__device__ __forceinline__ void glds16c(unsigned voff, const void* sbase, unsigned lds) {
  asm volatile("s_mov_b32 m0, %2\n\ts_nop 0\n\tglobal_load_lds_dwordx4 %0, %1 sc1" ::"v"(voff), "s"(sbase), "s"(lds) : "memory");
}
__device__ __forceinline__ bf16x4 trd(const LAS char* p) { return __builtin_amdgcn_ds_read_tr16_b64_v4i16((LAS bf16x4*)p); }
__device__ __forceinline__ bf16x8 cat8(bf16x4 a, bf16x4 b) { return (bf16x8){a[0], a[1], a[2], a[3], b[0], b[1], b[2], b[3]}; }
#define MFMA32(a, b, c) __builtin_amdgcn_mfma_f32_32x32x16_bf16(a, b, c, 0, 0, 0)
#define MFMA32K8(a, b, c) __builtin_amdgcn_mfma_f32_32x32x8bf16_1k(a, b, c, 0, 0, 0)
#define MFMA16(a, b, c) __builtin_amdgcn_mfma_f32_16x16x32_bf16(a, b, c, 0, 0, 0)
#define LD128(off) __builtin_amdgcn_raw_buffer_load_b128(rws, (off), 0, SC1)
#define LD64(off) __builtin_amdgcn_raw_buffer_load_b64(rws, (off), 0, SC1)
#define ST128(v, off) __builtin_amdgcn_raw_buffer_store_b128((v), rws, (off), 0, SC1)
#define ST64(v, off) __builtin_amdgcn_raw_buffer_store_b64((v), rws, (off), 0, SC1)

#define STAMP(k) do {} while (0)
#define STAMP_INIT() do {} while (0)
#define PSTAMP(i) do {} while (0)
#define STAMP_ZERO() do {} while (0)
#define STAMP_PA() do {} while (0)
#define STAMP_FLUSH() do {} while (0)

constexpr unsigned SPIN_CAP = 1u << 17;

__global__ __launch_bounds__(512, 2) void fused_kernel(Params p) {
  __shared__ __attribute__((aligned(16))) char smem[LDS_BYTES];
  LAS char* L = (LAS char*)smem;
  const unsigned lds0 = (unsigned)(uintptr_t)smem;
  const int tid = threadIdx.x, lane = tid & 63;
  const int w = __builtin_amdgcn_readfirstlane(tid >> 6);
  const int b = blockIdx.x, xcd = b & 7, bi = b >> 3;
  const int l31 = lane & 31, hh = lane >> 5, l15 = lane & 15, fg = lane >> 4;
  const __amdgpu_buffer_rsrc_t rws = __builtin_amdgcn_make_buffer_rsrc(p.ws, 0, 0x7fffffff, 0x00020000);
  const int aP = 4 * (xcd >> 1) + (bi >> 3), aj = 8 * (xcd & 1) + (bi & 7);
  const int hd = 4 * xcd + (bi >> 3), pp = bi & 7;
  const int R = 4 * xcd + (bi >> 3), cc = bi & 7;
  const int gs = xcd * 14 + bi;
  const bool is_gu = bi < 14 && gs < 108;
  const int dn = xcd * 5 + (bi - 14);
  const bool is_dn = bi >= 14 && bi < 19;
  STAMP_INIT();
  u32x2 tag2;
  auto read_epoch = [&]() {
    u64 e;
    asm volatile("s_load_dwordx2 %0, %1, 0x0\n\ts_waitcnt lgkmcnt(0)" : "=s"(e) : "s"(p.ws) : "memory");
    e += 1;
    tag2 = (u32x2){(unsigned)e, (unsigned)(e >> 32)};
  };

  auto rows_issue = [&](int lw, u32x4& ra0, u32x4& ra1, u32x4& rb0, u32x4& rb1) {
    if (lw == 0) ST128(((u32x4){0, 0, 0, 0}), WS_STATS + (R * 8 + cc) * 1024 + lane * 16);
    if (lw == 1 && is_gu && lane < 32) ST128(((u32x4){0, 0, 0, 0}), WS_ACT + (((gs >> 1) * 4 + 2 * (gs & 1) + (lane >> 4)) * 8 + (lane & 7)) * 32 + ((lane >> 3) & 1) * 16);
    const char* rb = (const char*)p.x + (size_t)(64 * R + 8 * cc + 2 * lw) * 1280;
    const unsigned o0 = lane * 16, o1 = 1024 + l15 * 16, o2 = o0 + 1280, o3 = o1 + 1280;
    asm volatile("global_load_dwordx4 %0, %4, %8\n\tglobal_load_dwordx4 %1, %5, %8\n\tglobal_load_dwordx4 %2, %6, %8\n\tglobal_load_dwordx4 %3, %7, %8"
                 : "=&v"(ra0), "=&v"(ra1), "=&v"(rb0), "=&v"(rb1) : "v"(o0), "v"(o1), "v"(o2), "v"(o3), "s"(rb) : "memory");
  };
  auto rows_finish = [&](int lw, const u32x4& ra0, const u32x4& ra1, const u32x4& rb0, const u32x4& rb1) {
    float mean[2], rs[2];
#pragma unroll
    for (int r = 0; r < 2; ++r) {
      const u32x4 c0 = r ? rb0 : ra0, c1 = r ? rb1 : ra1;
      float s0 = 0.f, s1 = 0.f;
#pragma unroll
      for (int e = 0; e < 4; ++e) { s0 = dot2(c0[e], 0x3f803f80u, s0); s1 = dot2(c1[e], 0x3f803f80u, s1); }
      mean[r] = wave_fsum(s0 + (lane < 16 ? s1 : 0.f)) * (1.f / D);
      const f32x2 mm = {mean[r], mean[r]};
      f32x2 q0 = {0.f, 0.f}, q1 = {0.f, 0.f};
#pragma unroll
      for (int e = 0; e < 4; ++e) { const f32x2 d0 = (f32x2){bf2f(c0[e]), bfhi(c0[e])} - mm, d1 = (f32x2){bf2f(c1[e]), bfhi(c1[e])} - mm; q0 += d0 * d0; q1 += d1 * d1; }
      const float var = wave_fsum((q0[0] + q0[1]) + (lane < 16 ? q1[0] + q1[1] : 0.f)) * (1.f / D);
      rs[r] = __builtin_amdgcn_rcpf(rbf(__builtin_amdgcn_sqrtf(rbf(rbf(var) + 1e-5f))));
    }
    if (lane < 2) ST128(((u32x4){__float_as_uint(rbf(lane ? mean[1] : mean[0])), __float_as_uint(lane ? rs[1] : rs[0]), tag2[0], tag2[1]}), WS_ST1 + (unsigned)(64 * R + 8 * cc + 2 * lw + lane) * 16);
  };

  auto lds_ctl_init = [&]() {
    if (tid == 0) { *(LAS unsigned*)(L + L_F1) = 0; *(LAS unsigned*)(L + L_C1) = 0; }
    STAMP_ZERO();
  };
  if (aj < 15 && w >= 4) {
    const int lw = w - 4;
    const unsigned xsw = (unsigned)(4 * (lw & 1) + (lane >> 4));
    const unsigned voff_x = (unsigned)(lane >> 3) * 1280u + (((unsigned)(lane & 7) ^ xsw) << 4);
    const unsigned ch = (unsigned)(lane & 15) ^ (((unsigned)(lane >> 4) << 2) | (unsigned)lw);
    const unsigned voff_w = (unsigned)(lane >> 4) * 3840u + ch * 16u;
    const char* xb = (const char*)p.x + (size_t)(128 * aP + 8 * lw) * 1280;
    const char* wb = (const char*)p.wqkv + (size_t)(4 * lw) * 3840 + 256 * aj;
    const unsigned lbase = lds0 + lw * 1024;
    const int rot = ((bi & 7) * 5 / 4 + (bi >> 3) * 5 / 2) % 10;
    auto issue = [&](int s) {
      const unsigned sl = lbase + (s >= 5 ? s - 5 : s) * L_SLOT;
      int ss = s + rot; if (ss >= 10) ss -= 10;
#pragma unroll
      for (int i = 0; i < 4; ++i) {
        glds16(voff_x, xb + (size_t)i * 32 * 1280 + ss * 128, sl + i * 4096);
        glds16(voff_w, wb + ((size_t)ss * 64 + i * 16) * 3840, sl + L_W + i * 4096);
      }
    };
    u32x4 ra0, ra1, rb0, rb1;
    rows_issue(lw, ra0, ra1, rb0, rb1);
    issue(0); issue(1);
    read_epoch();
    asm volatile("s_waitcnt vmcnt(8)" : "+v"(ra0), "+v"(ra1), "+v"(rb0), "+v"(rb1) :: "memory");
    BARRIER();
    PSTAMP(1);
    issue(2);
    rows_finish(lw, ra0, ra1, rb0, rb1);
    WAITV(9); BARRIER();
    issue(3); issue(4);
    WAITV(17); BARRIER();
    issue(5); issue(6);
    WAITV(24); BARRIER();
#pragma unroll 1
    for (int s = 3; s <= 5; ++s) { issue(s + 4); WAITV(24); BARRIER(); }
    const unsigned so = WS_ST1 + (unsigned)(128 * aP + 32 * lw + l31) * 16, co = WS_CS1 + (unsigned)((aj * 16 + fg) * 64 + 16 * lw + l15) * 16;
    u32x4 gs1 = LD128(so), gc0 = LD128(co), gc1 = LD128(co + 4096), gc2 = LD128(co + 8192), gc3 = LD128(co + 12288);
    WAITV(21); BARRIER();
    WAITV(13); BARRIER();
    WAITV(5); BARRIER();
    unsigned spins = 0;
    for (;;) {
      const bool ok = gs1[2] == tag2[0] && gs1[3] == tag2[1] && gc0[2] == tag2[0] && gc0[3] == tag2[1] && gc1[2] == tag2[0] && gc1[3] == tag2[1] && gc2[2] == tag2[0] && gc2[3] == tag2[1] && gc3[2] == tag2[0] && gc3[3] == tag2[1];
      if (__all(ok) || ++spins > SPIN_CAP) break;
      FENCE();
      gs1 = LD128(so); gc0 = LD128(co); gc1 = LD128(co + 4096); gc2 = LD128(co + 8192); gc3 = LD128(co + 12288);
    }
    if (lane < 32) *(LAS u32x2*)(L + L_RS + (32 * lw + l31) * 8) = (u32x2){gs1[0], gs1[1]};
    {
      const float c0 = swap_add(swap16_add((__uint_as_float(gc0[0]) + __uint_as_float(gc1[0])) + (__uint_as_float(gc2[0]) + __uint_as_float(gc3[0]))));
      const float c1 = swap_add(swap16_add((__uint_as_float(gc0[1]) + __uint_as_float(gc1[1])) + (__uint_as_float(gc2[1]) + __uint_as_float(gc3[1]))));
      if (lane < 16) *(LAS f32x2*)(L + L_CS + (32 * lw + 2 * lane) * 4) = (f32x2){c0, c1};
    }
    WAITL0(); BARRIER();
    BARRIER();
  } else if (w < 4) {
    if (aj < 15) {
      u32x4 wv[3];
#pragma unroll
      for (int i = 0; i < 3; ++i) { const int r = min(16 * i + (lane >> 2), 39); wv[i] = *(const u32x4*)((const char*)p.wqkv + (size_t)(40 * aP + r) * 3840 + 256 * aj + 64 * w + (lane & 3) * 16); }
      read_epoch();
      {
        float cs[8];
#pragma unroll
        for (int e2 = 0; e2 < 8; ++e2) cs[e2] = 0.f;
#pragma unroll
        for (int i = 0; i < 3; ++i) {
          const bool v = 16 * i + (lane >> 2) < 40;
#pragma unroll
          for (int e2 = 0; e2 < 4; ++e2) { cs[2 * e2] += v ? bf2f(wv[i][e2]) : 0.f; cs[2 * e2 + 1] += v ? bfhi(wv[i][e2]) : 0.f; }
        }
#pragma unroll
        for (int e2 = 0; e2 < 8; ++e2) cs[e2] = swap_add(swap16_add(dppadd<0x128>(dppadd<0x124>(cs[e2]))));
        const int e2 = (lane >> 2) & 3;
        const float c0 = e2 == 0 ? cs[0] : (e2 == 1 ? cs[2] : (e2 == 2 ? cs[4] : cs[6])), c1 = e2 == 0 ? cs[1] : (e2 == 1 ? cs[3] : (e2 == 2 ? cs[5] : cs[7]));
        if (lane < 16) ST128(((u32x4){__float_as_uint(c0), __float_as_uint(c1), tag2[0], tag2[1]}), WS_CS1 + (unsigned)((aj * 16 + aP) * 64 + 16 * w + 4 * (lane & 3) + e2) * 16);
      }
      PSTAMP(7);
      const int wm = w >> 1, wn = w & 1;
      unsigned xa[4], wa[4];
#pragma unroll
      for (int ks = 0; ks < 4; ++ks) xa[ks] = (unsigned)(64 * wm + l31) * 128u + ((((unsigned)(2 * ks + hh)) ^ (((unsigned)l31 >> 1) & 7u)) << 4);
      {
        const unsigned blk = (lane >> 4) & 1, q = (lane & 15) >> 2, pq = lane & 3;
#pragma unroll
        for (int j = 0; j < 2; ++j)
#pragma unroll
          for (int t = 0; t < 2; ++t) {
            const unsigned row = 8 * hh + 4 * t + q, chn = 4 * (2 * wn + (j ^ wm)) + 2 * blk + (pq >> 1), m = (q << 2) | (unsigned)(2 * hh + t);
            wa[j * 2 + t] = L_W + 256u * row + 16u * (chn ^ m) + 8u * (pq & 1);
          }
      }
      f32x16 a00 = {}, a01 = {}, a10 = {}, a11 = {};
      bf16x8 xf0[2], wf0[2], xf1[2], wf1[2];

      unsigned xb[4], wb[4];
#define SETSLOT(so) do { _Pragma("unroll") for (int k_ = 0; k_ < 4; ++k_) { xb[k_] = xa[k_] + (so); wb[k_] = wa[k_] + (so); } } while (0)
#define RD(XF, WF, ks) do { \
        XF[0] = *(const LAS bf16x8*)(L + xb[ks]); WF[0] = cat8(trd(L + wb[0] + (ks) * 4096), trd(L + wb[1] + (ks) * 4096)); \
        WF[1] = cat8(trd(L + wb[2] + (ks) * 4096), trd(L + wb[3] + (ks) * 4096)); XF[1] = *(const LAS bf16x8*)(L + xb[ks] + 4096); } while (0)
#define MM(XF, WF) do { \
        a00 = MFMA32(WF[0], XF[0], a00); a01 = MFMA32(WF[1], XF[0], a01); \
        a10 = MFMA32(WF[0], XF[1], a10); a11 = MFMA32(WF[1], XF[1], a11); } while (0)
#define PINALL() asm volatile("" : "+v"(a00), "+v"(a01), "+v"(a10), "+v"(a11))
#define ILV() do { __builtin_amdgcn_sched_group_barrier(0x8, 1, 0); __builtin_amdgcn_sched_group_barrier(0x100, 3, 0); \
        __builtin_amdgcn_sched_group_barrier(0x8, 1, 0); __builtin_amdgcn_sched_group_barrier(0x100, 3, 0); __builtin_amdgcn_sched_group_barrier(0x8, 2, 0); } while (0)
#define STEP(XN, WN, ksn, XC, WC) do { RD(XN, WN, ksn); MM(XC, WC); ILV(); PINALL(); SBAR(); } while (0)
      BARRIER();
      SETSLOT(0);
      RD(xf0, wf0, 0);
      SBAR();
#pragma unroll 1
      for (int s = 0; s < 9; ++s) {
        STEP(xf1, wf1, 1, xf0, wf0);
        STEP(xf0, wf0, 2, xf1, wf1);
        STEP(xf1, wf1, 3, xf0, wf0);
        SETSLOT((s >= 4 ? s - 4 : s + 1) * L_SLOT);
        WAITL0(); BARRIER();
        STEP(xf0, wf0, 0, xf1, wf1);
      }
      STEP(xf1, wf1, 1, xf0, wf0);
      STEP(xf0, wf0, 2, xf1, wf1);
      STEP(xf1, wf1, 3, xf0, wf0);
      MM(xf1, wf1); PINALL();
      PSTAMP(2);
      WAITL0(); BARRIER();
      PSTAMP(6);
      const float qs = aj < 5 ? 0.22360679774997896f * 1.4426950408889634f : 1.f;
      f32x2 rs0 = *(const LAS f32x2*)(L + L_RS + (64 * wm + l31) * 8), rs1 = *(const LAS f32x2*)(L + L_RS + (64 * wm + 32 + l31) * 8);
      const float sc0 = rs0[1] * qs, nm0 = -rs0[0] * sc0, sc1 = rs1[1] * qs, nm1 = -rs1[0] * sc1;
#pragma unroll
      for (int j = 0; j < 2; ++j) {
        const int fj = 64 * wn + 32 * (j ^ wm) + 4 * hh;
        const unsigned go = WS_QKV + (unsigned)(32 * aj + (fj >> 2)) * 16384u + (unsigned)(128 * aP + 64 * wm + l31) * 8u;
#pragma unroll
        for (int rq = 0; rq < 4; ++rq) {
          const f32x4 cs = *(const LAS f32x4*)(L + L_CS + (fj + 8 * rq) * 4);
          const f32x16& a0 = j == 0 ? a00 : a01; const f32x16& a1 = j == 0 ? a10 : a11;
          const f32x2 c01 = {cs[0], cs[1]}, c23 = {cs[2], cs[3]};
          const f32x2 u0 = (f32x2){a0[rq * 4], a0[rq * 4 + 1]} * sc0 + c01 * nm0, u1 = (f32x2){a0[rq * 4 + 2], a0[rq * 4 + 3]} * sc0 + c23 * nm0;
          const f32x2 u2 = (f32x2){a1[rq * 4], a1[rq * 4 + 1]} * sc1 + c01 * nm1, u3 = (f32x2){a1[rq * 4 + 2], a1[rq * 4 + 3]} * sc1 + c23 * nm1;
          ST64(((u32x2){pk2(u0[0], u0[1]), pk2(u1[0], u1[1])}), go + 2 * rq * 16384);
          ST64(((u32x2){pk2(u2[0], u2[1]), pk2(u3[0], u3[1])}), go + 2 * rq * 16384 + 256);
        }
      }
      PSTAMP(4);
      lds_ctl_init();
      WAITV(0); WAITL0();
      PSTAMP(5);
      BARRIER();
    } else {
      read_epoch();
    }
  } else {
    u32x4 ra0, ra1, rb0, rb1;
    rows_issue(w - 4, ra0, ra1, rb0, rb1);
    read_epoch();
    asm volatile("s_waitcnt vmcnt(0)" : "+v"(ra0), "+v"(ra1), "+v"(rb0), "+v"(rb1) :: "memory");
    rows_finish(w - 4, ra0, ra1, rb0, rb1);
  }
  if (aj >= 15) { lds_ctl_init(); WAITL0(); BARRIER(); }
  const unsigned tag32 = tag2[0] | 1u;
  if (aj < 15 && tid == 0) ST64(tag2, WS_FLAGA + (aP * 16 + aj) * 128);
  PSTAMP(3);
  STAMP_PA();

  const int nt = w & 3, kh = w >> 2;
  const int tok = 64 * R + 16 * nt + l15;
  u32x2 xr[5], wrv[5];
  unsigned wr5[5];
  {
    const int t = w;
    *(LAS u32x2*)(L + L_V + 5 * PL + tid * 8) = (u32x2){(pp > 0 || tid >= 256) ? 0x00003f80u : 0u, 0u};
    if (pp == 0) {
#pragma unroll
      for (int i = 0; i < 3; ++i) { const int n = tid + 512 * i; if (n < 1280) *(LAS u32x4*)(L + (n >> 7) * PL + (n & 127) * 16) = (u32x4){0, 0, 0, 0}; }
    }
    if (kh == 0) {
#pragma unroll
      for (int ft = 0; ft < 5; ++ft) {
        xr[ft] = *(const u32x2*)(p.x + (size_t)tok * D + 80 * cc + 16 * ft + 4 * fg);
        wrv[ft] = *(const u32x2*)(p.wr + 80 * cc + 16 * ft + 4 * fg);
      }
    }
    if (bi == 31) {
#pragma unroll
      for (int i = 0; i < 5; ++i) wr5[i] = ((const unsigned*)p.wr)[lane + 64 * i];
    }
    auto lds_wait = [&](int off, unsigned v) { unsigned sp = 0; while (__hip_atomic_load((LAS unsigned*)(L + off), __ATOMIC_RELAXED, __HIP_MEMORY_SCOPE_WORKGROUP) < v && ++sp < (SPIN_CAP << 4)) __builtin_amdgcn_s_sleep(0); FENCE(); };
    WAITL0();
    if (kh == 0) {
      if (lane == 0) __hip_atomic_fetch_add((LAS unsigned*)(L + L_C1), 1u, __ATOMIC_RELAXED, __HIP_MEMORY_SCOPE_WORKGROUP);
      lds_wait(L_F1, 1);
    } else {
      if (w == 4) {
        const int sec = lane < 4 ? 0 : (lane < 12 ? 1 : 2);
        const int k = lane < 4 ? lane : (lane < 12 ? lane - 4 : lane - 12);
        const int col = sec * 640 + 20 * hd + ((k & 1) ? 19 : 0);
        const int Pn = sec == 0 ? 2 * pp + (k >> 1) : 2 * pp - 2 + (k >> 1);
        const bool need = lane < 20 && Pn >= 0;
        const unsigned fo = WS_FLAGA + (unsigned)((need ? Pn : 2 * pp) * 16 + (col >> 7)) * 128;
        unsigned spins = 0;
        for (;;) {
          FENCE();
          const u32x2 f = LD64(fo);
          if (__all(!need || (f[0] == tag2[0] && f[1] == tag2[1])) || ++spins > SPIN_CAP) break;
          __builtin_amdgcn_s_sleep(1);
        }
        if (lane == 0) __hip_atomic_store((LAS unsigned*)(L + L_F1), 1u, __ATOMIC_RELAXED, __HIP_MEMORY_SCOPE_WORKGROUP);
        STAMP(4);
      } else lds_wait(L_F1, 1);
      const char* qb = (const char*)p.ws + WS_QKV;
#pragma unroll
      for (int i = 0; i < 10; ++i) {
        const int pc = nt + 4 * i, pl = pc >> 2, qt = pc & 3;
        const int tok0 = 256 * pp - 256 + 128 * qt;
        if (tok0 >= 0) glds16c(lane * 16, qb + (size_t)((pl < 5 ? 160 + 5 * hd + pl : 320 + 5 * hd + pl - 5)) * 16384 + tok0 * 8, lds0 + pl * PL + qt * 1024);
      }
    }
    bf16x8 qf0; bf16x4 qf1;
    {
      const unsigned qo = WS_QKV + (unsigned)(5 * hd) * 16384u + (unsigned)(256 * pp + 32 * t + l31) * 8u;
      const u32x2 a = LD64(qo + (2 * hh) * 16384), c = LD64(qo + (2 * hh + 1) * 16384);
      u32x2 z = LD64(qo + 4 * 16384);
      asm volatile("s_waitcnt vmcnt(0)" : "+v"(z) :: "memory");
      if (hh) z = (u32x2){0, 0};
      qf0 = __builtin_bit_cast(bf16x8, (u32x4){a[0], a[1], c[0], c[1]});
      qf1 = __builtin_bit_cast(bf16x4, z);
    }
    if (kh == 1 && lane == 0) __hip_atomic_fetch_add((LAS unsigned*)(L + L_C1), 1u, __ATOMIC_RELAXED, __HIP_MEMORY_SCOPE_WORKGROUP);
    lds_wait(L_C1, 8);
    if (w == 4) STAMP(5);
    unsigned wov[5];
#pragma unroll
    for (int j = 0; j < 5; ++j) { const int n = 64 * j + lane, rho = (n * 6554) >> 16, chn = n - 10 * rho; wov[j] = (unsigned)(8 * ((rho >> 2) & 3) + 4 * (rho >> 4) + (rho & 3)) * 1280u + chn * 16u; }
    const char* wob = (const char*)p.wo + 160 * cc + (size_t)nt * 32 * 1280;
    const unsigned wol = lds0 + L_WO + nt * 5120;
#define WOP(i) glds16(wov[(i) % 5], wob + (size_t)((i) / 5) * 4 * 32 * 1280, wol + ((i) / 5) * 4 * 5120 + ((i) % 5) * 1024)
#define WOP3(i) do { if (kh == 0) { WOP(i); WOP((i) + 1); WOP((i) + 2); } SBAR(); } while (0)
    if (w == 0) STAMP(28);
    const LAS char* kb = L + L_K + l31 * 8 + (2 * hh) * PL;
    const LAS char* kb4 = L + L_K + l31 * 8 + 4 * PL;
    float negm;
    {
      const u32x2 k0 = *(const LAS u32x2*)(kb + (t + 8) * 256), k1 = *(const LAS u32x2*)(kb + (t + 8) * 256 + PL), k2 = *(const LAS u32x2*)(kb4 + (t + 8) * 256);
      const u32x4 q0 = __builtin_bit_cast(u32x4, qf0); const u32x2 q1 = __builtin_bit_cast(u32x2, qf1);
      float d = dot2(q0[0], k0[0], 0.f); d = dot2(q0[1], k0[1], d); d = dot2(q0[2], k1[0], d); d = dot2(q0[3], k1[1], d);
      d = dot2(q1[0], k2[0], d); d = dot2(q1[1], k2[1], d);
      negm = -swap_add(d);
    }
    const LAS char* vb = L + L_V + min(4 * ((lane >> 4) & 1) + (lane & 3), 5) * PL + (4 * hh + ((lane & 15) >> 2)) * 8;
    f32x16 o = (f32x16){};
    {
      auto diag_ok = [](int r) { unsigned long long m = 0; for (int l = 0; l < 64; ++l) if ((r & 3) + 8 * (r >> 2) + 4 * (l >> 5) <= (l & 31)) m |= 1ull << l; return m; };
      f32x16 cpl, sa, sb;
      bf16x4 ka, kc, kd;
      bf16x8 vq = {};
      u32x4 pq = {0, 0, 0, 0};
#define KRD(kt) do { ka = *(const LAS bf16x4*)(kb + (kt) * 256); kc = *(const LAS bf16x4*)(kb + (kt) * 256 + PL); kd = *(const LAS bf16x4*)(kb4 + (kt) * 256); } while (0)
#define EX2(S, i) pk2(__builtin_amdgcn_exp2f(S[i]), __builtin_amdgcn_exp2f(S[(i) + 1]))
#define GRP(nds, nv) do { __builtin_amdgcn_sched_group_barrier(0x8, 1, 0); if (nds) __builtin_amdgcn_sched_group_barrier(0x100, nds, 0); __builtin_amdgcn_sched_group_barrier(0x2, nv, 0); } while (0)
#define HALF(SC, SN, C, ktv, ktk) do { \
        u32x4 p0_; const LAS char* vp_ = vb + (ktv) * 256; \
        SN = MFMA32(cat8(ka, kc), qf0, C); \
        const bf16x8 v0_ = cat8(trd(vp_), trd(vp_ + 64)), v1_ = cat8(trd(vp_ + 128), trd(vp_ + 192)); \
        p0_[0] = EX2(SC, 0); p0_[1] = EX2(SC, 2); \
        GRP(4, 6); asm volatile("" : "+v"(SN), "+v"(p0_[0]), "+v"(p0_[1])); SBAR(); \
        o = MFMA32(vq, __builtin_bit_cast(bf16x8, pq), o); \
        p0_[2] = EX2(SC, 4); p0_[3] = EX2(SC, 6); \
        GRP(0, 6); asm volatile("" : "+v"(o), "+v"(p0_[2]), "+v"(p0_[3])); SBAR(); \
        SN = MFMA32K8(kd, qf1, SN); \
        KRD(ktk); \
        pq[0] = EX2(SC, 8); pq[1] = EX2(SC, 10); \
        GRP(3, 6); asm volatile("" : "+v"(SN), "+v"(pq[0]), "+v"(pq[1])); SBAR(); \
        o = MFMA32(v0_, __builtin_bit_cast(bf16x8, p0_), o); \
        pq[2] = EX2(SC, 12); pq[3] = EX2(SC, 14); vq = v1_; \
        GRP(0, 6); asm volatile("" : "+v"(o), "+v"(pq[2]), "+v"(pq[3])); SBAR(); } while (0)
      KRD(t + 8);
#pragma unroll
      for (int r = 0; r < 16; ++r) cpl[r] = __builtin_amdgcn_inverse_ballot_w64(diag_ok(r)) ? negm : -INFINITY;
      sa = MFMA32(cat8(ka, kc), qf0, cpl); sa = MFMA32K8(kd, qf1, sa);
      KRD(t + 7);
#pragma unroll
      for (int r = 0; r < 16; ++r) cpl[r] = negm;
      asm volatile("" : "+v"(sa), "+v"(cpl)); SBAR();
      WOP3(0);
      HALF(sa, sb, cpl, t + 8, t + 6);
      WOP3(3);
      HALF(sb, sa, cpl, t + 7, t + 5);
      WOP3(6);
      HALF(sa, sb, cpl, t + 6, t + 4);
      WOP3(9);
      HALF(sb, sa, cpl, t + 5, t + 3);
      WOP3(12);
      HALF(sa, sb, cpl, t + 4, t + 2);
      WOP3(15);
      HALF(sb, sa, cpl, t + 3, t + 1);
      WOP3(18);
      HALF(sa, sb, cpl, t + 2, t);
#pragma unroll
      for (int r = 0; r < 16; ++r) cpl[r] = __builtin_amdgcn_inverse_ballot_w64(diag_ok(r)) ? -INFINITY : negm;
      asm volatile("" : "+v"(cpl)); SBAR();
      if (kh == 0) { WOP(21); WOP(22); WOP(23); WOP(24); }
      SBAR();
      HALF(sb, sa, cpl, t + 1, t);
      {
        const LAS char* vp_ = vb + t * 256;
        const bf16x8 v0_ = cat8(trd(vp_), trd(vp_ + 64)), v1_ = cat8(trd(vp_ + 128), trd(vp_ + 192));
        o = MFMA32(vq, __builtin_bit_cast(bf16x8, pq), o);
        u32x4 p0_, p1_;
#pragma unroll
        for (int e = 0; e < 4; ++e) p0_[e] = EX2(sa, 2 * e);
        o = MFMA32(v0_, __builtin_bit_cast(bf16x8, p0_), o);
#pragma unroll
        for (int e = 0; e < 4; ++e) p1_[e] = EX2(sa, 8 + 2 * e);
        o = MFMA32(v1_, __builtin_bit_cast(bf16x8, p1_), o);
      }
    }
    float lsum;
    { auto r = __builtin_amdgcn_permlane32_swap(__float_as_uint(o[8]), __float_as_uint(o[8]), false, false); lsum = __uint_as_float(r[1]); }
    if (__builtin_expect(__any(!(lsum < 1.0995116e12f)), 0)) {
      const int lo = pp >= 1 ? t : 8;
      float mx = -INFINITY;
#pragma unroll 1
      for (int kt = t + 8; kt >= lo; --kt) {
        const bf16x4 ka = *(const LAS bf16x4*)(kb + kt * 256), kc = *(const LAS bf16x4*)(kb + kt * 256 + PL), kd = *(const LAS bf16x4*)(kb4 + kt * 256);
        f32x16 s = MFMA32(cat8(ka, kc), qf0, (f32x16){});
        s = MFMA32K8(kd, qf1, s);
#pragma unroll
        for (int r = 0; r < 16; ++r) { const int kr = (r & 3) + 8 * (r >> 2) + 4 * hh; const bool ok = kt == t + 8 ? kr <= l31 : (kt == t ? kr > l31 : true); mx = fmaxf(mx, ok ? s[r] : -INFINITY); }
      }
      { auto r = __builtin_amdgcn_permlane32_swap(__float_as_uint(mx), __float_as_uint(mx), false, false); mx = fmaxf(__uint_as_float(r[0]), __uint_as_float(r[1])); }
      o = (f32x16){};
#pragma unroll 1
      for (int kt = t + 8; kt >= lo; --kt) {
        const bf16x4 ka = *(const LAS bf16x4*)(kb + kt * 256), kc = *(const LAS bf16x4*)(kb + kt * 256 + PL), kd = *(const LAS bf16x4*)(kb4 + kt * 256);
        const LAS char* vp_ = vb + kt * 256;
        const bf16x8 v0_ = cat8(trd(vp_), trd(vp_ + 64)), v1_ = cat8(trd(vp_ + 128), trd(vp_ + 192));
        f32x16 s = MFMA32(cat8(ka, kc), qf0, (f32x16){});
        s = MFMA32K8(kd, qf1, s);
        u32x4 p0_, p1_;
#pragma unroll
        for (int r = 0; r < 16; ++r) { const int kr = (r & 3) + 8 * (r >> 2) + 4 * hh; const bool ok = kt == t + 8 ? kr <= l31 : (kt == t ? kr > l31 : true); s[r] = ok ? __builtin_amdgcn_exp2f(s[r] - mx) : 0.f; }
#pragma unroll
        for (int e = 0; e < 4; ++e) { p0_[e] = pk2(s[2 * e], s[2 * e + 1]); p1_[e] = pk2(s[8 + 2 * e], s[8 + 2 * e + 1]); }
        o = MFMA32(v0_, __builtin_bit_cast(bf16x8, p0_), o);
        o = MFMA32(v1_, __builtin_bit_cast(bf16x8, p1_), o);
      }
      auto r = __builtin_amdgcn_permlane32_swap(__float_as_uint(o[8]), __float_as_uint(o[8]), false, false); lsum = __uint_as_float(r[1]);
    }
    if (w == 0) STAMP(6);
    STAMP(32 + w);
    {
      const float inv = 1.f / lsum;
      unsigned A0 = pk2(o[0] * inv, o[1] * inv), A1 = pk2(o[2] * inv, o[3] * inv), B0 = pk2(o[4] * inv, o[5] * inv), B1 = pk2(o[6] * inv, o[7] * inv);
      unsigned C0 = pk2(o[8] * inv, o[9] * inv), C1 = pk2(o[10] * inv, o[11] * inv);
      const int tt = pp * 16 + 2 * t + (l31 >> 4);
      const unsigned base = WS_O + (unsigned)tt * 80 * 256 + (l31 & 15) * 16 + (unsigned)(5 * (hd >> 1)) * 256;
      if ((hd & 1) == 0) {
        auto s0 = __builtin_amdgcn_permlane32_swap(A0, B0, false, false), s1 = __builtin_amdgcn_permlane32_swap(A1, B1, false, false);
        ST128(((u32x4){s0[0], s1[0], s0[1], s1[1]}), base + hh * 256);
        if (hh == 0) ST64(((u32x2){C0, C1}), base + 2 * 256);
      } else {
        auto u0 = __builtin_amdgcn_permlane32_swap(A0, C0, false, false), u1 = __builtin_amdgcn_permlane32_swap(A1, C1, false, false);
        const u32x4 v = hh ? (u32x4){B0, B1, u0[0], u1[0]} : (u32x4){u0[1], u1[1], B0, B1};
        ST128(v, base + (3 + hh) * 256);
        if (hh == 0) ST64(((u32x2){A0, A1}), base + 2 * 256 + 8);
      }
      WAITV(0);
      if (lane == 0) ST64(tag2, WS_FLAGO + (((pp * 8 + t) * 2 + (hd >> 4)) * 16 + (hd & 15)) * 8);
    }
  }
  if (w == 0) STAMP(7);
  STAMP(40 + w);

  BARRIER();
  if (w == 0) STAMP(8);

  f32x4 acc[5];
#pragma unroll
  for (int ft = 0; ft < 5; ++ft) acc[ft] = (f32x4){0, 0, 0, 0};
  {
    const LAS char* wp = L + L_WO + (unsigned)(kh * 320 + 4 * fg + ((lane & 15) >> 2)) * 160 + (lane & 3) * 8;
    bf16x8 wa[3][5];
#pragma unroll
    for (int s = 0; s < 3; ++s)
#pragma unroll
      for (int ft = 0; ft < 5; ++ft) wa[s][ft] = cat8(trd(wp + s * 5120 + ft * 32), trd(wp + s * 5120 + 2560 + ft * 32));
    const int tg = 4 * R + nt;
    const unsigned fo = WS_FLAGO + ((((tg >> 4) * 8 + ((tg >> 1) & 7)) * 2 + kh) * 16 + l15) * 8;
    unsigned spins = 0;
    for (;;) {
      FENCE();
      const u32x2 f = LD64(fo);
      if (__all(f[0] == tag2[0] && f[1] == tag2[1]) || ++spins > SPIN_CAP) break;
      __builtin_amdgcn_s_sleep(1);
    }
    FENCE();
    if (w == 0) STAMP(9);
    STAMP(48 + w);
    u32x4 of[10];
#pragma unroll
    for (int s = 0; s < 10; ++s) of[s] = LD128(WS_O + (unsigned)(tg * 80 + 4 * (10 * kh + s)) * 256 + lane * 16);
    SBAR();
#pragma unroll
    for (int s = 0; s < 10; ++s) {
      const bf16x8 bfr = __builtin_bit_cast(bf16x8, of[s]);
#pragma unroll
      for (int ft = 0; ft < 5; ++ft) acc[ft] = MFMA16(s < 3 ? wa[s < 3 ? s : 0][ft] : cat8(trd(wp + s * 5120 + ft * 32), trd(wp + s * 5120 + 2560 + ft * 32)), bfr, acc[ft]);
    }
  }
  STAMP(56 + w);
  if (kh == 1) {
#pragma unroll
    for (int ft = 0; ft < 5; ++ft) *(LAS f32x4*)(L + L_RED + ((nt * 5 + ft) * 64 + lane) * 16) = acc[ft];
  }
  WAITL0(); BARRIER();
  if (w == 0) STAMP(10);
  if (kh == 0) {
    float s1 = 0.f, s2 = 0.f, s3 = 0.f;
#pragma unroll
    for (int ft = 0; ft < 5; ++ft) {
      const f32x4 o2 = *(const LAS f32x4*)(L + L_RED + ((nt * 5 + ft) * 64 + lane) * 16);
      const unsigned a01 = pk2(acc[ft][0] + o2[0], acc[ft][1] + o2[1]), a23 = pk2(acc[ft][2] + o2[2], acc[ft][3] + o2[3]);
      const unsigned h01 = pk2(bf2f(xr[ft][0]) + bf2f(a01), bfhi(xr[ft][0]) + bfhi(a01)), h23 = pk2(bf2f(xr[ft][1]) + bf2f(a23), bfhi(xr[ft][1]) + bfhi(a23));
      *(LAS u32x2*)(L + L_HST + nt * 2560 + l15 * 160 + ft * 32 + fg * 8) = (u32x2){h01, h23};
      s1 = dot2(h01, 0x3f803f80u, s1); s1 = dot2(h23, 0x3f803f80u, s1);
      s2 = dot2(h01, h01, s2); s2 = dot2(h23, h23, s2);
      s3 = dot2(h01, wrv[ft][0], s3); s3 = dot2(h23, wrv[ft][1], s3);
    }
    WAITL0();
#pragma unroll
    for (int i = 0; i < 3; ++i) {
      const int n = lane + 64 * i;
      if (n < 160) ST128(*(const LAS u32x4*)(L + L_HST + nt * 2560 + n * 16), WS_H + (unsigned)((R * 8 + cc) * 64 + 16 * nt) * 160 + n * 16);
    }
    s1 = swap_add(swap16_add(s1)); s2 = swap_add(swap16_add(s2)); s3 = swap_add(swap16_add(s3));
    WAITV(0);
    if (lane < 16) ST128(((u32x4){__float_as_uint(s1), __float_as_uint(s2), __float_as_uint(s3), tag32}), WS_STATS + ((R * 8 + cc) * 64 + 16 * nt + l15) * 16);
    if (w == 0) STAMP(11);
    BARRIER();
  } else {
    BARRIER();
    const int rl = lane >> 1, grow = 8 * ((rl >> 2) & 3) + 4 * (rl >> 4) + (rl & 3);
    if (is_gu) {
      const unsigned vo = (unsigned)grow * (F * 2) + (lane & 1) * 16;
#pragma unroll 1
      for (int pc = nt; pc < 40; pc += 4) { const int m = pc >= 20, s = pc - 20 * m; glds16(vo, (const char*)(m ? p.wu : p.wg) + (size_t)s * 32 * F * 2 + gs * 32, lds0 + L_FW + pc * 1024); }
    } else if (is_dn) {
      const unsigned vo = (unsigned)grow * (D * 2) + (lane & 1) * 16;
#pragma unroll 1
      for (int pc = nt; pc < 54; pc += 4) glds16(vo, (const char*)p.wd + (size_t)pc * 32 * D * 2 + dn * 32, lds0 + L_FW + pc * 1024);
    }
  }
  auto rank64 = [&](unsigned k) {
    const u32x4 oa = *(const LAS u32x4*)(L + L_KEY + w * 32), ob = *(const LAS u32x4*)(L + L_KEY + w * 32 + 16);
    unsigned c = 0;
#pragma unroll
    for (int e = 0; e < 4; ++e) c += (oa[e] > k) + (ob[e] > k);
    *(LAS unsigned char*)(L + L_PART + lane * 8 + w) = (unsigned char)c;
    WAITL0(); BARRIER();
    const u32x2 pr = *(const LAS u32x2*)(L + L_PART + lane * 8);
    return __builtin_amdgcn_sad_u8(pr[0], 0u, __builtin_amdgcn_sad_u8(pr[1], 0u, 0u));
  };
  if (bi == 31) {
    float swr = 0.f;
#pragma unroll
    for (int i = 0; i < 5; ++i) swr = dot2(wr5[i], 0x3f803f80u, swr);
    swr = wave_fsum(swr);
    const int tl = 32 * w + l31;
    const unsigned so = WS_STATS + (unsigned)(((4 * xcd + (tl >> 6)) * 8 + 4 * hh) * 64 + (tl & 63)) * 16;
    float t1, t2, t3;
    unsigned spins = 0;
    for (;;) {
      FENCE();
      const u32x4 g0 = LD128(so), g1 = LD128(so + 1024), g2 = LD128(so + 2048), g3 = LD128(so + 3072);
      t1 = (__uint_as_float(g0[0]) + __uint_as_float(g1[0])) + (__uint_as_float(g2[0]) + __uint_as_float(g3[0]));
      t2 = (__uint_as_float(g0[1]) + __uint_as_float(g1[1])) + (__uint_as_float(g2[1]) + __uint_as_float(g3[1]));
      t3 = (__uint_as_float(g0[2]) + __uint_as_float(g1[2])) + (__uint_as_float(g2[2]) + __uint_as_float(g3[2]));
      if (__all(g0[3] == tag32 && g1[3] == tag32 && g2[3] == tag32 && g3[3] == tag32) || ++spins > SPIN_CAP) break;
    }
    t1 = swap_add(t1); t2 = swap_add(t2); t3 = swap_add(t3);
    const float mean = t1 * (1.f / D);
    const float var = fmaxf(t2 * (1.f / D) - mean * mean, 0.f);
    const float mb = rbf(mean);
    const float sd = rbf(__builtin_amdgcn_sqrtf(rbf(rbf(var) + 1e-5f)));
    const unsigned rb = pk2((t3 - mb * swr) * __builtin_amdgcn_rcpf(sd), 0.f) & 0xffffu;
    const unsigned key = ((rb ^ ((rb & 0x8000u) ? 0xffffu : 0x8000u)) << 16) | (unsigned)(2047 - (256 * xcd + tl));
    const unsigned ms = (__float_as_uint(mb) & 0xffff0000u) | (__float_as_uint(sd) >> 16);
    if (w == 0) STAMP(12);
    if (lane < 32) *(LAS unsigned*)(L + L_K32 + tl * 4) = key;
    WAITL0();
    unsigned c = 0;
#pragma unroll
    for (int i = 0; i < 4; ++i) {
      const u32x4 o4 = *(const LAS u32x4*)(L + L_K32 + (32 * w + 16 * hh + 4 * i) * 4);
#pragma unroll
      for (int e = 0; e < 4; ++e) c += o4[e] > key;
    }
    { auto r = __builtin_amdgcn_permlane32_swap(c, c, false, false); c = r[0] + r[1]; }
    if (lane < 32 && c < 8) { *(LAS unsigned*)(L + L_KEY + (8 * w + c) * 4) = key; *(LAS unsigned*)(L + L_KEY + 256 + (8 * w + c) * 4) = ms; }
    WAITL0(); BARRIER();
    const unsigned k = *(const LAS unsigned*)(L + L_KEY + lane * 4), m2 = *(const LAS unsigned*)(L + L_KEY + 256 + lane * 4);
    const unsigned rk = rank64(k);
    if (w == 0) { if (rk < 8) ST128(((u32x4){k, tag2[0], m2, tag2[1]}), WS_CAND + xcd * 128 + rk * 16); STAMP(26); }
    BARRIER();
  }
  if (w == 0) {
    u32x4 a;
    unsigned spins = 0;
    for (;;) {
      FENCE();
      a = LD128(WS_CAND + lane * 16);
      if (__all(a[1] == tag2[0] && a[3] == tag2[1]) || ++spins > SPIN_CAP) break;
      if (is_gu || is_dn) __builtin_amdgcn_s_sleep(1); else __builtin_amdgcn_s_sleep(8);
    }
    STAMP(13);
    *(LAS unsigned*)(L + L_KEY + lane * 4) = a[0]; *(LAS unsigned*)(L + L_KEY + 256 + lane * 4) = a[2];
    if (b == 0 && lane == 0) ST64(tag2, WS_EPOCH);
  }
  const int hc0 = (lane * 6554) >> 16, hc1 = ((64 + l15) * 6554) >> 16;
  const unsigned ho0 = (unsigned)hc0 * (64 * 160) + (lane - 10 * hc0) * 16, ho1 = (unsigned)hc1 * (64 * 160) + (64 + l15 - 10 * hc1) * 16;
  if (kh == 1) WAITV(0);
  WAITL0(); BARRIER();
  {
    const unsigned k = *(const LAS unsigned*)(L + L_KEY + lane * 4), m2 = *(const LAS unsigned*)(L + L_KEY + 256 + lane * 4);
    const unsigned rk = rank64(k);
    if (rk < 8) *(LAS u32x2*)(L + L_SEL + rk * 8) = (u32x2){k, m2};
    WAITL0();
  }
  if (w == 0) STAMP(14);

  auto h_row = [&](int row, u32x4& h0, u32x4& h1) {
    const unsigned rb = WS_H + (unsigned)((row >> 6) * 8 * 64 + (row & 63)) * 160;
    h0 = LD128(rb + ho0); h1 = LD128(rb + ho1);
  };
  auto ln8 = [&](const u32x4& h, float m, float iv) { u32x4 n;
#pragma unroll
    for (int e = 0; e < 4; ++e) { const unsigned d = pk2(bf2f(h[e]) - m, bfhi(h[e]) - m); n[e] = pk2(bf2f(d) * iv, bfhi(d) * iv); } return n; };
  auto out_row = [&]() {
    const int row = 64 * R + 8 * cc + w;
    const u32x2 sk = *(const LAS u32x2*)(L + L_SEL + (lane & 7) * 8);
    if (!__any(2047 - (int)(sk[0] & 0x7ff) == row)) {
      u32x4 h0, h1; h_row(row, h0, h1);
      const u32x4 g = LD128(WS_STATS + (unsigned)((R * 8 + (lane & 7)) * 64 + 8 * cc + w) * 16);
      float t1 = __uint_as_float(g[0]), t2 = __uint_as_float(g[1]);
      t1 = dppadd<0x141>(t1); t2 = dppadd<0x141>(t2);
      t1 = dppadd<0xb1>(t1); t2 = dppadd<0xb1>(t2);
      t1 = dppadd<0x4e>(t1); t2 = dppadd<0x4e>(t2);
      const float mean = t1 * (1.f / D);
      const float m = rbf(mean), iv = __builtin_amdgcn_rcpf(rbf(__builtin_amdgcn_sqrtf(rbf(rbf(fmaxf(t2 * (1.f / D) - mean * mean, 0.f)) + 1e-5f))));
      const u32x4 n0 = ln8(h0, m, iv), n1 = ln8(h1, m, iv);
      u32x4 r0, r1;
#pragma unroll
      for (int e = 0; e < 4; ++e) { r0[e] = pk2(bf2f(h0[e]) + bf2f(n0[e]), bfhi(h0[e]) + bfhi(n0[e])); r1[e] = pk2(bf2f(h1[e]) + bf2f(n1[e]), bfhi(h1[e]) + bfhi(n1[e])); }
      *(u32x4*)(p.out + (size_t)row * D + lane * 8) = r0;
      if (lane < 16) *(u32x4*)(p.out + (size_t)row * D + 512 + lane * 8) = r1;
    }
  };
  if (is_gu) {
    {
      const u32x2 sk = *(const LAS u32x2*)(L + L_SEL + w * 8);
      u32x4 h0, h1; h_row(2047 - (int)(sk[0] & 0x7ff), h0, h1);
      const float m = __uint_as_float(sk[1] & 0xffff0000u), iv = __builtin_amdgcn_rcpf(__uint_as_float(sk[1] << 16));
      *(LAS u32x4*)(L + L_XS + w * 1296 + lane * 16) = ln8(h0, m, iv);
      if (lane < 16) *(LAS u32x4*)(L + L_XS + w * 1296 + 1024 + lane * 16) = ln8(h1, m, iv);
    }
    WAITL0(); BARRIER();
    if (w == 0) STAMP(15);
    {
      const int m = w & 1, kq = w >> 1;
      f32x4 a4 = {0, 0, 0, 0}, b4 = {0, 0, 0, 0};
#pragma unroll
      for (int i = 0; i < 5; ++i) {
        const int s = 5 * kq + i;
        const LAS char* ap = L + L_FW + (m * 20 + s) * 1024 + lane * 8;
        const bf16x8 af = cat8(trd(ap), trd(ap + 512));
        const bf16x8 bfv = *(const LAS bf16x8*)(L + L_XS + (lane & 7) * 1296 + s * 64 + fg * 16);
        if (i & 1) b4 = MFMA16(af, bfv, b4); else a4 = MFMA16(af, bfv, a4);
      }
      *(LAS f32x4*)(L + L_FRED + (w * 64 + lane) * 16) = a4 + b4;
    }
    WAITL0(); BARRIER();
    if (w == 0) {
      f32x4 g4 = {0, 0, 0, 0}, u4 = {0, 0, 0, 0};
#pragma unroll
      for (int q = 0; q < 4; ++q) { g4 += *(const LAS f32x4*)(L + L_FRED + ((2 * q) * 64 + lane) * 16); u4 += *(const LAS f32x4*)(L + L_FRED + ((2 * q + 1) * 64 + lane) * 16); }
      float a[4];
#pragma unroll
      for (int e = 0; e < 4; ++e) { const float g = rbf(g4[e]), u = rbf(u4[e]); const float sg = rbf(g * __builtin_amdgcn_rcpf(1.f + __builtin_amdgcn_exp2f(g * -1.4426950408889634f))); a[e] = sg * u; }
      if (l15 < 8) ST128(((u32x4){pk2(a[0], a[1]), tag32, pk2(a[2], a[3]), tag32}), WS_ACT + (((gs >> 1) * 4 + 2 * (gs & 1) + (fg >> 1)) * 8 + l15) * 32 + (fg & 1) * 16);
      STAMP(16);
    }
    out_row();
  } else if (is_dn) {
    const u32x2 sk = *(const LAS u32x2*)(L + L_SEL + (lane & 7) * 8);
    const int row = 2047 - (int)(sk[0] & 0x7ff);
    u32x2 hv = {0, 0}, nv = {0, 0};
    float sig = 0.f;
    if (w == 0) {
      const int col = 16 * dn + 4 * fg, c = col / 80;
      hv = LD64(WS_H + (unsigned)(((row >> 6) * 8 + c) * 64 + (row & 63)) * 160 + (col - 80 * c) * 2);
      const unsigned rk = sk[0] >> 16, rbits = (rk & 0x8000u) ? (rk ^ 0x8000u) : (rk ^ 0xffffu);
      sig = rbf(__builtin_amdgcn_rcpf(1.f + __builtin_amdgcn_exp2f(bf2f(rbits) * -1.4426950408889634f)));
      const float m = __uint_as_float(sk[1] & 0xffff0000u), iv = __builtin_amdgcn_rcpf(__uint_as_float(sk[1] << 16));
#pragma unroll
      for (int e = 0; e < 2; ++e) { const unsigned d2 = pk2(bf2f(hv[e]) - m, bfhi(hv[e]) - m); nv[e] = pk2(bf2f(d2) * iv, bfhi(d2) * iv); }
      asm volatile("" : "+v"(nv), "+v"(hv), "+v"(sig));
    } else out_row();
    f32x4 a4 = {0, 0, 0, 0}, b4 = {0, 0, 0, 0};
    {
      bf16x8 af[7];
#pragma unroll
      for (int i = 0; i < 7; ++i) { const LAS char* ap = L + L_FW + min(w + 8 * i, 53) * 1024 + lane * 8; af[i] = cat8(trd(ap), trd(ap + 512)); }
      u32x4 g0[7], g1[7];
      unsigned spins = 0;
      for (;;) {
        FENCE();
        bool ok = true;
#pragma unroll
        for (int i = 0; i < 7; ++i) {
          const int s = min(w + 8 * i, 53);
          const unsigned ao = WS_ACT + ((s * 4 + fg) * 8 + (lane & 7)) * 32;
          g0[i] = LD128(ao); g1[i] = LD128(ao + 16);
        }
#pragma unroll
        for (int i = 0; i < 7; ++i) ok = ok && g0[i][1] == tag32 && g0[i][3] == tag32 && g1[i][1] == tag32 && g1[i][3] == tag32;
        if (__all(ok) || ++spins > SPIN_CAP) break;
      }
      if (w == 0) STAMP(17);
#pragma unroll
      for (int i = 0; i < 7; ++i) {
        const bf16x8 bv = __builtin_bit_cast(bf16x8, (u32x4){g0[i][0], g0[i][2], g1[i][0], g1[i][2]});
        if (i < 6 || w < 6) { if (i & 1) b4 = MFMA16(af[i], bv, b4); else a4 = MFMA16(af[i], bv, a4); }
      }
    }
    *(LAS f32x4*)(L + L_FRED + (w * 64 + lane) * 16) = a4 + b4;
    WAITL0(); BARRIER();
    if (w == 0) {
      f32x4 y = {0, 0, 0, 0};
#pragma unroll
      for (int q = 0; q < 8; ++q) y += *(const LAS f32x4*)(L + L_FRED + (q * 64 + lane) * 16);
      unsigned r2[2];
#pragma unroll
      for (int e = 0; e < 2; ++e) {
        const unsigned y2 = pk2(rbf(y[2 * e]) * sig, rbf(y[2 * e + 1]) * sig);
        const unsigned t2 = pk2(bf2f(nv[e]) + bf2f(y2), bfhi(nv[e]) + bfhi(y2));
        r2[e] = pk2(bf2f(hv[e]) + bf2f(t2), bfhi(hv[e]) + bfhi(t2));
      }
      if (l15 < 8) *(u32x2*)(p.out + (size_t)row * D + 16 * dn + 4 * fg) = (u32x2){r2[0], r2[1]};
      STAMP(18);
      out_row();
    }
  } else {
    out_row();
  }
  if (w == 0) STAMP(19);
  STAMP_FLUSH();
}

extern "C" void kernel_launch(void* const* d_in, const int* in_sizes, int n_in, void* d_out, int out_size, void* d_ws, size_t ws_size, hipStream_t stream) {
  Params p;
  p.x = (const u16*)d_in[0]; p.wqkv = (const u16*)d_in[1]; p.wo = (const u16*)d_in[2]; p.wr = (const u16*)d_in[4];
  p.wg = (const u16*)d_in[5]; p.wu = (const u16*)d_in[6]; p.wd = (const u16*)d_in[7]; p.out = (u16*)d_out; p.ws = (unsigned char*)d_ws;
  fused_kernel<<<256, 512, 0, stream>>>(p);
}
```

```cpp
#define ROT (((bi >> 3) * 5 / 2) % 10)
#include <hip/hip_runtime.h>
#include <stdint.h>

typedef unsigned short u16;
typedef short bf16x8 __attribute__((ext_vector_type(8)));
typedef short bf16x4 __attribute__((ext_vector_type(4)));
typedef __bf16 bf2 __attribute__((ext_vector_type(2)));
typedef float f32x16 __attribute__((ext_vector_type(16)));
typedef float f32x4 __attribute__((ext_vector_type(4)));
typedef float f32x2 __attribute__((ext_vector_type(2)));
typedef unsigned u32x4 __attribute__((ext_vector_type(4)));
typedef unsigned u32x2 __attribute__((ext_vector_type(2)));
typedef unsigned long long u64;
#define LAS __attribute__((address_space(3)))
#define SBAR() __builtin_amdgcn_sched_barrier(0)
#define BARRIER() asm volatile("s_barrier" ::: "memory")
#define WAITL0() asm volatile("s_waitcnt lgkmcnt(0)" ::: "memory")
#define WAITV(n) asm volatile("s_waitcnt vmcnt(" #n ")" ::: "memory")
#define FENCE() asm volatile("" ::: "memory")

constexpr int S = 2048, D = 640, F = 1728;
constexpr int SC1 = 16;

constexpr unsigned WS_EPOCH = 0;
constexpr unsigned WS_FLAGA = 4096;
constexpr unsigned WS_FLAGO = WS_FLAGA + 32768;
constexpr unsigned WS_CAND = WS_FLAGO + 16384;
constexpr unsigned WS_HDONE = WS_CAND + 4096;
constexpr unsigned WS_ACT = WS_HDONE + 4096;
constexpr unsigned WS_ST1 = 131072;
constexpr unsigned WS_CS1 = WS_ST1 + 32768;
constexpr unsigned WS_STATS = 524288;
constexpr unsigned WS_QKV = 1048576;
constexpr unsigned WS_O = WS_QKV + 480 * 16384;
constexpr unsigned WS_H = WS_O + S * D * 2;
#define STAMP_OFF (200u << 20)
#define NSTAMP 64

constexpr int L_SLOT = 32768, L_W = 16384;
constexpr int PL = 4128;
constexpr int L_K = 0, L_V = 5 * PL;
constexpr int L_WO = 45568;
constexpr int L_RED = 0;
constexpr int L_HST = 20480;
constexpr int L_FW = L_WO;
constexpr int L_XS = 0;
constexpr int L_FRED = 12288;
constexpr int L_MISC = L_WO + 640 * 160;
constexpr int L_RS = 0;
constexpr int L_CS = 1024;
constexpr int L_CNT = L_MISC + 3600;
constexpr int L_F1 = L_MISC + 3604, L_C1 = L_MISC + 3608;
constexpr int L_PART = L_MISC + 2048;
constexpr int L_K32 = L_MISC + 2560;
constexpr int L_HD = L_MISC + 3712;
constexpr int L_SEL = L_MISC + 3968;
constexpr int L_STAMP = L_MISC + 4096;
constexpr int L_KEY = L_MISC + 4608 + 256;
constexpr int LDS_BYTES = 163840;
static_assert(L_V + 6 * PL <= L_WO && L_KEY + 2048 <= LDS_BYTES, "lds map");

struct Params { const u16* x; const u16* wqkv; const u16* wo; const u16* wr; const u16* wg; const u16* wu; const u16* wd; u16* out; unsigned char* ws; };

__device__ __forceinline__ float bf2f(unsigned v) { return __uint_as_float(v << 16); }
__device__ __forceinline__ float bfhi(unsigned v) { return __uint_as_float(v & 0xffff0000u); }
__device__ __forceinline__ unsigned pk2(float a, float b) { f32x2 f = {a, b}; return __builtin_bit_cast(unsigned, __builtin_convertvector(f, bf2)); }
__device__ __forceinline__ float rbf(float f) { return __uint_as_float(pk2(f, 0.f) << 16); }
__device__ __forceinline__ float dot2(unsigned a, unsigned b, float c) { return __builtin_amdgcn_fdot2_f32_bf16(__builtin_bit_cast(bf2, a), __builtin_bit_cast(bf2, b), c, false); }
__device__ __forceinline__ float swap_add(float v) { auto r = __builtin_amdgcn_permlane32_swap(__float_as_uint(v), __float_as_uint(v), false, false); return __uint_as_float(r[0]) + __uint_as_float(r[1]); }
__device__ __forceinline__ float swap16_add(float v) { auto r = __builtin_amdgcn_permlane16_swap(__float_as_uint(v), __float_as_uint(v), false, false); return __uint_as_float(r[0]) + __uint_as_float(r[1]); }
template <int CTRL> __device__ __forceinline__ float dppadd(float v) { return v + __int_as_float(__builtin_amdgcn_update_dpp(0, __float_as_int(v), CTRL, 0xf, 0xf, false)); }
__device__ __forceinline__ float wave_fsum(float v) { v = dppadd<0x128>(v); v = dppadd<0x124>(v); v = dppadd<0x122>(v); v = dppadd<0x121>(v); return swap_add(swap16_add(v)); }
template <int CTRL> __device__ __forceinline__ unsigned dppmax(unsigned v) { return max(v, (unsigned)__builtin_amdgcn_update_dpp(0, (int)v, CTRL, 0xf, 0xf, false)); }
__device__ __forceinline__ unsigned wave_umax(unsigned v) {
  v = dppmax<0x128>(v); v = dppmax<0x124>(v); v = dppmax<0x122>(v); v = dppmax<0x121>(v);
  auto r = __builtin_amdgcn_permlane16_swap(v, v, false, false); v = max(r[0], r[1]);
  auto q = __builtin_amdgcn_permlane32_swap(v, v, false, false); return max(q[0], q[1]);
}
__device__ __forceinline__ void glds16(unsigned voff, const void* sbase, unsigned lds) {
  asm volatile("s_mov_b32 m0, %2\n\ts_nop 0\n\tglobal_load_lds_dwordx4 %0, %1" ::"v"(voff), "s"(sbase), "s"(lds) : "memory");
}
__device__ __forceinline__ void glds16c(unsigned voff, const void* sbase, unsigned lds) {
  asm volatile("s_mov_b32 m0, %2\n\ts_nop 0\n\tglobal_load_lds_dwordx4 %0, %1 sc1" ::"v"(voff), "s"(sbase), "s"(lds) : "memory");
}
__device__ __forceinline__ bf16x4 trd(const LAS char* p) { return __builtin_amdgcn_ds_read_tr16_b64_v4i16((LAS bf16x4*)p); }
__device__ __forceinline__ bf16x8 cat8(bf16x4 a, bf16x4 b) { return (bf16x8){a[0], a[1], a[2], a[3], b[0], b[1], b[2], b[3]}; }
#define MFMA32(a, b, c) __builtin_amdgcn_mfma_f32_32x32x16_bf16(a, b, c, 0, 0, 0)
#define MFMA32K8(a, b, c) __builtin_amdgcn_mfma_f32_32x32x8bf16_1k(a, b, c, 0, 0, 0)
#define MFMA16(a, b, c) __builtin_amdgcn_mfma_f32_16x16x32_bf16(a, b, c, 0, 0, 0)
#define LD128(off) __builtin_amdgcn_raw_buffer_load_b128(rws, (off), 0, SC1)
#define LD64(off) __builtin_amdgcn_raw_buffer_load_b64(rws, (off), 0, SC1)
#define ST128(v, off) __builtin_amdgcn_raw_buffer_store_b128((v), rws, (off), 0, SC1)
#define ST64(v, off) __builtin_amdgcn_raw_buffer_store_b64((v), rws, (off), 0, SC1)

#define STAMP(k) do {} while (0)
#define STAMP_INIT() do {} while (0)
#define PSTAMP(i) do {} while (0)
#define STAMP_ZERO() do {} while (0)
#define STAMP_PA() do {} while (0)
#define STAMP_FLUSH() do {} while (0)

#define DIST 3
constexpr unsigned SPIN_CAP = 1u << 17;

__global__ __launch_bounds__(512, 2) void fused_kernel(Params p) {
  __shared__ __attribute__((aligned(16))) char smem[LDS_BYTES];
  LAS char* L = (LAS char*)smem;
  const unsigned lds0 = (unsigned)(uintptr_t)smem;
  const int tid = threadIdx.x, lane = tid & 63;
  const int w = __builtin_amdgcn_readfirstlane(tid >> 6);
  const int b = blockIdx.x, xcd = b & 7, bi = b >> 3;
  const int l31 = lane & 31, hh = lane >> 5, l15 = lane & 15, fg = lane >> 4;
  const __amdgpu_buffer_rsrc_t rws = __builtin_amdgcn_make_buffer_rsrc(p.ws, 0, 0x7fffffff, 0x00020000);
  const int aP = 4 * (xcd >> 1) + (bi >> 3), aj = 8 * (xcd & 1) + (bi & 7);
  const int hd = 4 * xcd + (bi >> 3), pp = bi & 7;
  const int R = 4 * xcd + (bi >> 3), cc = bi & 7;
  const int gs = xcd * 14 + bi;
  const bool is_gu = bi < 14 && gs < 108;
  const int dn = xcd * 5 + (bi - 14);
  const bool is_dn = bi >= 14 && bi < 19;
  STAMP_INIT();
  u32x2 tag2;
  auto read_epoch = [&]() {
    u64 e;
    asm volatile("s_load_dwordx2 %0, %1, 0x0\n\ts_waitcnt lgkmcnt(0)" : "=s"(e) : "s"(p.ws) : "memory");
    e += 1;
    tag2 = (u32x2){(unsigned)e, (unsigned)(e >> 32)};
  };

  auto rows_issue = [&](int lw, u32x4& ra0, u32x4& ra1, u32x4& rb0, u32x4& rb1) {
    if (lw == 0) ST128(((u32x4){0, 0, 0, 0}), WS_STATS + (R * 8 + cc) * 1024 + lane * 16);
    if (lw == 1 && is_gu && lane < 32) ST128(((u32x4){0, 0, 0, 0}), WS_ACT + (((gs >> 1) * 4 + 2 * (gs & 1) + (lane >> 4)) * 8 + (lane & 7)) * 32 + ((lane >> 3) & 1) * 16);
    const char* rb = (const char*)p.x + (size_t)(64 * R + 8 * cc + 2 * lw) * 1280;
    ra0 = *(const u32x4*)(rb + lane * 16); ra1 = *(const u32x4*)(rb + 1024 + l15 * 16);
    rb0 = *(const u32x4*)(rb + 1280 + lane * 16); rb1 = *(const u32x4*)(rb + 1280 + 1024 + l15 * 16);
    FENCE();
  };
  auto rows_finish = [&](int lw, const u32x4& ra0, const u32x4& ra1, const u32x4& rb0, const u32x4& rb1) {
    float mean[2], rs[2];
#pragma unroll
    for (int r = 0; r < 2; ++r) {
      const u32x4 c0 = r ? rb0 : ra0, c1 = r ? rb1 : ra1;
      float s0 = 0.f, s1 = 0.f;
#pragma unroll
      for (int e = 0; e < 4; ++e) { s0 = dot2(c0[e], 0x3f803f80u, s0); s1 = dot2(c1[e], 0x3f803f80u, s1); }
      mean[r] = wave_fsum(s0 + (lane < 16 ? s1 : 0.f)) * (1.f / D);
      const f32x2 mm = {mean[r], mean[r]};
      f32x2 q0 = {0.f, 0.f}, q1 = {0.f, 0.f};
#pragma unroll
      for (int e = 0; e < 4; ++e) { const f32x2 d0 = (f32x2){bf2f(c0[e]), bfhi(c0[e])} - mm, d1 = (f32x2){bf2f(c1[e]), bfhi(c1[e])} - mm; q0 += d0 * d0; q1 += d1 * d1; }
      const float var = wave_fsum((q0[0] + q0[1]) + (lane < 16 ? q1[0] + q1[1] : 0.f)) * (1.f / D);
      rs[r] = __builtin_amdgcn_rcpf(rbf(__builtin_amdgcn_sqrtf(rbf(rbf(var) + 1e-5f))));
    }
    WAITV(0);
    if (lane < 2) ST128(((u32x4){__float_as_uint(rbf(lane ? mean[1] : mean[0])), __float_as_uint(lane ? rs[1] : rs[0]), tag2[0], tag2[1]}), WS_ST1 + (unsigned)(64 * R + 8 * cc + 2 * lw + lane) * 16);
  };

  auto lds_ctl_init = [&]() {
    if (tid == 0) { *(LAS unsigned*)(L + L_F1) = 0; *(LAS unsigned*)(L + L_C1) = 0; }
    STAMP_ZERO();
  };
  if (aj < 15 && w >= 4) {
    const int lw = w - 4;
    const unsigned xsw = (unsigned)(4 * (lw & 1) + (lane >> 4));
    const unsigned voff_x = (unsigned)(lane >> 3) * 1280u + (((unsigned)(lane & 7) ^ xsw) << 4);
    const unsigned ch = (unsigned)(lane & 15) ^ (((unsigned)(lane >> 4) << 2) | (unsigned)lw);
    const unsigned voff_w = (unsigned)(lane >> 4) * 3840u + ch * 16u;
    const char* xb = (const char*)p.x + (size_t)(128 * aP + 8 * lw) * 1280;
    const char* wb = (const char*)p.wqkv + (size_t)(4 * lw) * 3840 + 256 * aj;
    const unsigned lbase = lds0 + lw * 1024;
    const int rot = ROT;
    auto issue = [&](int s) {
      const unsigned sl = lbase + (s >= 5 ? s - 5 : s) * L_SLOT;
      int ss = s + rot; if (ss >= 10) ss -= 10;
#pragma unroll
      for (int i = 0; i < 4; ++i) {
        glds16(voff_x, xb + (size_t)i * 32 * 1280 + ss * 128, sl + i * 4096);
        glds16(voff_w, wb + ((size_t)ss * 64 + i * 16) * 3840, sl + L_W + i * 4096);
      }
    };
    BARRIER();
    issue(0); issue(1); issue(2);
    read_epoch();
    WAITV(16); BARRIER();
    PSTAMP(1);
#pragma unroll 1
    for (int s = 0; s <= 5; ++s) { issue(s + 3); WAITV(16); BARRIER(); }
    issue(9);
#define W7 13
    const unsigned so = WS_ST1 + (unsigned)(128 * aP + 32 * lw + l31) * 16, co = WS_CS1 + (unsigned)((aj * 16 + fg) * 64 + 16 * lw + l15) * 16;
    u32x4 gs1 = LD128(so), gc0 = LD128(co), gc1 = LD128(co + 4096), gc2 = LD128(co + 8192), gc3 = LD128(co + 12288);
    WAITV(21); BARRIER();
    WAITV(13); BARRIER();
    WAITV(5); BARRIER();
    unsigned spins = 0;
    for (;;) {
      const bool ok = gs1[2] == tag2[0] && gs1[3] == tag2[1] && gc0[2] == tag2[0] && gc0[3] == tag2[1] && gc1[2] == tag2[0] && gc1[3] == tag2[1] && gc2[2] == tag2[0] && gc2[3] == tag2[1] && gc3[2] == tag2[0] && gc3[3] == tag2[1];
      if (__all(ok) || ++spins > SPIN_CAP) break;
      FENCE();
      gs1 = LD128(so); gc0 = LD128(co); gc1 = LD128(co + 4096); gc2 = LD128(co + 8192); gc3 = LD128(co + 12288);
    }
    if (lane < 32) *(LAS u32x2*)(L + L_RS + (32 * lw + l31) * 8) = (u32x2){gs1[0], gs1[1]};
    {
      const float c0 = swap_add(swap16_add((__uint_as_float(gc0[0]) + __uint_as_float(gc1[0])) + (__uint_as_float(gc2[0]) + __uint_as_float(gc3[0]))));
      const float c1 = swap_add(swap16_add((__uint_as_float(gc0[1]) + __uint_as_float(gc1[1])) + (__uint_as_float(gc2[1]) + __uint_as_float(gc3[1]))));
      if (lane < 16) *(LAS f32x2*)(L + L_CS + (32 * lw + 2 * lane) * 4) = (f32x2){c0, c1};
    }
    WAITL0(); BARRIER();
    BARRIER();
  } else if (w < 4) {
    u32x4 ra0, ra1, rb0, rb1;
    rows_issue(w, ra0, ra1, rb0, rb1);
    if (aj < 15) {
      BARRIER();
      read_epoch();
      rows_finish(w, ra0, ra1, rb0, rb1);
      PSTAMP(7);
      const int wm = w >> 1, wn = w & 1;
      unsigned xa[4], wa[4];
#pragma unroll
      for (int ks = 0; ks < 4; ++ks) xa[ks] = (unsigned)(64 * wm + l31) * 128u + ((((unsigned)(2 * ks + hh)) ^ (((unsigned)l31 >> 1) & 7u)) << 4);
      {
        const unsigned blk = (lane >> 4) & 1, q = (lane & 15) >> 2, pq = lane & 3;
#pragma unroll
        for (int j = 0; j < 2; ++j)
#pragma unroll
          for (int t = 0; t < 2; ++t) {
            const unsigned row = 8 * hh + 4 * t + q, chn = 4 * (2 * wn + (j ^ wm)) + 2 * blk + (pq >> 1), m = (q << 2) | (unsigned)(2 * hh + t);
            wa[j * 2 + t] = L_W + 256u * row + 16u * (chn ^ m) + 8u * (pq & 1);
          }
      }
      f32x16 a00 = {}, a01 = {}, a10 = {}, a11 = {};
      bf16x8 xf0[2], wf0[2], xf1[2], wf1[2];

      unsigned xb[4], wb[4];
#define SETSLOT(so) do { _Pragma("unroll") for (int k_ = 0; k_ < 4; ++k_) { xb[k_] = xa[k_] + (so); wb[k_] = wa[k_] + (so); } } while (0)
#define RD(XF, WF, ks) do { \
        XF[0] = *(const LAS bf16x8*)(L + xb[ks]); WF[0] = cat8(trd(L + wb[0] + (ks) * 4096), trd(L + wb[1] + (ks) * 4096)); \
        WF[1] = cat8(trd(L + wb[2] + (ks) * 4096), trd(L + wb[3] + (ks) * 4096)); XF[1] = *(const LAS bf16x8*)(L + xb[ks] + 4096); } while (0)
#define MM(XF, WF) do { \
        a00 = MFMA32(WF[0], XF[0], a00); a01 = MFMA32(WF[1], XF[0], a01); \
        a10 = MFMA32(WF[0], XF[1], a10); a11 = MFMA32(WF[1], XF[1], a11); } while (0)
#define PINALL() asm volatile("" : "+v"(a00), "+v"(a01), "+v"(a10), "+v"(a11))
#define ILV() do { __builtin_amdgcn_sched_group_barrier(0x8, 1, 0); __builtin_amdgcn_sched_group_barrier(0x100, 3, 0); \
        __builtin_amdgcn_sched_group_barrier(0x8, 1, 0); __builtin_amdgcn_sched_group_barrier(0x100, 3, 0); __builtin_amdgcn_sched_group_barrier(0x8, 2, 0); } while (0)
#define STEP(XN, WN, ksn, XC, WC) do { RD(XN, WN, ksn); MM(XC, WC); ILV(); PINALL(); SBAR(); } while (0)
      BARRIER();
      SETSLOT(0);
      RD(xf0, wf0, 0);
      SBAR();
#pragma unroll 1
      for (int s = 0; s < 9; ++s) {
        STEP(xf1, wf1, 1, xf0, wf0);
        STEP(xf0, wf0, 2, xf1, wf1);
        STEP(xf1, wf1, 3, xf0, wf0);
        SETSLOT((s >= 4 ? s - 4 : s + 1) * L_SLOT);
        WAITL0(); BARRIER();
        STEP(xf0, wf0, 0, xf1, wf1);
      }
      STEP(xf1, wf1, 1, xf0, wf0);
      STEP(xf0, wf0, 2, xf1, wf1);
      STEP(xf1, wf1, 3, xf0, wf0);
      MM(xf1, wf1); PINALL();
      PSTAMP(2);
      WAITL0(); BARRIER();
      PSTAMP(6);
      const float qs = aj < 5 ? 0.22360679774997896f * 1.4426950408889634f : 1.f;
      f32x2 rs0 = *(const LAS f32x2*)(L + L_RS + (64 * wm + l31) * 8), rs1 = *(const LAS f32x2*)(L + L_RS + (64 * wm + 32 + l31) * 8);
      const float sc0 = rs0[1] * qs, nm0 = -rs0[0] * sc0, sc1 = rs1[1] * qs, nm1 = -rs1[0] * sc1;
#pragma unroll
      for (int j = 0; j < 2; ++j) {
        const int fj = 64 * wn + 32 * (j ^ wm) + 4 * hh;
        const unsigned go = WS_QKV + (unsigned)(32 * aj + (fj >> 2)) * 16384u + (unsigned)(128 * aP + 64 * wm + l31) * 8u;
#pragma unroll
        for (int rq = 0; rq < 4; ++rq) {
          const f32x4 cs = *(const LAS f32x4*)(L + L_CS + (fj + 8 * rq) * 4);
          const f32x16& a0 = j == 0 ? a00 : a01; const f32x16& a1 = j == 0 ? a10 : a11;
          const f32x2 c01 = {cs[0], cs[1]}, c23 = {cs[2], cs[3]};
          const f32x2 u0 = (f32x2){a0[rq * 4], a0[rq * 4 + 1]} * sc0 + c01 * nm0, u1 = (f32x2){a0[rq * 4 + 2], a0[rq * 4 + 3]} * sc0 + c23 * nm0;
          const f32x2 u2 = (f32x2){a1[rq * 4], a1[rq * 4 + 1]} * sc1 + c01 * nm1, u3 = (f32x2){a1[rq * 4 + 2], a1[rq * 4 + 3]} * sc1 + c23 * nm1;
          ST64(((u32x2){pk2(u0[0], u0[1]), pk2(u1[0], u1[1])}), go + 2 * rq * 16384);
          ST64(((u32x2){pk2(u2[0], u2[1]), pk2(u3[0], u3[1])}), go + 2 * rq * 16384 + 256);
        }
      }
      PSTAMP(4);
      lds_ctl_init();
      WAITV(0); WAITL0();
      PSTAMP(5);
      BARRIER();
    } else {
      read_epoch();
      rows_finish(w, ra0, ra1, rb0, rb1);
    }
  } else {
    read_epoch();
  }
  if (aj >= 15) {
    const int chn = min(32 * w + l31, 239);
    const char* wp0 = (const char*)p.wqkv + (size_t)(40 * aP + 20 * hh) * 3840 + chn * 16;
    u32x4 wv[20];
#pragma unroll
    for (int i = 0; i < 20; ++i) wv[i] = *(const u32x4*)(wp0 + (size_t)i * 3840);
    SBAR();
    f32x2 c2[4] = {{0.f, 0.f}, {0.f, 0.f}, {0.f, 0.f}, {0.f, 0.f}};
#pragma unroll
    for (int i = 0; i < 20; ++i)
#pragma unroll
      for (int e = 0; e < 4; ++e) c2[e] += (f32x2){bf2f(wv[i][e]), bfhi(wv[i][e])};
#pragma unroll
    for (int e = 0; e < 4; ++e) c2[e] = (f32x2){swap_add(c2[e][0]), swap_add(c2[e][1])};
    if (lane < 32 && 32 * w + l31 < 240) {
#pragma unroll
      for (int e = 0; e < 4; ++e) ST128(((u32x4){__float_as_uint(c2[e][0]), __float_as_uint(c2[e][1]), tag2[0], tag2[1]}), WS_CS1 + (unsigned)(((chn >> 4) * 16 + aP) * 64 + 4 * (chn & 15) + e) * 16);
    }
    lds_ctl_init(); WAITL0(); BARRIER();
  }
  const unsigned tag32 = tag2[0] | 1u;
  if (aj < 15 && tid == 0) ST64(tag2, WS_FLAGA + (aP * 16 + aj) * 128);
  PSTAMP(3);
  STAMP_PA();

  const int nt = w & 3, kh = w >> 2;
  const int tok = 64 * R + 16 * nt + l15;
  u32x2 xr[5], wrv[5];
  unsigned wr5[5];
  {
    const int t = w;
    *(LAS u32x2*)(L + L_V + 5 * PL + tid * 8) = (u32x2){(pp > 0 || tid >= 256) ? 0x00003f80u : 0u, 0u};
    if (pp == 0) {
#pragma unroll
      for (int i = 0; i < 3; ++i) { const int n = tid + 512 * i; if (n < 1280) *(LAS u32x4*)(L + (n >> 7) * PL + (n & 127) * 16) = (u32x4){0, 0, 0, 0}; }
    }
    if (kh == 0) {
#pragma unroll
      for (int ft = 0; ft < 5; ++ft) {
        xr[ft] = *(const u32x2*)(p.x + (size_t)tok * D + 80 * cc + 16 * ft + 4 * fg);
        wrv[ft] = *(const u32x2*)(p.wr + 80 * cc + 16 * ft + 4 * fg);
      }
    }
    if (bi == 31) {
#pragma unroll
      for (int i = 0; i < 5; ++i) wr5[i] = ((const unsigned*)p.wr)[lane + 64 * i];
    }
    auto lds_wait = [&](int off, unsigned v) { unsigned sp = 0; while (__hip_atomic_load((LAS unsigned*)(L + off), __ATOMIC_RELAXED, __HIP_MEMORY_SCOPE_WORKGROUP) < v && ++sp < (SPIN_CAP << 4)) __builtin_amdgcn_s_sleep(0); FENCE(); };
    WAITL0();
    if (kh == 0) {
      if (lane == 0) __hip_atomic_fetch_add((LAS unsigned*)(L + L_C1), 1u, __ATOMIC_RELAXED, __HIP_MEMORY_SCOPE_WORKGROUP);
      lds_wait(L_F1, 1);
    } else {
      if (w == 4) {
        const int sec = lane < 4 ? 0 : (lane < 12 ? 1 : 2);
        const int k = lane < 4 ? lane : (lane < 12 ? lane - 4 : lane - 12);
        const int col = sec * 640 + 20 * hd + ((k & 1) ? 19 : 0);
        const int Pn = sec == 0 ? 2 * pp + (k >> 1) : 2 * pp - 2 + (k >> 1);
        const bool need = lane < 20 && Pn >= 0;
        const unsigned fo = WS_FLAGA + (unsigned)((need ? Pn : 2 * pp) * 16 + (col >> 7)) * 128;
        unsigned spins = 0;
        for (;;) {
          FENCE();
          const u32x2 f = LD64(fo);
          if (__all(!need || (f[0] == tag2[0] && f[1] == tag2[1])) || ++spins > SPIN_CAP) break;
          __builtin_amdgcn_s_sleep(1);
        }
        if (lane == 0) __hip_atomic_store((LAS unsigned*)(L + L_F1), 1u, __ATOMIC_RELAXED, __HIP_MEMORY_SCOPE_WORKGROUP);
        STAMP(4);
      } else lds_wait(L_F1, 1);
      const char* qb = (const char*)p.ws + WS_QKV;
#pragma unroll
      for (int i = 0; i < 10; ++i) {
        const int pc = nt + 4 * i, pl = pc >> 2, qt = pc & 3;
        const int tok0 = 256 * pp - 256 + 128 * qt;
        if (tok0 >= 0) glds16c(lane * 16, qb + (size_t)((pl < 5 ? 160 + 5 * hd + pl : 320 + 5 * hd + pl - 5)) * 16384 + tok0 * 8, lds0 + pl * PL + qt * 1024);
      }
    }
    bf16x8 qf0; bf16x4 qf1;
    {
      const unsigned qo = WS_QKV + (unsigned)(5 * hd) * 16384u + (unsigned)(256 * pp + 32 * t + l31) * 8u;
      const u32x2 a = LD64(qo + (2 * hh) * 16384), c = LD64(qo + (2 * hh + 1) * 16384);
      u32x2 z = LD64(qo + 4 * 16384);
      asm volatile("s_waitcnt vmcnt(0)" : "+v"(z) :: "memory");
      if (hh) z = (u32x2){0, 0};
      qf0 = __builtin_bit_cast(bf16x8, (u32x4){a[0], a[1], c[0], c[1]});
      qf1 = __builtin_bit_cast(bf16x4, z);
    }
    if (kh == 1 && lane == 0) __hip_atomic_fetch_add((LAS unsigned*)(L + L_C1), 1u, __ATOMIC_RELAXED, __HIP_MEMORY_SCOPE_WORKGROUP);
    lds_wait(L_C1, 8);
    if (w == 4) STAMP(5);
    unsigned wov[5];
#pragma unroll
    for (int j = 0; j < 5; ++j) { const int n = 64 * j + lane, rho = (n * 6554) >> 16, chn = n - 10 * rho; wov[j] = (unsigned)(8 * ((rho >> 2) & 3) + 4 * (rho >> 4) + (rho & 3)) * 1280u + chn * 16u; }
    const char* wob = (const char*)p.wo + 160 * cc + (size_t)nt * 32 * 1280;
    const unsigned wol = lds0 + L_WO + nt * 5120;
#define WOP(i) glds16(wov[(i) % 5], wob + (size_t)((i) / 5) * 4 * 32 * 1280, wol + ((i) / 5) * 4 * 5120 + ((i) % 5) * 1024)
#define WOP3(i) do { if (kh == 0) { WOP(i); WOP((i) + 1); WOP((i) + 2); } SBAR(); } while (0)
    if (w == 0) STAMP(28);
    const LAS char* kb = L + L_K + l31 * 8 + (2 * hh) * PL;
    const LAS char* kb4 = L + L_K + l31 * 8 + 4 * PL;
    float negm;
    {
      const u32x2 k0 = *(const LAS u32x2*)(kb + (t + 8) * 256), k1 = *(const LAS u32x2*)(kb + (t + 8) * 256 + PL), k2 = *(const LAS u32x2*)(kb4 + (t + 8) * 256);
      const u32x4 q0 = __builtin_bit_cast(u32x4, qf0); const u32x2 q1 = __builtin_bit_cast(u32x2, qf1);
      float d = dot2(q0[0], k0[0], 0.f); d = dot2(q0[1], k0[1], d); d = dot2(q0[2], k1[0], d); d = dot2(q0[3], k1[1], d);
      d = dot2(q1[0], k2[0], d); d = dot2(q1[1], k2[1], d);
      negm = -swap_add(d);
    }
    const LAS char* vb = L + L_V + min(4 * ((lane >> 4) & 1) + (lane & 3), 5) * PL + (4 * hh + ((lane & 15) >> 2)) * 8;
    f32x16 o = (f32x16){};
    {
      auto diag_ok = [](int r) { unsigned long long m = 0; for (int l = 0; l < 64; ++l) if ((r & 3) + 8 * (r >> 2) + 4 * (l >> 5) <= (l & 31)) m |= 1ull << l; return m; };
      f32x16 cpl, sa, sb;
      bf16x4 ka, kc, kd;
      bf16x8 vq = {};
      u32x4 pq = {0, 0, 0, 0};
#define KRD(kt) do { ka = *(const LAS bf16x4*)(kb + (kt) * 256); kc = *(const LAS bf16x4*)(kb + (kt) * 256 + PL); kd = *(const LAS bf16x4*)(kb4 + (kt) * 256); } while (0)
#define EX2(S, i) pk2(__builtin_amdgcn_exp2f(S[i]), __builtin_amdgcn_exp2f(S[(i) + 1]))
#define GRP(nds, nv) do { __builtin_amdgcn_sched_group_barrier(0x8, 1, 0); if (nds) __builtin_amdgcn_sched_group_barrier(0x100, nds, 0); __builtin_amdgcn_sched_group_barrier(0x2, nv, 0); } while (0)
#define HALF(SC, SN, C, ktv, ktk) do { \
        u32x4 p0_; const LAS char* vp_ = vb + (ktv) * 256; \
        SN = MFMA32(cat8(ka, kc), qf0, C); \
        const bf16x8 v0_ = cat8(trd(vp_), trd(vp_ + 64)), v1_ = cat8(trd(vp_ + 128), trd(vp_ + 192)); \
        p0_[0] = EX2(SC, 0); p0_[1] = EX2(SC, 2); \
        GRP(4, 6); asm volatile("" : "+v"(SN), "+v"(p0_[0]), "+v"(p0_[1])); SBAR(); \
        o = MFMA32(vq, __builtin_bit_cast(bf16x8, pq), o); \
        p0_[2] = EX2(SC, 4); p0_[3] = EX2(SC, 6); \
        GRP(0, 6); asm volatile("" : "+v"(o), "+v"(p0_[2]), "+v"(p0_[3])); SBAR(); \
        SN = MFMA32K8(kd, qf1, SN); \
        KRD(ktk); \
        pq[0] = EX2(SC, 8); pq[1] = EX2(SC, 10); \
        GRP(3, 6); asm volatile("" : "+v"(SN), "+v"(pq[0]), "+v"(pq[1])); SBAR(); \
        o = MFMA32(v0_, __builtin_bit_cast(bf16x8, p0_), o); \
        pq[2] = EX2(SC, 12); pq[3] = EX2(SC, 14); vq = v1_; \
        GRP(0, 6); asm volatile("" : "+v"(o), "+v"(pq[2]), "+v"(pq[3])); SBAR(); } while (0)
      KRD(t + 8);
#pragma unroll
      for (int r = 0; r < 16; ++r) cpl[r] = __builtin_amdgcn_inverse_ballot_w64(diag_ok(r)) ? negm : -INFINITY;
      sa = MFMA32(cat8(ka, kc), qf0, cpl); sa = MFMA32K8(kd, qf1, sa);
      KRD(t + 7);
#pragma unroll
      for (int r = 0; r < 16; ++r) cpl[r] = negm;
      asm volatile("" : "+v"(sa), "+v"(cpl)); SBAR();
      WOP3(0);
      HALF(sa, sb, cpl, t + 8, t + 6);
      WOP3(3);
      HALF(sb, sa, cpl, t + 7, t + 5);
      WOP3(6);
      HALF(sa, sb, cpl, t + 6, t + 4);
      WOP3(9);
      HALF(sb, sa, cpl, t + 5, t + 3);
      WOP3(12);
      HALF(sa, sb, cpl, t + 4, t + 2);
      WOP3(15);
      HALF(sb, sa, cpl, t + 3, t + 1);
      WOP3(18);
      HALF(sa, sb, cpl, t + 2, t);
#pragma unroll
      for (int r = 0; r < 16; ++r) cpl[r] = __builtin_amdgcn_inverse_ballot_w64(diag_ok(r)) ? -INFINITY : negm;
      asm volatile("" : "+v"(cpl)); SBAR();
      if (kh == 0) { WOP(21); WOP(22); WOP(23); WOP(24); }
      SBAR();
      HALF(sb, sa, cpl, t + 1, t);
      {
        const LAS char* vp_ = vb + t * 256;
        const bf16x8 v0_ = cat8(trd(vp_), trd(vp_ + 64)), v1_ = cat8(trd(vp_ + 128), trd(vp_ + 192));
        o = MFMA32(vq, __builtin_bit_cast(bf16x8, pq), o);
        u32x4 p0_, p1_;
#pragma unroll
        for (int e = 0; e < 4; ++e) p0_[e] = EX2(sa, 2 * e);
        o = MFMA32(v0_, __builtin_bit_cast(bf16x8, p0_), o);
#pragma unroll
        for (int e = 0; e < 4; ++e) p1_[e] = EX2(sa, 8 + 2 * e);
        o = MFMA32(v1_, __builtin_bit_cast(bf16x8, p1_), o);
      }
    }
    float lsum;
    { auto r = __builtin_amdgcn_permlane32_swap(__float_as_uint(o[8]), __float_as_uint(o[8]), false, false); lsum = __uint_as_float(r[1]); }
    if (__builtin_expect(__any(!(lsum < 1.0995116e12f)), 0)) {
      const int lo = pp >= 1 ? t : 8;
      float mx = -INFINITY;
#pragma unroll 1
      for (int kt = t + 8; kt >= lo; --kt) {
        const bf16x4 ka = *(const LAS bf16x4*)(kb + kt * 256), kc = *(const LAS bf16x4*)(kb + kt * 256 + PL), kd = *(const LAS bf16x4*)(kb4 + kt * 256);
        f32x16 s = MFMA32(cat8(ka, kc), qf0, (f32x16){});
        s = MFMA32K8(kd, qf1, s);
#pragma unroll
        for (int r = 0; r < 16; ++r) { const int kr = (r & 3) + 8 * (r >> 2) + 4 * hh; const bool ok = kt == t + 8 ? kr <= l31 : (kt == t ? kr > l31 : true); mx = fmaxf(mx, ok ? s[r] : -INFINITY); }
      }
      { auto r = __builtin_amdgcn_permlane32_swap(__float_as_uint(mx), __float_as_uint(mx), false, false); mx = fmaxf(__uint_as_float(r[0]), __uint_as_float(r[1])); }
      o = (f32x16){};
#pragma unroll 1
      for (int kt = t + 8; kt >= lo; --kt) {
        const bf16x4 ka = *(const LAS bf16x4*)(kb + kt * 256), kc = *(const LAS bf16x4*)(kb + kt * 256 + PL), kd = *(const LAS bf16x4*)(kb4 + kt * 256);
        const LAS char* vp_ = vb + kt * 256;
        const bf16x8 v0_ = cat8(trd(vp_), trd(vp_ + 64)), v1_ = cat8(trd(vp_ + 128), trd(vp_ + 192));
        f32x16 s = MFMA32(cat8(ka, kc), qf0, (f32x16){});
        s = MFMA32K8(kd, qf1, s);
        u32x4 p0_, p1_;
#pragma unroll
        for (int r = 0; r < 16; ++r) { const int kr = (r & 3) + 8 * (r >> 2) + 4 * hh; const bool ok = kt == t + 8 ? kr <= l31 : (kt == t ? kr > l31 : true); s[r] = ok ? __builtin_amdgcn_exp2f(s[r] - mx) : 0.f; }
#pragma unroll
        for (int e = 0; e < 4; ++e) { p0_[e] = pk2(s[2 * e], s[2 * e + 1]); p1_[e] = pk2(s[8 + 2 * e], s[8 + 2 * e + 1]); }
        o = MFMA32(v0_, __builtin_bit_cast(bf16x8, p0_), o);
        o = MFMA32(v1_, __builtin_bit_cast(bf16x8, p1_), o);
      }
      auto r = __builtin_amdgcn_permlane32_swap(__float_as_uint(o[8]), __float_as_uint(o[8]), false, false); lsum = __uint_as_float(r[1]);
    }
    if (w == 0) STAMP(6);
    STAMP(32 + w);
    {
      const float inv = 1.f / lsum;
      unsigned A0 = pk2(o[0] * inv, o[1] * inv), A1 = pk2(o[2] * inv, o[3] * inv), B0 = pk2(o[4] * inv, o[5] * inv), B1 = pk2(o[6] * inv, o[7] * inv);
      unsigned C0 = pk2(o[8] * inv, o[9] * inv), C1 = pk2(o[10] * inv, o[11] * inv);
      const int tt = pp * 16 + 2 * t + (l31 >> 4);
      const unsigned base = WS_O + (unsigned)tt * 80 * 256 + (l31 & 15) * 16 + (unsigned)(5 * (hd >> 1)) * 256;
      if ((hd & 1) == 0) {
        auto s0 = __builtin_amdgcn_permlane32_swap(A0, B0, false, false), s1 = __builtin_amdgcn_permlane32_swap(A1, B1, false, false);
        ST128(((u32x4){s0[0], s1[0], s0[1], s1[1]}), base + hh * 256);
        if (hh == 0) ST64(((u32x2){C0, C1}), base + 2 * 256);
      } else {
        auto u0 = __builtin_amdgcn_permlane32_swap(A0, C0, false, false), u1 = __builtin_amdgcn_permlane32_swap(A1, C1, false, false);
        const u32x4 v = hh ? (u32x4){B0, B1, u0[0], u1[0]} : (u32x4){u0[1], u1[1], B0, B1};
        ST128(v, base + (3 + hh) * 256);
        if (hh == 0) ST64(((u32x2){A0, A1}), base + 2 * 256 + 8);
      }
      WAITV(0);
      if (lane == 0) ST64(tag2, WS_FLAGO + (((pp * 8 + t) * 2 + (hd >> 4)) * 16 + (hd & 15)) * 8);
    }
  }
  if (w == 0) STAMP(7);
  STAMP(40 + w);

  BARRIER();
  if (w == 0) STAMP(8);

  f32x4 acc[5];
#pragma unroll
  for (int ft = 0; ft < 5; ++ft) acc[ft] = (f32x4){0, 0, 0, 0};
  {
    const LAS char* wp = L + L_WO + (unsigned)(kh * 320 + 4 * fg + ((lane & 15) >> 2)) * 160 + (lane & 3) * 8;
    bf16x8 wa[3][5];
#pragma unroll
    for (int s = 0; s < 3; ++s)
#pragma unroll
      for (int ft = 0; ft < 5; ++ft) wa[s][ft] = cat8(trd(wp + s * 5120 + ft * 32), trd(wp + s * 5120 + 2560 + ft * 32));
    const int tg = 4 * R + nt;
    const unsigned fo = WS_FLAGO + ((((tg >> 4) * 8 + ((tg >> 1) & 7)) * 2 + kh) * 16 + l15) * 8;
    unsigned spins = 0;
    for (;;) {
      FENCE();
      const u32x2 f = LD64(fo);
      if (__all(f[0] == tag2[0] && f[1] == tag2[1]) || ++spins > SPIN_CAP) break;
      __builtin_amdgcn_s_sleep(1);
    }
    FENCE();
    if (w == 0) STAMP(9);
    STAMP(48 + w);
    u32x4 of[10];
#pragma unroll
    for (int s = 0; s < 10; ++s) of[s] = LD128(WS_O + (unsigned)(tg * 80 + 4 * (10 * kh + s)) * 256 + lane * 16);
    SBAR();
#pragma unroll
    for (int s = 0; s < 10; ++s) {
      const bf16x8 bfr = __builtin_bit_cast(bf16x8, of[s]);
#pragma unroll
      for (int ft = 0; ft < 5; ++ft) acc[ft] = MFMA16(s < 3 ? wa[s < 3 ? s : 0][ft] : cat8(trd(wp + s * 5120 + ft * 32), trd(wp + s * 5120 + 2560 + ft * 32)), bfr, acc[ft]);
    }
  }
  STAMP(56 + w);
  if (kh == 1) {
#pragma unroll
    for (int ft = 0; ft < 5; ++ft) *(LAS f32x4*)(L + L_RED + ((nt * 5 + ft) * 64 + lane) * 16) = acc[ft];
  }
  WAITL0(); BARRIER();
  if (w == 0) STAMP(10);
  if (kh == 0) {
    float s1 = 0.f, s2 = 0.f, s3 = 0.f;
#pragma unroll
    for (int ft = 0; ft < 5; ++ft) {
      const f32x4 o2 = *(const LAS f32x4*)(L + L_RED + ((nt * 5 + ft) * 64 + lane) * 16);
      const unsigned a01 = pk2(acc[ft][0] + o2[0], acc[ft][1] + o2[1]), a23 = pk2(acc[ft][2] + o2[2], acc[ft][3] + o2[3]);
      const unsigned h01 = pk2(bf2f(xr[ft][0]) + bf2f(a01), bfhi(xr[ft][0]) + bfhi(a01)), h23 = pk2(bf2f(xr[ft][1]) + bf2f(a23), bfhi(xr[ft][1]) + bfhi(a23));
      *(LAS u32x2*)(L + L_HST + nt * 2560 + l15 * 160 + ft * 32 + fg * 8) = (u32x2){h01, h23};
      s1 = dot2(h01, 0x3f803f80u, s1); s1 = dot2(h23, 0x3f803f80u, s1);
      s2 = dot2(h01, h01, s2); s2 = dot2(h23, h23, s2);
      s3 = dot2(h01, wrv[ft][0], s3); s3 = dot2(h23, wrv[ft][1], s3);
    }
    WAITL0();
#pragma unroll
    for (int i = 0; i < 3; ++i) {
      const int n = lane + 64 * i;
      if (n < 160) ST128(*(const LAS u32x4*)(L + L_HST + nt * 2560 + n * 16), WS_H + (unsigned)((R * 8 + cc) * 64 + 16 * nt) * 160 + n * 16);
    }
    s1 = swap_add(swap16_add(s1)); s2 = swap_add(swap16_add(s2)); s3 = swap_add(swap16_add(s3));
    WAITV(0);
    if (lane < 16) ST128(((u32x4){__float_as_uint(s1), __float_as_uint(s2), __float_as_uint(s3), tag32}), WS_STATS + ((R * 8 + cc) * 64 + 16 * nt + l15) * 16);
    if (w == 0) STAMP(11);
    BARRIER();
  } else {
    BARRIER();
    const int rl = lane >> 1, grow = 8 * ((rl >> 2) & 3) + 4 * (rl >> 4) + (rl & 3);
    if (is_gu) {
      const unsigned vo = (unsigned)grow * (F * 2) + (lane & 1) * 16;
#pragma unroll 1
      for (int pc = nt; pc < 40; pc += 4) { const int m = pc >= 20, s = pc - 20 * m; glds16(vo, (const char*)(m ? p.wu : p.wg) + (size_t)s * 32 * F * 2 + gs * 32, lds0 + L_FW + pc * 1024); }
    } else if (is_dn) {
      const unsigned vo = (unsigned)grow * (D * 2) + (lane & 1) * 16;
#pragma unroll 1
      for (int pc = nt; pc < 54; pc += 4) glds16(vo, (const char*)p.wd + (size_t)pc * 32 * D * 2 + dn * 32, lds0 + L_FW + pc * 1024);
    }
  }
  auto rank64 = [&](unsigned k) {
    const u32x4 oa = *(const LAS u32x4*)(L + L_KEY + w * 32), ob = *(const LAS u32x4*)(L + L_KEY + w * 32 + 16);
    unsigned c = 0;
#pragma unroll
    for (int e = 0; e < 4; ++e) c += (oa[e] > k) + (ob[e] > k);
    *(LAS unsigned char*)(L + L_PART + lane * 8 + w) = (unsigned char)c;
    WAITL0(); BARRIER();
    const u32x2 pr = *(const LAS u32x2*)(L + L_PART + lane * 8);
    return __builtin_amdgcn_sad_u8(pr[0], 0u, __builtin_amdgcn_sad_u8(pr[1], 0u, 0u));
  };
  if (bi == 31) {
    float swr = 0.f;
#pragma unroll
    for (int i = 0; i < 5; ++i) swr = dot2(wr5[i], 0x3f803f80u, swr);
    swr = wave_fsum(swr);
    const int tl = 32 * w + l31;
    const unsigned so = WS_STATS + (unsigned)(((4 * xcd + (tl >> 6)) * 8 + 4 * hh) * 64 + (tl & 63)) * 16;
    float t1, t2, t3;
    unsigned spins = 0;
    for (;;) {
      FENCE();
      const u32x4 g0 = LD128(so), g1 = LD128(so + 1024), g2 = LD128(so + 2048), g3 = LD128(so + 3072);
      t1 = (__uint_as_float(g0[0]) + __uint_as_float(g1[0])) + (__uint_as_float(g2[0]) + __uint_as_float(g3[0]));
      t2 = (__uint_as_float(g0[1]) + __uint_as_float(g1[1])) + (__uint_as_float(g2[1]) + __uint_as_float(g3[1]));
      t3 = (__uint_as_float(g0[2]) + __uint_as_float(g1[2])) + (__uint_as_float(g2[2]) + __uint_as_float(g3[2]));
      if (__all(g0[3] == tag32 && g1[3] == tag32 && g2[3] == tag32 && g3[3] == tag32) || ++spins > SPIN_CAP) break;
    }
    t1 = swap_add(t1); t2 = swap_add(t2); t3 = swap_add(t3);
    const float mean = t1 * (1.f / D);
    const float var = fmaxf(t2 * (1.f / D) - mean * mean, 0.f);
    const float mb = rbf(mean);
    const float sd = rbf(__builtin_amdgcn_sqrtf(rbf(rbf(var) + 1e-5f)));
    const unsigned rb = pk2((t3 - mb * swr) * __builtin_amdgcn_rcpf(sd), 0.f) & 0xffffu;
    const unsigned key = ((rb ^ ((rb & 0x8000u) ? 0xffffu : 0x8000u)) << 16) | (unsigned)(2047 - (256 * xcd + tl));
    const unsigned ms = (__float_as_uint(mb) & 0xffff0000u) | (__float_as_uint(sd) >> 16);
    if (w == 0) STAMP(12);
    if (lane < 32) *(LAS unsigned*)(L + L_K32 + tl * 4) = key;
    WAITL0();
    unsigned c = 0;
#pragma unroll
    for (int i = 0; i < 4; ++i) {
      const u32x4 o4 = *(const LAS u32x4*)(L + L_K32 + (32 * w + 16 * hh + 4 * i) * 4);
#pragma unroll
      for (int e = 0; e < 4; ++e) c += o4[e] > key;
    }
    { auto r = __builtin_amdgcn_permlane32_swap(c, c, false, false); c = r[0] + r[1]; }
    if (lane < 32 && c < 8) { *(LAS unsigned*)(L + L_KEY + (8 * w + c) * 4) = key; *(LAS unsigned*)(L + L_KEY + 256 + (8 * w + c) * 4) = ms; }
    WAITL0(); BARRIER();
    const unsigned k = *(const LAS unsigned*)(L + L_KEY + lane * 4), m2 = *(const LAS unsigned*)(L + L_KEY + 256 + lane * 4);
    const unsigned rk = rank64(k);
    if (w == 0) { if (rk < 8) ST128(((u32x4){k, tag2[0], m2, tag2[1]}), WS_CAND + xcd * 128 + rk * 16); STAMP(26); }
    BARRIER();
  }
  if (w == 0) {
    u32x4 a;
    unsigned spins = 0;
    for (;;) {
      FENCE();
      a = LD128(WS_CAND + lane * 16);
      if (__all(a[1] == tag2[0] && a[3] == tag2[1]) || ++spins > SPIN_CAP) break;
      if (is_gu || is_dn) __builtin_amdgcn_s_sleep(1); else __builtin_amdgcn_s_sleep(8);
    }
    STAMP(13);
    *(LAS unsigned*)(L + L_KEY + lane * 4) = a[0]; *(LAS unsigned*)(L + L_KEY + 256 + lane * 4) = a[2];
    if (b == 0 && lane == 0) ST64(tag2, WS_EPOCH);
  }
  const int hc0 = (lane * 6554) >> 16, hc1 = ((64 + l15) * 6554) >> 16;
  const unsigned ho0 = (unsigned)hc0 * (64 * 160) + (lane - 10 * hc0) * 16, ho1 = (unsigned)hc1 * (64 * 160) + (64 + l15 - 10 * hc1) * 16;
  if (kh == 1) WAITV(0);
  WAITL0(); BARRIER();
  {
    const unsigned k = *(const LAS unsigned*)(L + L_KEY + lane * 4), m2 = *(const LAS unsigned*)(L + L_KEY + 256 + lane * 4);
    const unsigned rk = rank64(k);
    if (rk < 8) *(LAS u32x2*)(L + L_SEL + rk * 8) = (u32x2){k, m2};
    WAITL0();
  }
  if (w == 0) STAMP(14);

  auto h_row = [&](int row, u32x4& h0, u32x4& h1) {
    const unsigned rb = WS_H + (unsigned)((row >> 6) * 8 * 64 + (row & 63)) * 160;
    h0 = LD128(rb + ho0); h1 = LD128(rb + ho1);
  };
  auto ln8 = [&](const u32x4& h, float m, float iv) { u32x4 n;
#pragma unroll
    for (int e = 0; e < 4; ++e) { const unsigned d = pk2(bf2f(h[e]) - m, bfhi(h[e]) - m); n[e] = pk2(bf2f(d) * iv, bfhi(d) * iv); } return n; };
  auto out_row = [&]() {
    const int row = 64 * R + 8 * cc + w;
    const u32x2 sk = *(const LAS u32x2*)(L + L_SEL + (lane & 7) * 8);
    if (!__any(2047 - (int)(sk[0] & 0x7ff) == row)) {
      u32x4 h0, h1; h_row(row, h0, h1);
      const u32x4 g = LD128(WS_STATS + (unsigned)((R * 8 + (lane & 7)) * 64 + 8 * cc + w) * 16);
      float t1 = __uint_as_float(g[0]), t2 = __uint_as_float(g[1]);
      t1 = dppadd<0x141>(t1); t2 = dppadd<0x141>(t2);
      t1 = dppadd<0xb1>(t1); t2 = dppadd<0xb1>(t2);
      t1 = dppadd<0x4e>(t1); t2 = dppadd<0x4e>(t2);
      const float mean = t1 * (1.f / D);
      const float m = rbf(mean), iv = __builtin_amdgcn_rcpf(rbf(__builtin_amdgcn_sqrtf(rbf(rbf(fmaxf(t2 * (1.f / D) - mean * mean, 0.f)) + 1e-5f))));
      const u32x4 n0 = ln8(h0, m, iv), n1 = ln8(h1, m, iv);
      u32x4 r0, r1;
#pragma unroll
      for (int e = 0; e < 4; ++e) { r0[e] = pk2(bf2f(h0[e]) + bf2f(n0[e]), bfhi(h0[e]) + bfhi(n0[e])); r1[e] = pk2(bf2f(h1[e]) + bf2f(n1[e]), bfhi(h1[e]) + bfhi(n1[e])); }
      *(u32x4*)(p.out + (size_t)row * D + lane * 8) = r0;
      if (lane < 16) *(u32x4*)(p.out + (size_t)row * D + 512 + lane * 8) = r1;
    }
  };
  if (is_gu) {
    {
      const u32x2 sk = *(const LAS u32x2*)(L + L_SEL + w * 8);
      u32x4 h0, h1; h_row(2047 - (int)(sk[0] & 0x7ff), h0, h1);
      const float m = __uint_as_float(sk[1] & 0xffff0000u), iv = __builtin_amdgcn_rcpf(__uint_as_float(sk[1] << 16));
      *(LAS u32x4*)(L + L_XS + w * 1296 + lane * 16) = ln8(h0, m, iv);
      if (lane < 16) *(LAS u32x4*)(L + L_XS + w * 1296 + 1024 + lane * 16) = ln8(h1, m, iv);
    }
    WAITL0(); BARRIER();
    if (w == 0) STAMP(15);
    {
      const int m = w & 1, kq = w >> 1;
      f32x4 a4 = {0, 0, 0, 0}, b4 = {0, 0, 0, 0};
#pragma unroll
      for (int i = 0; i < 5; ++i) {
        const int s = 5 * kq + i;
        const LAS char* ap = L + L_FW + (m * 20 + s) * 1024 + lane * 8;
        const bf16x8 af = cat8(trd(ap), trd(ap + 512));
        const bf16x8 bfv = *(const LAS bf16x8*)(L + L_XS + (lane & 7) * 1296 + s * 64 + fg * 16);
        if (i & 1) b4 = MFMA16(af, bfv, b4); else a4 = MFMA16(af, bfv, a4);
      }
      *(LAS f32x4*)(L + L_FRED + (w * 64 + lane) * 16) = a4 + b4;
    }
    WAITL0(); BARRIER();
    if (w == 0) {
      f32x4 g4 = {0, 0, 0, 0}, u4 = {0, 0, 0, 0};
#pragma unroll
      for (int q = 0; q < 4; ++q) { g4 += *(const LAS f32x4*)(L + L_FRED + ((2 * q) * 64 + lane) * 16); u4 += *(const LAS f32x4*)(L + L_FRED + ((2 * q + 1) * 64 + lane) * 16); }
      float a[4];
#pragma unroll
      for (int e = 0; e < 4; ++e) { const float g = rbf(g4[e]), u = rbf(u4[e]); const float sg = rbf(g * __builtin_amdgcn_rcpf(1.f + __builtin_amdgcn_exp2f(g * -1.4426950408889634f))); a[e] = sg * u; }
      if (l15 < 8) ST128(((u32x4){pk2(a[0], a[1]), tag32, pk2(a[2], a[3]), tag32}), WS_ACT + (((gs >> 1) * 4 + 2 * (gs & 1) + (fg >> 1)) * 8 + l15) * 32 + (fg & 1) * 16);
      STAMP(16);
    }
    out_row();
  } else if (is_dn) {
    const u32x2 sk = *(const LAS u32x2*)(L + L_SEL + (lane & 7) * 8);
    const int row = 2047 - (int)(sk[0] & 0x7ff);
    u32x2 hv = {0, 0}, nv = {0, 0};
    float sig = 0.f;
    if (w == 0) {
      const int col = 16 * dn + 4 * fg, c = col / 80;
      hv = LD64(WS_H + (unsigned)(((row >> 6) * 8 + c) * 64 + (row & 63)) * 160 + (col - 80 * c) * 2);
      const unsigned rk = sk[0] >> 16, rbits = (rk & 0x8000u) ? (rk ^ 0x8000u) : (rk ^ 0xffffu);
      sig = rbf(__builtin_amdgcn_rcpf(1.f + __builtin_amdgcn_exp2f(bf2f(rbits) * -1.4426950408889634f)));
      const float m = __uint_as_float(sk[1] & 0xffff0000u), iv = __builtin_amdgcn_rcpf(__uint_as_float(sk[1] << 16));
#pragma unroll
      for (int e = 0; e < 2; ++e) { const unsigned d2 = pk2(bf2f(hv[e]) - m, bfhi(hv[e]) - m); nv[e] = pk2(bf2f(d2) * iv, bfhi(d2) * iv); }
      asm volatile("" : "+v"(nv), "+v"(hv), "+v"(sig));
    } else out_row();
    f32x4 a4 = {0, 0, 0, 0}, b4 = {0, 0, 0, 0};
    {
      bf16x8 af[7];
#pragma unroll
      for (int i = 0; i < 7; ++i) { const LAS char* ap = L + L_FW + min(w + 8 * i, 53) * 1024 + lane * 8; af[i] = cat8(trd(ap), trd(ap + 512)); }
      u32x4 g0[7], g1[7];
      unsigned spins = 0;
      for (;;) {
        FENCE();
        bool ok = true;
#pragma unroll
        for (int i = 0; i < 7; ++i) {
          const int s = min(w + 8 * i, 53);
          const unsigned ao = WS_ACT + ((s * 4 + fg) * 8 + (lane & 7)) * 32;
          g0[i] = LD128(ao); g1[i] = LD128(ao + 16);
        }
#pragma unroll
        for (int i = 0; i < 7; ++i) ok = ok && g0[i][1] == tag32 && g0[i][3] == tag32 && g1[i][1] == tag32 && g1[i][3] == tag32;
        if (__all(ok) || ++spins > SPIN_CAP) break;
      }
      if (w == 0) STAMP(17);
#pragma unroll
      for (int i = 0; i < 7; ++i) {
        const bf16x8 bv = __builtin_bit_cast(bf16x8, (u32x4){g0[i][0], g0[i][2], g1[i][0], g1[i][2]});
        if (i < 6 || w < 6) { if (i & 1) b4 = MFMA16(af[i], bv, b4); else a4 = MFMA16(af[i], bv, a4); }
      }
    }
    *(LAS f32x4*)(L + L_FRED + (w * 64 + lane) * 16) = a4 + b4;
    WAITL0(); BARRIER();
    if (w == 0) {
      f32x4 y = {0, 0, 0, 0};
#pragma unroll
      for (int q = 0; q < 8; ++q) y += *(const LAS f32x4*)(L + L_FRED + (q * 64 + lane) * 16);
      unsigned r2[2];
#pragma unroll
      for (int e = 0; e < 2; ++e) {
        const unsigned y2 = pk2(rbf(y[2 * e]) * sig, rbf(y[2 * e + 1]) * sig);
        const unsigned t2 = pk2(bf2f(nv[e]) + bf2f(y2), bfhi(nv[e]) + bfhi(y2));
        r2[e] = pk2(bf2f(hv[e]) + bf2f(t2), bfhi(hv[e]) + bfhi(t2));
      }
      if (l15 < 8) *(u32x2*)(p.out + (size_t)row * D + 16 * dn + 4 * fg) = (u32x2){r2[0], r2[1]};
      STAMP(18);
      out_row();
    }
  } else {
    out_row();
  }
  if (w == 0) STAMP(19);
  STAMP_FLUSH();
}

extern "C" void kernel_launch(void* const* d_in, const int* in_sizes, int n_in, void* d_out, int out_size, void* d_ws, size_t ws_size, hipStream_t stream) {
  Params p;
  p.x = (const u16*)d_in[0]; p.wqkv = (const u16*)d_in[1]; p.wo = (const u16*)d_in[2]; p.wr = (const u16*)d_in[4];
  p.wg = (const u16*)d_in[5]; p.wu = (const u16*)d_in[6]; p.wd = (const u16*)d_in[7]; p.out = (u16*)d_out; p.ws = (unsigned char*)d_ws;
  fused_kernel<<<256, 512, 0, stream>>>(p);
}
```

```cpp
#include <hip/hip_runtime.h>
#include <stdint.h>

typedef unsigned short u16;
typedef short bf16x8 __attribute__((ext_vector_type(8)));
typedef short bf16x4 __attribute__((ext_vector_type(4)));
typedef __bf16 bf2 __attribute__((ext_vector_type(2)));
typedef float f32x16 __attribute__((ext_vector_type(16)));
typedef float f32x4 __attribute__((ext_vector_type(4)));
typedef float f32x2 __attribute__((ext_vector_type(2)));
typedef unsigned u32x4 __attribute__((ext_vector_type(4)));
typedef unsigned u32x2 __attribute__((ext_vector_type(2)));
typedef unsigned long long u64;
#define LAS __attribute__((address_space(3)))
#define SBAR() __builtin_amdgcn_sched_barrier(0)
#define BARRIER() asm volatile("s_barrier" ::: "memory")
#define WAITL0() asm volatile("s_waitcnt lgkmcnt(0)" ::: "memory")
#define WAITV(n) asm volatile("s_waitcnt vmcnt(" #n ")" ::: "memory")
#define FENCE() asm volatile("" ::: "memory")

constexpr int S = 2048, D = 640, F = 1728;
constexpr int SC1 = 16;

constexpr unsigned WS_EPOCH = 0;
constexpr unsigned WS_FLAGA = 4096;
constexpr unsigned WS_FLAGO = WS_FLAGA + 32768;
constexpr unsigned WS_CAND = WS_FLAGO + 16384;
constexpr unsigned WS_HDONE = WS_CAND + 4096;
constexpr unsigned WS_ACT = WS_HDONE + 4096;
constexpr unsigned WS_ST1 = 131072;
constexpr unsigned WS_CS1 = WS_ST1 + 32768;
constexpr unsigned WS_STATS = 524288;
constexpr unsigned WS_QKV = 1048576;
constexpr unsigned WS_O = WS_QKV + 480 * 16384;
constexpr unsigned WS_H = WS_O + S * D * 2;
#define STAMP_OFF (200u << 20)
#define NSTAMP 64

constexpr int L_SLOT = 32768, L_W = 16384;
constexpr int PL = 4128;
constexpr int L_K = 0, L_V = 5 * PL;
constexpr int L_WO = 45568;
constexpr int L_RED = 0;
constexpr int L_HST = 20480;
constexpr int L_FW = L_WO;
constexpr int L_XS = 0;
constexpr int L_FRED = 12288;
constexpr int L_MISC = L_WO + 640 * 160;
constexpr int L_RS = 0;
constexpr int L_CS = 1024;
constexpr int L_CNT = L_MISC + 3600;
constexpr int L_F1 = L_MISC + 3604, L_C1 = L_MISC + 3608;
constexpr int L_PART = L_MISC + 2048;
constexpr int L_K32 = L_MISC + 2560;
constexpr int L_HD = L_MISC + 3712;
constexpr int L_SEL = L_MISC + 3968;
constexpr int L_STAMP = L_MISC + 4096;
constexpr int L_KEY = L_MISC + 4608 + 256;
constexpr int LDS_BYTES = 163840;
static_assert(L_V + 6 * PL <= L_WO && L_KEY + 2048 <= LDS_BYTES, "lds map");

struct Params { const u16* x; const u16* wqkv; const u16* wo; const u16* wr; const u16* wg; const u16* wu; const u16* wd; u16* out; unsigned char* ws; };

__device__ __forceinline__ float bf2f(unsigned v) { return __uint_as_float(v << 16); }
__device__ __forceinline__ float bfhi(unsigned v) { return __uint_as_float(v & 0xffff0000u); }
__device__ __forceinline__ unsigned pk2(float a, float b) { f32x2 f = {a, b}; return __builtin_bit_cast(unsigned, __builtin_convertvector(f, bf2)); }
__device__ __forceinline__ float rbf(float f) { return __uint_as_float(pk2(f, 0.f) << 16); }
__device__ __forceinline__ float dot2(unsigned a, unsigned b, float c) { return __builtin_amdgcn_fdot2_f32_bf16(__builtin_bit_cast(bf2, a), __builtin_bit_cast(bf2, b), c, false); }
__device__ __forceinline__ float swap_add(float v) { auto r = __builtin_amdgcn_permlane32_swap(__float_as_uint(v), __float_as_uint(v), false, false); return __uint_as_float(r[0]) + __uint_as_float(r[1]); }
__device__ __forceinline__ float swap16_add(float v) { auto r = __builtin_amdgcn_permlane16_swap(__float_as_uint(v), __float_as_uint(v), false, false); return __uint_as_float(r[0]) + __uint_as_float(r[1]); }
template <int CTRL> __device__ __forceinline__ float dppadd(float v) { return v + __int_as_float(__builtin_amdgcn_update_dpp(0, __float_as_int(v), CTRL, 0xf, 0xf, false)); }
__device__ __forceinline__ float wave_fsum(float v) { v = dppadd<0x128>(v); v = dppadd<0x124>(v); v = dppadd<0x122>(v); v = dppadd<0x121>(v); return swap_add(swap16_add(v)); }
template <int CTRL> __device__ __forceinline__ unsigned dppmax(unsigned v) { return max(v, (unsigned)__builtin_amdgcn_update_dpp(0, (int)v, CTRL, 0xf, 0xf, false)); }
__device__ __forceinline__ unsigned wave_umax(unsigned v) {
  v = dppmax<0x128>(v); v = dppmax<0x124>(v); v = dppmax<0x122>(v); v = dppmax<0x121>(v);
  auto r = __builtin_amdgcn_permlane16_swap(v, v, false, false); v = max(r[0], r[1]);
  auto q = __builtin_amdgcn_permlane32_swap(v, v, false, false); return max(q[0], q[1]);
}
__device__ __forceinline__ void glds16(unsigned voff, const void* sbase, unsigned lds) {
  asm volatile("s_mov_b32 m0, %2\n\ts_nop 0\n\tglobal_load_lds_dwordx4 %0, %1" ::"v"(voff), "s"(sbase), "s"(lds) : "memory");
}
__device__ __forceinline__ void glds16c(unsigned voff, const void* sbase, unsigned lds) {
  asm volatile("s_mov_b32 m0, %2\n\ts_nop 0\n\tglobal_load_lds_dwordx4 %0, %1 sc1" ::"v"(voff), "s"(sbase), "s"(lds) : "memory");
}
__device__ __forceinline__ void touch64(unsigned voff, const void* sbase, unsigned lds) {
  asm volatile("s_mov_b32 m0, %2\n\ts_nop 0\n\tglobal_load_lds_dword %0, %1" ::"v"(voff), "s"(sbase), "s"(lds) : "memory");
}
__device__ __forceinline__ void glds16n(unsigned voff, const void* sbase, unsigned lds) {
  asm volatile("s_mov_b32 m0, %2\n\ts_nop 0\n\tglobal_load_lds_dwordx4 %0, %1 nt" ::"v"(voff), "s"(sbase), "s"(lds) : "memory");
}
#define GL_FW glds16
#define GL_WO glds16
#define GL_X glds16
#define GL_W glds16
__device__ __forceinline__ bf16x4 trd(const LAS char* p) { return __builtin_amdgcn_ds_read_tr16_b64_v4i16((LAS bf16x4*)p); }
__device__ __forceinline__ bf16x8 cat8(bf16x4 a, bf16x4 b) { return (bf16x8){a[0], a[1], a[2], a[3], b[0], b[1], b[2], b[3]}; }
#define MFMA32(a, b, c) __builtin_amdgcn_mfma_f32_32x32x16_bf16(a, b, c, 0, 0, 0)
#define MFMA32K8(a, b, c) __builtin_amdgcn_mfma_f32_32x32x8bf16_1k(a, b, c, 0, 0, 0)
#define MFMA16(a, b, c) __builtin_amdgcn_mfma_f32_16x16x32_bf16(a, b, c, 0, 0, 0)
#define LD128(off) __builtin_amdgcn_raw_buffer_load_b128(rws, (off), 0, SC1)
#define LD64(off) __builtin_amdgcn_raw_buffer_load_b64(rws, (off), 0, SC1)
#define ST128(v, off) __builtin_amdgcn_raw_buffer_store_b128((v), rws, (off), 0, SC1)
#define ST64(v, off) __builtin_amdgcn_raw_buffer_store_b64((v), rws, (off), 0, SC1)

#define STAMP(k) do {} while (0)
#define STAMP_INIT() do {} while (0)
#define PSTAMP(i) do {} while (0)
#define STAMP_ZERO() do {} while (0)
#define STAMP_PA() do {} while (0)
#define STAMP_FLUSH() do {} while (0)

#define ROT (((bi >> 3) * 5 / 2) % 10)
constexpr unsigned SPIN_CAP = 1u << 17;

__global__ __launch_bounds__(512, 2) void fused_kernel(Params p) {
  __shared__ __attribute__((aligned(16))) char smem[LDS_BYTES];
  LAS char* L = (LAS char*)smem;
  const unsigned lds0 = (unsigned)(uintptr_t)smem;
  const int tid = threadIdx.x, lane = tid & 63;
  const int w = __builtin_amdgcn_readfirstlane(tid >> 6);
  const int b = blockIdx.x, xcd = b & 7, bi = b >> 3;
  const int l31 = lane & 31, hh = lane >> 5, l15 = lane & 15, fg = lane >> 4;
  const __amdgpu_buffer_rsrc_t rws = __builtin_amdgcn_make_buffer_rsrc(p.ws, 0, 0x7fffffff, 0x00020000);
  const int aP = 4 * (xcd >> 1) + (bi >> 3), aj = 8 * (xcd & 1) + (bi & 7);
  const int hd = 4 * xcd + (bi >> 3), pp = bi & 7;
  const int R = 4 * xcd + (bi >> 3), cc = bi & 7;
  const int gs = xcd * 14 + bi;
  const bool is_gu = bi < 14 && gs < 108;
  const int dn = xcd * 5 + (bi - 14);
  const bool is_dn = bi >= 14 && bi < 19;
  STAMP_INIT();
  u32x2 tag2;
  auto read_epoch = [&]() {
    u64 e;
    asm volatile("s_load_dwordx2 %0, %1, 0x0\n\ts_waitcnt lgkmcnt(0)" : "=s"(e) : "s"(p.ws) : "memory");
    e += 1;
    tag2 = (u32x2){(unsigned)e, (unsigned)(e >> 32)};
  };

  auto rows_issue = [&](int lw, u32x4& ra0, u32x4& ra1, u32x4& rb0, u32x4& rb1) {
    if (lw == 0) ST128(((u32x4){0, 0, 0, 0}), WS_STATS + (R * 8 + cc) * 1024 + lane * 16);
    if (lw == 1 && is_gu && lane < 32) ST128(((u32x4){0, 0, 0, 0}), WS_ACT + (((gs >> 1) * 4 + 2 * (gs & 1) + (lane >> 4)) * 8 + (lane & 7)) * 32 + ((lane >> 3) & 1) * 16);
    const char* rb = (const char*)p.x + (size_t)(64 * R + 8 * cc + 2 * lw) * 1280;
    ra0 = *(const u32x4*)(rb + lane * 16); ra1 = *(const u32x4*)(rb + 1024 + l15 * 16);
    rb0 = *(const u32x4*)(rb + 1280 + lane * 16); rb1 = *(const u32x4*)(rb + 1280 + 1024 + l15 * 16);
    FENCE();
  };
  auto rows_finish = [&](int lw, const u32x4& ra0, const u32x4& ra1, const u32x4& rb0, const u32x4& rb1) {
    float mean[2], rs[2];
#pragma unroll
    for (int r = 0; r < 2; ++r) {
      const u32x4 c0 = r ? rb0 : ra0, c1 = r ? rb1 : ra1;
      float s0 = 0.f, s1 = 0.f;
#pragma unroll
      for (int e = 0; e < 4; ++e) { s0 = dot2(c0[e], 0x3f803f80u, s0); s1 = dot2(c1[e], 0x3f803f80u, s1); }
      mean[r] = wave_fsum(s0 + (lane < 16 ? s1 : 0.f)) * (1.f / D);
      const f32x2 mm = {mean[r], mean[r]};
      f32x2 q0 = {0.f, 0.f}, q1 = {0.f, 0.f};
#pragma unroll
      for (int e = 0; e < 4; ++e) { const f32x2 d0 = (f32x2){bf2f(c0[e]), bfhi(c0[e])} - mm, d1 = (f32x2){bf2f(c1[e]), bfhi(c1[e])} - mm; q0 += d0 * d0; q1 += d1 * d1; }
      const float var = wave_fsum((q0[0] + q0[1]) + (lane < 16 ? q1[0] + q1[1] : 0.f)) * (1.f / D);
      rs[r] = __builtin_amdgcn_rcpf(rbf(__builtin_amdgcn_sqrtf(rbf(rbf(var) + 1e-5f))));
    }
    WAITV(0);
    if (lane < 2) ST128(((u32x4){__float_as_uint(rbf(lane ? mean[1] : mean[0])), __float_as_uint(lane ? rs[1] : rs[0]), tag2[0], tag2[1]}), WS_ST1 + (unsigned)(64 * R + 8 * cc + 2 * lw + lane) * 16);
  };

  auto lds_ctl_init = [&]() {
    if (tid == 0) { *(LAS unsigned*)(L + L_F1) = 0; *(LAS unsigned*)(L + L_C1) = 0; }
    STAMP_ZERO();
  };
  if (aj < 15 && w >= 4) {
    const int lw = w - 4;
    const unsigned xsw = (unsigned)(4 * (lw & 1) + (lane >> 4));
    const unsigned voff_x = (unsigned)(lane >> 3) * 1280u + (((unsigned)(lane & 7) ^ xsw) << 4);
    const unsigned ch = (unsigned)(lane & 15) ^ (((unsigned)(lane >> 4) << 2) | (unsigned)lw);
    const unsigned voff_w = (unsigned)(lane >> 4) * 3840u + ch * 16u;
    const char* xb = (const char*)p.x + (size_t)(128 * aP + 8 * lw) * 1280;
    const char* wb = (const char*)p.wqkv + (size_t)(4 * lw) * 3840 + 256 * aj;
    const unsigned lbase = lds0 + lw * 1024;
    const int rot = ROT;
    auto issue = [&](int s) {
      const unsigned sl = lbase + (s & 3) * L_SLOT;
      int ss = s + rot; if (ss >= 10) ss -= 10;
#pragma unroll
      for (int i = 0; i < 4; ++i) {
        GL_X(voff_x, xb + (size_t)i * 32 * 1280 + ss * 128, sl + i * 4096);
        GL_W(voff_w, wb + ((size_t)ss * 64 + i * 16) * 3840, sl + L_W + i * 4096);
      }
    };
    BARRIER();
    issue(0); issue(1); issue(2);
    read_epoch();
    WAITV(16); BARRIER();
    PSTAMP(1);
#pragma unroll 1
    for (int s = 0; s <= 5; ++s) { issue(s + 3); WAITV(16); BARRIER(); }
    issue(9);
    const unsigned so = WS_ST1 + (unsigned)(128 * aP + 32 * lw + l31) * 16, co = WS_CS1 + (unsigned)((aj * 16 + fg) * 64 + 16 * lw + l15) * 16;
    u32x4 gs1 = LD128(so), gc0 = LD128(co), gc1 = LD128(co + 4096), gc2 = LD128(co + 8192), gc3 = LD128(co + 12288);
    WAITV(21); BARRIER();
    WAITV(13); BARRIER();
    WAITV(5); BARRIER();
    unsigned spins = 0;
    for (;;) {
      const bool ok = gs1[2] == tag2[0] && gs1[3] == tag2[1] && gc0[2] == tag2[0] && gc0[3] == tag2[1] && gc1[2] == tag2[0] && gc1[3] == tag2[1] && gc2[2] == tag2[0] && gc2[3] == tag2[1] && gc3[2] == tag2[0] && gc3[3] == tag2[1];
      if (__all(ok) || ++spins > SPIN_CAP) break;
      FENCE();
      gs1 = LD128(so); gc0 = LD128(co); gc1 = LD128(co + 4096); gc2 = LD128(co + 8192); gc3 = LD128(co + 12288);
    }
    if (lane < 32) *(LAS u32x2*)(L + L_RS + (32 * lw + l31) * 8) = (u32x2){gs1[0], gs1[1]};
    {
      const float c0 = swap_add(swap16_add((__uint_as_float(gc0[0]) + __uint_as_float(gc1[0])) + (__uint_as_float(gc2[0]) + __uint_as_float(gc3[0]))));
      const float c1 = swap_add(swap16_add((__uint_as_float(gc0[1]) + __uint_as_float(gc1[1])) + (__uint_as_float(gc2[1]) + __uint_as_float(gc3[1]))));
      if (lane < 16) *(LAS f32x2*)(L + L_CS + (32 * lw + 2 * lane) * 4) = (f32x2){c0, c1};
    }
    WAITL0(); BARRIER();
    BARRIER();
  } else if (w < 4) {
    u32x4 ra0, ra1, rb0, rb1;
    rows_issue(w, ra0, ra1, rb0, rb1);
    if (aj < 15) {
      BARRIER();
      read_epoch();
      rows_finish(w, ra0, ra1, rb0, rb1);
      PSTAMP(7);
      const int wm = w >> 1, wn = w & 1;
      unsigned xa[4], wa[4];
#pragma unroll
      for (int ks = 0; ks < 4; ++ks) xa[ks] = (unsigned)(64 * wm + l31) * 128u + ((((unsigned)(2 * ks + hh)) ^ (((unsigned)l31 >> 1) & 7u)) << 4);
      {
        const unsigned blk = (lane >> 4) & 1, q = (lane & 15) >> 2, pq = lane & 3;
#pragma unroll
        for (int j = 0; j < 2; ++j)
#pragma unroll
          for (int t = 0; t < 2; ++t) {
            const unsigned row = 8 * hh + 4 * t + q, chn = 4 * (2 * wn + (j ^ wm)) + 2 * blk + (pq >> 1), m = (q << 2) | (unsigned)(2 * hh + t);
            wa[j * 2 + t] = L_W + 256u * row + 16u * (chn ^ m) + 8u * (pq & 1);
          }
      }
      f32x16 a00 = {}, a01 = {}, a10 = {}, a11 = {};
      bf16x8 xf0[2], wf0[2], xf1[2], wf1[2];

      unsigned xb[4], wb[4];
#define SETSLOT(so) do { _Pragma("unroll") for (int k_ = 0; k_ < 4; ++k_) { xb[k_] = xa[k_] + (so); wb[k_] = wa[k_] + (so); } } while (0)
#define RD(XF, WF, ks) do { \
        XF[0] = *(const LAS bf16x8*)(L + xb[ks]); WF[0] = cat8(trd(L + wb[0] + (ks) * 4096), trd(L + wb[1] + (ks) * 4096)); \
        WF[1] = cat8(trd(L + wb[2] + (ks) * 4096), trd(L + wb[3] + (ks) * 4096)); XF[1] = *(const LAS bf16x8*)(L + xb[ks] + 4096); } while (0)
#define MM(XF, WF) do { \
        a00 = MFMA32(WF[0], XF[0], a00); a01 = MFMA32(WF[1], XF[0], a01); \
        a10 = MFMA32(WF[0], XF[1], a10); a11 = MFMA32(WF[1], XF[1], a11); } while (0)
#define PINALL() asm volatile("" : "+v"(a00), "+v"(a01), "+v"(a10), "+v"(a11))
#define ILV() do { __builtin_amdgcn_sched_group_barrier(0x8, 1, 0); __builtin_amdgcn_sched_group_barrier(0x100, 3, 0); \
        __builtin_amdgcn_sched_group_barrier(0x8, 1, 0); __builtin_amdgcn_sched_group_barrier(0x100, 3, 0); __builtin_amdgcn_sched_group_barrier(0x8, 2, 0); } while (0)
#define STEP(XN, WN, ksn, XC, WC) do { RD(XN, WN, ksn); MM(XC, WC); ILV(); PINALL(); SBAR(); } while (0)
      BARRIER();
      SETSLOT(0);
      RD(xf0, wf0, 0);
      SBAR();
#pragma unroll 1
      for (int s = 0; s < 9; ++s) {
        STEP(xf1, wf1, 1, xf0, wf0);
        STEP(xf0, wf0, 2, xf1, wf1);
        STEP(xf1, wf1, 3, xf0, wf0);
        SETSLOT(((s + 1) & 3) * L_SLOT);
        WAITL0(); BARRIER();
        STEP(xf0, wf0, 0, xf1, wf1);
      }
      STEP(xf1, wf1, 1, xf0, wf0);
      STEP(xf0, wf0, 2, xf1, wf1);
      STEP(xf1, wf1, 3, xf0, wf0);
      MM(xf1, wf1); PINALL();
      PSTAMP(2);
      WAITL0(); BARRIER();
      PSTAMP(6);
      const float qs = aj < 5 ? 0.22360679774997896f * 1.4426950408889634f : 1.f;
      f32x2 rs0 = *(const LAS f32x2*)(L + L_RS + (64 * wm + l31) * 8), rs1 = *(const LAS f32x2*)(L + L_RS + (64 * wm + 32 + l31) * 8);
      const float sc0 = rs0[1] * qs, nm0 = -rs0[0] * sc0, sc1 = rs1[1] * qs, nm1 = -rs1[0] * sc1;
#pragma unroll
      for (int j = 0; j < 2; ++j) {
        const int fj = 64 * wn + 32 * (j ^ wm) + 4 * hh;
        const unsigned go = WS_QKV + (unsigned)(32 * aj + (fj >> 2)) * 16384u + (unsigned)(128 * aP + 64 * wm + l31) * 8u;
#pragma unroll
        for (int rq = 0; rq < 4; ++rq) {
          const f32x4 cs = *(const LAS f32x4*)(L + L_CS + (fj + 8 * rq) * 4);
          const f32x16& a0 = j == 0 ? a00 : a01; const f32x16& a1 = j == 0 ? a10 : a11;
          const f32x2 c01 = {cs[0], cs[1]}, c23 = {cs[2], cs[3]};
          const f32x2 u0 = (f32x2){a0[rq * 4], a0[rq * 4 + 1]} * sc0 + c01 * nm0, u1 = (f32x2){a0[rq * 4 + 2], a0[rq * 4 + 3]} * sc0 + c23 * nm0;
          const f32x2 u2 = (f32x2){a1[rq * 4], a1[rq * 4 + 1]} * sc1 + c01 * nm1, u3 = (f32x2){a1[rq * 4 + 2], a1[rq * 4 + 3]} * sc1 + c23 * nm1;
          ST64(((u32x2){pk2(u0[0], u0[1]), pk2(u1[0], u1[1])}), go + 2 * rq * 16384);
          ST64(((u32x2){pk2(u2[0], u2[1]), pk2(u3[0], u3[1])}), go + 2 * rq * 16384 + 256);
        }
      }
      PSTAMP(4);
      lds_ctl_init();
      WAITV(0); WAITL0();
      PSTAMP(5);
      BARRIER();
    } else {
      read_epoch();
      rows_finish(w, ra0, ra1, rb0, rb1);
    }
  } else {
    read_epoch();
  }
  if (aj >= 15) {
    const int chn = min(32 * w + l31, 239);
    const char* wp0 = (const char*)p.wqkv + (size_t)(40 * aP + 20 * hh) * 3840 + chn * 16;
    u32x4 wv[20];
#pragma unroll
    for (int i = 0; i < 20; ++i) wv[i] = *(const u32x4*)(wp0 + (size_t)i * 3840);
    SBAR();
    f32x2 c2[4] = {{0.f, 0.f}, {0.f, 0.f}, {0.f, 0.f}, {0.f, 0.f}};
#pragma unroll
    for (int i = 0; i < 20; ++i)
#pragma unroll
      for (int e = 0; e < 4; ++e) c2[e] += (f32x2){bf2f(wv[i][e]), bfhi(wv[i][e])};
#pragma unroll
    for (int e = 0; e < 4; ++e) c2[e] = (f32x2){swap_add(c2[e][0]), swap_add(c2[e][1])};
    if (lane < 32 && 32 * w + l31 < 240) {
#pragma unroll
      for (int e = 0; e < 4; ++e) ST128(((u32x4){__float_as_uint(c2[e][0]), __float_as_uint(c2[e][1]), tag2[0], tag2[1]}), WS_CS1 + (unsigned)(((chn >> 4) * 16 + aP) * 64 + 4 * (chn & 15) + e) * 16);
    }
    lds_ctl_init(); WAITL0(); BARRIER();
  }
  const unsigned tag32 = tag2[0] | 1u;
  if (aj < 15 && tid == 0) ST64(tag2, WS_FLAGA + (aP * 16 + aj) * 128);
  PSTAMP(3);
  STAMP_PA();

  const int nt = w & 3, kh = w >> 2;
  const int tok = 64 * R + 16 * nt + l15;
  u32x2 xr[5], wrv[5];
  unsigned wr5[5];
  {
    const int t = w;
    *(LAS u32x2*)(L + L_V + 5 * PL + tid * 8) = (u32x2){(pp > 0 || tid >= 256) ? 0x00003f80u : 0u, 0u};
    if (pp == 0) {
#pragma unroll
      for (int i = 0; i < 3; ++i) { const int n = tid + 512 * i; if (n < 1280) *(LAS u32x4*)(L + (n >> 7) * PL + (n & 127) * 16) = (u32x4){0, 0, 0, 0}; }
    }
    if (kh == 0) {
#pragma unroll
      for (int ft = 0; ft < 5; ++ft) {
        xr[ft] = *(const u32x2*)(p.x + (size_t)tok * D + 80 * cc + 16 * ft + 4 * fg);
        wrv[ft] = *(const u32x2*)(p.wr + 80 * cc + 16 * ft + 4 * fg);
      }
    }
    if (bi == 31) {
#pragma unroll
      for (int i = 0; i < 5; ++i) wr5[i] = ((const unsigned*)p.wr)[lane + 64 * i];
    }
    auto lds_wait = [&](int off, unsigned v) { unsigned sp = 0; while (__hip_atomic_load((LAS unsigned*)(L + off), __ATOMIC_RELAXED, __HIP_MEMORY_SCOPE_WORKGROUP) < v && ++sp < (SPIN_CAP << 4)) __builtin_amdgcn_s_sleep(0); FENCE(); };
    WAITL0();
    if (kh == 0) {
      if (lane == 0) __hip_atomic_fetch_add((LAS unsigned*)(L + L_C1), 1u, __ATOMIC_RELAXED, __HIP_MEMORY_SCOPE_WORKGROUP);
      lds_wait(L_F1, 1);
    } else {
      if (w == 4) {
        const int sec = lane < 4 ? 0 : (lane < 12 ? 1 : 2);
        const int k = lane < 4 ? lane : (lane < 12 ? lane - 4 : lane - 12);
        const int col = sec * 640 + 20 * hd + ((k & 1) ? 19 : 0);
        const int Pn = sec == 0 ? 2 * pp + (k >> 1) : 2 * pp - 2 + (k >> 1);
        const bool need = lane < 20 && Pn >= 0;
        const unsigned fo = WS_FLAGA + (unsigned)((need ? Pn : 2 * pp) * 16 + (col >> 7)) * 128;
        unsigned spins = 0;
        for (;;) {
          FENCE();
          const u32x2 f = LD64(fo);
          if (__all(!need || (f[0] == tag2[0] && f[1] == tag2[1])) || ++spins > SPIN_CAP) break;
          __builtin_amdgcn_s_sleep(1);
        }
        if (lane == 0) __hip_atomic_store((LAS unsigned*)(L + L_F1), 1u, __ATOMIC_RELAXED, __HIP_MEMORY_SCOPE_WORKGROUP);
        STAMP(4);
      } else lds_wait(L_F1, 1);
      const char* qb = (const char*)p.ws + WS_QKV;
#pragma unroll
      for (int i = 0; i < 10; ++i) {
        const int pc = nt + 4 * i, pl = pc >> 2, qt = pc & 3;
        const int tok0 = 256 * pp - 256 + 128 * qt;
        if (tok0 >= 0) glds16c(lane * 16, qb + (size_t)((pl < 5 ? 160 + 5 * hd + pl : 320 + 5 * hd + pl - 5)) * 16384 + tok0 * 8, lds0 + pl * PL + qt * 1024);
      }
    }
    bf16x8 qf0; bf16x4 qf1;
    {
      const unsigned qo = WS_QKV + (unsigned)(5 * hd) * 16384u + (unsigned)(256 * pp + 32 * t + l31) * 8u;
      const u32x2 a = LD64(qo + (2 * hh) * 16384), c = LD64(qo + (2 * hh + 1) * 16384);
      u32x2 z = LD64(qo + 4 * 16384);
      asm volatile("s_waitcnt vmcnt(0)" : "+v"(z) :: "memory");
      if (hh) z = (u32x2){0, 0};
      qf0 = __builtin_bit_cast(bf16x8, (u32x4){a[0], a[1], c[0], c[1]});
      qf1 = __builtin_bit_cast(bf16x4, z);
    }
    if (kh == 1 && lane == 0) __hip_atomic_fetch_add((LAS unsigned*)(L + L_C1), 1u, __ATOMIC_RELAXED, __HIP_MEMORY_SCOPE_WORKGROUP);
    lds_wait(L_C1, 8);
    if (w == 4) STAMP(5);
    unsigned wov[5];
#pragma unroll
    for (int j = 0; j < 5; ++j) { const int n = 64 * j + lane, rho = (n * 6554) >> 16, chn = n - 10 * rho; wov[j] = (unsigned)(8 * ((rho >> 2) & 3) + 4 * (rho >> 4) + (rho & 3)) * 1280u + chn * 16u; }
    const char* wob = (const char*)p.wo + 160 * cc + (size_t)nt * 32 * 1280;
    const unsigned wol = lds0 + L_WO + nt * 5120;
#define WOP(i) GL_WO(wov[(i) % 5], wob + (size_t)((i) / 5) * 4 * 32 * 1280, wol + ((i) / 5) * 4 * 5120 + ((i) % 5) * 1024)
#define WOP3(i) do { if (kh == 0) { WOP(i); WOP((i) + 1); WOP((i) + 2); } SBAR(); } while (0)
    if (w == 0) STAMP(28);
    const LAS char* kb = L + L_K + l31 * 8 + (2 * hh) * PL;
    const LAS char* kb4 = L + L_K + l31 * 8 + 4 * PL;
    float negm;
    {
      const u32x2 k0 = *(const LAS u32x2*)(kb + (t + 8) * 256), k1 = *(const LAS u32x2*)(kb + (t + 8) * 256 + PL), k2 = *(const LAS u32x2*)(kb4 + (t + 8) * 256);
      const u32x4 q0 = __builtin_bit_cast(u32x4, qf0); const u32x2 q1 = __builtin_bit_cast(u32x2, qf1);
      float d = dot2(q0[0], k0[0], 0.f); d = dot2(q0[1], k0[1], d); d = dot2(q0[2], k1[0], d); d = dot2(q0[3], k1[1], d);
      d = dot2(q1[0], k2[0], d); d = dot2(q1[1], k2[1], d);
      negm = -swap_add(d);
    }
    const LAS char* vb = L + L_V + min(4 * ((lane >> 4) & 1) + (lane & 3), 5) * PL + (4 * hh + ((lane & 15) >> 2)) * 8;
    f32x16 o = (f32x16){};
    {
      auto diag_ok = [](int r) { unsigned long long m = 0; for (int l = 0; l < 64; ++l) if ((r & 3) + 8 * (r >> 2) + 4 * (l >> 5) <= (l & 31)) m |= 1ull << l; return m; };
      f32x16 cpl, sa, sb;
      bf16x4 ka, kc, kd;
      bf16x8 vq = {};
      u32x4 pq = {0, 0, 0, 0};
#define KRD(kt) do { ka = *(const LAS bf16x4*)(kb + (kt) * 256); kc = *(const LAS bf16x4*)(kb + (kt) * 256 + PL); kd = *(const LAS bf16x4*)(kb4 + (kt) * 256); } while (0)
#define EX2(S, i) pk2(__builtin_amdgcn_exp2f(S[i]), __builtin_amdgcn_exp2f(S[(i) + 1]))
#define GRP(nds, nv) do { __builtin_amdgcn_sched_group_barrier(0x8, 1, 0); if (nds) __builtin_amdgcn_sched_group_barrier(0x100, nds, 0); __builtin_amdgcn_sched_group_barrier(0x2, nv, 0); } while (0)
#define HALF(SC, SN, C, ktv, ktk) do { \
        u32x4 p0_; const LAS char* vp_ = vb + (ktv) * 256; \
        SN = MFMA32(cat8(ka, kc), qf0, C); \
        const bf16x8 v0_ = cat8(trd(vp_), trd(vp_ + 64)), v1_ = cat8(trd(vp_ + 128), trd(vp_ + 192)); \
        p0_[0] = EX2(SC, 0); p0_[1] = EX2(SC, 2); \
        GRP(4, 6); asm volatile("" : "+v"(SN), "+v"(p0_[0]), "+v"(p0_[1])); SBAR(); \
        o = MFMA32(vq, __builtin_bit_cast(bf16x8, pq), o); \
        p0_[2] = EX2(SC, 4); p0_[3] = EX2(SC, 6); \
        GRP(0, 6); asm volatile("" : "+v"(o), "+v"(p0_[2]), "+v"(p0_[3])); SBAR(); \
        SN = MFMA32K8(kd, qf1, SN); \
        KRD(ktk); \
        pq[0] = EX2(SC, 8); pq[1] = EX2(SC, 10); \
        GRP(3, 6); asm volatile("" : "+v"(SN), "+v"(pq[0]), "+v"(pq[1])); SBAR(); \
        o = MFMA32(v0_, __builtin_bit_cast(bf16x8, p0_), o); \
        pq[2] = EX2(SC, 12); pq[3] = EX2(SC, 14); vq = v1_; \
        GRP(0, 6); asm volatile("" : "+v"(o), "+v"(pq[2]), "+v"(pq[3])); SBAR(); } while (0)
      KRD(t + 8);
#pragma unroll
      for (int r = 0; r < 16; ++r) cpl[r] = __builtin_amdgcn_inverse_ballot_w64(diag_ok(r)) ? negm : -INFINITY;
      sa = MFMA32(cat8(ka, kc), qf0, cpl); sa = MFMA32K8(kd, qf1, sa);
      KRD(t + 7);
#pragma unroll
      for (int r = 0; r < 16; ++r) cpl[r] = negm;
      asm volatile("" : "+v"(sa), "+v"(cpl)); SBAR();
      WOP3(0);
      HALF(sa, sb, cpl, t + 8, t + 6);
      WOP3(3);
      HALF(sb, sa, cpl, t + 7, t + 5);
      WOP3(6);
      HALF(sa, sb, cpl, t + 6, t + 4);
      WOP3(9);
      HALF(sb, sa, cpl, t + 5, t + 3);
      WOP3(12);
      HALF(sa, sb, cpl, t + 4, t + 2);
      WOP3(15);
      HALF(sb, sa, cpl, t + 3, t + 1);
      WOP3(18);
      HALF(sa, sb, cpl, t + 2, t);
#pragma unroll
      for (int r = 0; r < 16; ++r) cpl[r] = __builtin_amdgcn_inverse_ballot_w64(diag_ok(r)) ? -INFINITY : negm;
      asm volatile("" : "+v"(cpl)); SBAR();
      if (kh == 0) { WOP(21); WOP(22); WOP(23); WOP(24); }
      SBAR();
      HALF(sb, sa, cpl, t + 1, t);
      {
        const LAS char* vp_ = vb + t * 256;
        const bf16x8 v0_ = cat8(trd(vp_), trd(vp_ + 64)), v1_ = cat8(trd(vp_ + 128), trd(vp_ + 192));
        o = MFMA32(vq, __builtin_bit_cast(bf16x8, pq), o);
        u32x4 p0_, p1_;
#pragma unroll
        for (int e = 0; e < 4; ++e) p0_[e] = EX2(sa, 2 * e);
        o = MFMA32(v0_, __builtin_bit_cast(bf16x8, p0_), o);
#pragma unroll
        for (int e = 0; e < 4; ++e) p1_[e] = EX2(sa, 8 + 2 * e);
        o = MFMA32(v1_, __builtin_bit_cast(bf16x8, p1_), o);
      }
    }
    float lsum;
    { auto r = __builtin_amdgcn_permlane32_swap(__float_as_uint(o[8]), __float_as_uint(o[8]), false, false); lsum = __uint_as_float(r[1]); }
    if (__builtin_expect(__any(!(lsum < 1.0995116e12f)), 0)) {
      const int lo = pp >= 1 ? t : 8;
      float mx = -INFINITY;
#pragma unroll 1
      for (int kt = t + 8; kt >= lo; --kt) {
        const bf16x4 ka = *(const LAS bf16x4*)(kb + kt * 256), kc = *(const LAS bf16x4*)(kb + kt * 256 + PL), kd = *(const LAS bf16x4*)(kb4 + kt * 256);
        f32x16 s = MFMA32(cat8(ka, kc), qf0, (f32x16){});
        s = MFMA32K8(kd, qf1, s);
#pragma unroll
        for (int r = 0; r < 16; ++r) { const int kr = (r & 3) + 8 * (r >> 2) + 4 * hh; const bool ok = kt == t + 8 ? kr <= l31 : (kt == t ? kr > l31 : true); mx = fmaxf(mx, ok ? s[r] : -INFINITY); }
      }
      { auto r = __builtin_amdgcn_permlane32_swap(__float_as_uint(mx), __float_as_uint(mx), false, false); mx = fmaxf(__uint_as_float(r[0]), __uint_as_float(r[1])); }
      o = (f32x16){};
#pragma unroll 1
      for (int kt = t + 8; kt >= lo; --kt) {
        const bf16x4 ka = *(const LAS bf16x4*)(kb + kt * 256), kc = *(const LAS bf16x4*)(kb + kt * 256 + PL), kd = *(const LAS bf16x4*)(kb4 + kt * 256);
        const LAS char* vp_ = vb + kt * 256;
        const bf16x8 v0_ = cat8(trd(vp_), trd(vp_ + 64)), v1_ = cat8(trd(vp_ + 128), trd(vp_ + 192));
        f32x16 s = MFMA32(cat8(ka, kc), qf0, (f32x16){});
        s = MFMA32K8(kd, qf1, s);
        u32x4 p0_, p1_;
#pragma unroll
        for (int r = 0; r < 16; ++r) { const int kr = (r & 3) + 8 * (r >> 2) + 4 * hh; const bool ok = kt == t + 8 ? kr <= l31 : (kt == t ? kr > l31 : true); s[r] = ok ? __builtin_amdgcn_exp2f(s[r] - mx) : 0.f; }
#pragma unroll
        for (int e = 0; e < 4; ++e) { p0_[e] = pk2(s[2 * e], s[2 * e + 1]); p1_[e] = pk2(s[8 + 2 * e], s[8 + 2 * e + 1]); }
        o = MFMA32(v0_, __builtin_bit_cast(bf16x8, p0_), o);
        o = MFMA32(v1_, __builtin_bit_cast(bf16x8, p1_), o);
      }
      auto r = __builtin_amdgcn_permlane32_swap(__float_as_uint(o[8]), __float_as_uint(o[8]), false, false); lsum = __uint_as_float(r[1]);
    }
    if (w == 0) STAMP(6);
    STAMP(32 + w);
    {
      const float inv = 1.f / lsum;
      unsigned A0 = pk2(o[0] * inv, o[1] * inv), A1 = pk2(o[2] * inv, o[3] * inv), B0 = pk2(o[4] * inv, o[5] * inv), B1 = pk2(o[6] * inv, o[7] * inv);
      unsigned C0 = pk2(o[8] * inv, o[9] * inv), C1 = pk2(o[10] * inv, o[11] * inv);
      const int tt = pp * 16 + 2 * t + (l31 >> 4);
      const unsigned base = WS_O + (unsigned)tt * 80 * 256 + (l31 & 15) * 16 + (unsigned)(5 * (hd >> 1)) * 256;
      if ((hd & 1) == 0) {
        auto s0 = __builtin_amdgcn_permlane32_swap(A0, B0, false, false), s1 = __builtin_amdgcn_permlane32_swap(A1, B1, false, false);
        ST128(((u32x4){s0[0], s1[0], s0[1], s1[1]}), base + hh * 256);
        if (hh == 0) ST64(((u32x2){C0, C1}), base + 2 * 256);
      } else {
        auto u0 = __builtin_amdgcn_permlane32_swap(A0, C0, false, false), u1 = __builtin_amdgcn_permlane32_swap(A1, C1, false, false);
        const u32x4 v = hh ? (u32x4){B0, B1, u0[0], u1[0]} : (u32x4){u0[1], u1[1], B0, B1};
        ST128(v, base + (3 + hh) * 256);
        if (hh == 0) ST64(((u32x2){A0, A1}), base + 2 * 256 + 8);
      }
      WAITV(0);
      if (lane == 0) ST64(tag2, WS_FLAGO + (((pp * 8 + t) * 2 + (hd >> 4)) * 16 + (hd & 15)) * 8);
    }
  }
  if (w == 0) STAMP(7);
  STAMP(40 + w);

  BARRIER();
  if (w == 0) STAMP(8);

  f32x4 acc[5];
#pragma unroll
  for (int ft = 0; ft < 5; ++ft) acc[ft] = (f32x4){0, 0, 0, 0};
  {
    const LAS char* wp = L + L_WO + (unsigned)(kh * 320 + 4 * fg + ((lane & 15) >> 2)) * 160 + (lane & 3) * 8;
    bf16x8 wa[3][5];
#pragma unroll
    for (int s = 0; s < 3; ++s)
#pragma unroll
      for (int ft = 0; ft < 5; ++ft) wa[s][ft] = cat8(trd(wp + s * 5120 + ft * 32), trd(wp + s * 5120 + 2560 + ft * 32));
    const int tg = 4 * R + nt;
    const unsigned fo = WS_FLAGO + ((((tg >> 4) * 8 + ((tg >> 1) & 7)) * 2 + kh) * 16 + l15) * 8;
    unsigned spins = 0;
    for (;;) {
      FENCE();
      const u32x2 f = LD64(fo);
      if (__all(f[0] == tag2[0] && f[1] == tag2[1]) || ++spins > SPIN_CAP) break;
      __builtin_amdgcn_s_sleep(1);
    }
    FENCE();
    if (w == 0) STAMP(9);
    STAMP(48 + w);
    u32x4 of[10];
#pragma unroll
    for (int s = 0; s < 10; ++s) of[s] = LD128(WS_O + (unsigned)(tg * 80 + 4 * (10 * kh + s)) * 256 + lane * 16);
    SBAR();
#pragma unroll
    for (int s = 0; s < 10; ++s) {
      const bf16x8 bfr = __builtin_bit_cast(bf16x8, of[s]);
#pragma unroll
      for (int ft = 0; ft < 5; ++ft) acc[ft] = MFMA16(s < 3 ? wa[s < 3 ? s : 0][ft] : cat8(trd(wp + s * 5120 + ft * 32), trd(wp + s * 5120 + 2560 + ft * 32)), bfr, acc[ft]);
    }
  }
  STAMP(56 + w);
  if (kh == 1) {
#pragma unroll
    for (int ft = 0; ft < 5; ++ft) *(LAS f32x4*)(L + L_RED + ((nt * 5 + ft) * 64 + lane) * 16) = acc[ft];
  }
  WAITL0(); BARRIER();
  if (w == 0) STAMP(10);
  if (kh == 0) {
    float s1 = 0.f, s2 = 0.f, s3 = 0.f;
#pragma unroll
    for (int ft = 0; ft < 5; ++ft) {
      const f32x4 o2 = *(const LAS f32x4*)(L + L_RED + ((nt * 5 + ft) * 64 + lane) * 16);
      const unsigned a01 = pk2(acc[ft][0] + o2[0], acc[ft][1] + o2[1]), a23 = pk2(acc[ft][2] + o2[2], acc[ft][3] + o2[3]);
      const unsigned h01 = pk2(bf2f(xr[ft][0]) + bf2f(a01), bfhi(xr[ft][0]) + bfhi(a01)), h23 = pk2(bf2f(xr[ft][1]) + bf2f(a23), bfhi(xr[ft][1]) + bfhi(a23));
      *(LAS u32x2*)(L + L_HST + nt * 2560 + l15 * 160 + ft * 32 + fg * 8) = (u32x2){h01, h23};
      s1 = dot2(h01, 0x3f803f80u, s1); s1 = dot2(h23, 0x3f803f80u, s1);
      s2 = dot2(h01, h01, s2); s2 = dot2(h23, h23, s2);
      s3 = dot2(h01, wrv[ft][0], s3); s3 = dot2(h23, wrv[ft][1], s3);
    }
    WAITL0();
#pragma unroll
    for (int i = 0; i < 3; ++i) {
      const int n = lane + 64 * i;
      if (n < 160) ST128(*(const LAS u32x4*)(L + L_HST + nt * 2560 + n * 16), WS_H + (unsigned)((R * 8 + cc) * 64 + 16 * nt) * 160 + n * 16);
    }
    s1 = swap_add(swap16_add(s1)); s2 = swap_add(swap16_add(s2)); s3 = swap_add(swap16_add(s3));
    WAITV(0);
    if (lane < 16) ST128(((u32x4){__float_as_uint(s1), __float_as_uint(s2), __float_as_uint(s3), tag32}), WS_STATS + ((R * 8 + cc) * 64 + 16 * nt + l15) * 16);
    if (w == 0) STAMP(11);
    BARRIER();
  } else {
    BARRIER();
    const int rl = lane >> 1, grow = 8 * ((rl >> 2) & 3) + 4 * (rl >> 4) + (rl & 3);
    if (is_gu) {
      const unsigned vo = (unsigned)grow * (F * 2) + (lane & 1) * 16;
#pragma unroll 1
      for (int pc = nt; pc < 40; pc += 4) { const int m = pc >= 20, s = pc - 20 * m; GL_FW(vo, (const char*)(m ? p.wu : p.wg) + (size_t)s * 32 * F * 2 + gs * 32, lds0 + L_FW + pc * 1024); }
    } else if (is_dn) {
      const unsigned vo = (unsigned)grow * (D * 2) + (lane & 1) * 16;
#pragma unroll 1
      for (int pc = nt; pc < 54; pc += 4) GL_FW(vo, (const char*)p.wd + (size_t)pc * 32 * D * 2 + dn * 32, lds0 + L_FW + pc * 1024);
    }
  }
  auto rank64 = [&](unsigned k) {
    const u32x4 oa = *(const LAS u32x4*)(L + L_KEY + w * 32), ob = *(const LAS u32x4*)(L + L_KEY + w * 32 + 16);
    unsigned c = 0;
#pragma unroll
    for (int e = 0; e < 4; ++e) c += (oa[e] > k) + (ob[e] > k);
    *(LAS unsigned char*)(L + L_PART + lane * 8 + w) = (unsigned char)c;
    WAITL0(); BARRIER();
    const u32x2 pr = *(const LAS u32x2*)(L + L_PART + lane * 8);
    return __builtin_amdgcn_sad_u8(pr[0], 0u, __builtin_amdgcn_sad_u8(pr[1], 0u, 0u));
  };
  if (bi == 31) {
    float swr = 0.f;
#pragma unroll
    for (int i = 0; i < 5; ++i) swr = dot2(wr5[i], 0x3f803f80u, swr);
    swr = wave_fsum(swr);
    const int tl = 32 * w + l31;
    const unsigned so = WS_STATS + (unsigned)(((4 * xcd + (tl >> 6)) * 8 + 4 * hh) * 64 + (tl & 63)) * 16;
    float t1, t2, t3;
    unsigned spins = 0;
    for (;;) {
      FENCE();
      const u32x4 g0 = LD128(so), g1 = LD128(so + 1024), g2 = LD128(so + 2048), g3 = LD128(so + 3072);
      t1 = (__uint_as_float(g0[0]) + __uint_as_float(g1[0])) + (__uint_as_float(g2[0]) + __uint_as_float(g3[0]));
      t2 = (__uint_as_float(g0[1]) + __uint_as_float(g1[1])) + (__uint_as_float(g2[1]) + __uint_as_float(g3[1]));
      t3 = (__uint_as_float(g0[2]) + __uint_as_float(g1[2])) + (__uint_as_float(g2[2]) + __uint_as_float(g3[2]));
      if (__all(g0[3] == tag32 && g1[3] == tag32 && g2[3] == tag32 && g3[3] == tag32) || ++spins > SPIN_CAP) break;
    }
    t1 = swap_add(t1); t2 = swap_add(t2); t3 = swap_add(t3);
    const float mean = t1 * (1.f / D);
    const float var = fmaxf(t2 * (1.f / D) - mean * mean, 0.f);
    const float mb = rbf(mean);
    const float sd = rbf(__builtin_amdgcn_sqrtf(rbf(rbf(var) + 1e-5f)));
    const unsigned rb = pk2((t3 - mb * swr) * __builtin_amdgcn_rcpf(sd), 0.f) & 0xffffu;
    const unsigned key = ((rb ^ ((rb & 0x8000u) ? 0xffffu : 0x8000u)) << 16) | (unsigned)(2047 - (256 * xcd + tl));
    const unsigned ms = (__float_as_uint(mb) & 0xffff0000u) | (__float_as_uint(sd) >> 16);
    if (w == 0) STAMP(12);
    if (lane < 32) *(LAS unsigned*)(L + L_K32 + tl * 4) = key;
    WAITL0();
    unsigned c = 0;
#pragma unroll
    for (int i = 0; i < 4; ++i) {
      const u32x4 o4 = *(const LAS u32x4*)(L + L_K32 + (32 * w + 16 * hh + 4 * i) * 4);
#pragma unroll
      for (int e = 0; e < 4; ++e) c += o4[e] > key;
    }
    { auto r = __builtin_amdgcn_permlane32_swap(c, c, false, false); c = r[0] + r[1]; }
    if (lane < 32 && c < 8) { *(LAS unsigned*)(L + L_KEY + (8 * w + c) * 4) = key; *(LAS unsigned*)(L + L_KEY + 256 + (8 * w + c) * 4) = ms; }
    WAITL0(); BARRIER();
    const unsigned k = *(const LAS unsigned*)(L + L_KEY + lane * 4), m2 = *(const LAS unsigned*)(L + L_KEY + 256 + lane * 4);
    const unsigned rk = rank64(k);
    if (w == 0) { if (rk < 8) ST128(((u32x4){k, tag2[0], m2, tag2[1]}), WS_CAND + xcd * 128 + rk * 16); STAMP(26); }
    BARRIER();
  }
  if (w == 0) {
    u32x4 a;
    unsigned spins = 0;
    for (;;) {
      FENCE();
      a = LD128(WS_CAND + lane * 16);
      if (__all(a[1] == tag2[0] && a[3] == tag2[1]) || ++spins > SPIN_CAP) break;
      if (is_gu || is_dn) __builtin_amdgcn_s_sleep(1); else __builtin_amdgcn_s_sleep(8);
    }
    STAMP(13);
    *(LAS unsigned*)(L + L_KEY + lane * 4) = a[0]; *(LAS unsigned*)(L + L_KEY + 256 + lane * 4) = a[2];
    if (b == 0 && lane == 0) ST64(tag2, WS_EPOCH);
  }
  const int hc0 = (lane * 6554) >> 16, hc1 = ((64 + l15) * 6554) >> 16;
  const unsigned ho0 = (unsigned)hc0 * (64 * 160) + (lane - 10 * hc0) * 16, ho1 = (unsigned)hc1 * (64 * 160) + (64 + l15 - 10 * hc1) * 16;
  if (kh == 1) WAITV(0);
  WAITL0(); BARRIER();
  {
    const unsigned k = *(const LAS unsigned*)(L + L_KEY + lane * 4), m2 = *(const LAS unsigned*)(L + L_KEY + 256 + lane * 4);
    const unsigned rk = rank64(k);
    if (rk < 8) *(LAS u32x2*)(L + L_SEL + rk * 8) = (u32x2){k, m2};
    WAITL0();
  }
  if (w == 0) STAMP(14);

  auto h_row = [&](int row, u32x4& h0, u32x4& h1) {
    const unsigned rb = WS_H + (unsigned)((row >> 6) * 8 * 64 + (row & 63)) * 160;
    h0 = LD128(rb + ho0); h1 = LD128(rb + ho1);
  };
  auto ln8 = [&](const u32x4& h, float m, float iv) { u32x4 n;
#pragma unroll
    for (int e = 0; e < 4; ++e) { const unsigned d = pk2(bf2f(h[e]) - m, bfhi(h[e]) - m); n[e] = pk2(bf2f(d) * iv, bfhi(d) * iv); } return n; };
  auto out_row = [&]() {
    const int row = 64 * R + 8 * cc + w;
    const u32x2 sk = *(const LAS u32x2*)(L + L_SEL + (lane & 7) * 8);
    if (!__any(2047 - (int)(sk[0] & 0x7ff) == row)) {
      u32x4 h0, h1; h_row(row, h0, h1);
      const u32x4 g = LD128(WS_STATS + (unsigned)((R * 8 + (lane & 7)) * 64 + 8 * cc + w) * 16);
      float t1 = __uint_as_float(g[0]), t2 = __uint_as_float(g[1]);
      t1 = dppadd<0x141>(t1); t2 = dppadd<0x141>(t2);
      t1 = dppadd<0xb1>(t1); t2 = dppadd<0xb1>(t2);
      t1 = dppadd<0x4e>(t1); t2 = dppadd<0x4e>(t2);
      const float mean = t1 * (1.f / D);
      const float m = rbf(mean), iv = __builtin_amdgcn_rcpf(rbf(__builtin_amdgcn_sqrtf(rbf(rbf(fmaxf(t2 * (1.f / D) - mean * mean, 0.f)) + 1e-5f))));
      const u32x4 n0 = ln8(h0, m, iv), n1 = ln8(h1, m, iv);
      u32x4 r0, r1;
#pragma unroll
      for (int e = 0; e < 4; ++e) { r0[e] = pk2(bf2f(h0[e]) + bf2f(n0[e]), bfhi(h0[e]) + bfhi(n0[e])); r1[e] = pk2(bf2f(h1[e]) + bf2f(n1[e]), bfhi(h1[e]) + bfhi(n1[e])); }
      *(u32x4*)(p.out + (size_t)row * D + lane * 8) = r0;
      if (lane < 16) *(u32x4*)(p.out + (size_t)row * D + 512 + lane * 8) = r1;
    }
  };
  if (is_gu) {
    {
      const u32x2 sk = *(const LAS u32x2*)(L + L_SEL + w * 8);
      u32x4 h0, h1; h_row(2047 - (int)(sk[0] & 0x7ff), h0, h1);
      const float m = __uint_as_float(sk[1] & 0xffff0000u), iv = __builtin_amdgcn_rcpf(__uint_as_float(sk[1] << 16));
      *(LAS u32x4*)(L + L_XS + w * 1296 + lane * 16) = ln8(h0, m, iv);
      if (lane < 16) *(LAS u32x4*)(L + L_XS + w * 1296 + 1024 + lane * 16) = ln8(h1, m, iv);
    }
    WAITL0(); BARRIER();
    if (w == 0) STAMP(15);
    {
      const int m = w & 1, kq = w >> 1;
      f32x4 a4 = {0, 0, 0, 0}, b4 = {0, 0, 0, 0};
#pragma unroll
      for (int i = 0; i < 5; ++i) {
        const int s = 5 * kq + i;
        const LAS char* ap = L + L_FW + (m * 20 + s) * 1024 + lane * 8;
        const bf16x8 af = cat8(trd(ap), trd(ap + 512));
        const bf16x8 bfv = *(const LAS bf16x8*)(L + L_XS + (lane & 7) * 1296 + s * 64 + fg * 16);
        if (i & 1) b4 = MFMA16(af, bfv, b4); else a4 = MFMA16(af, bfv, a4);
      }
      *(LAS f32x4*)(L + L_FRED + (w * 64 + lane) * 16) = a4 + b4;
    }
    WAITL0(); BARRIER();
    if (w == 0) {
      f32x4 g4 = {0, 0, 0, 0}, u4 = {0, 0, 0, 0};
#pragma unroll
      for (int q = 0; q < 4; ++q) { g4 += *(const LAS f32x4*)(L + L_FRED + ((2 * q) * 64 + lane) * 16); u4 += *(const LAS f32x4*)(L + L_FRED + ((2 * q + 1) * 64 + lane) * 16); }
      float a[4];
#pragma unroll
      for (int e = 0; e < 4; ++e) { const float g = rbf(g4[e]), u = rbf(u4[e]); const float sg = rbf(g * __builtin_amdgcn_rcpf(1.f + __builtin_amdgcn_exp2f(g * -1.4426950408889634f))); a[e] = sg * u; }
      if (l15 < 8) ST128(((u32x4){pk2(a[0], a[1]), tag32, pk2(a[2], a[3]), tag32}), WS_ACT + (((gs >> 1) * 4 + 2 * (gs & 1) + (fg >> 1)) * 8 + l15) * 32 + (fg & 1) * 16);
      STAMP(16);
    }
    out_row();
  } else if (is_dn) {
    const u32x2 sk = *(const LAS u32x2*)(L + L_SEL + (lane & 7) * 8);
    const int row = 2047 - (int)(sk[0] & 0x7ff);
    u32x2 hv = {0, 0}, nv = {0, 0};
    float sig = 0.f;
    if (w == 0) {
      const int col = 16 * dn + 4 * fg, c = col / 80;
      hv = LD64(WS_H + (unsigned)(((row >> 6) * 8 + c) * 64 + (row & 63)) * 160 + (col - 80 * c) * 2);
      const unsigned rk = sk[0] >> 16, rbits = (rk & 0x8000u) ? (rk ^ 0x8000u) : (rk ^ 0xffffu);
      sig = rbf(__builtin_amdgcn_rcpf(1.f + __builtin_amdgcn_exp2f(bf2f(rbits) * -1.4426950408889634f)));
      const float m = __uint_as_float(sk[1] & 0xffff0000u), iv = __builtin_amdgcn_rcpf(__uint_as_float(sk[1] << 16));
#pragma unroll
      for (int e = 0; e < 2; ++e) { const unsigned d2 = pk2(bf2f(hv[e]) - m, bfhi(hv[e]) - m); nv[e] = pk2(bf2f(d2) * iv, bfhi(d2) * iv); }
      asm volatile("" : "+v"(nv), "+v"(hv), "+v"(sig));
    } else out_row();
    f32x4 a4 = {0, 0, 0, 0}, b4 = {0, 0, 0, 0};
    {
      bf16x8 af[7];
#pragma unroll
      for (int i = 0; i < 7; ++i) { const LAS char* ap = L + L_FW + min(w + 8 * i, 53) * 1024 + lane * 8; af[i] = cat8(trd(ap), trd(ap + 512)); }
      u32x4 g0[7], g1[7];
      unsigned spins = 0;
      for (;;) {
        FENCE();
        bool ok = true;
#pragma unroll
        for (int i = 0; i < 7; ++i) {
          const int s = min(w + 8 * i, 53);
          const unsigned ao = WS_ACT + ((s * 4 + fg) * 8 + (lane & 7)) * 32;
          g0[i] = LD128(ao); g1[i] = LD128(ao + 16);
        }
#pragma unroll
        for (int i = 0; i < 7; ++i) ok = ok && g0[i][1] == tag32 && g0[i][3] == tag32 && g1[i][1] == tag32 && g1[i][3] == tag32;
        if (__all(ok) || ++spins > SPIN_CAP) break;
      }
      if (w == 0) STAMP(17);
#pragma unroll
      for (int i = 0; i < 7; ++i) {
        const bf16x8 bv = __builtin_bit_cast(bf16x8, (u32x4){g0[i][0], g0[i][2], g1[i][0], g1[i][2]});
        if (i < 6 || w < 6) { if (i & 1) b4 = MFMA16(af[i], bv, b4); else a4 = MFMA16(af[i], bv, a4); }
      }
    }
    *(LAS f32x4*)(L + L_FRED + (w * 64 + lane) * 16) = a4 + b4;
    WAITL0(); BARRIER();
    if (w == 0) {
      f32x4 y = {0, 0, 0, 0};
#pragma unroll
      for (int q = 0; q < 8; ++q) y += *(const LAS f32x4*)(L + L_FRED + (q * 64 + lane) * 16);
      unsigned r2[2];
#pragma unroll
      for (int e = 0; e < 2; ++e) {
        const unsigned y2 = pk2(rbf(y[2 * e]) * sig, rbf(y[2 * e + 1]) * sig);
        const unsigned t2 = pk2(bf2f(nv[e]) + bf2f(y2), bfhi(nv[e]) + bfhi(y2));
        r2[e] = pk2(bf2f(hv[e]) + bf2f(t2), bfhi(hv[e]) + bfhi(t2));
      }
      if (l15 < 8) *(u32x2*)(p.out + (size_t)row * D + 16 * dn + 4 * fg) = (u32x2){r2[0], r2[1]};
      STAMP(18);
      out_row();
    }
  } else {
    out_row();
  }
  if (w == 0) STAMP(19);
  STAMP_FLUSH();
}

extern "C" void kernel_launch(void* const* d_in, const int* in_sizes, int n_in, void* d_out, int out_size, void* d_ws, size_t ws_size, hipStream_t stream) {
  Params p;
  p.x = (const u16*)d_in[0]; p.wqkv = (const u16*)d_in[1]; p.wo = (const u16*)d_in[2]; p.wr = (const u16*)d_in[4];
  p.wg = (const u16*)d_in[5]; p.wu = (const u16*)d_in[6]; p.wd = (const u16*)d_in[7]; p.out = (u16*)d_out; p.ws = (unsigned char*)d_ws + ((ws_size - (16u << 20)) & ~(size_t)4095);
  fused_kernel<<<256, 512, 0, stream>>>(p);
}
```

```cpp
#include <hip/hip_runtime.h>
#include <stdint.h>

typedef unsigned short u16;
typedef short bf16x8 __attribute__((ext_vector_type(8)));
typedef short bf16x4 __attribute__((ext_vector_type(4)));
typedef __bf16 bf2 __attribute__((ext_vector_type(2)));
typedef float f32x16 __attribute__((ext_vector_type(16)));
typedef float f32x4 __attribute__((ext_vector_type(4)));
typedef float f32x2 __attribute__((ext_vector_type(2)));
typedef unsigned u32x4 __attribute__((ext_vector_type(4)));
typedef unsigned u32x2 __attribute__((ext_vector_type(2)));
typedef unsigned long long u64;
#define LAS __attribute__((address_space(3)))
#define SBAR() __builtin_amdgcn_sched_barrier(0)
#define BARRIER() asm volatile("s_barrier" ::: "memory")
#define WAITL0() asm volatile("s_waitcnt lgkmcnt(0)" ::: "memory")
#define WAITV(n) asm volatile("s_waitcnt vmcnt(" #n ")" ::: "memory")
#define FENCE() asm volatile("" ::: "memory")

constexpr int S = 2048, D = 640, F = 1728;
constexpr int SC1 = 16;

constexpr unsigned WS_EPOCH = 0;
constexpr unsigned WS_FLAGA = 4096;
constexpr unsigned WS_FLAGO = WS_FLAGA + 32768;
constexpr unsigned WS_CAND = WS_FLAGO + 16384;
constexpr unsigned WS_HDONE = WS_CAND + 4096;
constexpr unsigned WS_ACT = WS_HDONE + 4096;
constexpr unsigned WS_ST1 = 131072;
constexpr unsigned WS_CS1 = WS_ST1 + 32768;
constexpr unsigned WS_STATS = 524288;
constexpr unsigned WS_QKV = 1048576;
constexpr unsigned WS_O = WS_QKV + 480 * 16384;
constexpr unsigned WS_H = WS_O + S * D * 2;
#define WS_TAIL 1
#define STAMP_OFF (15u << 20)
#define NSTAMP 64

constexpr int L_SLOT = 32768, L_W = 16384;
constexpr int PL = 4128;
constexpr int L_K = 0, L_V = 5 * PL;
constexpr int L_WO = 45568;
constexpr int L_RED = 0;
constexpr int L_HST = 20480;
constexpr int L_FW = L_WO;
constexpr int L_XS = 0;
constexpr int L_FRED = 12288;
constexpr int L_MISC = L_WO + 640 * 160;
constexpr int L_RS = 0;
constexpr int L_CS = 1024;
constexpr int L_CNT = L_MISC + 3600;
constexpr int L_F1 = L_MISC + 3604, L_C1 = L_MISC + 3608;
constexpr int L_PART = L_MISC + 2048;
constexpr int L_K32 = L_MISC + 2560;
constexpr int L_HD = L_MISC + 3712;
constexpr int L_SEL = L_MISC + 3968;
constexpr int L_STAMP = L_MISC + 4096;
constexpr int L_KEY = L_MISC + 4608 + 256;
constexpr int LDS_BYTES = 163840;
static_assert(L_V + 6 * PL <= L_WO && L_KEY + 2048 <= LDS_BYTES, "lds map");

struct Params { const u16* x; const u16* wqkv; const u16* wo; const u16* wr; const u16* wg; const u16* wu; const u16* wd; u16* out; unsigned char* ws; };

__device__ __forceinline__ float bf2f(unsigned v) { return __uint_as_float(v << 16); }
__device__ __forceinline__ float bfhi(unsigned v) { return __uint_as_float(v & 0xffff0000u); }
__device__ __forceinline__ unsigned pk2(float a, float b) { f32x2 f = {a, b}; return __builtin_bit_cast(unsigned, __builtin_convertvector(f, bf2)); }
__device__ __forceinline__ float rbf(float f) { return __uint_as_float(pk2(f, 0.f) << 16); }
__device__ __forceinline__ float dot2(unsigned a, unsigned b, float c) { return __builtin_amdgcn_fdot2_f32_bf16(__builtin_bit_cast(bf2, a), __builtin_bit_cast(bf2, b), c, false); }
__device__ __forceinline__ float swap_add(float v) { auto r = __builtin_amdgcn_permlane32_swap(__float_as_uint(v), __float_as_uint(v), false, false); return __uint_as_float(r[0]) + __uint_as_float(r[1]); }
__device__ __forceinline__ float swap16_add(float v) { auto r = __builtin_amdgcn_permlane16_swap(__float_as_uint(v), __float_as_uint(v), false, false); return __uint_as_float(r[0]) + __uint_as_float(r[1]); }
template <int CTRL> __device__ __forceinline__ float dppadd(float v) { return v + __int_as_float(__builtin_amdgcn_update_dpp(0, __float_as_int(v), CTRL, 0xf, 0xf, false)); }
__device__ __forceinline__ float wave_fsum(float v) { v = dppadd<0x128>(v); v = dppadd<0x124>(v); v = dppadd<0x122>(v); v = dppadd<0x121>(v); return swap_add(swap16_add(v)); }
template <int CTRL> __device__ __forceinline__ unsigned dppmax(unsigned v) { return max(v, (unsigned)__builtin_amdgcn_update_dpp(0, (int)v, CTRL, 0xf, 0xf, false)); }
__device__ __forceinline__ unsigned wave_umax(unsigned v) {
  v = dppmax<0x128>(v); v = dppmax<0x124>(v); v = dppmax<0x122>(v); v = dppmax<0x121>(v);
  auto r = __builtin_amdgcn_permlane16_swap(v, v, false, false); v = max(r[0], r[1]);
  auto q = __builtin_amdgcn_permlane32_swap(v, v, false, false); return max(q[0], q[1]);
}
__device__ __forceinline__ void glds16(unsigned voff, const void* sbase, unsigned lds) {
  asm volatile("s_mov_b32 m0, %2\n\ts_nop 0\n\tglobal_load_lds_dwordx4 %0, %1" ::"v"(voff), "s"(sbase), "s"(lds) : "memory");
}
__device__ __forceinline__ void glds16c(unsigned voff, const void* sbase, unsigned lds) {
  asm volatile("s_mov_b32 m0, %2\n\ts_nop 0\n\tglobal_load_lds_dwordx4 %0, %1 sc1" ::"v"(voff), "s"(sbase), "s"(lds) : "memory");
}
__device__ __forceinline__ void touch64(unsigned voff, const void* sbase, unsigned lds) {
  asm volatile("s_mov_b32 m0, %2\n\ts_nop 0\n\tglobal_load_lds_dword %0, %1" ::"v"(voff), "s"(sbase), "s"(lds) : "memory");
}
__device__ __forceinline__ void glds16n(unsigned voff, const void* sbase, unsigned lds) {
  asm volatile("s_mov_b32 m0, %2\n\ts_nop 0\n\tglobal_load_lds_dwordx4 %0, %1 nt" ::"v"(voff), "s"(sbase), "s"(lds) : "memory");
}
#define GL_FW glds16
#define GL_WO glds16
#define GL_X glds16
#define GL_W glds16
__device__ __forceinline__ bf16x4 trd(const LAS char* p) { return __builtin_amdgcn_ds_read_tr16_b64_v4i16((LAS bf16x4*)p); }
__device__ __forceinline__ bf16x8 cat8(bf16x4 a, bf16x4 b) { return (bf16x8){a[0], a[1], a[2], a[3], b[0], b[1], b[2], b[3]}; }
#define MFMA32(a, b, c) __builtin_amdgcn_mfma_f32_32x32x16_bf16(a, b, c, 0, 0, 0)
#define MFMA32K8(a, b, c) __builtin_amdgcn_mfma_f32_32x32x8bf16_1k(a, b, c, 0, 0, 0)
#define MFMA16(a, b, c) __builtin_amdgcn_mfma_f32_16x16x32_bf16(a, b, c, 0, 0, 0)
#define LD128(off) __builtin_amdgcn_raw_buffer_load_b128(rws, (off), 0, SC1)
#define LD64(off) __builtin_amdgcn_raw_buffer_load_b64(rws, (off), 0, SC1)
#define ST128(v, off) __builtin_amdgcn_raw_buffer_store_b128((v), rws, (off), 0, SC1)
#define ST64(v, off) __builtin_amdgcn_raw_buffer_store_b64((v), rws, (off), 0, SC1)

#define STAMP(k) do {} while (0)
#define STAMP_INIT() do {} while (0)
#define PSTAMP(i) do {} while (0)
#define STAMP_ZERO() do {} while (0)
#define STAMP_PA() do {} while (0)
#define STAMP_FLUSH() do {} while (0)

#define ROT (((bi >> 3) * 5 / 2) % 10)
constexpr unsigned SPIN_CAP = 1u << 17;

__global__ __launch_bounds__(512, 2) void fused_kernel(Params p) {
  __shared__ __attribute__((aligned(16))) char smem[LDS_BYTES];
  LAS char* L = (LAS char*)smem;
  const unsigned lds0 = (unsigned)(uintptr_t)smem;
  const int tid = threadIdx.x, lane = tid & 63;
  const int w = __builtin_amdgcn_readfirstlane(tid >> 6);
  const int b = blockIdx.x, xcd = b & 7, bi = b >> 3;
  const int l31 = lane & 31, hh = lane >> 5, l15 = lane & 15, fg = lane >> 4;
  const __amdgpu_buffer_rsrc_t rws = __builtin_amdgcn_make_buffer_rsrc(p.ws, 0, 0x7fffffff, 0x00020000);
  const int aP = 4 * (xcd >> 1) + (bi >> 3), aj = 8 * (xcd & 1) + (bi & 7);
  const int hd = 4 * xcd + (bi >> 3), pp = bi & 7;
  const int R = 4 * xcd + (bi >> 3), cc = bi & 7;
  const int gs = xcd * 14 + bi;
  const bool is_gu = bi < 14 && gs < 108;
  const int dn = xcd * 5 + (bi - 14);
  const bool is_dn = bi >= 14 && bi < 19;
  STAMP_INIT();
  u32x2 tag2;
  auto read_epoch = [&]() {
    u64 e;
    asm volatile("s_load_dwordx2 %0, %1, 0x0\n\ts_waitcnt lgkmcnt(0)" : "=s"(e) : "s"(p.ws) : "memory");
    e += 1;
    tag2 = (u32x2){(unsigned)e, (unsigned)(e >> 32)};
  };

  auto rows_issue = [&](int lw, u32x4& ra0, u32x4& ra1, u32x4& rb0, u32x4& rb1) {
    if (lw == 0) ST128(((u32x4){0, 0, 0, 0}), WS_STATS + (R * 8 + cc) * 1024 + lane * 16);
    if (lw == 1 && is_gu && lane < 32) ST128(((u32x4){0, 0, 0, 0}), WS_ACT + (((gs >> 1) * 4 + 2 * (gs & 1) + (lane >> 4)) * 8 + (lane & 7)) * 32 + ((lane >> 3) & 1) * 16);
    const char* rb = (const char*)p.x + (size_t)(64 * R + 8 * cc + 2 * lw) * 1280;
    ra0 = *(const u32x4*)(rb + lane * 16); ra1 = *(const u32x4*)(rb + 1024 + l15 * 16);
    rb0 = *(const u32x4*)(rb + 1280 + lane * 16); rb1 = *(const u32x4*)(rb + 1280 + 1024 + l15 * 16);
    FENCE();
  };
  auto rows_finish = [&](int lw, const u32x4& ra0, const u32x4& ra1, const u32x4& rb0, const u32x4& rb1) {
    float mean[2], rs[2];
#pragma unroll
    for (int r = 0; r < 2; ++r) {
      const u32x4 c0 = r ? rb0 : ra0, c1 = r ? rb1 : ra1;
      float s0 = 0.f, s1 = 0.f;
#pragma unroll
      for (int e = 0; e < 4; ++e) { s0 = dot2(c0[e], 0x3f803f80u, s0); s1 = dot2(c1[e], 0x3f803f80u, s1); }
      mean[r] = wave_fsum(s0 + (lane < 16 ? s1 : 0.f)) * (1.f / D);
      const f32x2 mm = {mean[r], mean[r]};
      f32x2 q0 = {0.f, 0.f}, q1 = {0.f, 0.f};
#pragma unroll
      for (int e = 0; e < 4; ++e) { const f32x2 d0 = (f32x2){bf2f(c0[e]), bfhi(c0[e])} - mm, d1 = (f32x2){bf2f(c1[e]), bfhi(c1[e])} - mm; q0 += d0 * d0; q1 += d1 * d1; }
      const float var = wave_fsum((q0[0] + q0[1]) + (lane < 16 ? q1[0] + q1[1] : 0.f)) * (1.f / D);
      rs[r] = __builtin_amdgcn_rcpf(rbf(__builtin_amdgcn_sqrtf(rbf(rbf(var) + 1e-5f))));
    }
    WAITV(0);
    if (lane < 2) ST128(((u32x4){__float_as_uint(rbf(lane ? mean[1] : mean[0])), __float_as_uint(lane ? rs[1] : rs[0]), tag2[0], tag2[1]}), WS_ST1 + (unsigned)(64 * R + 8 * cc + 2 * lw + lane) * 16);
  };

  auto lds_ctl_init = [&]() {
    if (tid == 0) { *(LAS unsigned*)(L + L_F1) = 0; *(LAS unsigned*)(L + L_C1) = 0; }
    STAMP_ZERO();
  };
  if (aj < 15 && w >= 4) {
    const int lw = w - 4;
    const unsigned xsw = (unsigned)(4 * (lw & 1) + (lane >> 4));
    const unsigned voff_x = (unsigned)(lane >> 3) * 1280u + (((unsigned)(lane & 7) ^ xsw) << 4);
    const unsigned ch = (unsigned)(lane & 15) ^ (((unsigned)(lane >> 4) << 2) | (unsigned)lw);
    const unsigned voff_w = (unsigned)(lane >> 4) * 3840u + ch * 16u;
    const char* xb = (const char*)p.x + (size_t)(128 * aP + 8 * lw) * 1280;
    const char* wb = (const char*)p.wqkv + (size_t)(4 * lw) * 3840 + 256 * aj;
    const unsigned lbase = lds0 + lw * 1024;
    const int rot = ROT;
    auto issue = [&](int s) {
      const unsigned sl = lbase + (s & 3) * L_SLOT;
      int ss = s + rot; if (ss >= 10) ss -= 10;
#pragma unroll
      for (int i = 0; i < 4; ++i) {
        GL_X(voff_x, xb + (size_t)i * 32 * 1280 + ss * 128, sl + i * 4096);
        GL_W(voff_w, wb + ((size_t)ss * 64 + i * 16) * 3840, sl + L_W + i * 4096);
      }
    };
    BARRIER();
    issue(0); issue(1); issue(2);
    read_epoch();
    WAITV(16); BARRIER();
    PSTAMP(1);
#pragma unroll 1
    for (int s = 0; s <= 5; ++s) { issue(s + 3); WAITV(16); BARRIER(); }
    issue(9);
    const unsigned so = WS_ST1 + (unsigned)(128 * aP + 32 * lw + l31) * 16, co = WS_CS1 + (unsigned)((aj * 16 + fg) * 64 + 16 * lw + l15) * 16;
    u32x4 gs1 = LD128(so), gc0 = LD128(co), gc1 = LD128(co + 4096), gc2 = LD128(co + 8192), gc3 = LD128(co + 12288);
    WAITV(21); BARRIER();
    WAITV(13); BARRIER();
    WAITV(5); BARRIER();
    unsigned spins = 0;
    for (;;) {
      const bool ok = gs1[2] == tag2[0] && gs1[3] == tag2[1] && gc0[2] == tag2[0] && gc0[3] == tag2[1] && gc1[2] == tag2[0] && gc1[3] == tag2[1] && gc2[2] == tag2[0] && gc2[3] == tag2[1] && gc3[2] == tag2[0] && gc3[3] == tag2[1];
      if (__all(ok) || ++spins > SPIN_CAP) break;
      FENCE();
      gs1 = LD128(so); gc0 = LD128(co); gc1 = LD128(co + 4096); gc2 = LD128(co + 8192); gc3 = LD128(co + 12288);
    }
    if (lane < 32) *(LAS u32x2*)(L + L_RS + (32 * lw + l31) * 8) = (u32x2){gs1[0], gs1[1]};
    {
      const float c0 = swap_add(swap16_add((__uint_as_float(gc0[0]) + __uint_as_float(gc1[0])) + (__uint_as_float(gc2[0]) + __uint_as_float(gc3[0]))));
      const float c1 = swap_add(swap16_add((__uint_as_float(gc0[1]) + __uint_as_float(gc1[1])) + (__uint_as_float(gc2[1]) + __uint_as_float(gc3[1]))));
      if (lane < 16) *(LAS f32x2*)(L + L_CS + (32 * lw + 2 * lane) * 4) = (f32x2){c0, c1};
    }
    WAITL0(); BARRIER();
    BARRIER();
  } else if (w < 4) {
    u32x4 ra0, ra1, rb0, rb1;
    rows_issue(w, ra0, ra1, rb0, rb1);
    if (aj < 15) {
      BARRIER();
      read_epoch();
      rows_finish(w, ra0, ra1, rb0, rb1);
      PSTAMP(7);
      const int wm = w >> 1, wn = w & 1;
      unsigned xa[4], wa[4];
#pragma unroll
      for (int ks = 0; ks < 4; ++ks) xa[ks] = (unsigned)(64 * wm + l31) * 128u + ((((unsigned)(2 * ks + hh)) ^ (((unsigned)l31 >> 1) & 7u)) << 4);
      {
        const unsigned blk = (lane >> 4) & 1, q = (lane & 15) >> 2, pq = lane & 3;
#pragma unroll
        for (int j = 0; j < 2; ++j)
#pragma unroll
          for (int t = 0; t < 2; ++t) {
            const unsigned row = 8 * hh + 4 * t + q, chn = 4 * (2 * wn + (j ^ wm)) + 2 * blk + (pq >> 1), m = (q << 2) | (unsigned)(2 * hh + t);
            wa[j * 2 + t] = L_W + 256u * row + 16u * (chn ^ m) + 8u * (pq & 1);
          }
      }
      f32x16 a00 = {}, a01 = {}, a10 = {}, a11 = {};
      bf16x8 xf0[2], wf0[2], xf1[2], wf1[2];

      unsigned xb[4], wb[4];
#define SETSLOT(so) do { _Pragma("unroll") for (int k_ = 0; k_ < 4; ++k_) { xb[k_] = xa[k_] + (so); wb[k_] = wa[k_] + (so); } } while (0)
#define RD(XF, WF, ks) do { \
        XF[0] = *(const LAS bf16x8*)(L + xb[ks]); WF[0] = cat8(trd(L + wb[0] + (ks) * 4096), trd(L + wb[1] + (ks) * 4096)); \
        WF[1] = cat8(trd(L + wb[2] + (ks) * 4096), trd(L + wb[3] + (ks) * 4096)); XF[1] = *(const LAS bf16x8*)(L + xb[ks] + 4096); } while (0)
#define MM(XF, WF) do { \
        a00 = MFMA32(WF[0], XF[0], a00); a01 = MFMA32(WF[1], XF[0], a01); \
        a10 = MFMA32(WF[0], XF[1], a10); a11 = MFMA32(WF[1], XF[1], a11); } while (0)
#define PINALL() asm volatile("" : "+v"(a00), "+v"(a01), "+v"(a10), "+v"(a11))
#define ILV() do { __builtin_amdgcn_sched_group_barrier(0x8, 1, 0); __builtin_amdgcn_sched_group_barrier(0x100, 3, 0); \
        __builtin_amdgcn_sched_group_barrier(0x8, 1, 0); __builtin_amdgcn_sched_group_barrier(0x100, 3, 0); __builtin_amdgcn_sched_group_barrier(0x8, 2, 0); } while (0)
#define STEP(XN, WN, ksn, XC, WC) do { RD(XN, WN, ksn); MM(XC, WC); ILV(); PINALL(); SBAR(); } while (0)
      BARRIER();
      SETSLOT(0);
      RD(xf0, wf0, 0);
      SBAR();
#pragma unroll 1
      for (int s = 0; s < 9; ++s) {
        STEP(xf1, wf1, 1, xf0, wf0);
        STEP(xf0, wf0, 2, xf1, wf1);
        STEP(xf1, wf1, 3, xf0, wf0);
        SETSLOT(((s + 1) & 3) * L_SLOT);
        WAITL0(); BARRIER();
        STEP(xf0, wf0, 0, xf1, wf1);
      }
      STEP(xf1, wf1, 1, xf0, wf0);
      STEP(xf0, wf0, 2, xf1, wf1);
      STEP(xf1, wf1, 3, xf0, wf0);
      MM(xf1, wf1); PINALL();
      PSTAMP(2);
      WAITL0(); BARRIER();
      PSTAMP(6);
      const float qs = aj < 5 ? 0.22360679774997896f * 1.4426950408889634f : 1.f;
      f32x2 rs0 = *(const LAS f32x2*)(L + L_RS + (64 * wm + l31) * 8), rs1 = *(const LAS f32x2*)(L + L_RS + (64 * wm + 32 + l31) * 8);
      const float sc0 = rs0[1] * qs, nm0 = -rs0[0] * sc0, sc1 = rs1[1] * qs, nm1 = -rs1[0] * sc1;
#pragma unroll
      for (int j = 0; j < 2; ++j) {
        const int fj = 64 * wn + 32 * (j ^ wm) + 4 * hh;
        const unsigned go = WS_QKV + (unsigned)(32 * aj + (fj >> 2)) * 16384u + (unsigned)(128 * aP + 64 * wm + l31) * 8u;
#pragma unroll
        for (int rq = 0; rq < 4; ++rq) {
          const f32x4 cs = *(const LAS f32x4*)(L + L_CS + (fj + 8 * rq) * 4);
          const f32x16& a0 = j == 0 ? a00 : a01; const f32x16& a1 = j == 0 ? a10 : a11;
          const f32x2 c01 = {cs[0], cs[1]}, c23 = {cs[2], cs[3]};
          const f32x2 u0 = (f32x2){a0[rq * 4], a0[rq * 4 + 1]} * sc0 + c01 * nm0, u1 = (f32x2){a0[rq * 4 + 2], a0[rq * 4 + 3]} * sc0 + c23 * nm0;
          const f32x2 u2 = (f32x2){a1[rq * 4], a1[rq * 4 + 1]} * sc1 + c01 * nm1, u3 = (f32x2){a1[rq * 4 + 2], a1[rq * 4 + 3]} * sc1 + c23 * nm1;
          ST64(((u32x2){pk2(u0[0], u0[1]), pk2(u1[0], u1[1])}), go + 2 * rq * 16384);
          ST64(((u32x2){pk2(u2[0], u2[1]), pk2(u3[0], u3[1])}), go + 2 * rq * 16384 + 256);
        }
      }
      PSTAMP(4);
      lds_ctl_init();
      WAITV(0); WAITL0();
      PSTAMP(5);
      BARRIER();
    } else {
      read_epoch();
      rows_finish(w, ra0, ra1, rb0, rb1);
    }
  } else {
    read_epoch();
  }
  if (aj >= 15) {
    const int chn = min(32 * w + l31, 239);
    const char* wp0 = (const char*)p.wqkv + (size_t)(40 * aP + 20 * hh) * 3840 + chn * 16;
    u32x4 wv[20];
#pragma unroll
    for (int i = 0; i < 20; ++i) wv[i] = *(const u32x4*)(wp0 + (size_t)i * 3840);
    SBAR();
    f32x2 c2[4] = {{0.f, 0.f}, {0.f, 0.f}, {0.f, 0.f}, {0.f, 0.f}};
#pragma unroll
    for (int i = 0; i < 20; ++i)
#pragma unroll
      for (int e = 0; e < 4; ++e) c2[e] += (f32x2){bf2f(wv[i][e]), bfhi(wv[i][e])};
#pragma unroll
    for (int e = 0; e < 4; ++e) c2[e] = (f32x2){swap_add(c2[e][0]), swap_add(c2[e][1])};
    if (lane < 32 && 32 * w + l31 < 240) {
#pragma unroll
      for (int e = 0; e < 4; ++e) ST128(((u32x4){__float_as_uint(c2[e][0]), __float_as_uint(c2[e][1]), tag2[0], tag2[1]}), WS_CS1 + (unsigned)(((chn >> 4) * 16 + aP) * 64 + 4 * (chn & 15) + e) * 16);
    }
    lds_ctl_init(); WAITL0(); BARRIER();
  }
  const unsigned tag32 = tag2[0] | 1u;
  if (aj < 15 && tid == 0) ST64(tag2, WS_FLAGA + (aP * 16 + aj) * 128);
  PSTAMP(3);
  STAMP_PA();

  const int nt = w & 3, kh = w >> 2;
  const int tok = 64 * R + 16 * nt + l15;
  u32x2 xr[5], wrv[5];
  unsigned wr5[5];
  {
    const int t = w;
    *(LAS u32x2*)(L + L_V + 5 * PL + tid * 8) = (u32x2){(pp > 0 || tid >= 256) ? 0x00003f80u : 0u, 0u};
    if (pp == 0) {
#pragma unroll
      for (int i = 0; i < 3; ++i) { const int n = tid + 512 * i; if (n < 1280) *(LAS u32x4*)(L + (n >> 7) * PL + (n & 127) * 16) = (u32x4){0, 0, 0, 0}; }
    }
    if (kh == 0) {
#pragma unroll
      for (int ft = 0; ft < 5; ++ft) {
        xr[ft] = *(const u32x2*)(p.x + (size_t)tok * D + 80 * cc + 16 * ft + 4 * fg);
        wrv[ft] = *(const u32x2*)(p.wr + 80 * cc + 16 * ft + 4 * fg);
      }
    }
    if (bi == 31) {
#pragma unroll
      for (int i = 0; i < 5; ++i) wr5[i] = ((const unsigned*)p.wr)[lane + 64 * i];
    }
    f32x16 cdg, can;
    {
      auto diag_ok = [](int r) { unsigned long long m = 0; for (int l = 0; l < 64; ++l) if ((r & 3) + 8 * (r >> 2) + 4 * (l >> 5) <= (l & 31)) m |= 1ull << l; return m; };
#pragma unroll
      for (int r = 0; r < 16; ++r) { const bool ok = __builtin_amdgcn_inverse_ballot_w64(diag_ok(r)); cdg[r] = ok ? 0.f : -INFINITY; can[r] = ok ? -INFINITY : 0.f; }
      asm volatile("" : "+v"(cdg), "+v"(can));
    }
    auto lds_wait = [&](int off, unsigned v) { unsigned sp = 0; while (__hip_atomic_load((LAS unsigned*)(L + off), __ATOMIC_RELAXED, __HIP_MEMORY_SCOPE_WORKGROUP) < v && ++sp < (SPIN_CAP << 4)) __builtin_amdgcn_s_sleep(0); FENCE(); };
    WAITL0();
    if (kh == 0) {
      if (lane == 0) __hip_atomic_fetch_add((LAS unsigned*)(L + L_C1), 1u, __ATOMIC_RELAXED, __HIP_MEMORY_SCOPE_WORKGROUP);
      lds_wait(L_F1, 1);
    } else {
      if (w == 4) {
        const int sec = lane < 4 ? 0 : (lane < 12 ? 1 : 2);
        const int k = lane < 4 ? lane : (lane < 12 ? lane - 4 : lane - 12);
        const int col = sec * 640 + 20 * hd + ((k & 1) ? 19 : 0);
        const int Pn = sec == 0 ? 2 * pp + (k >> 1) : 2 * pp - 2 + (k >> 1);
        const bool need = lane < 20 && Pn >= 0;
        const unsigned fo = WS_FLAGA + (unsigned)((need ? Pn : 2 * pp) * 16 + (col >> 7)) * 128;
        unsigned spins = 0;
        for (;;) {
          FENCE();
          const u32x2 f = LD64(fo);
          if (__all(!need || (f[0] == tag2[0] && f[1] == tag2[1])) || ++spins > SPIN_CAP) break;
          __builtin_amdgcn_s_sleep(1);
        }
        if (lane == 0) __hip_atomic_store((LAS unsigned*)(L + L_F1), 1u, __ATOMIC_RELAXED, __HIP_MEMORY_SCOPE_WORKGROUP);
        STAMP(4);
      } else lds_wait(L_F1, 1);
      const char* qb = (const char*)p.ws + WS_QKV;
#pragma unroll
      for (int i = 0; i < 10; ++i) {
        const int pc = nt + 4 * i, pl = pc >> 2, qt = pc & 3;
        const int tok0 = 256 * pp - 256 + 128 * qt;
        if (tok0 >= 0) glds16c(lane * 16, qb + (size_t)((pl < 5 ? 160 + 5 * hd + pl : 320 + 5 * hd + pl - 5)) * 16384 + tok0 * 8, lds0 + pl * PL + qt * 1024);
      }
    }
    bf16x8 qf0; bf16x4 qf1;
    {
      const unsigned qo = WS_QKV + (unsigned)(5 * hd) * 16384u + (unsigned)(256 * pp + 32 * t + l31) * 8u;
      const u32x2 a = LD64(qo + (2 * hh) * 16384), c = LD64(qo + (2 * hh + 1) * 16384);
      u32x2 z = LD64(qo + 4 * 16384);
      asm volatile("s_waitcnt vmcnt(0)" : "+v"(z) :: "memory");
      if (hh) z = (u32x2){0, 0};
      qf0 = __builtin_bit_cast(bf16x8, (u32x4){a[0], a[1], c[0], c[1]});
      qf1 = __builtin_bit_cast(bf16x4, z);
    }
    if (kh == 1 && lane == 0) __hip_atomic_fetch_add((LAS unsigned*)(L + L_C1), 1u, __ATOMIC_RELAXED, __HIP_MEMORY_SCOPE_WORKGROUP);
    lds_wait(L_C1, 8);
    if (w == 4) STAMP(5);
    unsigned wov[5];
#pragma unroll
    for (int j = 0; j < 5; ++j) { const int n = 64 * j + lane, rho = (n * 6554) >> 16, chn = n - 10 * rho; wov[j] = (unsigned)(8 * ((rho >> 2) & 3) + 4 * (rho >> 4) + (rho & 3)) * 1280u + chn * 16u; }
    const char* wob = (const char*)p.wo + 160 * cc + (size_t)nt * 32 * 1280;
    const unsigned wol = lds0 + L_WO + nt * 5120;
#define WOP(i) GL_WO(wov[(i) % 5], wob + (size_t)((i) / 5) * 4 * 32 * 1280, wol + ((i) / 5) * 4 * 5120 + ((i) % 5) * 1024)
#define WOP3(i) do { if (kh == 0) { WOP(i); WOP((i) + 1); WOP((i) + 2); } SBAR(); } while (0)
    if (w == 0) STAMP(28);
    const LAS char* kb = L + L_K + l31 * 8 + (2 * hh) * PL;
    const LAS char* kb4 = L + (hh ? L_V + 5 * PL : L_K + 4 * PL) + l31 * 8;
    {
      const u32x2 k0 = *(const LAS u32x2*)(kb + (t + 8) * 256), k1 = *(const LAS u32x2*)(kb + (t + 8) * 256 + PL), k2 = *(const LAS u32x2*)(kb4 + (t + 8) * 256);
      const u32x4 q0 = __builtin_bit_cast(u32x4, qf0); u32x2 q1 = __builtin_bit_cast(u32x2, qf1);
      float d = dot2(q0[0], k0[0], 0.f); d = dot2(q0[1], k0[1], d); d = dot2(q0[2], k1[0], d); d = dot2(q0[3], k1[1], d);
      d = dot2(q1[0], k2[0], d); d = dot2(q1[1], k2[1], d);
      const unsigned nm = pk2(-swap_add(d), 0.f);
      if (hh) q1[0] = nm;
      qf1 = __builtin_bit_cast(bf16x4, q1);
    }
    const LAS char* vb = L + L_V + min(4 * ((lane >> 4) & 1) + (lane & 3), 5) * PL + (4 * hh + ((lane & 15) >> 2)) * 8;
    f32x16 o = (f32x16){};
    {
      f32x16 sa, sb;
      const f32x16 cz = {};
      bf16x4 ka, kc, kd;
      bf16x8 vq = {};
      u32x4 pq = {0, 0, 0, 0};
#define KRD(kt) do { ka = *(const LAS bf16x4*)(kb + (kt) * 256); kc = *(const LAS bf16x4*)(kb + (kt) * 256 + PL); kd = *(const LAS bf16x4*)(kb4 + (kt) * 256); } while (0)
#define EX2(S, i) pk2(__builtin_amdgcn_exp2f(S[i]), __builtin_amdgcn_exp2f(S[(i) + 1]))
#define GRP(nds, nv) do { __builtin_amdgcn_sched_group_barrier(0x8, 1, 0); if (nds) __builtin_amdgcn_sched_group_barrier(0x100, nds, 0); __builtin_amdgcn_sched_group_barrier(0x2, nv, 0); } while (0)
#define HALF(SC, SN, C, ktv, ktk) do { \
        u32x4 p0_; const LAS char* vp_ = vb + (ktv) * 256; \
        SN = MFMA32(cat8(ka, kc), qf0, C); \
        const bf16x8 v0_ = cat8(trd(vp_), trd(vp_ + 64)), v1_ = cat8(trd(vp_ + 128), trd(vp_ + 192)); \
        p0_[0] = EX2(SC, 0); p0_[1] = EX2(SC, 2); \
        GRP(4, 6); asm volatile("" : "+v"(SN), "+v"(p0_[0]), "+v"(p0_[1])); SBAR(); \
        o = MFMA32(vq, __builtin_bit_cast(bf16x8, pq), o); \
        p0_[2] = EX2(SC, 4); p0_[3] = EX2(SC, 6); \
        GRP(0, 6); asm volatile("" : "+v"(o), "+v"(p0_[2]), "+v"(p0_[3])); SBAR(); \
        SN = MFMA32K8(kd, qf1, SN); \
        KRD(ktk); \
        pq[0] = EX2(SC, 8); pq[1] = EX2(SC, 10); \
        GRP(3, 6); asm volatile("" : "+v"(SN), "+v"(pq[0]), "+v"(pq[1])); SBAR(); \
        o = MFMA32(v0_, __builtin_bit_cast(bf16x8, p0_), o); \
        pq[2] = EX2(SC, 12); pq[3] = EX2(SC, 14); vq = v1_; \
        GRP(0, 6); asm volatile("" : "+v"(o), "+v"(pq[2]), "+v"(pq[3])); SBAR(); } while (0)
      KRD(t + 8);
      sa = MFMA32(cat8(ka, kc), qf0, cdg); sa = MFMA32K8(kd, qf1, sa);
      KRD(t + 7);
      asm volatile("" : "+v"(sa)); SBAR();
      WOP3(0);
      HALF(sa, sb, cz, t + 8, t + 6);
      WOP3(3);
      HALF(sb, sa, cz, t + 7, t + 5);
      WOP3(6);
      HALF(sa, sb, cz, t + 6, t + 4);
      WOP3(9);
      HALF(sb, sa, cz, t + 5, t + 3);
      WOP3(12);
      HALF(sa, sb, cz, t + 4, t + 2);
      WOP3(15);
      HALF(sb, sa, cz, t + 3, t + 1);
      WOP3(18);
      HALF(sa, sb, cz, t + 2, t);
      if (kh == 0) { WOP(21); WOP(22); WOP(23); WOP(24); }
      SBAR();
      HALF(sb, sa, can, t + 1, t);
      {
        const LAS char* vp_ = vb + t * 256;
        const bf16x8 v0_ = cat8(trd(vp_), trd(vp_ + 64)), v1_ = cat8(trd(vp_ + 128), trd(vp_ + 192));
        o = MFMA32(vq, __builtin_bit_cast(bf16x8, pq), o);
        u32x4 p0_, p1_;
#pragma unroll
        for (int e = 0; e < 4; ++e) p0_[e] = EX2(sa, 2 * e);
        o = MFMA32(v0_, __builtin_bit_cast(bf16x8, p0_), o);
#pragma unroll
        for (int e = 0; e < 4; ++e) p1_[e] = EX2(sa, 8 + 2 * e);
        o = MFMA32(v1_, __builtin_bit_cast(bf16x8, p1_), o);
      }
    }
    float lsum;
    { auto r = __builtin_amdgcn_permlane32_swap(__float_as_uint(o[8]), __float_as_uint(o[8]), false, false); lsum = __uint_as_float(r[1]); }
    if (__builtin_expect(__any(!(lsum < 1.0995116e12f)), 0)) {
      const int lo = pp >= 1 ? t : 8;
      float mx = -INFINITY;
#pragma unroll 1
      for (int kt = t + 8; kt >= lo; --kt) {
        const bf16x4 ka = *(const LAS bf16x4*)(kb + kt * 256), kc = *(const LAS bf16x4*)(kb + kt * 256 + PL), kd = *(const LAS bf16x4*)(kb4 + kt * 256);
        f32x16 s = MFMA32(cat8(ka, kc), qf0, (f32x16){});
        s = MFMA32K8(kd, qf1, s);
#pragma unroll
        for (int r = 0; r < 16; ++r) { const int kr = (r & 3) + 8 * (r >> 2) + 4 * hh; const bool ok = kt == t + 8 ? kr <= l31 : (kt == t ? kr > l31 : true); mx = fmaxf(mx, ok ? s[r] : -INFINITY); }
      }
      { auto r = __builtin_amdgcn_permlane32_swap(__float_as_uint(mx), __float_as_uint(mx), false, false); mx = fmaxf(__uint_as_float(r[0]), __uint_as_float(r[1])); }
      o = (f32x16){};
#pragma unroll 1
      for (int kt = t + 8; kt >= lo; --kt) {
        const bf16x4 ka = *(const LAS bf16x4*)(kb + kt * 256), kc = *(const LAS bf16x4*)(kb + kt * 256 + PL), kd = *(const LAS bf16x4*)(kb4 + kt * 256);
        const LAS char* vp_ = vb + kt * 256;
        const bf16x8 v0_ = cat8(trd(vp_), trd(vp_ + 64)), v1_ = cat8(trd(vp_ + 128), trd(vp_ + 192));
        f32x16 s = MFMA32(cat8(ka, kc), qf0, (f32x16){});
        s = MFMA32K8(kd, qf1, s);
        u32x4 p0_, p1_;
#pragma unroll
        for (int r = 0; r < 16; ++r) { const int kr = (r & 3) + 8 * (r >> 2) + 4 * hh; const bool ok = kt == t + 8 ? kr <= l31 : (kt == t ? kr > l31 : true); s[r] = ok ? __builtin_amdgcn_exp2f(s[r] - mx) : 0.f; }
#pragma unroll
        for (int e = 0; e < 4; ++e) { p0_[e] = pk2(s[2 * e], s[2 * e + 1]); p1_[e] = pk2(s[8 + 2 * e], s[8 + 2 * e + 1]); }
        o = MFMA32(v0_, __builtin_bit_cast(bf16x8, p0_), o);
        o = MFMA32(v1_, __builtin_bit_cast(bf16x8, p1_), o);
      }
      auto r = __builtin_amdgcn_permlane32_swap(__float_as_uint(o[8]), __float_as_uint(o[8]), false, false); lsum = __uint_as_float(r[1]);
    }
    if (w == 0) STAMP(6);
    STAMP(32 + w);
    {
      const float inv = 1.f / lsum;
      unsigned A0 = pk2(o[0] * inv, o[1] * inv), A1 = pk2(o[2] * inv, o[3] * inv), B0 = pk2(o[4] * inv, o[5] * inv), B1 = pk2(o[6] * inv, o[7] * inv);
      unsigned C0 = pk2(o[8] * inv, o[9] * inv), C1 = pk2(o[10] * inv, o[11] * inv);
      const int tt = pp * 16 + 2 * t + (l31 >> 4);
      const unsigned base = WS_O + (unsigned)tt * 80 * 256 + (l31 & 15) * 16 + (unsigned)(5 * (hd >> 1)) * 256;
      if ((hd & 1) == 0) {
        auto s0 = __builtin_amdgcn_permlane32_swap(A0, B0, false, false), s1 = __builtin_amdgcn_permlane32_swap(A1, B1, false, false);
        ST128(((u32x4){s0[0], s1[0], s0[1], s1[1]}), base + hh * 256);
        if (hh == 0) ST64(((u32x2){C0, C1}), base + 2 * 256);
      } else {
        auto u0 = __builtin_amdgcn_permlane32_swap(A0, C0, false, false), u1 = __builtin_amdgcn_permlane32_swap(A1, C1, false, false);
        const u32x4 v = hh ? (u32x4){B0, B1, u0[0], u1[0]} : (u32x4){u0[1], u1[1], B0, B1};
        ST128(v, base + (3 + hh) * 256);
        if (hh == 0) ST64(((u32x2){A0, A1}), base + 2 * 256 + 8);
      }
      WAITV(0);
      if (lane == 0) ST64(tag2, WS_FLAGO + (((pp * 8 + t) * 2 + (hd >> 4)) * 16 + (hd & 15)) * 8);
    }
  }
  if (w == 0) STAMP(7);
  STAMP(40 + w);

  BARRIER();
  if (w == 0) STAMP(8);

  f32x4 acc[5];
#pragma unroll
  for (int ft = 0; ft < 5; ++ft) acc[ft] = (f32x4){0, 0, 0, 0};
  {
    const LAS char* wp = L + L_WO + (unsigned)(kh * 320 + 4 * fg + ((lane & 15) >> 2)) * 160 + (lane & 3) * 8;
    bf16x8 wa[3][5];
#pragma unroll
    for (int s = 0; s < 3; ++s)
#pragma unroll
      for (int ft = 0; ft < 5; ++ft) wa[s][ft] = cat8(trd(wp + s * 5120 + ft * 32), trd(wp + s * 5120 + 2560 + ft * 32));
    const int tg = 4 * R + nt;
    const unsigned fo = WS_FLAGO + ((((tg >> 4) * 8 + ((tg >> 1) & 7)) * 2 + kh) * 16 + l15) * 8;
    unsigned spins = 0;
    for (;;) {
      FENCE();
      const u32x2 f = LD64(fo);
      if (__all(f[0] == tag2[0] && f[1] == tag2[1]) || ++spins > SPIN_CAP) break;
      __builtin_amdgcn_s_sleep(1);
    }
    FENCE();
    if (w == 0) STAMP(9);
    STAMP(48 + w);
    u32x4 of[10];
#pragma unroll
    for (int s = 0; s < 10; ++s) of[s] = LD128(WS_O + (unsigned)(tg * 80 + 4 * (10 * kh + s)) * 256 + lane * 16);
    SBAR();
#pragma unroll
    for (int s = 0; s < 10; ++s) {
      const bf16x8 bfr = __builtin_bit_cast(bf16x8, of[s]);
#pragma unroll
      for (int ft = 0; ft < 5; ++ft) acc[ft] = MFMA16(s < 3 ? wa[s < 3 ? s : 0][ft] : cat8(trd(wp + s * 5120 + ft * 32), trd(wp + s * 5120 + 2560 + ft * 32)), bfr, acc[ft]);
    }
  }
  STAMP(56 + w);
  if (kh == 1) {
#pragma unroll
    for (int ft = 0; ft < 5; ++ft) *(LAS f32x4*)(L + L_RED + ((nt * 5 + ft) * 64 + lane) * 16) = acc[ft];
  }
  WAITL0(); BARRIER();
  if (w == 0) STAMP(10);
  if (kh == 0) {
    float s1 = 0.f, s2 = 0.f, s3 = 0.f;
#pragma unroll
    for (int ft = 0; ft < 5; ++ft) {
      const f32x4 o2 = *(const LAS f32x4*)(L + L_RED + ((nt * 5 + ft) * 64 + lane) * 16);
      const unsigned a01 = pk2(acc[ft][0] + o2[0], acc[ft][1] + o2[1]), a23 = pk2(acc[ft][2] + o2[2], acc[ft][3] + o2[3]);
      const unsigned h01 = pk2(bf2f(xr[ft][0]) + bf2f(a01), bfhi(xr[ft][0]) + bfhi(a01)), h23 = pk2(bf2f(xr[ft][1]) + bf2f(a23), bfhi(xr[ft][1]) + bfhi(a23));
      *(LAS u32x2*)(L + L_HST + nt * 2560 + l15 * 160 + ft * 32 + fg * 8) = (u32x2){h01, h23};
      s1 = dot2(h01, 0x3f803f80u, s1); s1 = dot2(h23, 0x3f803f80u, s1);
      s2 = dot2(h01, h01, s2); s2 = dot2(h23, h23, s2);
      s3 = dot2(h01, wrv[ft][0], s3); s3 = dot2(h23, wrv[ft][1], s3);
    }
    WAITL0();
#pragma unroll
    for (int i = 0; i < 3; ++i) {
      const int n = lane + 64 * i;
      if (n < 160) ST128(*(const LAS u32x4*)(L + L_HST + nt * 2560 + n * 16), WS_H + (unsigned)((R * 8 + cc) * 64 + 16 * nt) * 160 + n * 16);
    }
    s1 = swap_add(swap16_add(s1)); s2 = swap_add(swap16_add(s2)); s3 = swap_add(swap16_add(s3));
    WAITV(0);
    if (lane < 16) ST128(((u32x4){__float_as_uint(s1), __float_as_uint(s2), __float_as_uint(s3), tag32}), WS_STATS + ((R * 8 + cc) * 64 + 16 * nt + l15) * 16);
    if (w == 0) STAMP(11);
    BARRIER();
  } else {
    BARRIER();
    const int rl = lane >> 1, grow = 8 * ((rl >> 2) & 3) + 4 * (rl >> 4) + (rl & 3);
    if (is_gu) {
      const unsigned vo = (unsigned)grow * (F * 2) + (lane & 1) * 16;
#pragma unroll 1
      for (int pc = nt; pc < 40; pc += 4) { const int m = pc >= 20, s = pc - 20 * m; GL_FW(vo, (const char*)(m ? p.wu : p.wg) + (size_t)s * 32 * F * 2 + gs * 32, lds0 + L_FW + pc * 1024); }
    } else if (is_dn) {
      const unsigned vo = (unsigned)grow * (D * 2) + (lane & 1) * 16;
#pragma unroll 1
      for (int pc = nt; pc < 54; pc += 4) GL_FW(vo, (const char*)p.wd + (size_t)pc * 32 * D * 2 + dn * 32, lds0 + L_FW + pc * 1024);
    }
  }
  auto rank64 = [&](unsigned k) {
    const u32x4 oa = *(const LAS u32x4*)(L + L_KEY + w * 32), ob = *(const LAS u32x4*)(L + L_KEY + w * 32 + 16);
    unsigned c = 0;
#pragma unroll
    for (int e = 0; e < 4; ++e) c += (oa[e] > k) + (ob[e] > k);
    *(LAS unsigned char*)(L + L_PART + lane * 8 + w) = (unsigned char)c;
    WAITL0(); BARRIER();
    const u32x2 pr = *(const LAS u32x2*)(L + L_PART + lane * 8);
    return __builtin_amdgcn_sad_u8(pr[0], 0u, __builtin_amdgcn_sad_u8(pr[1], 0u, 0u));
  };
  if (bi == 31) {
    float swr = 0.f;
#pragma unroll
    for (int i = 0; i < 5; ++i) swr = dot2(wr5[i], 0x3f803f80u, swr);
    swr = wave_fsum(swr);
    const int tl = 32 * w + l31;
    const unsigned so = WS_STATS + (unsigned)(((4 * xcd + (tl >> 6)) * 8 + 4 * hh) * 64 + (tl & 63)) * 16;
    float t1, t2, t3;
    unsigned spins = 0;
    for (;;) {
      FENCE();
      const u32x4 g0 = LD128(so), g1 = LD128(so + 1024), g2 = LD128(so + 2048), g3 = LD128(so + 3072);
      t1 = (__uint_as_float(g0[0]) + __uint_as_float(g1[0])) + (__uint_as_float(g2[0]) + __uint_as_float(g3[0]));
      t2 = (__uint_as_float(g0[1]) + __uint_as_float(g1[1])) + (__uint_as_float(g2[1]) + __uint_as_float(g3[1]));
      t3 = (__uint_as_float(g0[2]) + __uint_as_float(g1[2])) + (__uint_as_float(g2[2]) + __uint_as_float(g3[2]));
      if (__all(g0[3] == tag32 && g1[3] == tag32 && g2[3] == tag32 && g3[3] == tag32) || ++spins > SPIN_CAP) break;
    }
    t1 = swap_add(t1); t2 = swap_add(t2); t3 = swap_add(t3);
    const float mean = t1 * (1.f / D);
    const float var = fmaxf(t2 * (1.f / D) - mean * mean, 0.f);
    const float mb = rbf(mean);
    const float sd = rbf(__builtin_amdgcn_sqrtf(rbf(rbf(var) + 1e-5f)));
    const unsigned rb = pk2((t3 - mb * swr) * __builtin_amdgcn_rcpf(sd), 0.f) & 0xffffu;
    const unsigned key = ((rb ^ ((rb & 0x8000u) ? 0xffffu : 0x8000u)) << 16) | (unsigned)(2047 - (256 * xcd + tl));
    const unsigned ms = (__float_as_uint(mb) & 0xffff0000u) | (__float_as_uint(sd) >> 16);
    if (w == 0) STAMP(12);
    if (lane < 32) *(LAS unsigned*)(L + L_K32 + tl * 4) = key;
    WAITL0();
    unsigned c = 0;
#pragma unroll
    for (int i = 0; i < 4; ++i) {
      const u32x4 o4 = *(const LAS u32x4*)(L + L_K32 + (32 * w + 16 * hh + 4 * i) * 4);
#pragma unroll
      for (int e = 0; e < 4; ++e) c += o4[e] > key;
    }
    { auto r = __builtin_amdgcn_permlane32_swap(c, c, false, false); c = r[0] + r[1]; }
    if (lane < 32 && c < 8) { *(LAS unsigned*)(L + L_KEY + (8 * w + c) * 4) = key; *(LAS unsigned*)(L + L_KEY + 256 + (8 * w + c) * 4) = ms; }
    WAITL0(); BARRIER();
    const unsigned k = *(const LAS unsigned*)(L + L_KEY + lane * 4), m2 = *(const LAS unsigned*)(L + L_KEY + 256 + lane * 4);
    const unsigned rk = rank64(k);
    if (w == 0) { if (rk < 8) ST128(((u32x4){k, tag2[0], m2, tag2[1]}), WS_CAND + xcd * 128 + rk * 16); STAMP(26); }
    BARRIER();
  }
  if (w == 0) {
    u32x4 a;
    unsigned spins = 0;
    for (;;) {
      FENCE();
      a = LD128(WS_CAND + lane * 16);
      if (__all(a[1] == tag2[0] && a[3] == tag2[1]) || ++spins > SPIN_CAP) break;
      if (is_gu || is_dn) __builtin_amdgcn_s_sleep(1); else __builtin_amdgcn_s_sleep(8);
    }
    STAMP(13);
    *(LAS unsigned*)(L + L_KEY + lane * 4) = a[0]; *(LAS unsigned*)(L + L_KEY + 256 + lane * 4) = a[2];
    if (b == 0 && lane == 0) ST64(tag2, WS_EPOCH);
  }
  const int hc0 = (lane * 6554) >> 16, hc1 = ((64 + l15) * 6554) >> 16;
  const unsigned ho0 = (unsigned)hc0 * (64 * 160) + (lane - 10 * hc0) * 16, ho1 = (unsigned)hc1 * (64 * 160) + (64 + l15 - 10 * hc1) * 16;
  if (kh == 1) WAITV(0);
  WAITL0(); BARRIER();
  {
    const unsigned k = *(const LAS unsigned*)(L + L_KEY + lane * 4), m2 = *(const LAS unsigned*)(L + L_KEY + 256 + lane * 4);
    const unsigned rk = rank64(k);
    if (rk < 8) *(LAS u32x2*)(L + L_SEL + rk * 8) = (u32x2){k, m2};
    WAITL0();
  }
  if (w == 0) STAMP(14);

  auto h_row = [&](int row, u32x4& h0, u32x4& h1) {
    const unsigned rb = WS_H + (unsigned)((row >> 6) * 8 * 64 + (row & 63)) * 160;
    h0 = LD128(rb + ho0); h1 = LD128(rb + ho1);
  };
  auto ln8 = [&](const u32x4& h, float m, float iv) { u32x4 n;
#pragma unroll
    for (int e = 0; e < 4; ++e) { const unsigned d = pk2(bf2f(h[e]) - m, bfhi(h[e]) - m); n[e] = pk2(bf2f(d) * iv, bfhi(d) * iv); } return n; };
  auto out_row = [&]() {
    const int row = 64 * R + 8 * cc + w;
    const u32x2 sk = *(const LAS u32x2*)(L + L_SEL + (lane & 7) * 8);
    if (!__any(2047 - (int)(sk[0] & 0x7ff) == row)) {
      u32x4 h0, h1; h_row(row, h0, h1);
      const u32x4 g = LD128(WS_STATS + (unsigned)((R * 8 + (lane & 7)) * 64 + 8 * cc + w) * 16);
      float t1 = __uint_as_float(g[0]), t2 = __uint_as_float(g[1]);
      t1 = dppadd<0x141>(t1); t2 = dppadd<0x141>(t2);
      t1 = dppadd<0xb1>(t1); t2 = dppadd<0xb1>(t2);
      t1 = dppadd<0x4e>(t1); t2 = dppadd<0x4e>(t2);
      const float mean = t1 * (1.f / D);
      const float m = rbf(mean), iv = __builtin_amdgcn_rcpf(rbf(__builtin_amdgcn_sqrtf(rbf(rbf(fmaxf(t2 * (1.f / D) - mean * mean, 0.f)) + 1e-5f))));
      const u32x4 n0 = ln8(h0, m, iv), n1 = ln8(h1, m, iv);
      u32x4 r0, r1;
#pragma unroll
      for (int e = 0; e < 4; ++e) { r0[e] = pk2(bf2f(h0[e]) + bf2f(n0[e]), bfhi(h0[e]) + bfhi(n0[e])); r1[e] = pk2(bf2f(h1[e]) + bf2f(n1[e]), bfhi(h1[e]) + bfhi(n1[e])); }
      *(u32x4*)(p.out + (size_t)row * D + lane * 8) = r0;
      if (lane < 16) *(u32x4*)(p.out + (size_t)row * D + 512 + lane * 8) = r1;
    }
  };
  if (is_gu) {
    {
      const u32x2 sk = *(const LAS u32x2*)(L + L_SEL + w * 8);
      u32x4 h0, h1; h_row(2047 - (int)(sk[0] & 0x7ff), h0, h1);
      const float m = __uint_as_float(sk[1] & 0xffff0000u), iv = __builtin_amdgcn_rcpf(__uint_as_float(sk[1] << 16));
      *(LAS u32x4*)(L + L_XS + w * 1296 + lane * 16) = ln8(h0, m, iv);
      if (lane < 16) *(LAS u32x4*)(L + L_XS + w * 1296 + 1024 + lane * 16) = ln8(h1, m, iv);
    }
    WAITL0(); BARRIER();
    if (w == 0) STAMP(15);
    {
      const int m = w & 1, kq = w >> 1;
      f32x4 a4 = {0, 0, 0, 0}, b4 = {0, 0, 0, 0};
#pragma unroll
      for (int i = 0; i < 5; ++i) {
        const int s = 5 * kq + i;
        const LAS char* ap = L + L_FW + (m * 20 + s) * 1024 + lane * 8;
        const bf16x8 af = cat8(trd(ap), trd(ap + 512));
        const bf16x8 bfv = *(const LAS bf16x8*)(L + L_XS + (lane & 7) * 1296 + s * 64 + fg * 16);
        if (i & 1) b4 = MFMA16(af, bfv, b4); else a4 = MFMA16(af, bfv, a4);
      }
      *(LAS f32x4*)(L + L_FRED + (w * 64 + lane) * 16) = a4 + b4;
    }
    WAITL0(); BARRIER();
    if (w == 0) {
      f32x4 g4 = {0, 0, 0, 0}, u4 = {0, 0, 0, 0};
#pragma unroll
      for (int q = 0; q < 4; ++q) { g4 += *(const LAS f32x4*)(L + L_FRED + ((2 * q) * 64 + lane) * 16); u4 += *(const LAS f32x4*)(L + L_FRED + ((2 * q + 1) * 64 + lane) * 16); }
      float a[4];
#pragma unroll
      for (int e = 0; e < 4; ++e) { const float g = rbf(g4[e]), u = rbf(u4[e]); const float sg = rbf(g * __builtin_amdgcn_rcpf(1.f + __builtin_amdgcn_exp2f(g * -1.4426950408889634f))); a[e] = sg * u; }
      if (l15 < 8) ST128(((u32x4){pk2(a[0], a[1]), tag32, pk2(a[2], a[3]), tag32}), WS_ACT + (((gs >> 1) * 4 + 2 * (gs & 1) + (fg >> 1)) * 8 + l15) * 32 + (fg & 1) * 16);
      STAMP(16);
    }
    out_row();
  } else if (is_dn) {
    const u32x2 sk = *(const LAS u32x2*)(L + L_SEL + (lane & 7) * 8);
    const int row = 2047 - (int)(sk[0] & 0x7ff);
    u32x2 hv = {0, 0}, nv = {0, 0};
    float sig = 0.f;
    if (w == 0) {
      const int col = 16 * dn + 4 * fg, c = col / 80;
      hv = LD64(WS_H + (unsigned)(((row >> 6) * 8 + c) * 64 + (row & 63)) * 160 + (col - 80 * c) * 2);
      const unsigned rk = sk[0] >> 16, rbits = (rk & 0x8000u) ? (rk ^ 0x8000u) : (rk ^ 0xffffu);
      sig = rbf(__builtin_amdgcn_rcpf(1.f + __builtin_amdgcn_exp2f(bf2f(rbits) * -1.4426950408889634f)));
      const float m = __uint_as_float(sk[1] & 0xffff0000u), iv = __builtin_amdgcn_rcpf(__uint_as_float(sk[1] << 16));
#pragma unroll
      for (int e = 0; e < 2; ++e) { const unsigned d2 = pk2(bf2f(hv[e]) - m, bfhi(hv[e]) - m); nv[e] = pk2(bf2f(d2) * iv, bfhi(d2) * iv); }
      asm volatile("" : "+v"(nv), "+v"(hv), "+v"(sig));
    } else out_row();
    f32x4 a4 = {0, 0, 0, 0}, b4 = {0, 0, 0, 0};
    {
      bf16x8 af[7];
#pragma unroll
      for (int i = 0; i < 7; ++i) { const LAS char* ap = L + L_FW + min(w + 8 * i, 53) * 1024 + lane * 8; af[i] = cat8(trd(ap), trd(ap + 512)); }
      u32x4 g0[7];
      unsigned spins = 0;
      for (;;) {
        FENCE();
        bool ok = true;
#pragma unroll
        for (int i = 0; i < 7; ++i) g0[i] = LD128(WS_ACT + ((min(w + 8 * i, 53) * 4 + fg) * 8 + (lane & 7)) * 32 + ((lane >> 3) & 1) * 16);
#pragma unroll
        for (int i = 0; i < 7; ++i) ok = ok && g0[i][1] == tag32 && g0[i][3] == tag32;
        if (__all(ok) || ++spins > SPIN_CAP) break;
      }
      if (w == 0) STAMP(17);
#pragma unroll
      for (int i = 0; i < 7; ++i) {
        const unsigned y0 = (unsigned)__builtin_amdgcn_update_dpp(0, (int)g0[i][0], 0x128, 0xf, 0xf, false), y1 = (unsigned)__builtin_amdgcn_update_dpp(0, (int)g0[i][2], 0x128, 0xf, 0xf, false);
        const bf16x8 bv = __builtin_bit_cast(bf16x8, (u32x4){g0[i][0], g0[i][2], y0, y1});
        if (i < 6 || w < 6) { if (i & 1) b4 = MFMA16(af[i], bv, b4); else a4 = MFMA16(af[i], bv, a4); }
      }
    }
    *(LAS f32x4*)(L + L_FRED + (w * 64 + lane) * 16) = a4 + b4;
    WAITL0(); BARRIER();
    if (w == 0) {
      f32x4 y = {0, 0, 0, 0};
#pragma unroll
      for (int q = 0; q < 8; ++q) y += *(const LAS f32x4*)(L + L_FRED + (q * 64 + lane) * 16);
      unsigned r2[2];
#pragma unroll
      for (int e = 0; e < 2; ++e) {
        const unsigned y2 = pk2(rbf(y[2 * e]) * sig, rbf(y[2 * e + 1]) * sig);
        const unsigned t2 = pk2(bf2f(nv[e]) + bf2f(y2), bfhi(nv[e]) + bfhi(y2));
        r2[e] = pk2(bf2f(hv[e]) + bf2f(t2), bfhi(hv[e]) + bfhi(t2));
      }
      if (l15 < 8) *(u32x2*)(p.out + (size_t)row * D + 16 * dn + 4 * fg) = (u32x2){r2[0], r2[1]};
      STAMP(18);
      out_row();
    }
  } else {
    out_row();
  }
  if (w == 0) STAMP(19);
  STAMP_FLUSH();
}

extern "C" void kernel_launch(void* const* d_in, const int* in_sizes, int n_in, void* d_out, int out_size, void* d_ws, size_t ws_size, hipStream_t stream) {
  Params p;
  p.x = (const u16*)d_in[0]; p.wqkv = (const u16*)d_in[1]; p.wo = (const u16*)d_in[2]; p.wr = (const u16*)d_in[4];
  p.wg = (const u16*)d_in[5]; p.wu = (const u16*)d_in[6]; p.wd = (const u16*)d_in[7]; p.out = (u16*)d_out; p.ws = (unsigned char*)d_ws + ((ws_size - (16u << 20)) & ~(size_t)4095);
  fused_kernel<<<256, 512, 0, stream>>>(p);
}
```

```cpp
#include <hip/hip_runtime.h>
#include <stdint.h>

typedef unsigned short u16;
typedef short bf16x8 __attribute__((ext_vector_type(8)));
typedef short bf16x4 __attribute__((ext_vector_type(4)));
typedef __bf16 bf2 __attribute__((ext_vector_type(2)));
typedef float f32x16 __attribute__((ext_vector_type(16)));
typedef float f32x4 __attribute__((ext_vector_type(4)));
typedef float f32x2 __attribute__((ext_vector_type(2)));
typedef unsigned u32x4 __attribute__((ext_vector_type(4)));
typedef unsigned u32x2 __attribute__((ext_vector_type(2)));
typedef unsigned long long u64;
#define LAS __attribute__((address_space(3)))
#define SBAR() __builtin_amdgcn_sched_barrier(0)
#define BARRIER() asm volatile("s_barrier" ::: "memory")
#define WAITL0() asm volatile("s_waitcnt lgkmcnt(0)" ::: "memory")
#define WAITV(n) asm volatile("s_waitcnt vmcnt(" #n ")" ::: "memory")
#define FENCE() asm volatile("" ::: "memory")

constexpr int S = 2048, D = 640, F = 1728;
constexpr int SC1 = 16;

constexpr unsigned WS_EPOCH = 0;
constexpr unsigned WS_FLAGA = 4096;
constexpr unsigned WS_FLAGO = WS_FLAGA + 32768;
constexpr unsigned WS_CAND = WS_FLAGO + 16384;
constexpr unsigned WS_HDONE = WS_CAND + 4096;
constexpr unsigned WS_ACT = WS_HDONE + 4096;
constexpr unsigned WS_ST1 = 131072;
constexpr unsigned WS_CS1 = WS_ST1 + 32768;
constexpr unsigned WS_STATS = 524288;
constexpr unsigned WS_QKV = 1048576;
constexpr unsigned WS_O = WS_QKV + 480 * 16384;
constexpr unsigned WS_H = WS_O + S * D * 2;
#define WS_TAIL 1
#define STAMP_OFF (15u << 20)
#define NSTAMP 64

constexpr int L_SLOT = 32768, L_W = 16384;
constexpr int PL = 4128;
constexpr int L_K = 0, L_V = 5 * PL;
constexpr int L_WO = 45568;
constexpr int L_RED = 0;
constexpr int L_HST = 20480;
constexpr int L_FW = L_WO;
constexpr int L_XS = 0;
constexpr int L_FRED = 12288;
constexpr int L_MISC = L_WO + 640 * 160;
constexpr int L_RS = 0;
constexpr int L_CS = 1024;
constexpr int L_CNT = L_MISC + 3600;
constexpr int L_F1 = L_MISC + 3604, L_C1 = L_MISC + 3608;
constexpr int L_PART = L_MISC + 2048;
constexpr int L_K32 = L_MISC + 2560;
constexpr int L_HD = L_MISC + 3712;
constexpr int L_SEL = L_MISC + 3968;
constexpr int L_STAMP = L_MISC + 4096;
constexpr int L_KEY = L_MISC + 4608 + 256;
constexpr int LDS_BYTES = 163840;
static_assert(L_V + 6 * PL <= L_WO && L_KEY + 2048 <= LDS_BYTES, "lds map");

struct Params { const u16* x; const u16* wqkv; const u16* wo; const u16* wr; const u16* wg; const u16* wu; const u16* wd; u16* out; unsigned char* ws; };

__device__ __forceinline__ float bf2f(unsigned v) { return __uint_as_float(v << 16); }
__device__ __forceinline__ float bfhi(unsigned v) { return __uint_as_float(v & 0xffff0000u); }
__device__ __forceinline__ unsigned pk2(float a, float b) { f32x2 f = {a, b}; return __builtin_bit_cast(unsigned, __builtin_convertvector(f, bf2)); }
__device__ __forceinline__ float rbf(float f) { return __uint_as_float(pk2(f, 0.f) << 16); }
__device__ __forceinline__ float dot2(unsigned a, unsigned b, float c) { return __builtin_amdgcn_fdot2_f32_bf16(__builtin_bit_cast(bf2, a), __builtin_bit_cast(bf2, b), c, false); }
__device__ __forceinline__ float swap_add(float v) { auto r = __builtin_amdgcn_permlane32_swap(__float_as_uint(v), __float_as_uint(v), false, false); return __uint_as_float(r[0]) + __uint_as_float(r[1]); }
__device__ __forceinline__ float swap16_add(float v) { auto r = __builtin_amdgcn_permlane16_swap(__float_as_uint(v), __float_as_uint(v), false, false); return __uint_as_float(r[0]) + __uint_as_float(r[1]); }
template <int CTRL> __device__ __forceinline__ float dppadd(float v) { return v + __int_as_float(__builtin_amdgcn_update_dpp(0, __float_as_int(v), CTRL, 0xf, 0xf, false)); }
__device__ __forceinline__ float wave_fsum(float v) { v = dppadd<0x128>(v); v = dppadd<0x124>(v); v = dppadd<0x122>(v); v = dppadd<0x121>(v); return swap_add(swap16_add(v)); }
template <int CTRL> __device__ __forceinline__ unsigned dppmax(unsigned v) { return max(v, (unsigned)__builtin_amdgcn_update_dpp(0, (int)v, CTRL, 0xf, 0xf, false)); }
__device__ __forceinline__ unsigned wave_umax(unsigned v) {
  v = dppmax<0x128>(v); v = dppmax<0x124>(v); v = dppmax<0x122>(v); v = dppmax<0x121>(v);
  auto r = __builtin_amdgcn_permlane16_swap(v, v, false, false); v = max(r[0], r[1]);
  auto q = __builtin_amdgcn_permlane32_swap(v, v, false, false); return max(q[0], q[1]);
}
__device__ __forceinline__ void glds16(unsigned voff, const void* sbase, unsigned lds) {
  asm volatile("s_mov_b32 m0, %2\n\ts_nop 0\n\tglobal_load_lds_dwordx4 %0, %1" ::"v"(voff), "s"(sbase), "s"(lds) : "memory");
}
__device__ __forceinline__ void glds16c(unsigned voff, const void* sbase, unsigned lds) {
  asm volatile("s_mov_b32 m0, %2\n\ts_nop 0\n\tglobal_load_lds_dwordx4 %0, %1 sc1" ::"v"(voff), "s"(sbase), "s"(lds) : "memory");
}
__device__ __forceinline__ void touch64(unsigned voff, const void* sbase, unsigned lds) {
  asm volatile("s_mov_b32 m0, %2\n\ts_nop 0\n\tglobal_load_lds_dword %0, %1" ::"v"(voff), "s"(sbase), "s"(lds) : "memory");
}
__device__ __forceinline__ void glds16n(unsigned voff, const void* sbase, unsigned lds) {
  asm volatile("s_mov_b32 m0, %2\n\ts_nop 0\n\tglobal_load_lds_dwordx4 %0, %1 nt" ::"v"(voff), "s"(sbase), "s"(lds) : "memory");
}
#define GL_FW glds16
#define GL_WO glds16
#define GL_X glds16
#define GL_W glds16
__device__ __forceinline__ void touch_ptr(const void* g, unsigned lds) {
  asm volatile("s_mov_b32 m0, %1\n\ts_nop 0\n\tglobal_load_lds_dword %0, off" ::"v"(g), "s"(lds) : "memory");
}
__device__ __forceinline__ bf16x4 trd(const LAS char* p) { return __builtin_amdgcn_ds_read_tr16_b64_v4i16((LAS bf16x4*)p); }
__device__ __forceinline__ bf16x8 cat8(bf16x4 a, bf16x4 b) { return (bf16x8){a[0], a[1], a[2], a[3], b[0], b[1], b[2], b[3]}; }
#define MFMA32(a, b, c) __builtin_amdgcn_mfma_f32_32x32x16_bf16(a, b, c, 0, 0, 0)
#define MFMA32K8(a, b, c) __builtin_amdgcn_mfma_f32_32x32x8bf16_1k(a, b, c, 0, 0, 0)
#define MFMA16(a, b, c) __builtin_amdgcn_mfma_f32_16x16x32_bf16(a, b, c, 0, 0, 0)
#define LD128(off) __builtin_amdgcn_raw_buffer_load_b128(rws, (off), 0, SC1)
#define LD64(off) __builtin_amdgcn_raw_buffer_load_b64(rws, (off), 0, SC1)
#define ST128(v, off) __builtin_amdgcn_raw_buffer_store_b128((v), rws, (off), 0, SC1)
#define ST64(v, off) __builtin_amdgcn_raw_buffer_store_b64((v), rws, (off), 0, SC1)

#define STAMP(k) do {} while (0)
#define STAMP_INIT() do {} while (0)
#define PSTAMP(i) do {} while (0)
#define STAMP_ZERO() do {} while (0)
#define STAMP_PA() do {} while (0)
#define STAMP_FLUSH() do {} while (0)

#define ROT (((bi >> 3) * 5 / 2) % 10)
constexpr unsigned SPIN_CAP = 1u << 17;

__global__ __launch_bounds__(512, 2) void fused_kernel(Params p) {
  __shared__ __attribute__((aligned(16))) char smem[LDS_BYTES];
  LAS char* L = (LAS char*)smem;
  const unsigned lds0 = (unsigned)(uintptr_t)smem;
  const int tid = threadIdx.x, lane = tid & 63;
  const int w = __builtin_amdgcn_readfirstlane(tid >> 6);
  const int b = blockIdx.x, xcd = b & 7, bi = b >> 3;
  const int l31 = lane & 31, hh = lane >> 5, l15 = lane & 15, fg = lane >> 4;
  const __amdgpu_buffer_rsrc_t rws = __builtin_amdgcn_make_buffer_rsrc(p.ws, 0, 0x7fffffff, 0x00020000);
  const int aP = 4 * (xcd >> 1) + (bi >> 3), aj = 8 * (xcd & 1) + (bi & 7);
  const int hd = 4 * xcd + (bi >> 3), pp = bi & 7;
  const int R = 4 * xcd + (bi >> 3), cc = bi & 7;
  const int gs = xcd * 14 + bi;
  const bool is_gu = bi < 14 && gs < 108;
  const int dn = xcd * 5 + (bi - 14);
  const bool is_dn = bi >= 14 && bi < 19;
  STAMP_INIT();
  u32x2 tag2;
  auto read_epoch = [&]() {
    u64 e;
    asm volatile("s_load_dwordx2 %0, %1, 0x0\n\ts_waitcnt lgkmcnt(0)" : "=s"(e) : "s"(p.ws) : "memory");
    e += 1;
    tag2 = (u32x2){(unsigned)e, (unsigned)(e >> 32)};
  };

  auto rows_issue = [&](int lw, u32x4& ra0, u32x4& ra1, u32x4& rb0, u32x4& rb1) {
    if (lw == 0) ST128(((u32x4){0, 0, 0, 0}), WS_STATS + (R * 8 + cc) * 1024 + lane * 16);
    if (lw == 1 && is_gu && lane < 32) ST128(((u32x4){0, 0, 0, 0}), WS_ACT + (((gs >> 1) * 4 + 2 * (gs & 1) + (lane >> 4)) * 8 + (lane & 7)) * 32 + ((lane >> 3) & 1) * 16);
    const char* rb = (const char*)p.x + (size_t)(64 * R + 8 * cc + 2 * lw) * 1280;
    ra0 = *(const u32x4*)(rb + lane * 16); ra1 = *(const u32x4*)(rb + 1024 + l15 * 16);
    rb0 = *(const u32x4*)(rb + 1280 + lane * 16); rb1 = *(const u32x4*)(rb + 1280 + 1024 + l15 * 16);
    FENCE();
  };
  auto rows_finish = [&](int lw, const u32x4& ra0, const u32x4& ra1, const u32x4& rb0, const u32x4& rb1) {
    float mean[2], rs[2];
#pragma unroll
    for (int r = 0; r < 2; ++r) {
      const u32x4 c0 = r ? rb0 : ra0, c1 = r ? rb1 : ra1;
      float s0 = 0.f, s1 = 0.f;
#pragma unroll
      for (int e = 0; e < 4; ++e) { s0 = dot2(c0[e], 0x3f803f80u, s0); s1 = dot2(c1[e], 0x3f803f80u, s1); }
      mean[r] = wave_fsum(s0 + (lane < 16 ? s1 : 0.f)) * (1.f / D);
      const f32x2 mm = {mean[r], mean[r]};
      f32x2 q0 = {0.f, 0.f}, q1 = {0.f, 0.f};
#pragma unroll
      for (int e = 0; e < 4; ++e) { const f32x2 d0 = (f32x2){bf2f(c0[e]), bfhi(c0[e])} - mm, d1 = (f32x2){bf2f(c1[e]), bfhi(c1[e])} - mm; q0 += d0 * d0; q1 += d1 * d1; }
      const float var = wave_fsum((q0[0] + q0[1]) + (lane < 16 ? q1[0] + q1[1] : 0.f)) * (1.f / D);
      rs[r] = __builtin_amdgcn_rcpf(rbf(__builtin_amdgcn_sqrtf(rbf(rbf(var) + 1e-5f))));
    }
    WAITV(0);
    if (lane < 2) ST128(((u32x4){__float_as_uint(rbf(lane ? mean[1] : mean[0])), __float_as_uint(lane ? rs[1] : rs[0]), tag2[0], tag2[1]}), WS_ST1 + (unsigned)(64 * R + 8 * cc + 2 * lw + lane) * 16);
  };

  auto lds_ctl_init = [&]() {
    if (tid == 0) { *(LAS unsigned*)(L + L_F1) = 0; *(LAS unsigned*)(L + L_C1) = 0; }
    STAMP_ZERO();
  };
  if (aj < 15 && w >= 4) {
    const int lw = w - 4;
    const unsigned xsw = (unsigned)(4 * (lw & 1) + (lane >> 4));
    const unsigned voff_x = (unsigned)(lane >> 3) * 1280u + (((unsigned)(lane & 7) ^ xsw) << 4);
    const unsigned ch = (unsigned)(lane & 15) ^ (((unsigned)(lane >> 4) << 2) | (unsigned)lw);
    const unsigned voff_w = (unsigned)(lane >> 4) * 3840u + ch * 16u;
    const char* xb = (const char*)p.x + (size_t)(128 * aP + 8 * lw) * 1280;
    const char* wb = (const char*)p.wqkv + (size_t)(4 * lw) * 3840 + 256 * aj;
    const unsigned lbase = lds0 + lw * 1024;
    const int rot = ROT;
    auto issue = [&](int s) {
      const unsigned sl = lbase + (s & 3) * L_SLOT;
      int ss = s + rot; if (ss >= 10) ss -= 10;
#pragma unroll
      for (int i = 0; i < 4; ++i) {
        GL_X(voff_x, xb + (size_t)i * 32 * 1280 + ss * 128, sl + i * 4096);
        GL_W(voff_w, wb + ((size_t)ss * 64 + i * 16) * 3840, sl + L_W + i * 4096);
      }
    };
    BARRIER();
    issue(0); issue(1); issue(2);
    read_epoch();
    WAITV(16); BARRIER();
    PSTAMP(1);
#pragma unroll 1
    for (int s = 0; s <= 5; ++s) { issue(s + 3); WAITV(16); BARRIER(); }
    issue(9);
    const unsigned so = WS_ST1 + (unsigned)(128 * aP + 32 * lw + l31) * 16, co = WS_CS1 + (unsigned)((aj * 16 + fg) * 64 + 16 * lw + l15) * 16;
    u32x4 gs1 = LD128(so), gc0 = LD128(co), gc1 = LD128(co + 4096), gc2 = LD128(co + 8192), gc3 = LD128(co + 12288);
    WAITV(21); BARRIER();
    WAITV(13); BARRIER();
    WAITV(5); BARRIER();
    unsigned spins = 0;
    for (;;) {
      const bool ok = gs1[2] == tag2[0] && gs1[3] == tag2[1] && gc0[2] == tag2[0] && gc0[3] == tag2[1] && gc1[2] == tag2[0] && gc1[3] == tag2[1] && gc2[2] == tag2[0] && gc2[3] == tag2[1] && gc3[2] == tag2[0] && gc3[3] == tag2[1];
      if (__all(ok) || ++spins > SPIN_CAP) break;
      FENCE();
      gs1 = LD128(so); gc0 = LD128(co); gc1 = LD128(co + 4096); gc2 = LD128(co + 8192); gc3 = LD128(co + 12288);
    }
    if (lane < 32) *(LAS u32x2*)(L + L_RS + (32 * lw + l31) * 8) = (u32x2){gs1[0], gs1[1]};
    {
      const float c0 = swap_add(swap16_add((__uint_as_float(gc0[0]) + __uint_as_float(gc1[0])) + (__uint_as_float(gc2[0]) + __uint_as_float(gc3[0]))));
      const float c1 = swap_add(swap16_add((__uint_as_float(gc0[1]) + __uint_as_float(gc1[1])) + (__uint_as_float(gc2[1]) + __uint_as_float(gc3[1]))));
      if (lane < 16) *(LAS f32x2*)(L + L_CS + (32 * lw + 2 * lane) * 4) = (f32x2){c0, c1};
    }
    WAITL0(); BARRIER();
    BARRIER();
  } else if (w < 4) {
    u32x4 ra0, ra1, rb0, rb1;
    rows_issue(w, ra0, ra1, rb0, rb1);
    if (aj < 15) {
      BARRIER();
      read_epoch();
      rows_finish(w, ra0, ra1, rb0, rb1);
      PSTAMP(7);
      const int wm = w >> 1, wn = w & 1;
      unsigned xa[4], wa[4];
#pragma unroll
      for (int ks = 0; ks < 4; ++ks) xa[ks] = (unsigned)(64 * wm + l31) * 128u + ((((unsigned)(2 * ks + hh)) ^ (((unsigned)l31 >> 1) & 7u)) << 4);
      {
        const unsigned blk = (lane >> 4) & 1, q = (lane & 15) >> 2, pq = lane & 3;
#pragma unroll
        for (int j = 0; j < 2; ++j)
#pragma unroll
          for (int t = 0; t < 2; ++t) {
            const unsigned row = 8 * hh + 4 * t + q, chn = 4 * (2 * wn + (j ^ wm)) + 2 * blk + (pq >> 1), m = (q << 2) | (unsigned)(2 * hh + t);
            wa[j * 2 + t] = L_W + 256u * row + 16u * (chn ^ m) + 8u * (pq & 1);
          }
      }
      f32x16 a00 = {}, a01 = {}, a10 = {}, a11 = {};
      bf16x8 xf0[2], wf0[2], xf1[2], wf1[2];

      unsigned xb[4], wb[4];
#define SETSLOT(so) do { _Pragma("unroll") for (int k_ = 0; k_ < 4; ++k_) { xb[k_] = xa[k_] + (so); wb[k_] = wa[k_] + (so); } } while (0)
#define RD(XF, WF, ks) do { \
        XF[0] = *(const LAS bf16x8*)(L + xb[ks]); WF[0] = cat8(trd(L + wb[0] + (ks) * 4096), trd(L + wb[1] + (ks) * 4096)); \
        WF[1] = cat8(trd(L + wb[2] + (ks) * 4096), trd(L + wb[3] + (ks) * 4096)); XF[1] = *(const LAS bf16x8*)(L + xb[ks] + 4096); } while (0)
#define MM(XF, WF) do { \
        a00 = MFMA32(WF[0], XF[0], a00); a01 = MFMA32(WF[1], XF[0], a01); \
        a10 = MFMA32(WF[0], XF[1], a10); a11 = MFMA32(WF[1], XF[1], a11); } while (0)
#define PINALL() asm volatile("" : "+v"(a00), "+v"(a01), "+v"(a10), "+v"(a11))
#define ILV() do { __builtin_amdgcn_sched_group_barrier(0x8, 1, 0); __builtin_amdgcn_sched_group_barrier(0x100, 3, 0); \
        __builtin_amdgcn_sched_group_barrier(0x8, 1, 0); __builtin_amdgcn_sched_group_barrier(0x100, 3, 0); __builtin_amdgcn_sched_group_barrier(0x8, 2, 0); } while (0)
#define STEP(XN, WN, ksn, XC, WC) do { RD(XN, WN, ksn); MM(XC, WC); ILV(); PINALL(); SBAR(); } while (0)
      BARRIER();
      SETSLOT(0);
      RD(xf0, wf0, 0);
      SBAR();
#pragma unroll 1
      for (int s = 0; s < 9; ++s) {
        STEP(xf1, wf1, 1, xf0, wf0);
        STEP(xf0, wf0, 2, xf1, wf1);
        STEP(xf1, wf1, 3, xf0, wf0);
        SETSLOT(((s + 1) & 3) * L_SLOT);
        WAITL0(); BARRIER();
        STEP(xf0, wf0, 0, xf1, wf1);
      }
      STEP(xf1, wf1, 1, xf0, wf0);
      STEP(xf0, wf0, 2, xf1, wf1);
      STEP(xf1, wf1, 3, xf0, wf0);
      MM(xf1, wf1); PINALL();
      PSTAMP(2);
      WAITL0(); BARRIER();
      PSTAMP(6);
      const float qs = aj < 5 ? 0.22360679774997896f * 1.4426950408889634f : 1.f;
      f32x2 rs0 = *(const LAS f32x2*)(L + L_RS + (64 * wm + l31) * 8), rs1 = *(const LAS f32x2*)(L + L_RS + (64 * wm + 32 + l31) * 8);
      const float sc0 = rs0[1] * qs, nm0 = -rs0[0] * sc0, sc1 = rs1[1] * qs, nm1 = -rs1[0] * sc1;
#pragma unroll
      for (int j = 0; j < 2; ++j) {
        const int fj = 64 * wn + 32 * (j ^ wm) + 4 * hh;
        const unsigned go = WS_QKV + (unsigned)(32 * aj + (fj >> 2)) * 16384u + (unsigned)(128 * aP + 64 * wm + l31) * 8u;
#pragma unroll
        for (int rq = 0; rq < 4; ++rq) {
          const f32x4 cs = *(const LAS f32x4*)(L + L_CS + (fj + 8 * rq) * 4);
          const f32x16& a0 = j == 0 ? a00 : a01; const f32x16& a1 = j == 0 ? a10 : a11;
          const f32x2 c01 = {cs[0], cs[1]}, c23 = {cs[2], cs[3]};
          const f32x2 u0 = (f32x2){a0[rq * 4], a0[rq * 4 + 1]} * sc0 + c01 * nm0, u1 = (f32x2){a0[rq * 4 + 2], a0[rq * 4 + 3]} * sc0 + c23 * nm0;
          const f32x2 u2 = (f32x2){a1[rq * 4], a1[rq * 4 + 1]} * sc1 + c01 * nm1, u3 = (f32x2){a1[rq * 4 + 2], a1[rq * 4 + 3]} * sc1 + c23 * nm1;
          ST64(((u32x2){pk2(u0[0], u0[1]), pk2(u1[0], u1[1])}), go + 2 * rq * 16384);
          ST64(((u32x2){pk2(u2[0], u2[1]), pk2(u3[0], u3[1])}), go + 2 * rq * 16384 + 256);
        }
      }
      PSTAMP(4);
      lds_ctl_init();
      WAITV(0); WAITL0();
      PSTAMP(5);
      BARRIER();
    } else {
      read_epoch();
      rows_finish(w, ra0, ra1, rb0, rb1);
    }
  } else {
    read_epoch();
  }
  if (aj >= 15) {
    const int chn = min(32 * w + l31, 239);
    const char* wp0 = (const char*)p.wqkv + (size_t)(40 * aP + 20 * hh) * 3840 + chn * 16;
    u32x4 wv[20];
#pragma unroll
    for (int i = 0; i < 20; ++i) wv[i] = *(const u32x4*)(wp0 + (size_t)i * 3840);
    SBAR();
    f32x2 c2[4] = {{0.f, 0.f}, {0.f, 0.f}, {0.f, 0.f}, {0.f, 0.f}};
#pragma unroll
    for (int i = 0; i < 20; ++i)
#pragma unroll
      for (int e = 0; e < 4; ++e) c2[e] += (f32x2){bf2f(wv[i][e]), bfhi(wv[i][e])};
#pragma unroll
    for (int e = 0; e < 4; ++e) c2[e] = (f32x2){swap_add(c2[e][0]), swap_add(c2[e][1])};
    if (lane < 32 && 32 * w + l31 < 240) {
#pragma unroll
      for (int e = 0; e < 4; ++e) ST128(((u32x4){__float_as_uint(c2[e][0]), __float_as_uint(c2[e][1]), tag2[0], tag2[1]}), WS_CS1 + (unsigned)(((chn >> 4) * 16 + aP) * 64 + 4 * (chn & 15) + e) * 16);
    }
    lds_ctl_init(); WAITL0(); BARRIER();
  }
  const unsigned tag32 = tag2[0] | 1u;
  if (aj < 15 && tid == 0) ST64(tag2, WS_FLAGA + (aP * 16 + aj) * 128);
  PSTAMP(3);
  STAMP_PA();

  const int nt = w & 3, kh = w >> 2;
  const int tok = 64 * R + 16 * nt + l15;
  u32x2 xr[5], wrv[5];
  unsigned wr5[5];
  {
    const int t = w;
    *(LAS u32x2*)(L + L_V + 5 * PL + tid * 8) = (u32x2){(pp > 0 || tid >= 256) ? 0x00003f80u : 0u, 0u};
    if (pp == 0) {
#pragma unroll
      for (int i = 0; i < 3; ++i) { const int n = tid + 512 * i; if (n < 1280) *(LAS u32x4*)(L + (n >> 7) * PL + (n & 127) * 16) = (u32x4){0, 0, 0, 0}; }
    }
    if (kh == 0) {
#pragma unroll
      for (int ft = 0; ft < 5; ++ft) {
        xr[ft] = *(const u32x2*)(p.x + (size_t)tok * D + 80 * cc + 16 * ft + 4 * fg);
        wrv[ft] = *(const u32x2*)(p.wr + 80 * cc + 16 * ft + 4 * fg);
      }
    }
    if (bi == 31) {
#pragma unroll
      for (int i = 0; i < 5; ++i) wr5[i] = ((const unsigned*)p.wr)[lane + 64 * i];
    }
    f32x16 cdg, can;
    {
      auto diag_ok = [](int r) { unsigned long long m = 0; for (int l = 0; l < 64; ++l) if ((r & 3) + 8 * (r >> 2) + 4 * (l >> 5) <= (l & 31)) m |= 1ull << l; return m; };
#pragma unroll
      for (int r = 0; r < 16; ++r) { const bool ok = __builtin_amdgcn_inverse_ballot_w64(diag_ok(r)); cdg[r] = ok ? 0.f : -INFINITY; can[r] = ok ? -INFINITY : 0.f; }
      asm volatile("" : "+v"(cdg), "+v"(can));
    }
    auto lds_wait = [&](int off, unsigned v) { unsigned sp = 0; while (__hip_atomic_load((LAS unsigned*)(L + off), __ATOMIC_RELAXED, __HIP_MEMORY_SCOPE_WORKGROUP) < v && ++sp < (SPIN_CAP << 4)) __builtin_amdgcn_s_sleep(0); FENCE(); };
    WAITL0();
    if (kh == 0) {
      if (lane == 0) __hip_atomic_fetch_add((LAS unsigned*)(L + L_C1), 1u, __ATOMIC_RELAXED, __HIP_MEMORY_SCOPE_WORKGROUP);
      lds_wait(L_F1, 1);
    } else {
      if (w == 4) {
        const int sec = lane < 4 ? 0 : (lane < 12 ? 1 : 2);
        const int k = lane < 4 ? lane : (lane < 12 ? lane - 4 : lane - 12);
        const int col = sec * 640 + 20 * hd + ((k & 1) ? 19 : 0);
        const int Pn = sec == 0 ? 2 * pp + (k >> 1) : 2 * pp - 2 + (k >> 1);
        const bool need = lane < 20 && Pn >= 0;
        const unsigned fo = WS_FLAGA + (unsigned)((need ? Pn : 2 * pp) * 16 + (col >> 7)) * 128;
        unsigned spins = 0;
        for (;;) {
          FENCE();
          const u32x2 f = LD64(fo);
          if (__all(!need || (f[0] == tag2[0] && f[1] == tag2[1])) || ++spins > SPIN_CAP) break;
          __builtin_amdgcn_s_sleep(1);
        }
        if (lane == 0) __hip_atomic_store((LAS unsigned*)(L + L_F1), 1u, __ATOMIC_RELAXED, __HIP_MEMORY_SCOPE_WORKGROUP);
        STAMP(4);
      } else lds_wait(L_F1, 1);
      const char* qb = (const char*)p.ws + WS_QKV;
#pragma unroll
      for (int i = 0; i < 10; ++i) {
        const int pc = nt + 4 * i, pl = pc >> 2, qt = pc & 3;
        const int tok0 = 256 * pp - 256 + 128 * qt;
        if (tok0 >= 0) glds16c(lane * 16, qb + (size_t)((pl < 5 ? 160 + 5 * hd + pl : 320 + 5 * hd + pl - 5)) * 16384 + tok0 * 8, lds0 + pl * PL + qt * 1024);
      }
    }
    bf16x8 qf0; bf16x4 qf1;
    {
      const unsigned qo = WS_QKV + (unsigned)(5 * hd) * 16384u + (unsigned)(256 * pp + 32 * t + l31) * 8u;
      const u32x2 a = LD64(qo + (2 * hh) * 16384), c = LD64(qo + (2 * hh + 1) * 16384);
      u32x2 z = LD64(qo + 4 * 16384);
      asm volatile("s_waitcnt vmcnt(0)" : "+v"(z) :: "memory");
      if (hh) z = (u32x2){0, 0};
      qf0 = __builtin_bit_cast(bf16x8, (u32x4){a[0], a[1], c[0], c[1]});
      qf1 = __builtin_bit_cast(bf16x4, z);
    }
    if (kh == 1 && lane == 0) __hip_atomic_fetch_add((LAS unsigned*)(L + L_C1), 1u, __ATOMIC_RELAXED, __HIP_MEMORY_SCOPE_WORKGROUP);
    lds_wait(L_C1, 8);
    if (w == 4) STAMP(5);
    unsigned wov[5];
#pragma unroll
    for (int j = 0; j < 5; ++j) { const int n = 64 * j + lane, rho = (n * 6554) >> 16, chn = n - 10 * rho; wov[j] = (unsigned)(8 * ((rho >> 2) & 3) + 4 * (rho >> 4) + (rho & 3)) * 1280u + chn * 16u; }
    const char* wob = (const char*)p.wo + 160 * cc + (size_t)nt * 32 * 1280;
    const unsigned wol = lds0 + L_WO + nt * 5120;
#define WOP(i) GL_WO(wov[(i) % 5], wob + (size_t)((i) / 5) * 4 * 32 * 1280, wol + ((i) / 5) * 4 * 5120 + ((i) % 5) * 1024)
#define WOP3(i) do { if (kh == 0) { WOP(i); WOP((i) + 1); WOP((i) + 2); } SBAR(); } while (0)
    if (kh == 0) {
      const char* tp;
      if (nt < 3) tp = (const char*)(nt == 0 ? p.wg : (nt == 1 ? p.wu : p.wd)) + (size_t)min(b * 68 + lane, 17279) * 128;
      else tp = (const char*)(lane < 4 ? p.wg : (lane < 8 ? p.wu : p.wd)) + (size_t)min(b * 68 + 64 + (lane & 3), 17279) * 128;
      if (nt < 3 || lane < 12) touch_ptr(tp, lds0 + L_MISC + 8192);
    }
    if (w == 0) STAMP(28);
    const LAS char* kb = L + L_K + l31 * 8 + (2 * hh) * PL;
    const LAS char* kb4 = L + (hh ? L_V + 5 * PL : L_K + 4 * PL) + l31 * 8;
    {
      const u32x2 k0 = *(const LAS u32x2*)(kb + (t + 8) * 256), k1 = *(const LAS u32x2*)(kb + (t + 8) * 256 + PL), k2 = *(const LAS u32x2*)(kb4 + (t + 8) * 256);
      const u32x4 q0 = __builtin_bit_cast(u32x4, qf0); u32x2 q1 = __builtin_bit_cast(u32x2, qf1);
      float d = dot2(q0[0], k0[0], 0.f); d = dot2(q0[1], k0[1], d); d = dot2(q0[2], k1[0], d); d = dot2(q0[3], k1[1], d);
      d = dot2(q1[0], k2[0], d); d = dot2(q1[1], k2[1], d);
      const unsigned nm = pk2(-swap_add(d), 0.f);
      if (hh) q1[0] = nm;
      qf1 = __builtin_bit_cast(bf16x4, q1);
    }
    const LAS char* vb = L + L_V + min(4 * ((lane >> 4) & 1) + (lane & 3), 5) * PL + (4 * hh + ((lane & 15) >> 2)) * 8;
    f32x16 o = (f32x16){};
    {
      f32x16 sa, sb;
      const f32x16 cz = {};
      bf16x4 ka, kc, kd;
      bf16x8 vq = {};
      u32x4 pq = {0, 0, 0, 0};
#define KRD(kt) do { ka = *(const LAS bf16x4*)(kb + (kt) * 256); kc = *(const LAS bf16x4*)(kb + (kt) * 256 + PL); kd = *(const LAS bf16x4*)(kb4 + (kt) * 256); } while (0)
#define EX2(S, i) pk2(__builtin_amdgcn_exp2f(S[i]), __builtin_amdgcn_exp2f(S[(i) + 1]))
#define GRP(nds, nv) do { __builtin_amdgcn_sched_group_barrier(0x8, 1, 0); if (nds) __builtin_amdgcn_sched_group_barrier(0x100, nds, 0); __builtin_amdgcn_sched_group_barrier(0x2, nv, 0); } while (0)
#define HALF(SC, SN, C, ktv, ktk) do { \
        u32x4 p0_; const LAS char* vp_ = vb + (ktv) * 256; \
        SN = MFMA32(cat8(ka, kc), qf0, C); \
        const bf16x8 v0_ = cat8(trd(vp_), trd(vp_ + 64)), v1_ = cat8(trd(vp_ + 128), trd(vp_ + 192)); \
        p0_[0] = EX2(SC, 0); p0_[1] = EX2(SC, 2); \
        GRP(4, 6); asm volatile("" : "+v"(SN), "+v"(p0_[0]), "+v"(p0_[1])); SBAR(); \
        o = MFMA32(vq, __builtin_bit_cast(bf16x8, pq), o); \
        p0_[2] = EX2(SC, 4); p0_[3] = EX2(SC, 6); \
        GRP(0, 6); asm volatile("" : "+v"(o), "+v"(p0_[2]), "+v"(p0_[3])); SBAR(); \
        SN = MFMA32K8(kd, qf1, SN); \
        KRD(ktk); \
        pq[0] = EX2(SC, 8); pq[1] = EX2(SC, 10); \
        GRP(3, 6); asm volatile("" : "+v"(SN), "+v"(pq[0]), "+v"(pq[1])); SBAR(); \
        o = MFMA32(v0_, __builtin_bit_cast(bf16x8, p0_), o); \
        pq[2] = EX2(SC, 12); pq[3] = EX2(SC, 14); vq = v1_; \
        GRP(0, 6); asm volatile("" : "+v"(o), "+v"(pq[2]), "+v"(pq[3])); SBAR(); } while (0)
      KRD(t + 8);
      sa = MFMA32(cat8(ka, kc), qf0, cdg); sa = MFMA32K8(kd, qf1, sa);
      KRD(t + 7);
      asm volatile("" : "+v"(sa)); SBAR();
      WOP3(0);
      HALF(sa, sb, cz, t + 8, t + 6);
      WOP3(3);
      HALF(sb, sa, cz, t + 7, t + 5);
      WOP3(6);
      HALF(sa, sb, cz, t + 6, t + 4);
      WOP3(9);
      HALF(sb, sa, cz, t + 5, t + 3);
      WOP3(12);
      HALF(sa, sb, cz, t + 4, t + 2);
      WOP3(15);
      HALF(sb, sa, cz, t + 3, t + 1);
      WOP3(18);
      HALF(sa, sb, cz, t + 2, t);
      if (kh == 0) { WOP(21); WOP(22); WOP(23); WOP(24); }
      SBAR();
      HALF(sb, sa, can, t + 1, t);
      {
        const LAS char* vp_ = vb + t * 256;
        const bf16x8 v0_ = cat8(trd(vp_), trd(vp_ + 64)), v1_ = cat8(trd(vp_ + 128), trd(vp_ + 192));
        o = MFMA32(vq, __builtin_bit_cast(bf16x8, pq), o);
        u32x4 p0_, p1_;
#pragma unroll
        for (int e = 0; e < 4; ++e) p0_[e] = EX2(sa, 2 * e);
        o = MFMA32(v0_, __builtin_bit_cast(bf16x8, p0_), o);
#pragma unroll
        for (int e = 0; e < 4; ++e) p1_[e] = EX2(sa, 8 + 2 * e);
        o = MFMA32(v1_, __builtin_bit_cast(bf16x8, p1_), o);
      }
    }
    float lsum;
    { auto r = __builtin_amdgcn_permlane32_swap(__float_as_uint(o[8]), __float_as_uint(o[8]), false, false); lsum = __uint_as_float(r[1]); }
    if (__builtin_expect(__any(!(lsum < 1.0995116e12f)), 0)) {
      const int lo = pp >= 1 ? t : 8;
      float mx = -INFINITY;
#pragma unroll 1
      for (int kt = t + 8; kt >= lo; --kt) {
        const bf16x4 ka = *(const LAS bf16x4*)(kb + kt * 256), kc = *(const LAS bf16x4*)(kb + kt * 256 + PL), kd = *(const LAS bf16x4*)(kb4 + kt * 256);
        f32x16 s = MFMA32(cat8(ka, kc), qf0, (f32x16){});
        s = MFMA32K8(kd, qf1, s);
#pragma unroll
        for (int r = 0; r < 16; ++r) { const int kr = (r & 3) + 8 * (r >> 2) + 4 * hh; const bool ok = kt == t + 8 ? kr <= l31 : (kt == t ? kr > l31 : true); mx = fmaxf(mx, ok ? s[r] : -INFINITY); }
      }
      { auto r = __builtin_amdgcn_permlane32_swap(__float_as_uint(mx), __float_as_uint(mx), false, false); mx = fmaxf(__uint_as_float(r[0]), __uint_as_float(r[1])); }
      o = (f32x16){};
#pragma unroll 1
      for (int kt = t + 8; kt >= lo; --kt) {
        const bf16x4 ka = *(const LAS bf16x4*)(kb + kt * 256), kc = *(const LAS bf16x4*)(kb + kt * 256 + PL), kd = *(const LAS bf16x4*)(kb4 + kt * 256);
        const LAS char* vp_ = vb + kt * 256;
        const bf16x8 v0_ = cat8(trd(vp_), trd(vp_ + 64)), v1_ = cat8(trd(vp_ + 128), trd(vp_ + 192));
        f32x16 s = MFMA32(cat8(ka, kc), qf0, (f32x16){});
        s = MFMA32K8(kd, qf1, s);
        u32x4 p0_, p1_;
#pragma unroll
        for (int r = 0; r < 16; ++r) { const int kr = (r & 3) + 8 * (r >> 2) + 4 * hh; const bool ok = kt == t + 8 ? kr <= l31 : (kt == t ? kr > l31 : true); s[r] = ok ? __builtin_amdgcn_exp2f(s[r] - mx) : 0.f; }
#pragma unroll
        for (int e = 0; e < 4; ++e) { p0_[e] = pk2(s[2 * e], s[2 * e + 1]); p1_[e] = pk2(s[8 + 2 * e], s[8 + 2 * e + 1]); }
        o = MFMA32(v0_, __builtin_bit_cast(bf16x8, p0_), o);
        o = MFMA32(v1_, __builtin_bit_cast(bf16x8, p1_), o);
      }
      auto r = __builtin_amdgcn_permlane32_swap(__float_as_uint(o[8]), __float_as_uint(o[8]), false, false); lsum = __uint_as_float(r[1]);
    }
    if (w == 0) STAMP(6);
    STAMP(32 + w);
    {
      const float inv = 1.f / lsum;
      unsigned A0 = pk2(o[0] * inv, o[1] * inv), A1 = pk2(o[2] * inv, o[3] * inv), B0 = pk2(o[4] * inv, o[5] * inv), B1 = pk2(o[6] * inv, o[7] * inv);
      unsigned C0 = pk2(o[8] * inv, o[9] * inv), C1 = pk2(o[10] * inv, o[11] * inv);
      const int tt = pp * 16 + 2 * t + (l31 >> 4);
      const unsigned base = WS_O + (unsigned)tt * 80 * 256 + (l31 & 15) * 16 + (unsigned)(5 * (hd >> 1)) * 256;
      if ((hd & 1) == 0) {
        auto s0 = __builtin_amdgcn_permlane32_swap(A0, B0, false, false), s1 = __builtin_amdgcn_permlane32_swap(A1, B1, false, false);
        ST128(((u32x4){s0[0], s1[0], s0[1], s1[1]}), base + hh * 256);
        if (hh == 0) ST64(((u32x2){C0, C1}), base + 2 * 256);
      } else {
        auto u0 = __builtin_amdgcn_permlane32_swap(A0, C0, false, false), u1 = __builtin_amdgcn_permlane32_swap(A1, C1, false, false);
        const u32x4 v = hh ? (u32x4){B0, B1, u0[0], u1[0]} : (u32x4){u0[1], u1[1], B0, B1};
        ST128(v, base + (3 + hh) * 256);
        if (hh == 0) ST64(((u32x2){A0, A1}), base + 2 * 256 + 8);
      }
      WAITV(0);
      if (lane == 0) ST64(tag2, WS_FLAGO + (((pp * 8 + t) * 2 + (hd >> 4)) * 16 + (hd & 15)) * 8);
    }
  }
  if (w == 0) STAMP(7);
  STAMP(40 + w);

  BARRIER();
  if (w == 0) STAMP(8);

  f32x4 acc[5];
#pragma unroll
  for (int ft = 0; ft < 5; ++ft) acc[ft] = (f32x4){0, 0, 0, 0};
  {
    const LAS char* wp = L + L_WO + (unsigned)(kh * 320 + 4 * fg + ((lane & 15) >> 2)) * 160 + (lane & 3) * 8;
    bf16x8 wa[3][5];
#pragma unroll
    for (int s = 0; s < 3; ++s)
#pragma unroll
      for (int ft = 0; ft < 5; ++ft) wa[s][ft] = cat8(trd(wp + s * 5120 + ft * 32), trd(wp + s * 5120 + 2560 + ft * 32));
    const int tg = 4 * R + nt;
    const unsigned fo = WS_FLAGO + ((((tg >> 4) * 8 + ((tg >> 1) & 7)) * 2 + kh) * 16 + l15) * 8;
    unsigned spins = 0;
    for (;;) {
      FENCE();
      const u32x2 f = LD64(fo);
      if (__all(f[0] == tag2[0] && f[1] == tag2[1]) || ++spins > SPIN_CAP) break;
      __builtin_amdgcn_s_sleep(1);
    }
    FENCE();
    if (w == 0) STAMP(9);
    STAMP(48 + w);
    u32x4 of[10];
#pragma unroll
    for (int s = 0; s < 10; ++s) of[s] = LD128(WS_O + (unsigned)(tg * 80 + 4 * (10 * kh + s)) * 256 + lane * 16);
    SBAR();
#pragma unroll
    for (int s = 0; s < 10; ++s) {
      const bf16x8 bfr = __builtin_bit_cast(bf16x8, of[s]);
#pragma unroll
      for (int ft = 0; ft < 5; ++ft) acc[ft] = MFMA16(s < 3 ? wa[s < 3 ? s : 0][ft] : cat8(trd(wp + s * 5120 + ft * 32), trd(wp + s * 5120 + 2560 + ft * 32)), bfr, acc[ft]);
    }
  }
  STAMP(56 + w);
  if (kh == 1) {
#pragma unroll
    for (int ft = 0; ft < 5; ++ft) *(LAS f32x4*)(L + L_RED + ((nt * 5 + ft) * 64 + lane) * 16) = acc[ft];
  }
  WAITL0(); BARRIER();
  if (w == 0) STAMP(10);
  if (kh == 0) {
    float s1 = 0.f, s2 = 0.f, s3 = 0.f;
#pragma unroll
    for (int ft = 0; ft < 5; ++ft) {
      const f32x4 o2 = *(const LAS f32x4*)(L + L_RED + ((nt * 5 + ft) * 64 + lane) * 16);
      const unsigned a01 = pk2(acc[ft][0] + o2[0], acc[ft][1] + o2[1]), a23 = pk2(acc[ft][2] + o2[2], acc[ft][3] + o2[3]);
      const unsigned h01 = pk2(bf2f(xr[ft][0]) + bf2f(a01), bfhi(xr[ft][0]) + bfhi(a01)), h23 = pk2(bf2f(xr[ft][1]) + bf2f(a23), bfhi(xr[ft][1]) + bfhi(a23));
      *(LAS u32x2*)(L + L_HST + nt * 2560 + l15 * 160 + ft * 32 + fg * 8) = (u32x2){h01, h23};
      s1 = dot2(h01, 0x3f803f80u, s1); s1 = dot2(h23, 0x3f803f80u, s1);
      s2 = dot2(h01, h01, s2); s2 = dot2(h23, h23, s2);
      s3 = dot2(h01, wrv[ft][0], s3); s3 = dot2(h23, wrv[ft][1], s3);
    }
    WAITL0();
#pragma unroll
    for (int i = 0; i < 3; ++i) {
      const int n = lane + 64 * i;
      if (n < 160) ST128(*(const LAS u32x4*)(L + L_HST + nt * 2560 + n * 16), WS_H + (unsigned)((R * 8 + cc) * 64 + 16 * nt) * 160 + n * 16);
    }
    s1 = swap_add(swap16_add(s1)); s2 = swap_add(swap16_add(s2)); s3 = swap_add(swap16_add(s3));
    WAITV(0);
    if (lane < 16) ST128(((u32x4){__float_as_uint(s1), __float_as_uint(s2), __float_as_uint(s3), tag32}), WS_STATS + ((R * 8 + cc) * 64 + 16 * nt + l15) * 16);
    if (w == 0) STAMP(11);
    BARRIER();
  } else {
    BARRIER();
    const int rl = lane >> 1, grow = 8 * ((rl >> 2) & 3) + 4 * (rl >> 4) + (rl & 3);
    if (is_gu) {
      const unsigned vo = (unsigned)grow * (F * 2) + (lane & 1) * 16;
#pragma unroll 1
      for (int pc = nt; pc < 40; pc += 4) { const int m = pc >= 20, s = pc - 20 * m; GL_FW(vo, (const char*)(m ? p.wu : p.wg) + (size_t)s * 32 * F * 2 + gs * 32, lds0 + L_FW + pc * 1024); }
    } else if (is_dn) {
      const unsigned vo = (unsigned)grow * (D * 2) + (lane & 1) * 16;
#pragma unroll 1
      for (int pc = nt; pc < 54; pc += 4) GL_FW(vo, (const char*)p.wd + (size_t)pc * 32 * D * 2 + dn * 32, lds0 + L_FW + pc * 1024);
    }
  }
  auto rank64 = [&](unsigned k) {
    const u32x4 oa = *(const LAS u32x4*)(L + L_KEY + w * 32), ob = *(const LAS u32x4*)(L + L_KEY + w * 32 + 16);
    unsigned c = 0;
#pragma unroll
    for (int e = 0; e < 4; ++e) c += (oa[e] > k) + (ob[e] > k);
    *(LAS unsigned char*)(L + L_PART + lane * 8 + w) = (unsigned char)c;
    WAITL0(); BARRIER();
    const u32x2 pr = *(const LAS u32x2*)(L + L_PART + lane * 8);
    return __builtin_amdgcn_sad_u8(pr[0], 0u, __builtin_amdgcn_sad_u8(pr[1], 0u, 0u));
  };
  if (bi == 31) {
    float swr = 0.f;
#pragma unroll
    for (int i = 0; i < 5; ++i) swr = dot2(wr5[i], 0x3f803f80u, swr);
    swr = wave_fsum(swr);
    const int tl = 32 * w + l31;
    const unsigned so = WS_STATS + (unsigned)(((4 * xcd + (tl >> 6)) * 8 + 4 * hh) * 64 + (tl & 63)) * 16;
    float t1, t2, t3;
    unsigned spins = 0;
    for (;;) {
      FENCE();
      const u32x4 g0 = LD128(so), g1 = LD128(so + 1024), g2 = LD128(so + 2048), g3 = LD128(so + 3072);
      t1 = (__uint_as_float(g0[0]) + __uint_as_float(g1[0])) + (__uint_as_float(g2[0]) + __uint_as_float(g3[0]));
      t2 = (__uint_as_float(g0[1]) + __uint_as_float(g1[1])) + (__uint_as_float(g2[1]) + __uint_as_float(g3[1]));
      t3 = (__uint_as_float(g0[2]) + __uint_as_float(g1[2])) + (__uint_as_float(g2[2]) + __uint_as_float(g3[2]));
      if (__all(g0[3] == tag32 && g1[3] == tag32 && g2[3] == tag32 && g3[3] == tag32) || ++spins > SPIN_CAP) break;
    }
    t1 = swap_add(t1); t2 = swap_add(t2); t3 = swap_add(t3);
    const float mean = t1 * (1.f / D);
    const float var = fmaxf(t2 * (1.f / D) - mean * mean, 0.f);
    const float mb = rbf(mean);
    const float sd = rbf(__builtin_amdgcn_sqrtf(rbf(rbf(var) + 1e-5f)));
    const unsigned rb = pk2((t3 - mb * swr) * __builtin_amdgcn_rcpf(sd), 0.f) & 0xffffu;
    const unsigned key = ((rb ^ ((rb & 0x8000u) ? 0xffffu : 0x8000u)) << 16) | (unsigned)(2047 - (256 * xcd + tl));
    const unsigned ms = (__float_as_uint(mb) & 0xffff0000u) | (__float_as_uint(sd) >> 16);
    if (w == 0) STAMP(12);
    if (lane < 32) *(LAS unsigned*)(L + L_K32 + tl * 4) = key;
    WAITL0();
    unsigned c = 0;
#pragma unroll
    for (int i = 0; i < 4; ++i) {
      const u32x4 o4 = *(const LAS u32x4*)(L + L_K32 + (32 * w + 16 * hh + 4 * i) * 4);
#pragma unroll
      for (int e = 0; e < 4; ++e) c += o4[e] > key;
    }
    { auto r = __builtin_amdgcn_permlane32_swap(c, c, false, false); c = r[0] + r[1]; }
    if (lane < 32 && c < 8) { *(LAS unsigned*)(L + L_KEY + (8 * w + c) * 4) = key; *(LAS unsigned*)(L + L_KEY + 256 + (8 * w + c) * 4) = ms; }
    WAITL0(); BARRIER();
    const unsigned k = *(const LAS unsigned*)(L + L_KEY + lane * 4), m2 = *(const LAS unsigned*)(L + L_KEY + 256 + lane * 4);
    const unsigned rk = rank64(k);
    if (w == 0) { if (rk < 8) ST128(((u32x4){k, tag2[0], m2, tag2[1]}), WS_CAND + xcd * 128 + rk * 16); STAMP(26); }
    BARRIER();
  }
  if (w == 0) {
    u32x4 a;
    unsigned spins = 0;
    for (;;) {
      FENCE();
      a = LD128(WS_CAND + lane * 16);
      if (__all(a[1] == tag2[0] && a[3] == tag2[1]) || ++spins > SPIN_CAP) break;
      if (is_gu || is_dn) __builtin_amdgcn_s_sleep(1); else __builtin_amdgcn_s_sleep(8);
    }
    STAMP(13);
    *(LAS unsigned*)(L + L_KEY + lane * 4) = a[0]; *(LAS unsigned*)(L + L_KEY + 256 + lane * 4) = a[2];
    if (b == 0 && lane == 0) ST64(tag2, WS_EPOCH);
  }
  const int hc0 = (lane * 6554) >> 16, hc1 = ((64 + l15) * 6554) >> 16;
  const unsigned ho0 = (unsigned)hc0 * (64 * 160) + (lane - 10 * hc0) * 16, ho1 = (unsigned)hc1 * (64 * 160) + (64 + l15 - 10 * hc1) * 16;
  if (kh == 1) WAITV(0);
  WAITL0(); BARRIER();
  {
    const unsigned k = *(const LAS unsigned*)(L + L_KEY + lane * 4), m2 = *(const LAS unsigned*)(L + L_KEY + 256 + lane * 4);
    const unsigned rk = rank64(k);
    if (rk < 8) *(LAS u32x2*)(L + L_SEL + rk * 8) = (u32x2){k, m2};
    WAITL0();
  }
  if (w == 0) STAMP(14);

  auto h_row = [&](int row, u32x4& h0, u32x4& h1) {
    const unsigned rb = WS_H + (unsigned)((row >> 6) * 8 * 64 + (row & 63)) * 160;
    h0 = LD128(rb + ho0); h1 = LD128(rb + ho1);
  };
  auto ln8 = [&](const u32x4& h, float m, float iv) { u32x4 n;
#pragma unroll
    for (int e = 0; e < 4; ++e) { const unsigned d = pk2(bf2f(h[e]) - m, bfhi(h[e]) - m); n[e] = pk2(bf2f(d) * iv, bfhi(d) * iv); } return n; };
  auto out_row = [&]() {
    const int row = 64 * R + 8 * cc + w;
    const u32x2 sk = *(const LAS u32x2*)(L + L_SEL + (lane & 7) * 8);
    if (!__any(2047 - (int)(sk[0] & 0x7ff) == row)) {
      u32x4 h0, h1; h_row(row, h0, h1);
      const u32x4 g = LD128(WS_STATS + (unsigned)((R * 8 + (lane & 7)) * 64 + 8 * cc + w) * 16);
      float t1 = __uint_as_float(g[0]), t2 = __uint_as_float(g[1]);
      t1 = dppadd<0x141>(t1); t2 = dppadd<0x141>(t2);
      t1 = dppadd<0xb1>(t1); t2 = dppadd<0xb1>(t2);
      t1 = dppadd<0x4e>(t1); t2 = dppadd<0x4e>(t2);
      const float mean = t1 * (1.f / D);
      const float m = rbf(mean), iv = __builtin_amdgcn_rcpf(rbf(__builtin_amdgcn_sqrtf(rbf(rbf(fmaxf(t2 * (1.f / D) - mean * mean, 0.f)) + 1e-5f))));
      const u32x4 n0 = ln8(h0, m, iv), n1 = ln8(h1, m, iv);
      u32x4 r0, r1;
#pragma unroll
      for (int e = 0; e < 4; ++e) { r0[e] = pk2(bf2f(h0[e]) + bf2f(n0[e]), bfhi(h0[e]) + bfhi(n0[e])); r1[e] = pk2(bf2f(h1[e]) + bf2f(n1[e]), bfhi(h1[e]) + bfhi(n1[e])); }
      *(u32x4*)(p.out + (size_t)row * D + lane * 8) = r0;
      if (lane < 16) *(u32x4*)(p.out + (size_t)row * D + 512 + lane * 8) = r1;
    }
  };
  if (is_gu) {
    {
      const u32x2 sk = *(const LAS u32x2*)(L + L_SEL + w * 8);
      u32x4 h0, h1; h_row(2047 - (int)(sk[0] & 0x7ff), h0, h1);
      const float m = __uint_as_float(sk[1] & 0xffff0000u), iv = __builtin_amdgcn_rcpf(__uint_as_float(sk[1] << 16));
      *(LAS u32x4*)(L + L_XS + w * 1296 + lane * 16) = ln8(h0, m, iv);
      if (lane < 16) *(LAS u32x4*)(L + L_XS + w * 1296 + 1024 + lane * 16) = ln8(h1, m, iv);
    }
    WAITL0(); BARRIER();
    if (w == 0) STAMP(15);
    {
      const int m = w & 1, kq = w >> 1;
      f32x4 a4 = {0, 0, 0, 0}, b4 = {0, 0, 0, 0};
#pragma unroll
      for (int i = 0; i < 5; ++i) {
        const int s = 5 * kq + i;
        const LAS char* ap = L + L_FW + (m * 20 + s) * 1024 + lane * 8;
        const bf16x8 af = cat8(trd(ap), trd(ap + 512));
        const bf16x8 bfv = *(const LAS bf16x8*)(L + L_XS + (lane & 7) * 1296 + s * 64 + fg * 16);
        if (i & 1) b4 = MFMA16(af, bfv, b4); else a4 = MFMA16(af, bfv, a4);
      }
      *(LAS f32x4*)(L + L_FRED + (w * 64 + lane) * 16) = a4 + b4;
    }
    WAITL0(); BARRIER();
    if (w == 0) {
      f32x4 g4 = {0, 0, 0, 0}, u4 = {0, 0, 0, 0};
#pragma unroll
      for (int q = 0; q < 4; ++q) { g4 += *(const LAS f32x4*)(L + L_FRED + ((2 * q) * 64 + lane) * 16); u4 += *(const LAS f32x4*)(L + L_FRED + ((2 * q + 1) * 64 + lane) * 16); }
      float a[4];
#pragma unroll
      for (int e = 0; e < 4; ++e) { const float g = rbf(g4[e]), u = rbf(u4[e]); const float sg = rbf(g * __builtin_amdgcn_rcpf(1.f + __builtin_amdgcn_exp2f(g * -1.4426950408889634f))); a[e] = sg * u; }
      if (l15 < 8) ST128(((u32x4){pk2(a[0], a[1]), tag32, pk2(a[2], a[3]), tag32}), WS_ACT + (((gs >> 1) * 4 + 2 * (gs & 1) + (fg >> 1)) * 8 + l15) * 32 + (fg & 1) * 16);
      STAMP(16);
    }
    out_row();
  } else if (is_dn) {
    const u32x2 sk = *(const LAS u32x2*)(L + L_SEL + (lane & 7) * 8);
    const int row = 2047 - (int)(sk[0] & 0x7ff);
    u32x2 hv = {0, 0}, nv = {0, 0};
    float sig = 0.f;
    if (w == 0) {
      const int col = 16 * dn + 4 * fg, c = col / 80;
      hv = LD64(WS_H + (unsigned)(((row >> 6) * 8 + c) * 64 + (row & 63)) * 160 + (col - 80 * c) * 2);
      const unsigned rk = sk[0] >> 16, rbits = (rk & 0x8000u) ? (rk ^ 0x8000u) : (rk ^ 0xffffu);
      sig = rbf(__builtin_amdgcn_rcpf(1.f + __builtin_amdgcn_exp2f(bf2f(rbits) * -1.4426950408889634f)));
      const float m = __uint_as_float(sk[1] & 0xffff0000u), iv = __builtin_amdgcn_rcpf(__uint_as_float(sk[1] << 16));
#pragma unroll
      for (int e = 0; e < 2; ++e) { const unsigned d2 = pk2(bf2f(hv[e]) - m, bfhi(hv[e]) - m); nv[e] = pk2(bf2f(d2) * iv, bfhi(d2) * iv); }
      asm volatile("" : "+v"(nv), "+v"(hv), "+v"(sig));
    } else out_row();
    f32x4 a4 = {0, 0, 0, 0}, b4 = {0, 0, 0, 0};
    {
      bf16x8 af[7];
#pragma unroll
      for (int i = 0; i < 7; ++i) { const LAS char* ap = L + L_FW + min(w + 8 * i, 53) * 1024 + lane * 8; af[i] = cat8(trd(ap), trd(ap + 512)); }
      u32x4 g0[7];
      unsigned spins = 0;
      for (;;) {
        FENCE();
        bool ok = true;
#pragma unroll
        for (int i = 0; i < 7; ++i) g0[i] = LD128(WS_ACT + ((min(w + 8 * i, 53) * 4 + fg) * 8 + (lane & 7)) * 32 + ((lane >> 3) & 1) * 16);
#pragma unroll
        for (int i = 0; i < 7; ++i) ok = ok && g0[i][1] == tag32 && g0[i][3] == tag32;
        if (__all(ok) || ++spins > SPIN_CAP) break;
      }
      if (w == 0) STAMP(17);
#pragma unroll
      for (int i = 0; i < 7; ++i) {
        const unsigned y0 = (unsigned)__builtin_amdgcn_update_dpp(0, (int)g0[i][0], 0x128, 0xf, 0xf, false), y1 = (unsigned)__builtin_amdgcn_update_dpp(0, (int)g0[i][2], 0x128, 0xf, 0xf, false);
        const bf16x8 bv = __builtin_bit_cast(bf16x8, (u32x4){g0[i][0], g0[i][2], y0, y1});
        if (i < 6 || w < 6) { if (i & 1) b4 = MFMA16(af[i], bv, b4); else a4 = MFMA16(af[i], bv, a4); }
      }
    }
    *(LAS f32x4*)(L + L_FRED + (w * 64 + lane) * 16) = a4 + b4;
    WAITL0(); BARRIER();
    if (w == 0) {
      f32x4 y = {0, 0, 0, 0};
#pragma unroll
      for (int q = 0; q < 8; ++q) y += *(const LAS f32x4*)(L + L_FRED + (q * 64 + lane) * 16);
      unsigned r2[2];
#pragma unroll
      for (int e = 0; e < 2; ++e) {
        const unsigned y2 = pk2(rbf(y[2 * e]) * sig, rbf(y[2 * e + 1]) * sig);
        const unsigned t2 = pk2(bf2f(nv[e]) + bf2f(y2), bfhi(nv[e]) + bfhi(y2));
        r2[e] = pk2(bf2f(hv[e]) + bf2f(t2), bfhi(hv[e]) + bfhi(t2));
      }
      if (l15 < 8) *(u32x2*)(p.out + (size_t)row * D + 16 * dn + 4 * fg) = (u32x2){r2[0], r2[1]};
      STAMP(18);
      out_row();
    }
  } else {
    out_row();
  }
  if (w == 0) STAMP(19);
  STAMP_FLUSH();
}

extern "C" void kernel_launch(void* const* d_in, const int* in_sizes, int n_in, void* d_out, int out_size, void* d_ws, size_t ws_size, hipStream_t stream) {
  Params p;
  p.x = (const u16*)d_in[0]; p.wqkv = (const u16*)d_in[1]; p.wo = (const u16*)d_in[2]; p.wr = (const u16*)d_in[4];
  p.wg = (const u16*)d_in[5]; p.wu = (const u16*)d_in[6]; p.wd = (const u16*)d_in[7]; p.out = (u16*)d_out; p.ws = (unsigned char*)d_ws + ((ws_size - (16u << 20)) & ~(size_t)4095);
  fused_kernel<<<256, 512, 0, stream>>>(p);
}
```

```cpp
#include <hip/hip_runtime.h>
#include <stdint.h>

typedef unsigned short u16;
typedef short bf16x8 __attribute__((ext_vector_type(8)));
typedef short bf16x4 __attribute__((ext_vector_type(4)));
typedef __bf16 bf2 __attribute__((ext_vector_type(2)));
typedef float f32x16 __attribute__((ext_vector_type(16)));
typedef float f32x4 __attribute__((ext_vector_type(4)));
typedef float f32x2 __attribute__((ext_vector_type(2)));
typedef unsigned u32x4 __attribute__((ext_vector_type(4)));
typedef unsigned u32x2 __attribute__((ext_vector_type(2)));
typedef unsigned long long u64;
#define LAS __attribute__((address_space(3)))
#define SBAR() __builtin_amdgcn_sched_barrier(0)
#define BARRIER() asm volatile("s_barrier" ::: "memory")
#define WAITL0() asm volatile("s_waitcnt lgkmcnt(0)" ::: "memory")
#define WAITV(n) asm volatile("s_waitcnt vmcnt(" #n ")" ::: "memory")
#define FENCE() asm volatile("" ::: "memory")

constexpr int S = 2048, D = 640, F = 1728;
constexpr int SC1 = 16;

constexpr unsigned WS_EPOCH = 0;
constexpr unsigned WS_FLAGA = 4096;
constexpr unsigned WS_FLAGO = WS_FLAGA + 32768;
constexpr unsigned WS_CAND = WS_FLAGO + 16384;
constexpr unsigned WS_HDONE = WS_CAND + 4096;
constexpr unsigned WS_ACT = WS_HDONE + 4096;
constexpr unsigned WS_ST1 = 131072;
constexpr unsigned WS_CS1 = WS_ST1 + 32768;
constexpr unsigned WS_STATS = 524288;
constexpr unsigned WS_QKV = 1048576;
constexpr unsigned WS_O = WS_QKV + 480 * 16384;
constexpr unsigned WS_H = WS_O + S * D * 2;
#define WS_TAIL 1
#define STAMP_OFF (15u << 20)
#define NSTAMP 64

constexpr int L_SLOT = 32768, L_W = 16384;
constexpr int PL = 4128;
constexpr int L_K = 0, L_V = 5 * PL;
constexpr int L_WO = 45568;
constexpr int L_RED = 0;
constexpr int L_HST = 20480;
constexpr int L_FW = L_WO;
constexpr int L_XS = 0;
constexpr int L_FRED = 12288;
constexpr int L_MISC = L_WO + 640 * 160;
constexpr int L_RS = 0;
constexpr int L_CS = 1024;
constexpr int L_CNT = L_MISC + 3600;
constexpr int L_F1 = L_MISC + 3604, L_C1 = L_MISC + 3608;
constexpr int L_PART = L_MISC + 2048;
constexpr int L_K32 = L_MISC + 2560;
constexpr int L_HD = L_MISC + 3712;
constexpr int L_SEL = L_MISC + 3968;
constexpr int L_STAMP = L_MISC + 4096;
constexpr int L_KEY = L_MISC + 4608 + 256;
constexpr int LDS_BYTES = 163840;
static_assert(L_V + 6 * PL <= L_WO && L_KEY + 2048 <= LDS_BYTES, "lds map");

struct Params { const u16* x; const u16* wqkv; const u16* wo; const u16* wr; const u16* wg; const u16* wu; const u16* wd; u16* out; unsigned char* ws; };

__device__ __forceinline__ float bf2f(unsigned v) { return __uint_as_float(v << 16); }
__device__ __forceinline__ float bfhi(unsigned v) { return __uint_as_float(v & 0xffff0000u); }
__device__ __forceinline__ unsigned pk2(float a, float b) { f32x2 f = {a, b}; return __builtin_bit_cast(unsigned, __builtin_convertvector(f, bf2)); }
__device__ __forceinline__ float rbf(float f) { return __uint_as_float(pk2(f, 0.f) << 16); }
__device__ __forceinline__ float dot2(unsigned a, unsigned b, float c) { return __builtin_amdgcn_fdot2_f32_bf16(__builtin_bit_cast(bf2, a), __builtin_bit_cast(bf2, b), c, false); }
__device__ __forceinline__ float swap_add(float v) { auto r = __builtin_amdgcn_permlane32_swap(__float_as_uint(v), __float_as_uint(v), false, false); return __uint_as_float(r[0]) + __uint_as_float(r[1]); }
__device__ __forceinline__ float swap16_add(float v) { auto r = __builtin_amdgcn_permlane16_swap(__float_as_uint(v), __float_as_uint(v), false, false); return __uint_as_float(r[0]) + __uint_as_float(r[1]); }
template <int CTRL> __device__ __forceinline__ float dppadd(float v) { return v + __int_as_float(__builtin_amdgcn_update_dpp(0, __float_as_int(v), CTRL, 0xf, 0xf, false)); }
__device__ __forceinline__ float wave_fsum(float v) { v = dppadd<0x128>(v); v = dppadd<0x124>(v); v = dppadd<0x122>(v); v = dppadd<0x121>(v); return swap_add(swap16_add(v)); }
template <int CTRL> __device__ __forceinline__ unsigned dppmax(unsigned v) { return max(v, (unsigned)__builtin_amdgcn_update_dpp(0, (int)v, CTRL, 0xf, 0xf, false)); }
__device__ __forceinline__ unsigned wave_umax(unsigned v) {
  v = dppmax<0x128>(v); v = dppmax<0x124>(v); v = dppmax<0x122>(v); v = dppmax<0x121>(v);
  auto r = __builtin_amdgcn_permlane16_swap(v, v, false, false); v = max(r[0], r[1]);
  auto q = __builtin_amdgcn_permlane32_swap(v, v, false, false); return max(q[0], q[1]);
}
__device__ __forceinline__ void glds16(unsigned voff, const void* sbase, unsigned lds) {
  asm volatile("s_mov_b32 m0, %2\n\ts_nop 0\n\tglobal_load_lds_dwordx4 %0, %1" ::"v"(voff), "s"(sbase), "s"(lds) : "memory");
}
__device__ __forceinline__ void glds16c(unsigned voff, const void* sbase, unsigned lds) {
  asm volatile("s_mov_b32 m0, %2\n\ts_nop 0\n\tglobal_load_lds_dwordx4 %0, %1 sc1" ::"v"(voff), "s"(sbase), "s"(lds) : "memory");
}
__device__ __forceinline__ void touch64(unsigned voff, const void* sbase, unsigned lds) {
  asm volatile("s_mov_b32 m0, %2\n\ts_nop 0\n\tglobal_load_lds_dword %0, %1" ::"v"(voff), "s"(sbase), "s"(lds) : "memory");
}
__device__ __forceinline__ void glds16n(unsigned voff, const void* sbase, unsigned lds) {
  asm volatile("s_mov_b32 m0, %2\n\ts_nop 0\n\tglobal_load_lds_dwordx4 %0, %1 nt" ::"v"(voff), "s"(sbase), "s"(lds) : "memory");
}
#define GL_FW glds16
#define GL_WO glds16
#define GL_X glds16
#define GL_W glds16
__device__ __forceinline__ void touch_ptr(const void* g, unsigned lds) {
  asm volatile("s_mov_b32 m0, %1\n\ts_nop 0\n\tglobal_load_lds_dword %0, off" ::"v"(g), "s"(lds) : "memory");
}
__device__ __forceinline__ bf16x4 trd(const LAS char* p) { return __builtin_amdgcn_ds_read_tr16_b64_v4i16((LAS bf16x4*)p); }
__device__ __forceinline__ bf16x8 cat8(bf16x4 a, bf16x4 b) { return (bf16x8){a[0], a[1], a[2], a[3], b[0], b[1], b[2], b[3]}; }
#define MFMA32(a, b, c) __builtin_amdgcn_mfma_f32_32x32x16_bf16(a, b, c, 0, 0, 0)
#define MFMA32K8(a, b, c) __builtin_amdgcn_mfma_f32_32x32x8bf16_1k(a, b, c, 0, 0, 0)
#define MFMA16(a, b, c) __builtin_amdgcn_mfma_f32_16x16x32_bf16(a, b, c, 0, 0, 0)
#define LD128(off) __builtin_amdgcn_raw_buffer_load_b128(rws, (off), 0, SC1)
#define LD64(off) __builtin_amdgcn_raw_buffer_load_b64(rws, (off), 0, SC1)
#define ST128(v, off) __builtin_amdgcn_raw_buffer_store_b128((v), rws, (off), 0, SC1)
#define ST64(v, off) __builtin_amdgcn_raw_buffer_store_b64((v), rws, (off), 0, SC1)

#define STAMP(k) do {} while (0)
#define STAMP_INIT() do {} while (0)
#define PSTAMP(i) do {} while (0)
#define STAMP_ZERO() do {} while (0)
#define STAMP_PA() do {} while (0)
#define STAMP_FLUSH() do {} while (0)

#define ROT (((bi >> 3) * 5 / 2) % 10)
constexpr unsigned SPIN_CAP = 1u << 17;

__global__ __launch_bounds__(512, 2) void fused_kernel(Params p) {
  __shared__ __attribute__((aligned(16))) char smem[LDS_BYTES];
  LAS char* L = (LAS char*)smem;
  const unsigned lds0 = (unsigned)(uintptr_t)smem;
  const int tid = threadIdx.x, lane = tid & 63;
  const int w = __builtin_amdgcn_readfirstlane(tid >> 6);
  const int b = blockIdx.x, xcd = b & 7, bi = b >> 3;
  const int l31 = lane & 31, hh = lane >> 5, l15 = lane & 15, fg = lane >> 4;
  const __amdgpu_buffer_rsrc_t rws = __builtin_amdgcn_make_buffer_rsrc(p.ws, 0, 0x7fffffff, 0x00020000);
  const int aP = 4 * (xcd >> 1) + (bi >> 3), aj = 8 * (xcd & 1) + (bi & 7);
  const int hd = 4 * xcd + (bi >> 3), pp = bi & 7;
  const int R = 4 * xcd + (bi >> 3), cc = bi & 7;
  const int gs = xcd * 14 + bi;
  const bool is_gu = bi < 14 && gs < 108;
  const int dn = xcd * 5 + (bi - 14);
  const bool is_dn = bi >= 14 && bi < 19;
  STAMP_INIT();
  u32x2 tag2;
  auto read_epoch = [&]() {
    u64 e;
    asm volatile("s_load_dwordx2 %0, %1, 0x0\n\ts_waitcnt lgkmcnt(0)" : "=s"(e) : "s"(p.ws) : "memory");
    e += 1;
    tag2 = (u32x2){(unsigned)e, (unsigned)(e >> 32)};
  };

  auto rows_issue = [&](int lw, u32x4& ra0, u32x4& ra1, u32x4& rb0, u32x4& rb1) {
    if (lw == 0) ST128(((u32x4){0, 0, 0, 0}), WS_STATS + (R * 8 + cc) * 1024 + lane * 16);
    if (lw == 1 && is_gu && lane < 32) ST128(((u32x4){0, 0, 0, 0}), WS_ACT + (((gs >> 1) * 4 + 2 * (gs & 1) + (lane >> 4)) * 8 + (lane & 7)) * 32 + ((lane >> 3) & 1) * 16);
    const char* rb = (const char*)p.x + (size_t)(64 * R + 8 * cc + 2 * lw) * 1280;
    ra0 = *(const u32x4*)(rb + lane * 16); ra1 = *(const u32x4*)(rb + 1024 + l15 * 16);
    rb0 = *(const u32x4*)(rb + 1280 + lane * 16); rb1 = *(const u32x4*)(rb + 1280 + 1024 + l15 * 16);
    FENCE();
  };
  auto rows_finish = [&](int lw, const u32x4& ra0, const u32x4& ra1, const u32x4& rb0, const u32x4& rb1) {
    float mean[2], rs[2];
#pragma unroll
    for (int r = 0; r < 2; ++r) {
      const u32x4 c0 = r ? rb0 : ra0, c1 = r ? rb1 : ra1;
      float s0 = 0.f, s1 = 0.f;
#pragma unroll
      for (int e = 0; e < 4; ++e) { s0 = dot2(c0[e], 0x3f803f80u, s0); s1 = dot2(c1[e], 0x3f803f80u, s1); }
      mean[r] = wave_fsum(s0 + (lane < 16 ? s1 : 0.f)) * (1.f / D);
      const f32x2 mm = {mean[r], mean[r]};
      f32x2 q0 = {0.f, 0.f}, q1 = {0.f, 0.f};
#pragma unroll
      for (int e = 0; e < 4; ++e) { const f32x2 d0 = (f32x2){bf2f(c0[e]), bfhi(c0[e])} - mm, d1 = (f32x2){bf2f(c1[e]), bfhi(c1[e])} - mm; q0 += d0 * d0; q1 += d1 * d1; }
      const float var = wave_fsum((q0[0] + q0[1]) + (lane < 16 ? q1[0] + q1[1] : 0.f)) * (1.f / D);
      rs[r] = __builtin_amdgcn_rcpf(rbf(__builtin_amdgcn_sqrtf(rbf(rbf(var) + 1e-5f))));
    }
    WAITV(0);
    if (lane < 2) ST128(((u32x4){__float_as_uint(rbf(lane ? mean[1] : mean[0])), __float_as_uint(lane ? rs[1] : rs[0]), tag2[0], tag2[1]}), WS_ST1 + (unsigned)(64 * R + 8 * cc + 2 * lw + lane) * 16);
  };

  auto lds_ctl_init = [&]() {
    if (tid == 0) { *(LAS unsigned*)(L + L_F1) = 0; *(LAS unsigned*)(L + L_C1) = 0; }
    STAMP_ZERO();
  };
  if (aj < 15 && w >= 4) {
    const int lw = w - 4;
    const unsigned xsw = (unsigned)(4 * (lw & 1) + (lane >> 4));
    const unsigned voff_x = (unsigned)(lane >> 3) * 1280u + (((unsigned)(lane & 7) ^ xsw) << 4);
    const unsigned ch = (unsigned)(lane & 15) ^ (((unsigned)(lane >> 4) << 2) | (unsigned)lw);
    const unsigned voff_w = (unsigned)(lane >> 4) * 3840u + ch * 16u;
    const char* xb = (const char*)p.x + (size_t)(128 * aP + 8 * lw) * 1280;
    const char* wb = (const char*)p.wqkv + (size_t)(4 * lw) * 3840 + 256 * aj;
    const unsigned lbase = lds0 + lw * 1024;
    const int rot = ROT;
    auto issue = [&](int s) {
      const unsigned sl = lbase + (s & 3) * L_SLOT;
      int ss = s + rot; if (ss >= 10) ss -= 10;
#pragma unroll
      for (int i = 0; i < 4; ++i) {
        GL_X(voff_x, xb + (size_t)i * 32 * 1280 + ss * 128, sl + i * 4096);
        GL_W(voff_w, wb + ((size_t)ss * 64 + i * 16) * 3840, sl + L_W + i * 4096);
      }
    };
    BARRIER();
    issue(0); issue(1); issue(2);
    {
      u64 pc; unsigned len;
      asm volatile("s_getpc_b64 %0\nLfk_here:\n\ts_mov_b32 %1, Lfk_code_end-Lfk_here" : "=s"(pc), "=s"(len));
      const char* cb = (const char*)(pc & ~(u64)127);
      const unsigned o0 = (unsigned)(lw * 64 + lane) * 128u;
      const bool on = (bi & 7) == 0;
      touch_ptr(on ? cb + min(o0, len) : xb, lds0 + L_MISC + 8192);
      touch_ptr(on ? cb + min(o0 + 32768u, len) : xb, lds0 + L_MISC + 8192);
    }
    read_epoch();
    WAITV(18); BARRIER();
    PSTAMP(1);
    issue(3); WAITV(18); BARRIER();
    issue(4); WAITV(18); BARRIER();
#pragma unroll 1
    for (int s = 2; s <= 5; ++s) { issue(s + 3); WAITV(16); BARRIER(); }
    issue(9);
    const unsigned so = WS_ST1 + (unsigned)(128 * aP + 32 * lw + l31) * 16, co = WS_CS1 + (unsigned)((aj * 16 + fg) * 64 + 16 * lw + l15) * 16;
    u32x4 gs1 = LD128(so), gc0 = LD128(co), gc1 = LD128(co + 4096), gc2 = LD128(co + 8192), gc3 = LD128(co + 12288);
    WAITV(21); BARRIER();
    WAITV(13); BARRIER();
    WAITV(5); BARRIER();
    unsigned spins = 0;
    for (;;) {
      const bool ok = gs1[2] == tag2[0] && gs1[3] == tag2[1] && gc0[2] == tag2[0] && gc0[3] == tag2[1] && gc1[2] == tag2[0] && gc1[3] == tag2[1] && gc2[2] == tag2[0] && gc2[3] == tag2[1] && gc3[2] == tag2[0] && gc3[3] == tag2[1];
      if (__all(ok) || ++spins > SPIN_CAP) break;
      FENCE();
      gs1 = LD128(so); gc0 = LD128(co); gc1 = LD128(co + 4096); gc2 = LD128(co + 8192); gc3 = LD128(co + 12288);
    }
    if (lane < 32) *(LAS u32x2*)(L + L_RS + (32 * lw + l31) * 8) = (u32x2){gs1[0], gs1[1]};
    {
      const float c0 = swap_add(swap16_add((__uint_as_float(gc0[0]) + __uint_as_float(gc1[0])) + (__uint_as_float(gc2[0]) + __uint_as_float(gc3[0]))));
      const float c1 = swap_add(swap16_add((__uint_as_float(gc0[1]) + __uint_as_float(gc1[1])) + (__uint_as_float(gc2[1]) + __uint_as_float(gc3[1]))));
      if (lane < 16) *(LAS f32x2*)(L + L_CS + (32 * lw + 2 * lane) * 4) = (f32x2){c0, c1};
    }
    WAITL0(); BARRIER();
    BARRIER();
  } else if (w < 4) {
    u32x4 ra0, ra1, rb0, rb1;
    rows_issue(w, ra0, ra1, rb0, rb1);
    if (aj < 15) {
      BARRIER();
      read_epoch();
      rows_finish(w, ra0, ra1, rb0, rb1);
      PSTAMP(7);
      const int wm = w >> 1, wn = w & 1;
      unsigned xa[4], wa[4];
#pragma unroll
      for (int ks = 0; ks < 4; ++ks) xa[ks] = (unsigned)(64 * wm + l31) * 128u + ((((unsigned)(2 * ks + hh)) ^ (((unsigned)l31 >> 1) & 7u)) << 4);
      {
        const unsigned blk = (lane >> 4) & 1, q = (lane & 15) >> 2, pq = lane & 3;
#pragma unroll
        for (int j = 0; j < 2; ++j)
#pragma unroll
          for (int t = 0; t < 2; ++t) {
            const unsigned row = 8 * hh + 4 * t + q, chn = 4 * (2 * wn + (j ^ wm)) + 2 * blk + (pq >> 1), m = (q << 2) | (unsigned)(2 * hh + t);
            wa[j * 2 + t] = L_W + 256u * row + 16u * (chn ^ m) + 8u * (pq & 1);
          }
      }
      f32x16 a00 = {}, a01 = {}, a10 = {}, a11 = {};
      bf16x8 xf0[2], wf0[2], xf1[2], wf1[2];

      unsigned xb[4], wb[4];
#define SETSLOT(so) do { _Pragma("unroll") for (int k_ = 0; k_ < 4; ++k_) { xb[k_] = xa[k_] + (so); wb[k_] = wa[k_] + (so); } } while (0)
#define RD(XF, WF, ks) do { \
        XF[0] = *(const LAS bf16x8*)(L + xb[ks]); WF[0] = cat8(trd(L + wb[0] + (ks) * 4096), trd(L + wb[1] + (ks) * 4096)); \
        WF[1] = cat8(trd(L + wb[2] + (ks) * 4096), trd(L + wb[3] + (ks) * 4096)); XF[1] = *(const LAS bf16x8*)(L + xb[ks] + 4096); } while (0)
#define MM(XF, WF) do { \
        a00 = MFMA32(WF[0], XF[0], a00); a01 = MFMA32(WF[1], XF[0], a01); \
        a10 = MFMA32(WF[0], XF[1], a10); a11 = MFMA32(WF[1], XF[1], a11); } while (0)
#define PINALL() asm volatile("" : "+v"(a00), "+v"(a01), "+v"(a10), "+v"(a11))
#define ILV() do { __builtin_amdgcn_sched_group_barrier(0x8, 1, 0); __builtin_amdgcn_sched_group_barrier(0x100, 3, 0); \
        __builtin_amdgcn_sched_group_barrier(0x8, 1, 0); __builtin_amdgcn_sched_group_barrier(0x100, 3, 0); __builtin_amdgcn_sched_group_barrier(0x8, 2, 0); } while (0)
#define STEP(XN, WN, ksn, XC, WC) do { RD(XN, WN, ksn); MM(XC, WC); ILV(); PINALL(); SBAR(); } while (0)
      BARRIER();
      SETSLOT(0);
      RD(xf0, wf0, 0);
      SBAR();
#pragma unroll 1
      for (int s = 0; s < 9; ++s) {
        STEP(xf1, wf1, 1, xf0, wf0);
        STEP(xf0, wf0, 2, xf1, wf1);
        STEP(xf1, wf1, 3, xf0, wf0);
        SETSLOT(((s + 1) & 3) * L_SLOT);
        WAITL0(); BARRIER();
        STEP(xf0, wf0, 0, xf1, wf1);
      }
      STEP(xf1, wf1, 1, xf0, wf0);
      STEP(xf0, wf0, 2, xf1, wf1);
      STEP(xf1, wf1, 3, xf0, wf0);
      MM(xf1, wf1); PINALL();
      PSTAMP(2);
      WAITL0(); BARRIER();
      PSTAMP(6);
      const float qs = aj < 5 ? 0.22360679774997896f * 1.4426950408889634f : 1.f;
      f32x2 rs0 = *(const LAS f32x2*)(L + L_RS + (64 * wm + l31) * 8), rs1 = *(const LAS f32x2*)(L + L_RS + (64 * wm + 32 + l31) * 8);
      const float sc0 = rs0[1] * qs, nm0 = -rs0[0] * sc0, sc1 = rs1[1] * qs, nm1 = -rs1[0] * sc1;
#pragma unroll
      for (int j = 0; j < 2; ++j) {
        const int fj = 64 * wn + 32 * (j ^ wm) + 4 * hh;
        const unsigned go = WS_QKV + (unsigned)(32 * aj + (fj >> 2)) * 16384u + (unsigned)(128 * aP + 64 * wm + l31) * 8u;
#pragma unroll
        for (int rq = 0; rq < 4; ++rq) {
          const f32x4 cs = *(const LAS f32x4*)(L + L_CS + (fj + 8 * rq) * 4);
          const f32x16& a0 = j == 0 ? a00 : a01; const f32x16& a1 = j == 0 ? a10 : a11;
          const f32x2 c01 = {cs[0], cs[1]}, c23 = {cs[2], cs[3]};
          const f32x2 u0 = (f32x2){a0[rq * 4], a0[rq * 4 + 1]} * sc0 + c01 * nm0, u1 = (f32x2){a0[rq * 4 + 2], a0[rq * 4 + 3]} * sc0 + c23 * nm0;
          const f32x2 u2 = (f32x2){a1[rq * 4], a1[rq * 4 + 1]} * sc1 + c01 * nm1, u3 = (f32x2){a1[rq * 4 + 2], a1[rq * 4 + 3]} * sc1 + c23 * nm1;
          ST64(((u32x2){pk2(u0[0], u0[1]), pk2(u1[0], u1[1])}), go + 2 * rq * 16384);
          ST64(((u32x2){pk2(u2[0], u2[1]), pk2(u3[0], u3[1])}), go + 2 * rq * 16384 + 256);
        }
      }
      PSTAMP(4);
      lds_ctl_init();
      WAITV(0); WAITL0();
      PSTAMP(5);
      BARRIER();
    } else {
      read_epoch();
      rows_finish(w, ra0, ra1, rb0, rb1);
    }
  } else {
    read_epoch();
  }
  if (aj >= 15) {
    const int chn = min(32 * w + l31, 239);
    const char* wp0 = (const char*)p.wqkv + (size_t)(40 * aP + 20 * hh) * 3840 + chn * 16;
    u32x4 wv[20];
#pragma unroll
    for (int i = 0; i < 20; ++i) wv[i] = *(const u32x4*)(wp0 + (size_t)i * 3840);
    SBAR();
    f32x2 c2[4] = {{0.f, 0.f}, {0.f, 0.f}, {0.f, 0.f}, {0.f, 0.f}};
#pragma unroll
    for (int i = 0; i < 20; ++i)
#pragma unroll
      for (int e = 0; e < 4; ++e) c2[e] += (f32x2){bf2f(wv[i][e]), bfhi(wv[i][e])};
#pragma unroll
    for (int e = 0; e < 4; ++e) c2[e] = (f32x2){swap_add(c2[e][0]), swap_add(c2[e][1])};
    if (lane < 32 && 32 * w + l31 < 240) {
#pragma unroll
      for (int e = 0; e < 4; ++e) ST128(((u32x4){__float_as_uint(c2[e][0]), __float_as_uint(c2[e][1]), tag2[0], tag2[1]}), WS_CS1 + (unsigned)(((chn >> 4) * 16 + aP) * 64 + 4 * (chn & 15) + e) * 16);
    }
    lds_ctl_init(); WAITL0(); BARRIER();
  }
  const unsigned tag32 = tag2[0] | 1u;
  if (aj < 15 && tid == 0) ST64(tag2, WS_FLAGA + (aP * 16 + aj) * 128);
  PSTAMP(3);
  STAMP_PA();

  const int nt = w & 3, kh = w >> 2;
  const int tok = 64 * R + 16 * nt + l15;
  u32x2 xr[5], wrv[5];
  unsigned wr5[5];
  {
    const int t = w;
    *(LAS u32x2*)(L + L_V + 5 * PL + tid * 8) = (u32x2){(pp > 0 || tid >= 256) ? 0x00003f80u : 0u, 0u};
    if (pp == 0) {
#pragma unroll
      for (int i = 0; i < 3; ++i) { const int n = tid + 512 * i; if (n < 1280) *(LAS u32x4*)(L + (n >> 7) * PL + (n & 127) * 16) = (u32x4){0, 0, 0, 0}; }
    }
    if (kh == 0) {
#pragma unroll
      for (int ft = 0; ft < 5; ++ft) {
        xr[ft] = *(const u32x2*)(p.x + (size_t)tok * D + 80 * cc + 16 * ft + 4 * fg);
        wrv[ft] = *(const u32x2*)(p.wr + 80 * cc + 16 * ft + 4 * fg);
      }
    }
    if (bi == 31) {
#pragma unroll
      for (int i = 0; i < 5; ++i) wr5[i] = ((const unsigned*)p.wr)[lane + 64 * i];
    }
    f32x16 cdg, can;
    {
      auto diag_ok = [](int r) { unsigned long long m = 0; for (int l = 0; l < 64; ++l) if ((r & 3) + 8 * (r >> 2) + 4 * (l >> 5) <= (l & 31)) m |= 1ull << l; return m; };
#pragma unroll
      for (int r = 0; r < 16; ++r) { const bool ok = __builtin_amdgcn_inverse_ballot_w64(diag_ok(r)); cdg[r] = ok ? 0.f : -INFINITY; can[r] = ok ? -INFINITY : 0.f; }
      asm volatile("" : "+v"(cdg), "+v"(can));
    }
    auto lds_wait = [&](int off, unsigned v) { unsigned sp = 0; while (__hip_atomic_load((LAS unsigned*)(L + off), __ATOMIC_RELAXED, __HIP_MEMORY_SCOPE_WORKGROUP) < v && ++sp < (SPIN_CAP << 4)) __builtin_amdgcn_s_sleep(0); FENCE(); };
    WAITL0();
    if (kh == 0) {
      if (lane == 0) __hip_atomic_fetch_add((LAS unsigned*)(L + L_C1), 1u, __ATOMIC_RELAXED, __HIP_MEMORY_SCOPE_WORKGROUP);
      lds_wait(L_F1, 1);
    } else {
      if (w == 4) {
        const int sec = lane < 4 ? 0 : (lane < 12 ? 1 : 2);
        const int k = lane < 4 ? lane : (lane < 12 ? lane - 4 : lane - 12);
        const int col = sec * 640 + 20 * hd + ((k & 1) ? 19 : 0);
        const int Pn = sec == 0 ? 2 * pp + (k >> 1) : 2 * pp - 2 + (k >> 1);
        const bool need = lane < 20 && Pn >= 0;
        const unsigned fo = WS_FLAGA + (unsigned)((need ? Pn : 2 * pp) * 16 + (col >> 7)) * 128;
        unsigned spins = 0;
        for (;;) {
          FENCE();
          const u32x2 f = LD64(fo);
          if (__all(!need || (f[0] == tag2[0] && f[1] == tag2[1])) || ++spins > SPIN_CAP) break;
          __builtin_amdgcn_s_sleep(1);
        }
        if (lane == 0) __hip_atomic_store((LAS unsigned*)(L + L_F1), 1u, __ATOMIC_RELAXED, __HIP_MEMORY_SCOPE_WORKGROUP);
        STAMP(4);
      } else lds_wait(L_F1, 1);
      const char* qb = (const char*)p.ws + WS_QKV;
#pragma unroll
      for (int i = 0; i < 10; ++i) {
        const int pc = nt + 4 * i, pl = pc >> 2, qt = pc & 3;
        const int tok0 = 256 * pp - 256 + 128 * qt;
        if (tok0 >= 0) glds16c(lane * 16, qb + (size_t)((pl < 5 ? 160 + 5 * hd + pl : 320 + 5 * hd + pl - 5)) * 16384 + tok0 * 8, lds0 + pl * PL + qt * 1024);
      }
    }
    bf16x8 qf0; bf16x4 qf1;
    {
      const unsigned qo = WS_QKV + (unsigned)(5 * hd) * 16384u + (unsigned)(256 * pp + 32 * t + l31) * 8u;
      const u32x2 a = LD64(qo + (2 * hh) * 16384), c = LD64(qo + (2 * hh + 1) * 16384);
      u32x2 z = LD64(qo + 4 * 16384);
      asm volatile("s_waitcnt vmcnt(0)" : "+v"(z) :: "memory");
      if (hh) z = (u32x2){0, 0};
      qf0 = __builtin_bit_cast(bf16x8, (u32x4){a[0], a[1], c[0], c[1]});
      qf1 = __builtin_bit_cast(bf16x4, z);
    }
    if (kh == 1 && lane == 0) __hip_atomic_fetch_add((LAS unsigned*)(L + L_C1), 1u, __ATOMIC_RELAXED, __HIP_MEMORY_SCOPE_WORKGROUP);
    lds_wait(L_C1, 8);
    if (w == 4) STAMP(5);
    unsigned wov[5];
#pragma unroll
    for (int j = 0; j < 5; ++j) { const int n = 64 * j + lane, rho = (n * 6554) >> 16, chn = n - 10 * rho; wov[j] = (unsigned)(8 * ((rho >> 2) & 3) + 4 * (rho >> 4) + (rho & 3)) * 1280u + chn * 16u; }
    const char* wob = (const char*)p.wo + 160 * cc + (size_t)nt * 32 * 1280;
    const unsigned wol = lds0 + L_WO + nt * 5120;
#define WOP(i) GL_WO(wov[(i) % 5], wob + (size_t)((i) / 5) * 4 * 32 * 1280, wol + ((i) / 5) * 4 * 5120 + ((i) % 5) * 1024)
#define WOP3(i) do { if (kh == 0) { WOP(i); WOP((i) + 1); WOP((i) + 2); } SBAR(); } while (0)
    if (w == 0) STAMP(28);
    const LAS char* kb = L + L_K + l31 * 8 + (2 * hh) * PL;
    const LAS char* kb4 = L + (hh ? L_V + 5 * PL : L_K + 4 * PL) + l31 * 8;
    {
      const u32x2 k0 = *(const LAS u32x2*)(kb + (t + 8) * 256), k1 = *(const LAS u32x2*)(kb + (t + 8) * 256 + PL), k2 = *(const LAS u32x2*)(kb4 + (t + 8) * 256);
      const u32x4 q0 = __builtin_bit_cast(u32x4, qf0); u32x2 q1 = __builtin_bit_cast(u32x2, qf1);
      float d = dot2(q0[0], k0[0], 0.f); d = dot2(q0[1], k0[1], d); d = dot2(q0[2], k1[0], d); d = dot2(q0[3], k1[1], d);
      d = dot2(q1[0], k2[0], d); d = dot2(q1[1], k2[1], d);
      const unsigned nm = pk2(-swap_add(d), 0.f);
      if (hh) q1[0] = nm;
      qf1 = __builtin_bit_cast(bf16x4, q1);
    }
    const LAS char* vb = L + L_V + min(4 * ((lane >> 4) & 1) + (lane & 3), 5) * PL + (4 * hh + ((lane & 15) >> 2)) * 8;
    f32x16 o = (f32x16){};
    {
      f32x16 sa, sb;
      const f32x16 cz = {};
      bf16x4 ka, kc, kd;
      bf16x8 vq = {};
      u32x4 pq = {0, 0, 0, 0};
#define KRD(kt) do { ka = *(const LAS bf16x4*)(kb + (kt) * 256); kc = *(const LAS bf16x4*)(kb + (kt) * 256 + PL); kd = *(const LAS bf16x4*)(kb4 + (kt) * 256); } while (0)
#define EX2(S, i) pk2(__builtin_amdgcn_exp2f(S[i]), __builtin_amdgcn_exp2f(S[(i) + 1]))
#define GRP(nds, nv) do { __builtin_amdgcn_sched_group_barrier(0x8, 1, 0); if (nds) __builtin_amdgcn_sched_group_barrier(0x100, nds, 0); __builtin_amdgcn_sched_group_barrier(0x2, nv, 0); } while (0)
#define HALF(SC, SN, C, ktv, ktk) do { \
        u32x4 p0_; const LAS char* vp_ = vb + (ktv) * 256; \
        SN = MFMA32(cat8(ka, kc), qf0, C); \
        const bf16x8 v0_ = cat8(trd(vp_), trd(vp_ + 64)), v1_ = cat8(trd(vp_ + 128), trd(vp_ + 192)); \
        p0_[0] = EX2(SC, 0); p0_[1] = EX2(SC, 2); \
        GRP(4, 6); asm volatile("" : "+v"(SN), "+v"(p0_[0]), "+v"(p0_[1])); SBAR(); \
        o = MFMA32(vq, __builtin_bit_cast(bf16x8, pq), o); \
        p0_[2] = EX2(SC, 4); p0_[3] = EX2(SC, 6); \
        GRP(0, 6); asm volatile("" : "+v"(o), "+v"(p0_[2]), "+v"(p0_[3])); SBAR(); \
        SN = MFMA32K8(kd, qf1, SN); \
        KRD(ktk); \
        pq[0] = EX2(SC, 8); pq[1] = EX2(SC, 10); \
        GRP(3, 6); asm volatile("" : "+v"(SN), "+v"(pq[0]), "+v"(pq[1])); SBAR(); \
        o = MFMA32(v0_, __builtin_bit_cast(bf16x8, p0_), o); \
        pq[2] = EX2(SC, 12); pq[3] = EX2(SC, 14); vq = v1_; \
        GRP(0, 6); asm volatile("" : "+v"(o), "+v"(pq[2]), "+v"(pq[3])); SBAR(); } while (0)
      KRD(t + 8);
      sa = MFMA32(cat8(ka, kc), qf0, cdg); sa = MFMA32K8(kd, qf1, sa);
      KRD(t + 7);
      asm volatile("" : "+v"(sa)); SBAR();
      WOP3(0);
      HALF(sa, sb, cz, t + 8, t + 6);
      WOP3(3);
      HALF(sb, sa, cz, t + 7, t + 5);
      WOP3(6);
      HALF(sa, sb, cz, t + 6, t + 4);
      WOP3(9);
      HALF(sb, sa, cz, t + 5, t + 3);
      WOP3(12);
      HALF(sa, sb, cz, t + 4, t + 2);
      WOP3(15);
      HALF(sb, sa, cz, t + 3, t + 1);
      WOP3(18);
      HALF(sa, sb, cz, t + 2, t);
      if (kh == 0) { WOP(21); WOP(22); WOP(23); WOP(24); }
      SBAR();
      HALF(sb, sa, can, t + 1, t);
      {
        const LAS char* vp_ = vb + t * 256;
        const bf16x8 v0_ = cat8(trd(vp_), trd(vp_ + 64)), v1_ = cat8(trd(vp_ + 128), trd(vp_ + 192));
        o = MFMA32(vq, __builtin_bit_cast(bf16x8, pq), o);
        u32x4 p0_, p1_;
#pragma unroll
        for (int e = 0; e < 4; ++e) p0_[e] = EX2(sa, 2 * e);
        o = MFMA32(v0_, __builtin_bit_cast(bf16x8, p0_), o);
#pragma unroll
        for (int e = 0; e < 4; ++e) p1_[e] = EX2(sa, 8 + 2 * e);
        o = MFMA32(v1_, __builtin_bit_cast(bf16x8, p1_), o);
      }
    }
    float lsum;
    { auto r = __builtin_amdgcn_permlane32_swap(__float_as_uint(o[8]), __float_as_uint(o[8]), false, false); lsum = __uint_as_float(r[1]); }
    if (__builtin_expect(__any(!(lsum < 1.0995116e12f)), 0)) {
      const int lo = pp >= 1 ? t : 8;
      float mx = -INFINITY;
#pragma unroll 1
      for (int kt = t + 8; kt >= lo; --kt) {
        const bf16x4 ka = *(const LAS bf16x4*)(kb + kt * 256), kc = *(const LAS bf16x4*)(kb + kt * 256 + PL), kd = *(const LAS bf16x4*)(kb4 + kt * 256);
        f32x16 s = MFMA32(cat8(ka, kc), qf0, (f32x16){});
        s = MFMA32K8(kd, qf1, s);
#pragma unroll
        for (int r = 0; r < 16; ++r) { const int kr = (r & 3) + 8 * (r >> 2) + 4 * hh; const bool ok = kt == t + 8 ? kr <= l31 : (kt == t ? kr > l31 : true); mx = fmaxf(mx, ok ? s[r] : -INFINITY); }
      }
      { auto r = __builtin_amdgcn_permlane32_swap(__float_as_uint(mx), __float_as_uint(mx), false, false); mx = fmaxf(__uint_as_float(r[0]), __uint_as_float(r[1])); }
      o = (f32x16){};
#pragma unroll 1
      for (int kt = t + 8; kt >= lo; --kt) {
        const bf16x4 ka = *(const LAS bf16x4*)(kb + kt * 256), kc = *(const LAS bf16x4*)(kb + kt * 256 + PL), kd = *(const LAS bf16x4*)(kb4 + kt * 256);
        const LAS char* vp_ = vb + kt * 256;
        const bf16x8 v0_ = cat8(trd(vp_), trd(vp_ + 64)), v1_ = cat8(trd(vp_ + 128), trd(vp_ + 192));
        f32x16 s = MFMA32(cat8(ka, kc), qf0, (f32x16){});
        s = MFMA32K8(kd, qf1, s);
        u32x4 p0_, p1_;
#pragma unroll
        for (int r = 0; r < 16; ++r) { const int kr = (r & 3) + 8 * (r >> 2) + 4 * hh; const bool ok = kt == t + 8 ? kr <= l31 : (kt == t ? kr > l31 : true); s[r] = ok ? __builtin_amdgcn_exp2f(s[r] - mx) : 0.f; }
#pragma unroll
        for (int e = 0; e < 4; ++e) { p0_[e] = pk2(s[2 * e], s[2 * e + 1]); p1_[e] = pk2(s[8 + 2 * e], s[8 + 2 * e + 1]); }
        o = MFMA32(v0_, __builtin_bit_cast(bf16x8, p0_), o);
        o = MFMA32(v1_, __builtin_bit_cast(bf16x8, p1_), o);
      }
      auto r = __builtin_amdgcn_permlane32_swap(__float_as_uint(o[8]), __float_as_uint(o[8]), false, false); lsum = __uint_as_float(r[1]);
    }
    if (w == 0) STAMP(6);
    STAMP(32 + w);
    {
      const float inv = 1.f / lsum;
      unsigned A0 = pk2(o[0] * inv, o[1] * inv), A1 = pk2(o[2] * inv, o[3] * inv), B0 = pk2(o[4] * inv, o[5] * inv), B1 = pk2(o[6] * inv, o[7] * inv);
      unsigned C0 = pk2(o[8] * inv, o[9] * inv), C1 = pk2(o[10] * inv, o[11] * inv);
      const int tt = pp * 16 + 2 * t + (l31 >> 4);
      const unsigned base = WS_O + (unsigned)tt * 80 * 256 + (l31 & 15) * 16 + (unsigned)(5 * (hd >> 1)) * 256;
      if ((hd & 1) == 0) {
        auto s0 = __builtin_amdgcn_permlane32_swap(A0, B0, false, false), s1 = __builtin_amdgcn_permlane32_swap(A1, B1, false, false);
        ST128(((u32x4){s0[0], s1[0], s0[1], s1[1]}), base + hh * 256);
        if (hh == 0) ST64(((u32x2){C0, C1}), base + 2 * 256);
      } else {
        auto u0 = __builtin_amdgcn_permlane32_swap(A0, C0, false, false), u1 = __builtin_amdgcn_permlane32_swap(A1, C1, false, false);
        const u32x4 v = hh ? (u32x4){B0, B1, u0[0], u1[0]} : (u32x4){u0[1], u1[1], B0, B1};
        ST128(v, base + (3 + hh) * 256);
        if (hh == 0) ST64(((u32x2){A0, A1}), base + 2 * 256 + 8);
      }
      WAITV(0);
      if (lane == 0) ST64(tag2, WS_FLAGO + (((pp * 8 + t) * 2 + (hd >> 4)) * 16 + (hd & 15)) * 8);
    }
  }
  if (w == 0) STAMP(7);
  STAMP(40 + w);

  BARRIER();
  if (w == 0) STAMP(8);

  f32x4 acc[5];
#pragma unroll
  for (int ft = 0; ft < 5; ++ft) acc[ft] = (f32x4){0, 0, 0, 0};
  {
    const LAS char* wp = L + L_WO + (unsigned)(kh * 320 + 4 * fg + ((lane & 15) >> 2)) * 160 + (lane & 3) * 8;
    bf16x8 wa[3][5];
#pragma unroll
    for (int s = 0; s < 3; ++s)
#pragma unroll
      for (int ft = 0; ft < 5; ++ft) wa[s][ft] = cat8(trd(wp + s * 5120 + ft * 32), trd(wp + s * 5120 + 2560 + ft * 32));
    const int tg = 4 * R + nt;
    const unsigned fo = WS_FLAGO + ((((tg >> 4) * 8 + ((tg >> 1) & 7)) * 2 + kh) * 16 + l15) * 8;
    unsigned spins = 0;
    for (;;) {
      FENCE();
      const u32x2 f = LD64(fo);
      if (__all(f[0] == tag2[0] && f[1] == tag2[1]) || ++spins > SPIN_CAP) break;
      __builtin_amdgcn_s_sleep(1);
    }
    FENCE();
    if (w == 0) STAMP(9);
    STAMP(48 + w);
    u32x4 of[10];
#pragma unroll
    for (int s = 0; s < 10; ++s) of[s] = LD128(WS_O + (unsigned)(tg * 80 + 4 * (10 * kh + s)) * 256 + lane * 16);
    SBAR();
#pragma unroll
    for (int s = 0; s < 10; ++s) {
      const bf16x8 bfr = __builtin_bit_cast(bf16x8, of[s]);
#pragma unroll
      for (int ft = 0; ft < 5; ++ft) acc[ft] = MFMA16(s < 3 ? wa[s < 3 ? s : 0][ft] : cat8(trd(wp + s * 5120 + ft * 32), trd(wp + s * 5120 + 2560 + ft * 32)), bfr, acc[ft]);
    }
  }
  STAMP(56 + w);
  if (kh == 1) {
#pragma unroll
    for (int ft = 0; ft < 5; ++ft) *(LAS f32x4*)(L + L_RED + ((nt * 5 + ft) * 64 + lane) * 16) = acc[ft];
  }
  WAITL0(); BARRIER();
  if (w == 0) STAMP(10);
  if (kh == 0) {
    float s1 = 0.f, s2 = 0.f, s3 = 0.f;
#pragma unroll
    for (int ft = 0; ft < 5; ++ft) {
      const f32x4 o2 = *(const LAS f32x4*)(L + L_RED + ((nt * 5 + ft) * 64 + lane) * 16);
      const unsigned a01 = pk2(acc[ft][0] + o2[0], acc[ft][1] + o2[1]), a23 = pk2(acc[ft][2] + o2[2], acc[ft][3] + o2[3]);
      const unsigned h01 = pk2(bf2f(xr[ft][0]) + bf2f(a01), bfhi(xr[ft][0]) + bfhi(a01)), h23 = pk2(bf2f(xr[ft][1]) + bf2f(a23), bfhi(xr[ft][1]) + bfhi(a23));
      *(LAS u32x2*)(L + L_HST + nt * 2560 + l15 * 160 + ft * 32 + fg * 8) = (u32x2){h01, h23};
      s1 = dot2(h01, 0x3f803f80u, s1); s1 = dot2(h23, 0x3f803f80u, s1);
      s2 = dot2(h01, h01, s2); s2 = dot2(h23, h23, s2);
      s3 = dot2(h01, wrv[ft][0], s3); s3 = dot2(h23, wrv[ft][1], s3);
    }
    WAITL0();
#pragma unroll
    for (int i = 0; i < 3; ++i) {
      const int n = lane + 64 * i;
      if (n < 160) ST128(*(const LAS u32x4*)(L + L_HST + nt * 2560 + n * 16), WS_H + (unsigned)((R * 8 + cc) * 64 + 16 * nt) * 160 + n * 16);
    }
    s1 = swap_add(swap16_add(s1)); s2 = swap_add(swap16_add(s2)); s3 = swap_add(swap16_add(s3));
    WAITV(0);
    if (lane < 16) ST128(((u32x4){__float_as_uint(s1), __float_as_uint(s2), __float_as_uint(s3), tag32}), WS_STATS + ((R * 8 + cc) * 64 + 16 * nt + l15) * 16);
    if (w == 0) STAMP(11);
    BARRIER();
  } else {
    BARRIER();
    const int rl = lane >> 1, grow = 8 * ((rl >> 2) & 3) + 4 * (rl >> 4) + (rl & 3);
    if (is_gu) {
      const unsigned vo = (unsigned)grow * (F * 2) + (lane & 1) * 16;
#pragma unroll 1
      for (int pc = nt; pc < 40; pc += 4) { const int m = pc >= 20, s = pc - 20 * m; GL_FW(vo, (const char*)(m ? p.wu : p.wg) + (size_t)s * 32 * F * 2 + gs * 32, lds0 + L_FW + pc * 1024); }
    } else if (is_dn) {
      const unsigned vo = (unsigned)grow * (D * 2) + (lane & 1) * 16;
#pragma unroll 1
      for (int pc = nt; pc < 54; pc += 4) GL_FW(vo, (const char*)p.wd + (size_t)pc * 32 * D * 2 + dn * 32, lds0 + L_FW + pc * 1024);
    }
  }
  auto rank64 = [&](unsigned k) {
    const u32x4 oa = *(const LAS u32x4*)(L + L_KEY + w * 32), ob = *(const LAS u32x4*)(L + L_KEY + w * 32 + 16);
    unsigned c = 0;
#pragma unroll
    for (int e = 0; e < 4; ++e) c += (oa[e] > k) + (ob[e] > k);
    *(LAS unsigned char*)(L + L_PART + lane * 8 + w) = (unsigned char)c;
    WAITL0(); BARRIER();
    const u32x2 pr = *(const LAS u32x2*)(L + L_PART + lane * 8);
    return __builtin_amdgcn_sad_u8(pr[0], 0u, __builtin_amdgcn_sad_u8(pr[1], 0u, 0u));
  };
  if (bi == 31) {
    float swr = 0.f;
#pragma unroll
    for (int i = 0; i < 5; ++i) swr = dot2(wr5[i], 0x3f803f80u, swr);
    swr = wave_fsum(swr);
    const int tl = 32 * w + l31;
    const unsigned so = WS_STATS + (unsigned)(((4 * xcd + (tl >> 6)) * 8 + 4 * hh) * 64 + (tl & 63)) * 16;
    float t1, t2, t3;
    unsigned spins = 0;
    for (;;) {
      FENCE();
      const u32x4 g0 = LD128(so), g1 = LD128(so + 1024), g2 = LD128(so + 2048), g3 = LD128(so + 3072);
      t1 = (__uint_as_float(g0[0]) + __uint_as_float(g1[0])) + (__uint_as_float(g2[0]) + __uint_as_float(g3[0]));
      t2 = (__uint_as_float(g0[1]) + __uint_as_float(g1[1])) + (__uint_as_float(g2[1]) + __uint_as_float(g3[1]));
      t3 = (__uint_as_float(g0[2]) + __uint_as_float(g1[2])) + (__uint_as_float(g2[2]) + __uint_as_float(g3[2]));
      if (__all(g0[3] == tag32 && g1[3] == tag32 && g2[3] == tag32 && g3[3] == tag32) || ++spins > SPIN_CAP) break;
    }
    t1 = swap_add(t1); t2 = swap_add(t2); t3 = swap_add(t3);
    const float mean = t1 * (1.f / D);
    const float var = fmaxf(t2 * (1.f / D) - mean * mean, 0.f);
    const float mb = rbf(mean);
    const float sd = rbf(__builtin_amdgcn_sqrtf(rbf(rbf(var) + 1e-5f)));
    const unsigned rb = pk2((t3 - mb * swr) * __builtin_amdgcn_rcpf(sd), 0.f) & 0xffffu;
    const unsigned key = ((rb ^ ((rb & 0x8000u) ? 0xffffu : 0x8000u)) << 16) | (unsigned)(2047 - (256 * xcd + tl));
    const unsigned ms = (__float_as_uint(mb) & 0xffff0000u) | (__float_as_uint(sd) >> 16);
    if (w == 0) STAMP(12);
    if (lane < 32) *(LAS unsigned*)(L + L_K32 + tl * 4) = key;
    WAITL0();
    unsigned c = 0;
#pragma unroll
    for (int i = 0; i < 4; ++i) {
      const u32x4 o4 = *(const LAS u32x4*)(L + L_K32 + (32 * w + 16 * hh + 4 * i) * 4);
#pragma unroll
      for (int e = 0; e < 4; ++e) c += o4[e] > key;
    }
    { auto r = __builtin_amdgcn_permlane32_swap(c, c, false, false); c = r[0] + r[1]; }
    if (lane < 32 && c < 8) { *(LAS unsigned*)(L + L_KEY + (8 * w + c) * 4) = key; *(LAS unsigned*)(L + L_KEY + 256 + (8 * w + c) * 4) = ms; }
    WAITL0(); BARRIER();
    const unsigned k = *(const LAS unsigned*)(L + L_KEY + lane * 4), m2 = *(const LAS unsigned*)(L + L_KEY + 256 + lane * 4);
    const unsigned rk = rank64(k);
    if (w == 0) { if (rk < 8) ST128(((u32x4){k, tag2[0], m2, tag2[1]}), WS_CAND + xcd * 128 + rk * 16); STAMP(26); }
    BARRIER();
  }
  if (w == 0) {
    u32x4 a;
    unsigned spins = 0;
    for (;;) {
      FENCE();
      a = LD128(WS_CAND + lane * 16);
      if (__all(a[1] == tag2[0] && a[3] == tag2[1]) || ++spins > SPIN_CAP) break;
      if (is_gu || is_dn) __builtin_amdgcn_s_sleep(1); else __builtin_amdgcn_s_sleep(8);
    }
    STAMP(13);
    *(LAS unsigned*)(L + L_KEY + lane * 4) = a[0]; *(LAS unsigned*)(L + L_KEY + 256 + lane * 4) = a[2];
    if (b == 0 && lane == 0) ST64(tag2, WS_EPOCH);
  }
  const int hc0 = (lane * 6554) >> 16, hc1 = ((64 + l15) * 6554) >> 16;
  const unsigned ho0 = (unsigned)hc0 * (64 * 160) + (lane - 10 * hc0) * 16, ho1 = (unsigned)hc1 * (64 * 160) + (64 + l15 - 10 * hc1) * 16;
  if (kh == 1) WAITV(0);
  WAITL0(); BARRIER();
  {
    const unsigned k = *(const LAS unsigned*)(L + L_KEY + lane * 4), m2 = *(const LAS unsigned*)(L + L_KEY + 256 + lane * 4);
    const unsigned rk = rank64(k);
    if (rk < 8) *(LAS u32x2*)(L + L_SEL + rk * 8) = (u32x2){k, m2};
    WAITL0();
  }
  if (w == 0) STAMP(14);

  auto h_row = [&](int row, u32x4& h0, u32x4& h1) {
    const unsigned rb = WS_H + (unsigned)((row >> 6) * 8 * 64 + (row & 63)) * 160;
    h0 = LD128(rb + ho0); h1 = LD128(rb + ho1);
  };
  auto ln8 = [&](const u32x4& h, float m, float iv) { u32x4 n;
#pragma unroll
    for (int e = 0; e < 4; ++e) { const unsigned d = pk2(bf2f(h[e]) - m, bfhi(h[e]) - m); n[e] = pk2(bf2f(d) * iv, bfhi(d) * iv); } return n; };
  auto out_row = [&]() {
    const int row = 64 * R + 8 * cc + w;
    const u32x2 sk = *(const LAS u32x2*)(L + L_SEL + (lane & 7) * 8);
    if (!__any(2047 - (int)(sk[0] & 0x7ff) == row)) {
      u32x4 h0, h1; h_row(row, h0, h1);
      const u32x4 g = LD128(WS_STATS + (unsigned)((R * 8 + (lane & 7)) * 64 + 8 * cc + w) * 16);
      float t1 = __uint_as_float(g[0]), t2 = __uint_as_float(g[1]);
      t1 = dppadd<0x141>(t1); t2 = dppadd<0x141>(t2);
      t1 = dppadd<0xb1>(t1); t2 = dppadd<0xb1>(t2);
      t1 = dppadd<0x4e>(t1); t2 = dppadd<0x4e>(t2);
      const float mean = t1 * (1.f / D);
      const float m = rbf(mean), iv = __builtin_amdgcn_rcpf(rbf(__builtin_amdgcn_sqrtf(rbf(rbf(fmaxf(t2 * (1.f / D) - mean * mean, 0.f)) + 1e-5f))));
      const u32x4 n0 = ln8(h0, m, iv), n1 = ln8(h1, m, iv);
      u32x4 r0, r1;
#pragma unroll
      for (int e = 0; e < 4; ++e) { r0[e] = pk2(bf2f(h0[e]) + bf2f(n0[e]), bfhi(h0[e]) + bfhi(n0[e])); r1[e] = pk2(bf2f(h1[e]) + bf2f(n1[e]), bfhi(h1[e]) + bfhi(n1[e])); }
      *(u32x4*)(p.out + (size_t)row * D + lane * 8) = r0;
      if (lane < 16) *(u32x4*)(p.out + (size_t)row * D + 512 + lane * 8) = r1;
    }
  };
  if (is_gu) {
    {
      const u32x2 sk = *(const LAS u32x2*)(L + L_SEL + w * 8);
      u32x4 h0, h1; h_row(2047 - (int)(sk[0] & 0x7ff), h0, h1);
      const float m = __uint_as_float(sk[1] & 0xffff0000u), iv = __builtin_amdgcn_rcpf(__uint_as_float(sk[1] << 16));
      *(LAS u32x4*)(L + L_XS + w * 1296 + lane * 16) = ln8(h0, m, iv);
      if (lane < 16) *(LAS u32x4*)(L + L_XS + w * 1296 + 1024 + lane * 16) = ln8(h1, m, iv);
    }
    WAITL0(); BARRIER();
    if (w == 0) STAMP(15);
    {
      const int m = w & 1, kq = w >> 1;
      f32x4 a4 = {0, 0, 0, 0}, b4 = {0, 0, 0, 0};
#pragma unroll
      for (int i = 0; i < 5; ++i) {
        const int s = 5 * kq + i;
        const LAS char* ap = L + L_FW + (m * 20 + s) * 1024 + lane * 8;
        const bf16x8 af = cat8(trd(ap), trd(ap + 512));
        const bf16x8 bfv = *(const LAS bf16x8*)(L + L_XS + (lane & 7) * 1296 + s * 64 + fg * 16);
        if (i & 1) b4 = MFMA16(af, bfv, b4); else a4 = MFMA16(af, bfv, a4);
      }
      *(LAS f32x4*)(L + L_FRED + (w * 64 + lane) * 16) = a4 + b4;
    }
    WAITL0(); BARRIER();
    if (w == 0) {
      f32x4 g4 = {0, 0, 0, 0}, u4 = {0, 0, 0, 0};
#pragma unroll
      for (int q = 0; q < 4; ++q) { g4 += *(const LAS f32x4*)(L + L_FRED + ((2 * q) * 64 + lane) * 16); u4 += *(const LAS f32x4*)(L + L_FRED + ((2 * q + 1) * 64 + lane) * 16); }
      float a[4];
#pragma unroll
      for (int e = 0; e < 4; ++e) { const float g = rbf(g4[e]), u = rbf(u4[e]); const float sg = rbf(g * __builtin_amdgcn_rcpf(1.f + __builtin_amdgcn_exp2f(g * -1.4426950408889634f))); a[e] = sg * u; }
      if (l15 < 8) ST128(((u32x4){pk2(a[0], a[1]), tag32, pk2(a[2], a[3]), tag32}), WS_ACT + (((gs >> 1) * 4 + 2 * (gs & 1) + (fg >> 1)) * 8 + l15) * 32 + (fg & 1) * 16);
      STAMP(16);
    }
    out_row();
  } else if (is_dn) {
    const u32x2 sk = *(const LAS u32x2*)(L + L_SEL + (lane & 7) * 8);
    const int row = 2047 - (int)(sk[0] & 0x7ff);
    u32x2 hv = {0, 0}, nv = {0, 0};
    float sig = 0.f;
    if (w == 0) {
      const int col = 16 * dn + 4 * fg, c = col / 80;
      hv = LD64(WS_H + (unsigned)(((row >> 6) * 8 + c) * 64 + (row & 63)) * 160 + (col - 80 * c) * 2);
      const unsigned rk = sk[0] >> 16, rbits = (rk & 0x8000u) ? (rk ^ 0x8000u) : (rk ^ 0xffffu);
      sig = rbf(__builtin_amdgcn_rcpf(1.f + __builtin_amdgcn_exp2f(bf2f(rbits) * -1.4426950408889634f)));
      const float m = __uint_as_float(sk[1] & 0xffff0000u), iv = __builtin_amdgcn_rcpf(__uint_as_float(sk[1] << 16));
#pragma unroll
      for (int e = 0; e < 2; ++e) { const unsigned d2 = pk2(bf2f(hv[e]) - m, bfhi(hv[e]) - m); nv[e] = pk2(bf2f(d2) * iv, bfhi(d2) * iv); }
      asm volatile("" : "+v"(nv), "+v"(hv), "+v"(sig));
    } else out_row();
    f32x4 a4 = {0, 0, 0, 0}, b4 = {0, 0, 0, 0};
    {
      bf16x8 af[7];
#pragma unroll
      for (int i = 0; i < 7; ++i) { const LAS char* ap = L + L_FW + min(w + 8 * i, 53) * 1024 + lane * 8; af[i] = cat8(trd(ap), trd(ap + 512)); }
      u32x4 g0[7];
      unsigned spins = 0;
      for (;;) {
        FENCE();
        bool ok = true;
#pragma unroll
        for (int i = 0; i < 7; ++i) g0[i] = LD128(WS_ACT + ((min(w + 8 * i, 53) * 4 + fg) * 8 + (lane & 7)) * 32 + ((lane >> 3) & 1) * 16);
#pragma unroll
        for (int i = 0; i < 7; ++i) ok = ok && g0[i][1] == tag32 && g0[i][3] == tag32;
        if (__all(ok) || ++spins > SPIN_CAP) break;
      }
      if (w == 0) STAMP(17);
#pragma unroll
      for (int i = 0; i < 7; ++i) {
        const unsigned y0 = (unsigned)__builtin_amdgcn_update_dpp(0, (int)g0[i][0], 0x128, 0xf, 0xf, false), y1 = (unsigned)__builtin_amdgcn_update_dpp(0, (int)g0[i][2], 0x128, 0xf, 0xf, false);
        const bf16x8 bv = __builtin_bit_cast(bf16x8, (u32x4){g0[i][0], g0[i][2], y0, y1});
        if (i < 6 || w < 6) { if (i & 1) b4 = MFMA16(af[i], bv, b4); else a4 = MFMA16(af[i], bv, a4); }
      }
    }
    *(LAS f32x4*)(L + L_FRED + (w * 64 + lane) * 16) = a4 + b4;
    WAITL0(); BARRIER();
    if (w == 0) {
      f32x4 y = {0, 0, 0, 0};
#pragma unroll
      for (int q = 0; q < 8; ++q) y += *(const LAS f32x4*)(L + L_FRED + (q * 64 + lane) * 16);
      unsigned r2[2];
#pragma unroll
      for (int e = 0; e < 2; ++e) {
        const unsigned y2 = pk2(rbf(y[2 * e]) * sig, rbf(y[2 * e + 1]) * sig);
        const unsigned t2 = pk2(bf2f(nv[e]) + bf2f(y2), bfhi(nv[e]) + bfhi(y2));
        r2[e] = pk2(bf2f(hv[e]) + bf2f(t2), bfhi(hv[e]) + bfhi(t2));
      }
      if (l15 < 8) *(u32x2*)(p.out + (size_t)row * D + 16 * dn + 4 * fg) = (u32x2){r2[0], r2[1]};
      STAMP(18);
      out_row();
    }
  } else {
    out_row();
  }
  if (w == 0) STAMP(19);
  asm volatile("Lfk_code_end:");
  STAMP_FLUSH();
}

extern "C" void kernel_launch(void* const* d_in, const int* in_sizes, int n_in, void* d_out, int out_size, void* d_ws, size_t ws_size, hipStream_t stream) {
  Params p;
  p.x = (const u16*)d_in[0]; p.wqkv = (const u16*)d_in[1]; p.wo = (const u16*)d_in[2]; p.wr = (const u16*)d_in[4];
  p.wg = (const u16*)d_in[5]; p.wu = (const u16*)d_in[6]; p.wd = (const u16*)d_in[7]; p.out = (u16*)d_out; p.ws = (unsigned char*)d_ws + ((ws_size - (16u << 20)) & ~(size_t)4095);
  fused_kernel<<<256, 512, 0, stream>>>(p);
}
```

```cpp
#include <hip/hip_runtime.h>
#include <stdint.h>

typedef unsigned short u16;
typedef short bf16x8 __attribute__((ext_vector_type(8)));
typedef short bf16x4 __attribute__((ext_vector_type(4)));
typedef __bf16 bf2 __attribute__((ext_vector_type(2)));
typedef float f32x16 __attribute__((ext_vector_type(16)));
typedef float f32x4 __attribute__((ext_vector_type(4)));
typedef float f32x2 __attribute__((ext_vector_type(2)));
typedef unsigned u32x4 __attribute__((ext_vector_type(4)));
typedef unsigned u32x2 __attribute__((ext_vector_type(2)));
typedef unsigned long long u64;
#define LAS __attribute__((address_space(3)))
#define SBAR() __builtin_amdgcn_sched_barrier(0)
#define BARRIER() asm volatile("s_barrier" ::: "memory")
#define WAITL0() asm volatile("s_waitcnt lgkmcnt(0)" ::: "memory")
#define WAITV(n) asm volatile("s_waitcnt vmcnt(" #n ")" ::: "memory")
#define FENCE() asm volatile("" ::: "memory")

constexpr int S = 2048, D = 640, F = 1728;
constexpr int SC1 = 16;

constexpr unsigned WS_EPOCH = 0;
constexpr unsigned WS_FLAGA = 4096;
constexpr unsigned WS_FLAGO = WS_FLAGA + 32768;
constexpr unsigned WS_CAND = WS_FLAGO + 16384;
constexpr unsigned WS_HDONE = WS_CAND + 4096;
constexpr unsigned WS_ACT = WS_HDONE + 4096;
constexpr unsigned WS_ST1 = 131072;
constexpr unsigned WS_CS1 = WS_ST1 + 32768;
constexpr unsigned WS_STATS = 524288;
constexpr unsigned WS_QKV = 1048576;
constexpr unsigned WS_O = WS_QKV + 480 * 16384;
constexpr unsigned WS_H = WS_O + S * D * 2;
#define WS_TAIL 1
#define STAMP_OFF (15u << 20)
#define NSTAMP 64

constexpr int L_SLOT = 32768, L_W = 16384;
constexpr int PL = 4128;
constexpr int L_K = 0, L_V = 5 * PL;
constexpr int L_WO = 45568;
constexpr int L_ROWS = 131072;
constexpr int L_RED = 0;
constexpr int L_HST = 20480;
constexpr int L_FW = L_WO;
constexpr int L_XS = 0;
constexpr int L_FRED = 12288;
constexpr int L_MISC = L_WO + 640 * 160;
constexpr int L_RS = 0;
constexpr int L_CS = 1024;
constexpr int L_CNT = L_MISC + 3600;
constexpr int L_F1 = L_MISC + 3604, L_C1 = L_MISC + 3608;
constexpr int L_PART = L_MISC + 2048;
constexpr int L_K32 = L_MISC + 2560;
constexpr int L_HD = L_MISC + 3712;
constexpr int L_SEL = L_MISC + 3968;
constexpr int L_STAMP = L_MISC + 4096;
constexpr int L_KEY = L_MISC + 4608 + 256;
constexpr int LDS_BYTES = 163840;
static_assert(L_V + 6 * PL <= L_WO && L_KEY + 2048 <= LDS_BYTES, "lds map");

struct Params { const u16* x; const u16* wqkv; const u16* wo; const u16* wr; const u16* wg; const u16* wu; const u16* wd; u16* out; unsigned char* ws; };

__device__ __forceinline__ float bf2f(unsigned v) { return __uint_as_float(v << 16); }
__device__ __forceinline__ float bfhi(unsigned v) { return __uint_as_float(v & 0xffff0000u); }
__device__ __forceinline__ unsigned pk2(float a, float b) { f32x2 f = {a, b}; return __builtin_bit_cast(unsigned, __builtin_convertvector(f, bf2)); }
__device__ __forceinline__ float rbf(float f) { return __uint_as_float(pk2(f, 0.f) << 16); }
__device__ __forceinline__ float dot2(unsigned a, unsigned b, float c) { return __builtin_amdgcn_fdot2_f32_bf16(__builtin_bit_cast(bf2, a), __builtin_bit_cast(bf2, b), c, false); }
__device__ __forceinline__ float swap_add(float v) { auto r = __builtin_amdgcn_permlane32_swap(__float_as_uint(v), __float_as_uint(v), false, false); return __uint_as_float(r[0]) + __uint_as_float(r[1]); }
__device__ __forceinline__ float swap16_add(float v) { auto r = __builtin_amdgcn_permlane16_swap(__float_as_uint(v), __float_as_uint(v), false, false); return __uint_as_float(r[0]) + __uint_as_float(r[1]); }
template <int CTRL> __device__ __forceinline__ float dppadd(float v) { return v + __int_as_float(__builtin_amdgcn_update_dpp(0, __float_as_int(v), CTRL, 0xf, 0xf, false)); }
__device__ __forceinline__ float wave_fsum(float v) { v = dppadd<0x128>(v); v = dppadd<0x124>(v); v = dppadd<0x122>(v); v = dppadd<0x121>(v); return swap_add(swap16_add(v)); }
template <int CTRL> __device__ __forceinline__ unsigned dppmax(unsigned v) { return max(v, (unsigned)__builtin_amdgcn_update_dpp(0, (int)v, CTRL, 0xf, 0xf, false)); }
__device__ __forceinline__ unsigned wave_umax(unsigned v) {
  v = dppmax<0x128>(v); v = dppmax<0x124>(v); v = dppmax<0x122>(v); v = dppmax<0x121>(v);
  auto r = __builtin_amdgcn_permlane16_swap(v, v, false, false); v = max(r[0], r[1]);
  auto q = __builtin_amdgcn_permlane32_swap(v, v, false, false); return max(q[0], q[1]);
}
__device__ __forceinline__ void glds16(unsigned voff, const void* sbase, unsigned lds) {
  asm volatile("s_mov_b32 m0, %2\n\ts_nop 0\n\tglobal_load_lds_dwordx4 %0, %1" ::"v"(voff), "s"(sbase), "s"(lds) : "memory");
}
__device__ __forceinline__ void glds16c(unsigned voff, const void* sbase, unsigned lds) {
  asm volatile("s_mov_b32 m0, %2\n\ts_nop 0\n\tglobal_load_lds_dwordx4 %0, %1 sc1" ::"v"(voff), "s"(sbase), "s"(lds) : "memory");
}
__device__ __forceinline__ void touch64(unsigned voff, const void* sbase, unsigned lds) {
  asm volatile("s_mov_b32 m0, %2\n\ts_nop 0\n\tglobal_load_lds_dword %0, %1" ::"v"(voff), "s"(sbase), "s"(lds) : "memory");
}
__device__ __forceinline__ void glds16n(unsigned voff, const void* sbase, unsigned lds) {
  asm volatile("s_mov_b32 m0, %2\n\ts_nop 0\n\tglobal_load_lds_dwordx4 %0, %1 nt" ::"v"(voff), "s"(sbase), "s"(lds) : "memory");
}
#define GL_FW glds16
#define GL_WO glds16
#define GL_X glds16
#define GL_W glds16
__device__ __forceinline__ void touch_ptr(const void* g, unsigned lds) {
  asm volatile("s_mov_b32 m0, %1\n\ts_nop 0\n\tglobal_load_lds_dword %0, off" ::"v"(g), "s"(lds) : "memory");
}
__device__ __forceinline__ bf16x4 trd(const LAS char* p) { return __builtin_amdgcn_ds_read_tr16_b64_v4i16((LAS bf16x4*)p); }
__device__ __forceinline__ bf16x8 cat8(bf16x4 a, bf16x4 b) { return (bf16x8){a[0], a[1], a[2], a[3], b[0], b[1], b[2], b[3]}; }
#define MFMA32(a, b, c) __builtin_amdgcn_mfma_f32_32x32x16_bf16(a, b, c, 0, 0, 0)
#define MFMA32K8(a, b, c) __builtin_amdgcn_mfma_f32_32x32x8bf16_1k(a, b, c, 0, 0, 0)
#define MFMA16(a, b, c) __builtin_amdgcn_mfma_f32_16x16x32_bf16(a, b, c, 0, 0, 0)
#define LD128(off) __builtin_amdgcn_raw_buffer_load_b128(rws, (off), 0, SC1)
#define LD64(off) __builtin_amdgcn_raw_buffer_load_b64(rws, (off), 0, SC1)
#define ST128(v, off) __builtin_amdgcn_raw_buffer_store_b128((v), rws, (off), 0, SC1)
#define ST64(v, off) __builtin_amdgcn_raw_buffer_store_b64((v), rws, (off), 0, SC1)

#define STAMP(k) do {} while (0)
#define STAMP_INIT() do {} while (0)
#define PSTAMP(i) do {} while (0)
#define STAMP_ZERO() do {} while (0)
#define STAMP_PA() do {} while (0)
#define STAMP_FLUSH() do {} while (0)

#define ROT (((bi >> 3) * 5 / 2) % 10)
constexpr unsigned SPIN_CAP = 1u << 17;

__global__ __launch_bounds__(512, 2) void fused_kernel(Params p) {
  __shared__ __attribute__((aligned(16))) char smem[LDS_BYTES];
  LAS char* L = (LAS char*)smem;
  const unsigned lds0 = (unsigned)(uintptr_t)smem;
  const int tid = threadIdx.x, lane = tid & 63;
  const int w = __builtin_amdgcn_readfirstlane(tid >> 6);
  const int b = blockIdx.x, xcd = b & 7, bi = b >> 3;
  const int l31 = lane & 31, hh = lane >> 5, l15 = lane & 15, fg = lane >> 4;
  const __amdgpu_buffer_rsrc_t rws = __builtin_amdgcn_make_buffer_rsrc(p.ws, 0, 16u << 20, 0x00020000);
  const int aP = 4 * (xcd >> 1) + (bi >> 3), aj = 8 * (xcd & 1) + (bi & 7);
  const int hd = 4 * xcd + (bi >> 3), pp = bi & 7;
  const int R = 4 * xcd + (bi >> 3), cc = bi & 7;
  const int gs = xcd * 14 + bi;
  const bool is_gu = bi < 14 && gs < 108;
  const int dn = xcd * 5 + (bi - 14);
  const bool is_dn = bi >= 14 && bi < 19;
  STAMP_INIT();
  u32x2 tag2;
  auto read_epoch = [&]() {
    u64 e;
    asm volatile("s_load_dwordx2 %0, %1, 0x0\n\ts_waitcnt lgkmcnt(0)" : "=s"(e) : "s"(p.ws) : "memory");
    e += 1;
    tag2 = (u32x2){(unsigned)e, (unsigned)(e >> 32)};
  };

  auto rows_dma = [&](int lw) {
    const char* rb = (const char*)p.x + (size_t)(64 * R + 8 * cc + 2 * lw) * 1280;
    glds16(lane * 16, rb, lds0 + L_ROWS + lw * 2560);
    glds16(lane * 16, rb + 1024, lds0 + L_ROWS + lw * 2560 + 1024);
    if (lane < 32) glds16(lane * 16, rb + 2048, lds0 + L_ROWS + lw * 2560 + 2048);
  };
  auto slot_clears = [&]() {
    const unsigned so = WS_STATS + (R * 8 + cc) * 1024 + lane * 16, ao = WS_ACT + (((gs >> 1) * 4 + 2 * (gs & 1) + ((lane >> 4) & 1)) * 8 + (lane & 7)) * 32 + ((lane >> 3) & 1) * 16;
    const unsigned off = w == 0 ? so : ((w == 1 && is_gu && lane < 32) ? ao : 0x80000000u);
    ST128(((u32x4){0, 0, 0, 0}), off);
  };
  auto rows_finish = [&]() {
    float mean[2], rs[2];
#pragma unroll
    for (int r = 0; r < 2; ++r) {
      const u32x4 c0 = *(const LAS u32x4*)(L + L_ROWS + (2 * w + r) * 1280 + lane * 16), c1 = *(const LAS u32x4*)(L + L_ROWS + (2 * w + r) * 1280 + 1024 + l15 * 16);
      float s0 = 0.f, s1 = 0.f;
#pragma unroll
      for (int e = 0; e < 4; ++e) { s0 = dot2(c0[e], 0x3f803f80u, s0); s1 = dot2(c1[e], 0x3f803f80u, s1); }
      mean[r] = wave_fsum(s0 + (lane < 16 ? s1 : 0.f)) * (1.f / D);
      const f32x2 mm = {mean[r], mean[r]};
      f32x2 q0 = {0.f, 0.f}, q1 = {0.f, 0.f};
#pragma unroll
      for (int e = 0; e < 4; ++e) { const f32x2 d0 = (f32x2){bf2f(c0[e]), bfhi(c0[e])} - mm, d1 = (f32x2){bf2f(c1[e]), bfhi(c1[e])} - mm; q0 += d0 * d0; q1 += d1 * d1; }
      const float var = wave_fsum((q0[0] + q0[1]) + (lane < 16 ? q1[0] + q1[1] : 0.f)) * (1.f / D);
      rs[r] = __builtin_amdgcn_rcpf(rbf(__builtin_amdgcn_sqrtf(rbf(rbf(var) + 1e-5f))));
    }
    WAITV(0);
    ST128(((u32x4){__float_as_uint(rbf(lane ? mean[1] : mean[0])), __float_as_uint(lane ? rs[1] : rs[0]), tag2[0], tag2[1]}), lane < 2 ? WS_ST1 + (unsigned)(64 * R + 8 * cc + 2 * w + lane) * 16 : 0x80000000u);
  };

  auto lds_ctl_init = [&]() {
    if (tid == 0) { *(LAS unsigned*)(L + L_F1) = 0; *(LAS unsigned*)(L + L_C1) = 0; }
    STAMP_ZERO();
  };
  if (aj < 15 && w >= 4) {
    const int lw = w - 4;
    const unsigned xsw = (unsigned)(4 * (lw & 1) + (lane >> 4));
    const unsigned voff_x = (unsigned)(lane >> 3) * 1280u + (((unsigned)(lane & 7) ^ xsw) << 4);
    const unsigned ch = (unsigned)(lane & 15) ^ (((unsigned)(lane >> 4) << 2) | (unsigned)lw);
    const unsigned voff_w = (unsigned)(lane >> 4) * 3840u + ch * 16u;
    const char* xb = (const char*)p.x + (size_t)(128 * aP + 8 * lw) * 1280;
    const char* wb = (const char*)p.wqkv + (size_t)(4 * lw) * 3840 + 256 * aj;
    const unsigned lbase = lds0 + lw * 1024;
    const int rot = ROT;
    auto issue = [&](int s) {
      const unsigned sl = lbase + (s & 3) * L_SLOT;
      int ss = s + rot; if (ss >= 10) ss -= 10;
#pragma unroll
      for (int i = 0; i < 4; ++i) {
        GL_X(voff_x, xb + (size_t)i * 32 * 1280 + ss * 128, sl + i * 4096);
        GL_W(voff_w, wb + ((size_t)ss * 64 + i * 16) * 3840, sl + L_W + i * 4096);
      }
    };
    rows_dma(lw);
    issue(0);
    read_epoch();
    WAITV(8); BARRIER();
    issue(1); issue(2);
    WAITV(16); BARRIER();
    PSTAMP(1);
#pragma unroll 1
    for (int s = 0; s <= 5; ++s) { issue(s + 3); WAITV(16); BARRIER(); }
    issue(9);
    const unsigned so = WS_ST1 + (unsigned)(128 * aP + 32 * lw + l31) * 16, co = WS_CS1 + (unsigned)((aj * 16 + fg) * 64 + 16 * lw + l15) * 16;
    u32x4 gs1 = LD128(so), gc0 = LD128(co), gc1 = LD128(co + 4096), gc2 = LD128(co + 8192), gc3 = LD128(co + 12288);
    WAITV(21); BARRIER();
    WAITV(13); BARRIER();
    WAITV(5); BARRIER();
    unsigned spins = 0;
    for (;;) {
      const bool ok = gs1[2] == tag2[0] && gs1[3] == tag2[1] && gc0[2] == tag2[0] && gc0[3] == tag2[1] && gc1[2] == tag2[0] && gc1[3] == tag2[1] && gc2[2] == tag2[0] && gc2[3] == tag2[1] && gc3[2] == tag2[0] && gc3[3] == tag2[1];
      if (__all(ok) || ++spins > SPIN_CAP) break;
      FENCE();
      gs1 = LD128(so); gc0 = LD128(co); gc1 = LD128(co + 4096); gc2 = LD128(co + 8192); gc3 = LD128(co + 12288);
    }
    if (lane < 32) *(LAS u32x2*)(L + L_RS + (32 * lw + l31) * 8) = (u32x2){gs1[0], gs1[1]};
    {
      const float c0 = swap_add(swap16_add((__uint_as_float(gc0[0]) + __uint_as_float(gc1[0])) + (__uint_as_float(gc2[0]) + __uint_as_float(gc3[0]))));
      const float c1 = swap_add(swap16_add((__uint_as_float(gc0[1]) + __uint_as_float(gc1[1])) + (__uint_as_float(gc2[1]) + __uint_as_float(gc3[1]))));
      if (lane < 16) *(LAS f32x2*)(L + L_CS + (32 * lw + 2 * lane) * 4) = (f32x2){c0, c1};
    }
    WAITL0(); BARRIER();
    BARRIER();
  } else if (w < 4) {
    slot_clears();
    read_epoch();
    BARRIER();
    rows_finish();
    if ((bi & 7) == 0) {
      u64 pc; unsigned len;
      asm volatile("s_getpc_b64 %0\nLfk_here:\n\ts_mov_b32 %1, Lfk_code_end-Lfk_here" : "=s"(pc), "=s"(len));
      const char* cb = (const char*)(pc & ~(u64)127);
      const unsigned o0 = (unsigned)(w * 64 + lane) * 128u;
      touch_ptr(cb + min(o0, len), lds0 + L_MISC + 8192);
      touch_ptr(cb + min(o0 + 32768u, len), lds0 + L_MISC + 8192);
    }
    if (aj < 15) {
      PSTAMP(7);
      const int wm = w >> 1, wn = w & 1;
      unsigned xa[4], wa[4];
#pragma unroll
      for (int ks = 0; ks < 4; ++ks) xa[ks] = (unsigned)(64 * wm + l31) * 128u + ((((unsigned)(2 * ks + hh)) ^ (((unsigned)l31 >> 1) & 7u)) << 4);
      {
        const unsigned blk = (lane >> 4) & 1, q = (lane & 15) >> 2, pq = lane & 3;
#pragma unroll
        for (int j = 0; j < 2; ++j)
#pragma unroll
          for (int t = 0; t < 2; ++t) {
            const unsigned row = 8 * hh + 4 * t + q, chn = 4 * (2 * wn + (j ^ wm)) + 2 * blk + (pq >> 1), m = (q << 2) | (unsigned)(2 * hh + t);
            wa[j * 2 + t] = L_W + 256u * row + 16u * (chn ^ m) + 8u * (pq & 1);
          }
      }
      f32x16 a00 = {}, a01 = {}, a10 = {}, a11 = {};
      bf16x8 xf0[2], wf0[2], xf1[2], wf1[2];

      unsigned xb[4], wb[4];
#define SETSLOT(so) do { _Pragma("unroll") for (int k_ = 0; k_ < 4; ++k_) { xb[k_] = xa[k_] + (so); wb[k_] = wa[k_] + (so); } } while (0)
#define RD(XF, WF, ks) do { \
        XF[0] = *(const LAS bf16x8*)(L + xb[ks]); WF[0] = cat8(trd(L + wb[0] + (ks) * 4096), trd(L + wb[1] + (ks) * 4096)); \
        WF[1] = cat8(trd(L + wb[2] + (ks) * 4096), trd(L + wb[3] + (ks) * 4096)); XF[1] = *(const LAS bf16x8*)(L + xb[ks] + 4096); } while (0)
#define MM(XF, WF) do { \
        a00 = MFMA32(WF[0], XF[0], a00); a01 = MFMA32(WF[1], XF[0], a01); \
        a10 = MFMA32(WF[0], XF[1], a10); a11 = MFMA32(WF[1], XF[1], a11); } while (0)
#define PINALL() asm volatile("" : "+v"(a00), "+v"(a01), "+v"(a10), "+v"(a11))
#define ILV() do { __builtin_amdgcn_sched_group_barrier(0x8, 1, 0); __builtin_amdgcn_sched_group_barrier(0x100, 3, 0); \
        __builtin_amdgcn_sched_group_barrier(0x8, 1, 0); __builtin_amdgcn_sched_group_barrier(0x100, 3, 0); __builtin_amdgcn_sched_group_barrier(0x8, 2, 0); } while (0)
#define STEP(XN, WN, ksn, XC, WC) do { RD(XN, WN, ksn); MM(XC, WC); ILV(); PINALL(); SBAR(); } while (0)
      BARRIER();
      SETSLOT(0);
      RD(xf0, wf0, 0);
      SBAR();
#pragma unroll 1
      for (int s = 0; s < 9; ++s) {
        STEP(xf1, wf1, 1, xf0, wf0);
        STEP(xf0, wf0, 2, xf1, wf1);
        STEP(xf1, wf1, 3, xf0, wf0);
        SETSLOT(((s + 1) & 3) * L_SLOT);
        WAITL0(); BARRIER();
        STEP(xf0, wf0, 0, xf1, wf1);
      }
      STEP(xf1, wf1, 1, xf0, wf0);
      STEP(xf0, wf0, 2, xf1, wf1);
      STEP(xf1, wf1, 3, xf0, wf0);
      MM(xf1, wf1); PINALL();
      PSTAMP(2);
      WAITL0(); BARRIER();
      PSTAMP(6);
      const float qs = aj < 5 ? 0.22360679774997896f * 1.4426950408889634f : 1.f;
      f32x2 rs0 = *(const LAS f32x2*)(L + L_RS + (64 * wm + l31) * 8), rs1 = *(const LAS f32x2*)(L + L_RS + (64 * wm + 32 + l31) * 8);
      const float sc0 = rs0[1] * qs, nm0 = -rs0[0] * sc0, sc1 = rs1[1] * qs, nm1 = -rs1[0] * sc1;
#pragma unroll
      for (int j = 0; j < 2; ++j) {
        const int fj = 64 * wn + 32 * (j ^ wm) + 4 * hh;
        const unsigned go = WS_QKV + (unsigned)(32 * aj + (fj >> 2)) * 16384u + (unsigned)(128 * aP + 64 * wm + l31) * 8u;
#pragma unroll
        for (int rq = 0; rq < 4; ++rq) {
          const f32x4 cs = *(const LAS f32x4*)(L + L_CS + (fj + 8 * rq) * 4);
          const f32x16& a0 = j == 0 ? a00 : a01; const f32x16& a1 = j == 0 ? a10 : a11;
          const f32x2 c01 = {cs[0], cs[1]}, c23 = {cs[2], cs[3]};
          const f32x2 u0 = (f32x2){a0[rq * 4], a0[rq * 4 + 1]} * sc0 + c01 * nm0, u1 = (f32x2){a0[rq * 4 + 2], a0[rq * 4 + 3]} * sc0 + c23 * nm0;
          const f32x2 u2 = (f32x2){a1[rq * 4], a1[rq * 4 + 1]} * sc1 + c01 * nm1, u3 = (f32x2){a1[rq * 4 + 2], a1[rq * 4 + 3]} * sc1 + c23 * nm1;
          ST64(((u32x2){pk2(u0[0], u0[1]), pk2(u1[0], u1[1])}), go + 2 * rq * 16384);
          ST64(((u32x2){pk2(u2[0], u2[1]), pk2(u3[0], u3[1])}), go + 2 * rq * 16384 + 256);
        }
      }
      PSTAMP(4);
      lds_ctl_init();
      WAITV(0); WAITL0();
      PSTAMP(5);
      BARRIER();
    }
  } else {
    rows_dma(w - 4);
    read_epoch();
    WAITV(0); BARRIER();
  }
  if (aj >= 15) {
    const int chn = min(32 * w + l31, 239);
    const char* wp0 = (const char*)p.wqkv + (size_t)(40 * aP + 20 * hh) * 3840 + chn * 16;
    u32x4 wv[20];
#pragma unroll
    for (int i = 0; i < 20; ++i) wv[i] = *(const u32x4*)(wp0 + (size_t)i * 3840);
    SBAR();
    f32x2 c2[4] = {{0.f, 0.f}, {0.f, 0.f}, {0.f, 0.f}, {0.f, 0.f}};
#pragma unroll
    for (int i = 0; i < 20; ++i)
#pragma unroll
      for (int e = 0; e < 4; ++e) c2[e] += (f32x2){bf2f(wv[i][e]), bfhi(wv[i][e])};
#pragma unroll
    for (int e = 0; e < 4; ++e) c2[e] = (f32x2){swap_add(c2[e][0]), swap_add(c2[e][1])};
    if (lane < 32 && 32 * w + l31 < 240) {
#pragma unroll
      for (int e = 0; e < 4; ++e) ST128(((u32x4){__float_as_uint(c2[e][0]), __float_as_uint(c2[e][1]), tag2[0], tag2[1]}), WS_CS1 + (unsigned)(((chn >> 4) * 16 + aP) * 64 + 4 * (chn & 15) + e) * 16);
    }
    lds_ctl_init(); WAITL0(); BARRIER();
  }
  const unsigned tag32 = tag2[0] | 1u;
  if (aj < 15 && tid == 0) ST64(tag2, WS_FLAGA + (aP * 16 + aj) * 128);
  PSTAMP(3);
  STAMP_PA();

  const int nt = w & 3, kh = w >> 2;
  const int tok = 64 * R + 16 * nt + l15;
  u32x2 xr[5], wrv[5];
  unsigned wr5[5];
  {
    const int t = w;
    *(LAS u32x2*)(L + L_V + 5 * PL + tid * 8) = (u32x2){(pp > 0 || tid >= 256) ? 0x00003f80u : 0u, 0u};
    if (pp == 0) {
#pragma unroll
      for (int i = 0; i < 3; ++i) { const int n = tid + 512 * i; if (n < 1280) *(LAS u32x4*)(L + (n >> 7) * PL + (n & 127) * 16) = (u32x4){0, 0, 0, 0}; }
    }
    if (kh == 0) {
#pragma unroll
      for (int ft = 0; ft < 5; ++ft) {
        xr[ft] = *(const u32x2*)(p.x + (size_t)tok * D + 80 * cc + 16 * ft + 4 * fg);
        wrv[ft] = *(const u32x2*)(p.wr + 80 * cc + 16 * ft + 4 * fg);
      }
    }
    if (bi == 31) {
#pragma unroll
      for (int i = 0; i < 5; ++i) wr5[i] = ((const unsigned*)p.wr)[lane + 64 * i];
    }
    f32x16 cdg, can;
    {
      auto diag_ok = [](int r) { unsigned long long m = 0; for (int l = 0; l < 64; ++l) if ((r & 3) + 8 * (r >> 2) + 4 * (l >> 5) <= (l & 31)) m |= 1ull << l; return m; };
#pragma unroll
      for (int r = 0; r < 16; ++r) { const bool ok = __builtin_amdgcn_inverse_ballot_w64(diag_ok(r)); cdg[r] = ok ? 0.f : -INFINITY; can[r] = ok ? -INFINITY : 0.f; }
      asm volatile("" : "+v"(cdg), "+v"(can));
    }
    auto lds_wait = [&](int off, unsigned v) { unsigned sp = 0; while (__hip_atomic_load((LAS unsigned*)(L + off), __ATOMIC_RELAXED, __HIP_MEMORY_SCOPE_WORKGROUP) < v && ++sp < (SPIN_CAP << 4)) __builtin_amdgcn_s_sleep(0); FENCE(); };
    WAITL0();
    if (kh == 0) {
      if (lane == 0) __hip_atomic_fetch_add((LAS unsigned*)(L + L_C1), 1u, __ATOMIC_RELAXED, __HIP_MEMORY_SCOPE_WORKGROUP);
      lds_wait(L_F1, 1);
    } else {
      if (w == 4) {
        const int sec = lane < 4 ? 0 : (lane < 12 ? 1 : 2);
        const int k = lane < 4 ? lane : (lane < 12 ? lane - 4 : lane - 12);
        const int col = sec * 640 + 20 * hd + ((k & 1) ? 19 : 0);
        const int Pn = sec == 0 ? 2 * pp + (k >> 1) : 2 * pp - 2 + (k >> 1);
        const bool need = lane < 20 && Pn >= 0;
        const unsigned fo = WS_FLAGA + (unsigned)((need ? Pn : 2 * pp) * 16 + (col >> 7)) * 128;
        unsigned spins = 0;
        for (;;) {
          FENCE();
          const u32x2 f = LD64(fo);
          if (__all(!need || (f[0] == tag2[0] && f[1] == tag2[1])) || ++spins > SPIN_CAP) break;
          __builtin_amdgcn_s_sleep(1);
        }
        if (lane == 0) __hip_atomic_store((LAS unsigned*)(L + L_F1), 1u, __ATOMIC_RELAXED, __HIP_MEMORY_SCOPE_WORKGROUP);
        STAMP(4);
      } else lds_wait(L_F1, 1);
      const char* qb = (const char*)p.ws + WS_QKV;
#pragma unroll
      for (int i = 0; i < 10; ++i) {
        const int pc = nt + 4 * i, pl = pc >> 2, qt = pc & 3;
        const int tok0 = 256 * pp - 256 + 128 * qt;
        if (tok0 >= 0) glds16c(lane * 16, qb + (size_t)((pl < 5 ? 160 + 5 * hd + pl : 320 + 5 * hd + pl - 5)) * 16384 + tok0 * 8, lds0 + pl * PL + qt * 1024);
      }
    }
    bf16x8 qf0; bf16x4 qf1;
    {
      const unsigned qo = WS_QKV + (unsigned)(5 * hd) * 16384u + (unsigned)(256 * pp + 32 * t + l31) * 8u;
      const u32x2 a = LD64(qo + (2 * hh) * 16384), c = LD64(qo + (2 * hh + 1) * 16384);
      u32x2 z = LD64(qo + 4 * 16384);
      asm volatile("s_waitcnt vmcnt(0)" : "+v"(z) :: "memory");
      if (hh) z = (u32x2){0, 0};
      qf0 = __builtin_bit_cast(bf16x8, (u32x4){a[0], a[1], c[0], c[1]});
      qf1 = __builtin_bit_cast(bf16x4, z);
    }
    if (kh == 1 && lane == 0) __hip_atomic_fetch_add((LAS unsigned*)(L + L_C1), 1u, __ATOMIC_RELAXED, __HIP_MEMORY_SCOPE_WORKGROUP);
    lds_wait(L_C1, 8);
    if (w == 4) STAMP(5);
    unsigned wov[5];
#pragma unroll
    for (int j = 0; j < 5; ++j) { const int n = 64 * j + lane, rho = (n * 6554) >> 16, chn = n - 10 * rho; wov[j] = (unsigned)(8 * ((rho >> 2) & 3) + 4 * (rho >> 4) + (rho & 3)) * 1280u + chn * 16u; }
    const char* wob = (const char*)p.wo + 160 * cc + (size_t)nt * 32 * 1280;
    const unsigned wol = lds0 + L_WO + nt * 5120;
#define WOP(i) GL_WO(wov[(i) % 5], wob + (size_t)((i) / 5) * 4 * 32 * 1280, wol + ((i) / 5) * 4 * 5120 + ((i) % 5) * 1024)
#define WOP3(i) do { if (kh == 0) { WOP(i); WOP((i) + 1); WOP((i) + 2); } SBAR(); } while (0)
    if (w == 0) STAMP(28);
    const LAS char* kb = L + L_K + l31 * 8 + (2 * hh) * PL;
    const LAS char* kb4 = L + (hh ? L_V + 5 * PL : L_K + 4 * PL) + l31 * 8;
    {
      const u32x2 k0 = *(const LAS u32x2*)(kb + (t + 8) * 256), k1 = *(const LAS u32x2*)(kb + (t + 8) * 256 + PL), k2 = *(const LAS u32x2*)(kb4 + (t + 8) * 256);
      const u32x4 q0 = __builtin_bit_cast(u32x4, qf0); u32x2 q1 = __builtin_bit_cast(u32x2, qf1);
      float d = dot2(q0[0], k0[0], 0.f); d = dot2(q0[1], k0[1], d); d = dot2(q0[2], k1[0], d); d = dot2(q0[3], k1[1], d);
      d = dot2(q1[0], k2[0], d); d = dot2(q1[1], k2[1], d);
      const unsigned nm = pk2(-swap_add(d), 0.f);
      if (hh) q1[0] = nm;
      qf1 = __builtin_bit_cast(bf16x4, q1);
    }
    const LAS char* vb = L + L_V + min(4 * ((lane >> 4) & 1) + (lane & 3), 5) * PL + (4 * hh + ((lane & 15) >> 2)) * 8;
    f32x16 o = (f32x16){};
    {
      f32x16 sa, sb;
      const f32x16 cz = {};
      bf16x4 ka, kc, kd;
      bf16x8 vq = {};
      u32x4 pq = {0, 0, 0, 0};
#define KRD(kt) do { ka = *(const LAS bf16x4*)(kb + (kt) * 256); kc = *(const LAS bf16x4*)(kb + (kt) * 256 + PL); kd = *(const LAS bf16x4*)(kb4 + (kt) * 256); } while (0)
#define EX2(S, i) pk2(__builtin_amdgcn_exp2f(S[i]), __builtin_amdgcn_exp2f(S[(i) + 1]))
#define GRP(nds, nv) do { __builtin_amdgcn_sched_group_barrier(0x8, 1, 0); if (nds) __builtin_amdgcn_sched_group_barrier(0x100, nds, 0); __builtin_amdgcn_sched_group_barrier(0x2, nv, 0); } while (0)
#define HALF(SC, SN, C, ktv, ktk) do { \
        u32x4 p0_; const LAS char* vp_ = vb + (ktv) * 256; \
        SN = MFMA32(cat8(ka, kc), qf0, C); \
        const bf16x8 v0_ = cat8(trd(vp_), trd(vp_ + 64)), v1_ = cat8(trd(vp_ + 128), trd(vp_ + 192)); \
        p0_[0] = EX2(SC, 0); p0_[1] = EX2(SC, 2); \
        GRP(4, 6); asm volatile("" : "+v"(SN), "+v"(p0_[0]), "+v"(p0_[1])); SBAR(); \
        o = MFMA32(vq, __builtin_bit_cast(bf16x8, pq), o); \
        p0_[2] = EX2(SC, 4); p0_[3] = EX2(SC, 6); \
        GRP(0, 6); asm volatile("" : "+v"(o), "+v"(p0_[2]), "+v"(p0_[3])); SBAR(); \
        SN = MFMA32K8(kd, qf1, SN); \
        KRD(ktk); \
        pq[0] = EX2(SC, 8); pq[1] = EX2(SC, 10); \
        GRP(3, 6); asm volatile("" : "+v"(SN), "+v"(pq[0]), "+v"(pq[1])); SBAR(); \
        o = MFMA32(v0_, __builtin_bit_cast(bf16x8, p0_), o); \
        pq[2] = EX2(SC, 12); pq[3] = EX2(SC, 14); vq = v1_; \
        GRP(0, 6); asm volatile("" : "+v"(o), "+v"(pq[2]), "+v"(pq[3])); SBAR(); } while (0)
      KRD(t + 8);
      sa = MFMA32(cat8(ka, kc), qf0, cdg); sa = MFMA32K8(kd, qf1, sa);
      KRD(t + 7);
      asm volatile("" : "+v"(sa)); SBAR();
      WOP3(0);
      HALF(sa, sb, cz, t + 8, t + 6);
      WOP3(3);
      HALF(sb, sa, cz, t + 7, t + 5);
      WOP3(6);
      HALF(sa, sb, cz, t + 6, t + 4);
      WOP3(9);
      HALF(sb, sa, cz, t + 5, t + 3);
      WOP3(12);
      HALF(sa, sb, cz, t + 4, t + 2);
      WOP3(15);
      HALF(sb, sa, cz, t + 3, t + 1);
      WOP3(18);
      HALF(sa, sb, cz, t + 2, t);
      if (kh == 0) { WOP(21); WOP(22); WOP(23); WOP(24); }
      SBAR();
      HALF(sb, sa, can, t + 1, t);
      {
        const LAS char* vp_ = vb + t * 256;
        const bf16x8 v0_ = cat8(trd(vp_), trd(vp_ + 64)), v1_ = cat8(trd(vp_ + 128), trd(vp_ + 192));
        o = MFMA32(vq, __builtin_bit_cast(bf16x8, pq), o);
        u32x4 p0_, p1_;
#pragma unroll
        for (int e = 0; e < 4; ++e) p0_[e] = EX2(sa, 2 * e);
        o = MFMA32(v0_, __builtin_bit_cast(bf16x8, p0_), o);
#pragma unroll
        for (int e = 0; e < 4; ++e) p1_[e] = EX2(sa, 8 + 2 * e);
        o = MFMA32(v1_, __builtin_bit_cast(bf16x8, p1_), o);
      }
    }
    float lsum;
    { auto r = __builtin_amdgcn_permlane32_swap(__float_as_uint(o[8]), __float_as_uint(o[8]), false, false); lsum = __uint_as_float(r[1]); }
    if (__builtin_expect(__any(!(lsum < 1.0995116e12f)), 0)) {
      const int lo = pp >= 1 ? t : 8;
      float mx = -INFINITY;
#pragma unroll 1
      for (int kt = t + 8; kt >= lo; --kt) {
        const bf16x4 ka = *(const LAS bf16x4*)(kb + kt * 256), kc = *(const LAS bf16x4*)(kb + kt * 256 + PL), kd = *(const LAS bf16x4*)(kb4 + kt * 256);
        f32x16 s = MFMA32(cat8(ka, kc), qf0, (f32x16){});
        s = MFMA32K8(kd, qf1, s);
#pragma unroll
        for (int r = 0; r < 16; ++r) { const int kr = (r & 3) + 8 * (r >> 2) + 4 * hh; const bool ok = kt == t + 8 ? kr <= l31 : (kt == t ? kr > l31 : true); mx = fmaxf(mx, ok ? s[r] : -INFINITY); }
      }
      { auto r = __builtin_amdgcn_permlane32_swap(__float_as_uint(mx), __float_as_uint(mx), false, false); mx = fmaxf(__uint_as_float(r[0]), __uint_as_float(r[1])); }
      o = (f32x16){};
#pragma unroll 1
      for (int kt = t + 8; kt >= lo; --kt) {
        const bf16x4 ka = *(const LAS bf16x4*)(kb + kt * 256), kc = *(const LAS bf16x4*)(kb + kt * 256 + PL), kd = *(const LAS bf16x4*)(kb4 + kt * 256);
        const LAS char* vp_ = vb + kt * 256;
        const bf16x8 v0_ = cat8(trd(vp_), trd(vp_ + 64)), v1_ = cat8(trd(vp_ + 128), trd(vp_ + 192));
        f32x16 s = MFMA32(cat8(ka, kc), qf0, (f32x16){});
        s = MFMA32K8(kd, qf1, s);
        u32x4 p0_, p1_;
#pragma unroll
        for (int r = 0; r < 16; ++r) { const int kr = (r & 3) + 8 * (r >> 2) + 4 * hh; const bool ok = kt == t + 8 ? kr <= l31 : (kt == t ? kr > l31 : true); s[r] = ok ? __builtin_amdgcn_exp2f(s[r] - mx) : 0.f; }
#pragma unroll
        for (int e = 0; e < 4; ++e) { p0_[e] = pk2(s[2 * e], s[2 * e + 1]); p1_[e] = pk2(s[8 + 2 * e], s[8 + 2 * e + 1]); }
        o = MFMA32(v0_, __builtin_bit_cast(bf16x8, p0_), o);
        o = MFMA32(v1_, __builtin_bit_cast(bf16x8, p1_), o);
      }
      auto r = __builtin_amdgcn_permlane32_swap(__float_as_uint(o[8]), __float_as_uint(o[8]), false, false); lsum = __uint_as_float(r[1]);
    }
    if (w == 0) STAMP(6);
    STAMP(32 + w);
    {
      const float inv = 1.f / lsum;
      unsigned A0 = pk2(o[0] * inv, o[1] * inv), A1 = pk2(o[2] * inv, o[3] * inv), B0 = pk2(o[4] * inv, o[5] * inv), B1 = pk2(o[6] * inv, o[7] * inv);
      unsigned C0 = pk2(o[8] * inv, o[9] * inv), C1 = pk2(o[10] * inv, o[11] * inv);
      const int tt = pp * 16 + 2 * t + (l31 >> 4);
      const unsigned base = WS_O + (unsigned)tt * 80 * 256 + (l31 & 15) * 16 + (unsigned)(5 * (hd >> 1)) * 256;
      if ((hd & 1) == 0) {
        auto s0 = __builtin_amdgcn_permlane32_swap(A0, B0, false, false), s1 = __builtin_amdgcn_permlane32_swap(A1, B1, false, false);
        ST128(((u32x4){s0[0], s1[0], s0[1], s1[1]}), base + hh * 256);
        if (hh == 0) ST64(((u32x2){C0, C1}), base + 2 * 256);
      } else {
        auto u0 = __builtin_amdgcn_permlane32_swap(A0, C0, false, false), u1 = __builtin_amdgcn_permlane32_swap(A1, C1, false, false);
        const u32x4 v = hh ? (u32x4){B0, B1, u0[0], u1[0]} : (u32x4){u0[1], u1[1], B0, B1};
        ST128(v, base + (3 + hh) * 256);
        if (hh == 0) ST64(((u32x2){A0, A1}), base + 2 * 256 + 8);
      }
      WAITV(0);
      if (lane == 0) ST64(tag2, WS_FLAGO + (((pp * 8 + t) * 2 + (hd >> 4)) * 16 + (hd & 15)) * 8);
    }
  }
  if (w == 0) STAMP(7);
  STAMP(40 + w);

  BARRIER();
  if (w == 0) STAMP(8);

  f32x4 acc[5];
#pragma unroll
  for (int ft = 0; ft < 5; ++ft) acc[ft] = (f32x4){0, 0, 0, 0};
  {
    const LAS char* wp = L + L_WO + (unsigned)(kh * 320 + 4 * fg + ((lane & 15) >> 2)) * 160 + (lane & 3) * 8;
    bf16x8 wa[3][5];
#pragma unroll
    for (int s = 0; s < 3; ++s)
#pragma unroll
      for (int ft = 0; ft < 5; ++ft) wa[s][ft] = cat8(trd(wp + s * 5120 + ft * 32), trd(wp + s * 5120 + 2560 + ft * 32));
    const int tg = 4 * R + nt;
    const unsigned fo = WS_FLAGO + ((((tg >> 4) * 8 + ((tg >> 1) & 7)) * 2 + kh) * 16 + l15) * 8;
    unsigned spins = 0;
    for (;;) {
      FENCE();
      const u32x2 f = LD64(fo);
      if (__all(f[0] == tag2[0] && f[1] == tag2[1]) || ++spins > SPIN_CAP) break;
      __builtin_amdgcn_s_sleep(1);
    }
    FENCE();
    if (w == 0) STAMP(9);
    STAMP(48 + w);
    u32x4 of[10];
#pragma unroll
    for (int s = 0; s < 10; ++s) of[s] = LD128(WS_O + (unsigned)(tg * 80 + 4 * (10 * kh + s)) * 256 + lane * 16);
    SBAR();
#pragma unroll
    for (int s = 0; s < 10; ++s) {
      const bf16x8 bfr = __builtin_bit_cast(bf16x8, of[s]);
#pragma unroll
      for (int ft = 0; ft < 5; ++ft) acc[ft] = MFMA16(s < 3 ? wa[s < 3 ? s : 0][ft] : cat8(trd(wp + s * 5120 + ft * 32), trd(wp + s * 5120 + 2560 + ft * 32)), bfr, acc[ft]);
    }
  }
  STAMP(56 + w);
  if (kh == 1) {
#pragma unroll
    for (int ft = 0; ft < 5; ++ft) *(LAS f32x4*)(L + L_RED + ((nt * 5 + ft) * 64 + lane) * 16) = acc[ft];
  }
  WAITL0(); BARRIER();
  if (w == 0) STAMP(10);
  if (kh == 0) {
    float s1 = 0.f, s2 = 0.f, s3 = 0.f;
#pragma unroll
    for (int ft = 0; ft < 5; ++ft) {
      const f32x4 o2 = *(const LAS f32x4*)(L + L_RED + ((nt * 5 + ft) * 64 + lane) * 16);
      const unsigned a01 = pk2(acc[ft][0] + o2[0], acc[ft][1] + o2[1]), a23 = pk2(acc[ft][2] + o2[2], acc[ft][3] + o2[3]);
      const unsigned h01 = pk2(bf2f(xr[ft][0]) + bf2f(a01), bfhi(xr[ft][0]) + bfhi(a01)), h23 = pk2(bf2f(xr[ft][1]) + bf2f(a23), bfhi(xr[ft][1]) + bfhi(a23));
      *(LAS u32x2*)(L + L_HST + nt * 2560 + l15 * 160 + ft * 32 + fg * 8) = (u32x2){h01, h23};
      s1 = dot2(h01, 0x3f803f80u, s1); s1 = dot2(h23, 0x3f803f80u, s1);
      s2 = dot2(h01, h01, s2); s2 = dot2(h23, h23, s2);
      s3 = dot2(h01, wrv[ft][0], s3); s3 = dot2(h23, wrv[ft][1], s3);
    }
    WAITL0();
#pragma unroll
    for (int i = 0; i < 3; ++i) {
      const int n = lane + 64 * i;
      if (n < 160) ST128(*(const LAS u32x4*)(L + L_HST + nt * 2560 + n * 16), WS_H + (unsigned)((R * 8 + cc) * 64 + 16 * nt) * 160 + n * 16);
    }
    s1 = swap_add(swap16_add(s1)); s2 = swap_add(swap16_add(s2)); s3 = swap_add(swap16_add(s3));
    WAITV(0);
    if (lane < 16) ST128(((u32x4){__float_as_uint(s1), __float_as_uint(s2), __float_as_uint(s3), tag32}), WS_STATS + ((R * 8 + cc) * 64 + 16 * nt + l15) * 16);
    if (w == 0) STAMP(11);
    BARRIER();
  } else {
    BARRIER();
    const int rl = lane >> 1, grow = 8 * ((rl >> 2) & 3) + 4 * (rl >> 4) + (rl & 3);
    if (is_gu) {
      const unsigned vo = (unsigned)grow * (F * 2) + (lane & 1) * 16;
#pragma unroll 1
      for (int pc = nt; pc < 40; pc += 4) { const int m = pc >= 20, s = pc - 20 * m; GL_FW(vo, (const char*)(m ? p.wu : p.wg) + (size_t)s * 32 * F * 2 + gs * 32, lds0 + L_FW + pc * 1024); }
    } else if (is_dn) {
      const unsigned vo = (unsigned)grow * (D * 2) + (lane & 1) * 16;
#pragma unroll 1
      for (int pc = nt; pc < 54; pc += 4) GL_FW(vo, (const char*)p.wd + (size_t)pc * 32 * D * 2 + dn * 32, lds0 + L_FW + pc * 1024);
    }
  }
  auto rank64 = [&](unsigned k) {
    const u32x4 oa = *(const LAS u32x4*)(L + L_KEY + w * 32), ob = *(const LAS u32x4*)(L + L_KEY + w * 32 + 16);
    unsigned c = 0;
#pragma unroll
    for (int e = 0; e < 4; ++e) c += (oa[e] > k) + (ob[e] > k);
    *(LAS unsigned char*)(L + L_PART + lane * 8 + w) = (unsigned char)c;
    WAITL0(); BARRIER();
    const u32x2 pr = *(const LAS u32x2*)(L + L_PART + lane * 8);
    return __builtin_amdgcn_sad_u8(pr[0], 0u, __builtin_amdgcn_sad_u8(pr[1], 0u, 0u));
  };
  if (bi == 31) {
    float swr = 0.f;
#pragma unroll
    for (int i = 0; i < 5; ++i) swr = dot2(wr5[i], 0x3f803f80u, swr);
    swr = wave_fsum(swr);
    const int tl = 32 * w + l31;
    const unsigned so = WS_STATS + (unsigned)(((4 * xcd + (tl >> 6)) * 8 + 4 * hh) * 64 + (tl & 63)) * 16;
    float t1, t2, t3;
    unsigned spins = 0;
    for (;;) {
      FENCE();
      const u32x4 g0 = LD128(so), g1 = LD128(so + 1024), g2 = LD128(so + 2048), g3 = LD128(so + 3072);
      t1 = (__uint_as_float(g0[0]) + __uint_as_float(g1[0])) + (__uint_as_float(g2[0]) + __uint_as_float(g3[0]));
      t2 = (__uint_as_float(g0[1]) + __uint_as_float(g1[1])) + (__uint_as_float(g2[1]) + __uint_as_float(g3[1]));
      t3 = (__uint_as_float(g0[2]) + __uint_as_float(g1[2])) + (__uint_as_float(g2[2]) + __uint_as_float(g3[2]));
      if (__all(g0[3] == tag32 && g1[3] == tag32 && g2[3] == tag32 && g3[3] == tag32) || ++spins > SPIN_CAP) break;
    }
    t1 = swap_add(t1); t2 = swap_add(t2); t3 = swap_add(t3);
    const float mean = t1 * (1.f / D);
    const float var = fmaxf(t2 * (1.f / D) - mean * mean, 0.f);
    const float mb = rbf(mean);
    const float sd = rbf(__builtin_amdgcn_sqrtf(rbf(rbf(var) + 1e-5f)));
    const unsigned rb = pk2((t3 - mb * swr) * __builtin_amdgcn_rcpf(sd), 0.f) & 0xffffu;
    const unsigned key = ((rb ^ ((rb & 0x8000u) ? 0xffffu : 0x8000u)) << 16) | (unsigned)(2047 - (256 * xcd + tl));
    const unsigned ms = (__float_as_uint(mb) & 0xffff0000u) | (__float_as_uint(sd) >> 16);
    if (w == 0) STAMP(12);
    if (lane < 32) *(LAS unsigned*)(L + L_K32 + tl * 4) = key;
    WAITL0();
    unsigned c = 0;
#pragma unroll
    for (int i = 0; i < 4; ++i) {
      const u32x4 o4 = *(const LAS u32x4*)(L + L_K32 + (32 * w + 16 * hh + 4 * i) * 4);
#pragma unroll
      for (int e = 0; e < 4; ++e) c += o4[e] > key;
    }
    { auto r = __builtin_amdgcn_permlane32_swap(c, c, false, false); c = r[0] + r[1]; }
    if (lane < 32 && c < 8) { *(LAS unsigned*)(L + L_KEY + (8 * w + c) * 4) = key; *(LAS unsigned*)(L + L_KEY + 256 + (8 * w + c) * 4) = ms; }
    WAITL0(); BARRIER();
    const unsigned k = *(const LAS unsigned*)(L + L_KEY + lane * 4), m2 = *(const LAS unsigned*)(L + L_KEY + 256 + lane * 4);
    const unsigned rk = rank64(k);
    if (w == 0) { if (rk < 8) ST128(((u32x4){k, tag2[0], m2, tag2[1]}), WS_CAND + xcd * 128 + rk * 16); STAMP(26); }
    BARRIER();
  }
  if (w == 0) {
    u32x4 a;
    unsigned spins = 0;
    for (;;) {
      FENCE();
      a = LD128(WS_CAND + lane * 16);
      if (__all(a[1] == tag2[0] && a[3] == tag2[1]) || ++spins > SPIN_CAP) break;
      if (is_gu || is_dn) __builtin_amdgcn_s_sleep(1); else __builtin_amdgcn_s_sleep(8);
    }
    STAMP(13);
    *(LAS unsigned*)(L + L_KEY + lane * 4) = a[0]; *(LAS unsigned*)(L + L_KEY + 256 + lane * 4) = a[2];
    if (b == 0 && lane == 0) ST64(tag2, WS_EPOCH);
  }
  const int hc0 = (lane * 6554) >> 16, hc1 = ((64 + l15) * 6554) >> 16;
  const unsigned ho0 = (unsigned)hc0 * (64 * 160) + (lane - 10 * hc0) * 16, ho1 = (unsigned)hc1 * (64 * 160) + (64 + l15 - 10 * hc1) * 16;
  if (kh == 1) WAITV(0);
  WAITL0(); BARRIER();
  {
    const unsigned k = *(const LAS unsigned*)(L + L_KEY + lane * 4), m2 = *(const LAS unsigned*)(L + L_KEY + 256 + lane * 4);
    const unsigned rk = rank64(k);
    if (rk < 8) *(LAS u32x2*)(L + L_SEL + rk * 8) = (u32x2){k, m2};
    WAITL0();
  }
  if (w == 0) STAMP(14);

  auto h_row = [&](int row, u32x4& h0, u32x4& h1) {
    const unsigned rb = WS_H + (unsigned)((row >> 6) * 8 * 64 + (row & 63)) * 160;
    h0 = LD128(rb + ho0); h1 = LD128(rb + ho1);
  };
  auto ln8 = [&](const u32x4& h, float m, float iv) { u32x4 n;
#pragma unroll
    for (int e = 0; e < 4; ++e) { const unsigned d = pk2(bf2f(h[e]) - m, bfhi(h[e]) - m); n[e] = pk2(bf2f(d) * iv, bfhi(d) * iv); } return n; };
  auto out_row = [&]() {
    const int row = 64 * R + 8 * cc + w;
    const u32x2 sk = *(const LAS u32x2*)(L + L_SEL + (lane & 7) * 8);
    if (!__any(2047 - (int)(sk[0] & 0x7ff) == row)) {
      u32x4 h0, h1; h_row(row, h0, h1);
      const u32x4 g = LD128(WS_STATS + (unsigned)((R * 8 + (lane & 7)) * 64 + 8 * cc + w) * 16);
      float t1 = __uint_as_float(g[0]), t2 = __uint_as_float(g[1]);
      t1 = dppadd<0x141>(t1); t2 = dppadd<0x141>(t2);
      t1 = dppadd<0xb1>(t1); t2 = dppadd<0xb1>(t2);
      t1 = dppadd<0x4e>(t1); t2 = dppadd<0x4e>(t2);
      const float mean = t1 * (1.f / D);
      const float m = rbf(mean), iv = __builtin_amdgcn_rcpf(rbf(__builtin_amdgcn_sqrtf(rbf(rbf(fmaxf(t2 * (1.f / D) - mean * mean, 0.f)) + 1e-5f))));
      const u32x4 n0 = ln8(h0, m, iv), n1 = ln8(h1, m, iv);
      u32x4 r0, r1;
#pragma unroll
      for (int e = 0; e < 4; ++e) { r0[e] = pk2(bf2f(h0[e]) + bf2f(n0[e]), bfhi(h0[e]) + bfhi(n0[e])); r1[e] = pk2(bf2f(h1[e]) + bf2f(n1[e]), bfhi(h1[e]) + bfhi(n1[e])); }
      *(u32x4*)(p.out + (size_t)row * D + lane * 8) = r0;
      if (lane < 16) *(u32x4*)(p.out + (size_t)row * D + 512 + lane * 8) = r1;
    }
  };
  if (is_gu) {
    {
      const u32x2 sk = *(const LAS u32x2*)(L + L_SEL + w * 8);
      u32x4 h0, h1; h_row(2047 - (int)(sk[0] & 0x7ff), h0, h1);
      const float m = __uint_as_float(sk[1] & 0xffff0000u), iv = __builtin_amdgcn_rcpf(__uint_as_float(sk[1] << 16));
      *(LAS u32x4*)(L + L_XS + w * 1296 + lane * 16) = ln8(h0, m, iv);
      if (lane < 16) *(LAS u32x4*)(L + L_XS + w * 1296 + 1024 + lane * 16) = ln8(h1, m, iv);
    }
    WAITL0(); BARRIER();
    if (w == 0) STAMP(15);
    {
      const int m = w & 1, kq = w >> 1;
      f32x4 a4 = {0, 0, 0, 0}, b4 = {0, 0, 0, 0};
#pragma unroll
      for (int i = 0; i < 5; ++i) {
        const int s = 5 * kq + i;
        const LAS char* ap = L + L_FW + (m * 20 + s) * 1024 + lane * 8;
        const bf16x8 af = cat8(trd(ap), trd(ap + 512));
        const bf16x8 bfv = *(const LAS bf16x8*)(L + L_XS + (lane & 7) * 1296 + s * 64 + fg * 16);
        if (i & 1) b4 = MFMA16(af, bfv, b4); else a4 = MFMA16(af, bfv, a4);
      }
      *(LAS f32x4*)(L + L_FRED + (w * 64 + lane) * 16) = a4 + b4;
    }
    WAITL0(); BARRIER();
    if (w == 0) {
      f32x4 g4 = {0, 0, 0, 0}, u4 = {0, 0, 0, 0};
#pragma unroll
      for (int q = 0; q < 4; ++q) { g4 += *(const LAS f32x4*)(L + L_FRED + ((2 * q) * 64 + lane) * 16); u4 += *(const LAS f32x4*)(L + L_FRED + ((2 * q + 1) * 64 + lane) * 16); }
      float a[4];
#pragma unroll
      for (int e = 0; e < 4; ++e) { const float g = rbf(g4[e]), u = rbf(u4[e]); const float sg = rbf(g * __builtin_amdgcn_rcpf(1.f + __builtin_amdgcn_exp2f(g * -1.4426950408889634f))); a[e] = sg * u; }
      if (l15 < 8) ST128(((u32x4){pk2(a[0], a[1]), tag32, pk2(a[2], a[3]), tag32}), WS_ACT + (((gs >> 1) * 4 + 2 * (gs & 1) + (fg >> 1)) * 8 + l15) * 32 + (fg & 1) * 16);
      STAMP(16);
    }
    out_row();
  } else if (is_dn) {
    const u32x2 sk = *(const LAS u32x2*)(L + L_SEL + (lane & 7) * 8);
    const int row = 2047 - (int)(sk[0] & 0x7ff);
    u32x2 hv = {0, 0}, nv = {0, 0};
    float sig = 0.f;
    if (w == 0) {
      const int col = 16 * dn + 4 * fg, c = col / 80;
      hv = LD64(WS_H + (unsigned)(((row >> 6) * 8 + c) * 64 + (row & 63)) * 160 + (col - 80 * c) * 2);
      const unsigned rk = sk[0] >> 16, rbits = (rk & 0x8000u) ? (rk ^ 0x8000u) : (rk ^ 0xffffu);
      sig = rbf(__builtin_amdgcn_rcpf(1.f + __builtin_amdgcn_exp2f(bf2f(rbits) * -1.4426950408889634f)));
      const float m = __uint_as_float(sk[1] & 0xffff0000u), iv = __builtin_amdgcn_rcpf(__uint_as_float(sk[1] << 16));
#pragma unroll
      for (int e = 0; e < 2; ++e) { const unsigned d2 = pk2(bf2f(hv[e]) - m, bfhi(hv[e]) - m); nv[e] = pk2(bf2f(d2) * iv, bfhi(d2) * iv); }
      asm volatile("" : "+v"(nv), "+v"(hv), "+v"(sig));
    } else out_row();
    f32x4 a4 = {0, 0, 0, 0}, b4 = {0, 0, 0, 0};
    {
      bf16x8 af[7];
#pragma unroll
      for (int i = 0; i < 7; ++i) { const LAS char* ap = L + L_FW + min(w + 8 * i, 53) * 1024 + lane * 8; af[i] = cat8(trd(ap), trd(ap + 512)); }
      u32x4 g0[7];
      unsigned spins = 0;
      for (;;) {
        FENCE();
        bool ok = true;
#pragma unroll
        for (int i = 0; i < 7; ++i) g0[i] = LD128(WS_ACT + ((min(w + 8 * i, 53) * 4 + fg) * 8 + (lane & 7)) * 32 + ((lane >> 3) & 1) * 16);
#pragma unroll
        for (int i = 0; i < 7; ++i) ok = ok && g0[i][1] == tag32 && g0[i][3] == tag32;
        if (__all(ok) || ++spins > SPIN_CAP) break;
      }
      if (w == 0) STAMP(17);
#pragma unroll
      for (int i = 0; i < 7; ++i) {
        const unsigned y0 = (unsigned)__builtin_amdgcn_update_dpp(0, (int)g0[i][0], 0x128, 0xf, 0xf, false), y1 = (unsigned)__builtin_amdgcn_update_dpp(0, (int)g0[i][2], 0x128, 0xf, 0xf, false);
        const bf16x8 bv = __builtin_bit_cast(bf16x8, (u32x4){g0[i][0], g0[i][2], y0, y1});
        if (i < 6 || w < 6) { if (i & 1) b4 = MFMA16(af[i], bv, b4); else a4 = MFMA16(af[i], bv, a4); }
      }
    }
    *(LAS f32x4*)(L + L_FRED + (w * 64 + lane) * 16) = a4 + b4;
    WAITL0(); BARRIER();
    if (w == 0) {
      f32x4 y = {0, 0, 0, 0};
#pragma unroll
      for (int q = 0; q < 8; ++q) y += *(const LAS f32x4*)(L + L_FRED + (q * 64 + lane) * 16);
      unsigned r2[2];
#pragma unroll
      for (int e = 0; e < 2; ++e) {
        const unsigned y2 = pk2(rbf(y[2 * e]) * sig, rbf(y[2 * e + 1]) * sig);
        const unsigned t2 = pk2(bf2f(nv[e]) + bf2f(y2), bfhi(nv[e]) + bfhi(y2));
        r2[e] = pk2(bf2f(hv[e]) + bf2f(t2), bfhi(hv[e]) + bfhi(t2));
      }
      if (l15 < 8) *(u32x2*)(p.out + (size_t)row * D + 16 * dn + 4 * fg) = (u32x2){r2[0], r2[1]};
      STAMP(18);
      out_row();
    }
  } else {
    out_row();
  }
  if (w == 0) STAMP(19);
  asm volatile("Lfk_code_end:");
  STAMP_FLUSH();
}

extern "C" void kernel_launch(void* const* d_in, const int* in_sizes, int n_in, void* d_out, int out_size, void* d_ws, size_t ws_size, hipStream_t stream) {
  Params p;
  p.x = (const u16*)d_in[0]; p.wqkv = (const u16*)d_in[1]; p.wo = (const u16*)d_in[2]; p.wr = (const u16*)d_in[4];
  p.wg = (const u16*)d_in[5]; p.wu = (const u16*)d_in[6]; p.wd = (const u16*)d_in[7]; p.out = (u16*)d_out; p.ws = (unsigned char*)d_ws + ((ws_size - (16u << 20)) & ~(size_t)4095);
  fused_kernel<<<256, 512, 0, stream>>>(p);
}
```

```cpp
#include <hip/hip_runtime.h>
#include <stdint.h>

typedef unsigned short u16;
typedef short bf16x8 __attribute__((ext_vector_type(8)));
typedef short bf16x4 __attribute__((ext_vector_type(4)));
typedef __bf16 bf2 __attribute__((ext_vector_type(2)));
typedef float f32x16 __attribute__((ext_vector_type(16)));
typedef float f32x4 __attribute__((ext_vector_type(4)));
typedef float f32x2 __attribute__((ext_vector_type(2)));
typedef unsigned u32x4 __attribute__((ext_vector_type(4)));
typedef unsigned u32x2 __attribute__((ext_vector_type(2)));
typedef unsigned long long u64;
#define LAS __attribute__((address_space(3)))
#define SBAR() __builtin_amdgcn_sched_barrier(0)
#define BARRIER() asm volatile("s_barrier" ::: "memory")
#define WAITL0() asm volatile("s_waitcnt lgkmcnt(0)" ::: "memory")
#define WAITV(n) asm volatile("s_waitcnt vmcnt(" #n ")" ::: "memory")
#define FENCE() asm volatile("" ::: "memory")

constexpr int S = 2048, D = 640, F = 1728;
constexpr int SC1 = 16;

constexpr unsigned WS_EPOCH = 0;
constexpr unsigned WS_FLAGA = 4096;
constexpr unsigned WS_FLAGO = WS_FLAGA + 32768;
constexpr unsigned WS_CAND = WS_FLAGO + 16384;
constexpr unsigned WS_HDONE = WS_CAND + 4096;
constexpr unsigned WS_ACT = WS_HDONE + 4096;
constexpr unsigned WS_ST1 = 131072;
constexpr unsigned WS_CS1 = WS_ST1 + 32768;
constexpr unsigned WS_STATS = 524288;
constexpr unsigned WS_QKV = 1048576;
constexpr unsigned WS_O = WS_QKV + 480 * 16384;
constexpr unsigned WS_H = WS_O + S * D * 2;
#define WS_TAIL 1
#define STAMP_OFF (15u << 20)
#define NSTAMP 64

constexpr int L_SLOT = 32768, L_W = 16384;
constexpr int PL = 4128;
constexpr int L_K = 0, L_V = 5 * PL;
constexpr int L_WO = 45568;
constexpr int L_ROWS = 131072;
constexpr int L_RED = 0;
constexpr int L_HST = 20480;
constexpr int L_FW = L_WO;
constexpr int L_XS = 0;
constexpr int L_FRED = 12288;
constexpr int L_MISC = L_WO + 640 * 160;
constexpr int L_RS = 0;
constexpr int L_CS = 1024;
constexpr int L_CNT = L_MISC + 3600;
constexpr int L_F1 = L_MISC + 3604, L_C1 = L_MISC + 3608;
constexpr int L_PART = L_MISC + 2048;
constexpr int L_K32 = L_MISC + 2560;
constexpr int L_HD = L_MISC + 3712;
constexpr int L_SEL = L_MISC + 3968;
constexpr int L_STAMP = L_MISC + 4096;
constexpr int L_KEY = L_MISC + 4608 + 256;
constexpr int LDS_BYTES = 163840;
static_assert(L_V + 6 * PL <= L_WO && L_KEY + 2048 <= LDS_BYTES, "lds map");

struct Params { const u16* x; const u16* wqkv; const u16* wo; const u16* wr; const u16* wg; const u16* wu; const u16* wd; u16* out; unsigned char* ws; };

__device__ __forceinline__ float bf2f(unsigned v) { return __uint_as_float(v << 16); }
__device__ __forceinline__ float bfhi(unsigned v) { return __uint_as_float(v & 0xffff0000u); }
__device__ __forceinline__ unsigned pk2(float a, float b) { f32x2 f = {a, b}; return __builtin_bit_cast(unsigned, __builtin_convertvector(f, bf2)); }
__device__ __forceinline__ float rbf(float f) { return __uint_as_float(pk2(f, 0.f) << 16); }
__device__ __forceinline__ float dot2(unsigned a, unsigned b, float c) { return __builtin_amdgcn_fdot2_f32_bf16(__builtin_bit_cast(bf2, a), __builtin_bit_cast(bf2, b), c, false); }
__device__ __forceinline__ float swap_add(float v) { auto r = __builtin_amdgcn_permlane32_swap(__float_as_uint(v), __float_as_uint(v), false, false); return __uint_as_float(r[0]) + __uint_as_float(r[1]); }
__device__ __forceinline__ float swap16_add(float v) { auto r = __builtin_amdgcn_permlane16_swap(__float_as_uint(v), __float_as_uint(v), false, false); return __uint_as_float(r[0]) + __uint_as_float(r[1]); }
template <int CTRL> __device__ __forceinline__ float dppadd(float v) { return v + __int_as_float(__builtin_amdgcn_update_dpp(0, __float_as_int(v), CTRL, 0xf, 0xf, false)); }
__device__ __forceinline__ float wave_fsum(float v) { v = dppadd<0x128>(v); v = dppadd<0x124>(v); v = dppadd<0x122>(v); v = dppadd<0x121>(v); return swap_add(swap16_add(v)); }
template <int CTRL> __device__ __forceinline__ unsigned dppmax(unsigned v) { return max(v, (unsigned)__builtin_amdgcn_update_dpp(0, (int)v, CTRL, 0xf, 0xf, false)); }
__device__ __forceinline__ unsigned wave_umax(unsigned v) {
  v = dppmax<0x128>(v); v = dppmax<0x124>(v); v = dppmax<0x122>(v); v = dppmax<0x121>(v);
  auto r = __builtin_amdgcn_permlane16_swap(v, v, false, false); v = max(r[0], r[1]);
  auto q = __builtin_amdgcn_permlane32_swap(v, v, false, false); return max(q[0], q[1]);
}
__device__ __forceinline__ void glds16(unsigned voff, const void* sbase, unsigned lds) {
  asm volatile("s_mov_b32 m0, %2\n\ts_nop 0\n\tglobal_load_lds_dwordx4 %0, %1" ::"v"(voff), "s"(sbase), "s"(lds) : "memory");
}
__device__ __forceinline__ void glds16c(unsigned voff, const void* sbase, unsigned lds) {
  asm volatile("s_mov_b32 m0, %2\n\ts_nop 0\n\tglobal_load_lds_dwordx4 %0, %1 sc1" ::"v"(voff), "s"(sbase), "s"(lds) : "memory");
}
__device__ __forceinline__ void touch64(unsigned voff, const void* sbase, unsigned lds) {
  asm volatile("s_mov_b32 m0, %2\n\ts_nop 0\n\tglobal_load_lds_dword %0, %1" ::"v"(voff), "s"(sbase), "s"(lds) : "memory");
}
__device__ __forceinline__ void glds16n(unsigned voff, const void* sbase, unsigned lds) {
  asm volatile("s_mov_b32 m0, %2\n\ts_nop 0\n\tglobal_load_lds_dwordx4 %0, %1 nt" ::"v"(voff), "s"(sbase), "s"(lds) : "memory");
}
#define GL_FW glds16
#define GL_WO glds16
#define GL_X glds16
#define GL_W glds16
__device__ __forceinline__ void touch_ptr(const void* g, unsigned lds) {
  asm volatile("s_mov_b32 m0, %1\n\ts_nop 0\n\tglobal_load_lds_dword %0, off" ::"v"(g), "s"(lds) : "memory");
}
__device__ __forceinline__ bf16x4 trd(const LAS char* p) { return __builtin_amdgcn_ds_read_tr16_b64_v4i16((LAS bf16x4*)p); }
__device__ __forceinline__ bf16x8 cat8(bf16x4 a, bf16x4 b) { return (bf16x8){a[0], a[1], a[2], a[3], b[0], b[1], b[2], b[3]}; }
#define MFMA32(a, b, c) __builtin_amdgcn_mfma_f32_32x32x16_bf16(a, b, c, 0, 0, 0)
#define MFMA32K8(a, b, c) __builtin_amdgcn_mfma_f32_32x32x8bf16_1k(a, b, c, 0, 0, 0)
#define MFMA16(a, b, c) __builtin_amdgcn_mfma_f32_16x16x32_bf16(a, b, c, 0, 0, 0)
#define LD128(off) __builtin_amdgcn_raw_buffer_load_b128(rws, (off), 0, SC1)
#define LD64(off) __builtin_amdgcn_raw_buffer_load_b64(rws, (off), 0, SC1)
#define ST128(v, off) __builtin_amdgcn_raw_buffer_store_b128((v), rws, (off), 0, SC1)
#define ST64(v, off) __builtin_amdgcn_raw_buffer_store_b64((v), rws, (off), 0, SC1)

#define STAMP(k) do {} while (0)
#define STAMP_INIT() do {} while (0)
#define PSTAMP(i) do {} while (0)
#define STAMP_ZERO() do {} while (0)
#define STAMP_PA() do {} while (0)
#define STAMP_FLUSH() do {} while (0)

#define ROT (((bi >> 3) * 5 / 2) % 10)
constexpr unsigned SPIN_CAP = 1u << 17;

__global__ __launch_bounds__(512, 2) void fused_kernel(Params p) {
  __shared__ __attribute__((aligned(16))) char smem[LDS_BYTES];
  LAS char* L = (LAS char*)smem;
  const unsigned lds0 = (unsigned)(uintptr_t)smem;
  const int tid = threadIdx.x, lane = tid & 63;
  const int w = __builtin_amdgcn_readfirstlane(tid >> 6);
  const int b = blockIdx.x, xcd = b & 7, bi = b >> 3;
  const int l31 = lane & 31, hh = lane >> 5, l15 = lane & 15, fg = lane >> 4;
  const __amdgpu_buffer_rsrc_t rws = __builtin_amdgcn_make_buffer_rsrc(p.ws, 0, 16u << 20, 0x00020000);
  const int aP = 4 * (xcd >> 1) + (bi >> 3), aj = 8 * (xcd & 1) + (bi & 7);
  const int hd = 4 * xcd + (bi >> 3), pp = bi & 7;
  const int R = 4 * xcd + (bi >> 3), cc = bi & 7;
  const int gs = xcd * 14 + bi;
  const bool is_gu = bi < 14 && gs < 108;
  const int dn = xcd * 5 + (bi - 14);
  const bool is_dn = bi >= 14 && bi < 19;
  asm volatile("Lfk_start:");
  STAMP_INIT();
  u32x2 tag2;
  auto read_epoch = [&]() {
    u64 e;
    asm volatile("s_load_dwordx2 %0, %1, 0x0\n\ts_waitcnt lgkmcnt(0)" : "=s"(e) : "s"(p.ws) : "memory");
    e += 1;
    tag2 = (u32x2){(unsigned)e, (unsigned)(e >> 32)};
  };

  auto rows_dma = [&](int lw) {
    const char* rb = (const char*)p.x + (size_t)(64 * R + 8 * cc + 2 * lw) * 1280;
    glds16(lane * 16, rb, lds0 + L_ROWS + lw * 2560);
    glds16(lane * 16, rb + 1024, lds0 + L_ROWS + lw * 2560 + 1024);
    if (lane < 32) glds16(lane * 16, rb + 2048, lds0 + L_ROWS + lw * 2560 + 2048);
  };
  auto slot_clears = [&]() {
    const unsigned so = WS_STATS + (R * 8 + cc) * 1024 + lane * 16, ao = WS_ACT + (((gs >> 1) * 4 + 2 * (gs & 1) + ((lane >> 4) & 1)) * 8 + (lane & 7)) * 32 + ((lane >> 3) & 1) * 16;
    const unsigned off = w == 0 ? so : ((w == 1 && is_gu && lane < 32) ? ao : 0x80000000u);
    ST128(((u32x4){0, 0, 0, 0}), off);
  };
  auto rows_finish = [&]() {
    float mean[2], rs[2];
#pragma unroll
    for (int r = 0; r < 2; ++r) {
      const u32x4 c0 = *(const LAS u32x4*)(L + L_ROWS + (2 * w + r) * 1280 + lane * 16), c1 = *(const LAS u32x4*)(L + L_ROWS + (2 * w + r) * 1280 + 1024 + l15 * 16);
      float s0 = 0.f, s1 = 0.f;
#pragma unroll
      for (int e = 0; e < 4; ++e) { s0 = dot2(c0[e], 0x3f803f80u, s0); s1 = dot2(c1[e], 0x3f803f80u, s1); }
      mean[r] = wave_fsum(s0 + (lane < 16 ? s1 : 0.f)) * (1.f / D);
      const f32x2 mm = {mean[r], mean[r]};
      f32x2 q0 = {0.f, 0.f}, q1 = {0.f, 0.f};
#pragma unroll
      for (int e = 0; e < 4; ++e) { const f32x2 d0 = (f32x2){bf2f(c0[e]), bfhi(c0[e])} - mm, d1 = (f32x2){bf2f(c1[e]), bfhi(c1[e])} - mm; q0 += d0 * d0; q1 += d1 * d1; }
      const float var = wave_fsum((q0[0] + q0[1]) + (lane < 16 ? q1[0] + q1[1] : 0.f)) * (1.f / D);
      rs[r] = __builtin_amdgcn_rcpf(rbf(__builtin_amdgcn_sqrtf(rbf(rbf(var) + 1e-5f))));
    }
    WAITV(0);
    ST128(((u32x4){__float_as_uint(rbf(lane ? mean[1] : mean[0])), __float_as_uint(lane ? rs[1] : rs[0]), tag2[0], tag2[1]}), lane < 2 ? WS_ST1 + (unsigned)(64 * R + 8 * cc + 2 * w + lane) * 16 : 0x80000000u);
  };

  auto code_touch = [&](int lw) {
    u64 pc; unsigned back, len;
    asm volatile("s_getpc_b64 %0\nLfk_here%=:\n\ts_mov_b32 %1, Lfk_here%=-Lfk_start\n\ts_mov_b32 %2, Lfk_code_end-Lfk_start" : "=s"(pc), "=s"(back), "=s"(len));
    const char* cb = (const char*)((pc - back) & ~(u64)127);
    touch_ptr(cb + min((unsigned)((bi * 4 + lw) * 3 + min(lane, 2)) * 128u, len), lds0 + L_MISC + 8192);
  };
  auto lds_ctl_init = [&]() {
    if (tid == 0) { *(LAS unsigned*)(L + L_F1) = 0; *(LAS unsigned*)(L + L_C1) = 0; }
    STAMP_ZERO();
  };
  if (aj < 15 && w >= 4) {
    const int lw = w - 4;
    const unsigned xsw = (unsigned)(4 * (lw & 1) + (lane >> 4));
    const unsigned voff_x = (unsigned)(lane >> 3) * 1280u + (((unsigned)(lane & 7) ^ xsw) << 4);
    const unsigned ch = (unsigned)(lane & 15) ^ (((unsigned)(lane >> 4) << 2) | (unsigned)lw);
    const unsigned voff_w = (unsigned)(lane >> 4) * 3840u + ch * 16u;
    const char* xb = (const char*)p.x + (size_t)(128 * aP + 8 * lw) * 1280;
    const char* wb = (const char*)p.wqkv + (size_t)(4 * lw) * 3840 + 256 * aj;
    const unsigned lbase = lds0 + lw * 1024;
    const int rot = ROT;
    auto issue = [&](int s) {
      const unsigned sl = lbase + (s & 3) * L_SLOT;
      int ss = s + rot; if (ss >= 10) ss -= 10;
#pragma unroll
      for (int i = 0; i < 4; ++i) {
        GL_X(voff_x, xb + (size_t)i * 32 * 1280 + ss * 128, sl + i * 4096);
        GL_W(voff_w, wb + ((size_t)ss * 64 + i * 16) * 3840, sl + L_W + i * 4096);
      }
    };
    rows_dma(lw);
    issue(0);
    code_touch(lw);
    read_epoch();
    WAITV(9); BARRIER();
    issue(1); issue(2);
    WAITV(17); BARRIER();
    PSTAMP(1);
#pragma unroll 1
    for (int s = 0; s <= 5; ++s) { issue(s + 3); WAITV(16); BARRIER(); }
    issue(9);
    const unsigned so = WS_ST1 + (unsigned)(128 * aP + 32 * lw + l31) * 16, co = WS_CS1 + (unsigned)((aj * 16 + fg) * 64 + 16 * lw + l15) * 16;
    u32x4 gs1 = LD128(so), gc0 = LD128(co), gc1 = LD128(co + 4096), gc2 = LD128(co + 8192), gc3 = LD128(co + 12288);
    WAITV(21); BARRIER();
    WAITV(13); BARRIER();
    WAITV(5); BARRIER();
    unsigned spins = 0;
#pragma clang loop unroll(disable)
    for (;;) {
      const bool ok = gs1[2] == tag2[0] && gs1[3] == tag2[1] && gc0[2] == tag2[0] && gc0[3] == tag2[1] && gc1[2] == tag2[0] && gc1[3] == tag2[1] && gc2[2] == tag2[0] && gc2[3] == tag2[1] && gc3[2] == tag2[0] && gc3[3] == tag2[1];
      if (__all(ok) || ++spins > SPIN_CAP) break;
      FENCE();
      gs1 = LD128(so); gc0 = LD128(co); gc1 = LD128(co + 4096); gc2 = LD128(co + 8192); gc3 = LD128(co + 12288);
    }
    if (lane < 32) *(LAS u32x2*)(L + L_RS + (32 * lw + l31) * 8) = (u32x2){gs1[0], gs1[1]};
    {
      const float c0 = swap_add(swap16_add((__uint_as_float(gc0[0]) + __uint_as_float(gc1[0])) + (__uint_as_float(gc2[0]) + __uint_as_float(gc3[0]))));
      const float c1 = swap_add(swap16_add((__uint_as_float(gc0[1]) + __uint_as_float(gc1[1])) + (__uint_as_float(gc2[1]) + __uint_as_float(gc3[1]))));
      if (lane < 16) *(LAS f32x2*)(L + L_CS + (32 * lw + 2 * lane) * 4) = (f32x2){c0, c1};
    }
    WAITL0(); BARRIER();
    BARRIER();
  } else if (w < 4) {
    slot_clears();
    read_epoch();
    BARRIER();
    rows_finish();
    if (aj < 15) {
      PSTAMP(7);
      const int wm = w >> 1, wn = w & 1;
      unsigned xa[4], wa[4];
#pragma unroll
      for (int ks = 0; ks < 4; ++ks) xa[ks] = (unsigned)(64 * wm + l31) * 128u + ((((unsigned)(2 * ks + hh)) ^ (((unsigned)l31 >> 1) & 7u)) << 4);
      {
        const unsigned blk = (lane >> 4) & 1, q = (lane & 15) >> 2, pq = lane & 3;
#pragma unroll
        for (int j = 0; j < 2; ++j)
#pragma unroll
          for (int t = 0; t < 2; ++t) {
            const unsigned row = 8 * hh + 4 * t + q, chn = 4 * (2 * wn + (j ^ wm)) + 2 * blk + (pq >> 1), m = (q << 2) | (unsigned)(2 * hh + t);
            wa[j * 2 + t] = L_W + 256u * row + 16u * (chn ^ m) + 8u * (pq & 1);
          }
      }
      f32x16 a00 = {}, a01 = {}, a10 = {}, a11 = {};
      bf16x8 xf0[2], wf0[2], xf1[2], wf1[2];

      unsigned xb[4], wb[4];
#define SETSLOT(so) do { _Pragma("unroll") for (int k_ = 0; k_ < 4; ++k_) { xb[k_] = xa[k_] + (so); wb[k_] = wa[k_] + (so); } } while (0)
#define RD(XF, WF, ks) do { \
        XF[0] = *(const LAS bf16x8*)(L + xb[ks]); WF[0] = cat8(trd(L + wb[0] + (ks) * 4096), trd(L + wb[1] + (ks) * 4096)); \
        WF[1] = cat8(trd(L + wb[2] + (ks) * 4096), trd(L + wb[3] + (ks) * 4096)); XF[1] = *(const LAS bf16x8*)(L + xb[ks] + 4096); } while (0)
#define MM(XF, WF) do { \
        a00 = MFMA32(WF[0], XF[0], a00); a01 = MFMA32(WF[1], XF[0], a01); \
        a10 = MFMA32(WF[0], XF[1], a10); a11 = MFMA32(WF[1], XF[1], a11); } while (0)
#define PINALL() asm volatile("" : "+v"(a00), "+v"(a01), "+v"(a10), "+v"(a11))
#define ILV() do { __builtin_amdgcn_sched_group_barrier(0x8, 1, 0); __builtin_amdgcn_sched_group_barrier(0x100, 3, 0); \
        __builtin_amdgcn_sched_group_barrier(0x8, 1, 0); __builtin_amdgcn_sched_group_barrier(0x100, 3, 0); __builtin_amdgcn_sched_group_barrier(0x8, 2, 0); } while (0)
#define STEP(XN, WN, ksn, XC, WC) do { RD(XN, WN, ksn); MM(XC, WC); ILV(); PINALL(); SBAR(); } while (0)
      BARRIER();
      SETSLOT(0);
      RD(xf0, wf0, 0);
      SBAR();
#pragma unroll 1
      for (int s = 0; s < 9; ++s) {
        STEP(xf1, wf1, 1, xf0, wf0);
        STEP(xf0, wf0, 2, xf1, wf1);
        STEP(xf1, wf1, 3, xf0, wf0);
        SETSLOT(((s + 1) & 3) * L_SLOT);
        WAITL0(); BARRIER();
        STEP(xf0, wf0, 0, xf1, wf1);
      }
      STEP(xf1, wf1, 1, xf0, wf0);
      STEP(xf0, wf0, 2, xf1, wf1);
      STEP(xf1, wf1, 3, xf0, wf0);
      MM(xf1, wf1); PINALL();
      PSTAMP(2);
      WAITL0(); BARRIER();
      PSTAMP(6);
      const float qs = aj < 5 ? 0.22360679774997896f * 1.4426950408889634f : 1.f;
      f32x2 rs0 = *(const LAS f32x2*)(L + L_RS + (64 * wm + l31) * 8), rs1 = *(const LAS f32x2*)(L + L_RS + (64 * wm + 32 + l31) * 8);
      const float sc0 = rs0[1] * qs, nm0 = -rs0[0] * sc0, sc1 = rs1[1] * qs, nm1 = -rs1[0] * sc1;
#pragma unroll
      for (int j = 0; j < 2; ++j) {
        const int fj = 64 * wn + 32 * (j ^ wm) + 4 * hh;
        const unsigned go = WS_QKV + (unsigned)(32 * aj + (fj >> 2)) * 16384u + (unsigned)(128 * aP + 64 * wm + l31) * 8u;
#pragma unroll
        for (int rq = 0; rq < 4; ++rq) {
          const f32x4 cs = *(const LAS f32x4*)(L + L_CS + (fj + 8 * rq) * 4);
          const f32x16& a0 = j == 0 ? a00 : a01; const f32x16& a1 = j == 0 ? a10 : a11;
          const f32x2 c01 = {cs[0], cs[1]}, c23 = {cs[2], cs[3]};
          const f32x2 u0 = (f32x2){a0[rq * 4], a0[rq * 4 + 1]} * sc0 + c01 * nm0, u1 = (f32x2){a0[rq * 4 + 2], a0[rq * 4 + 3]} * sc0 + c23 * nm0;
          const f32x2 u2 = (f32x2){a1[rq * 4], a1[rq * 4 + 1]} * sc1 + c01 * nm1, u3 = (f32x2){a1[rq * 4 + 2], a1[rq * 4 + 3]} * sc1 + c23 * nm1;
          ST64(((u32x2){pk2(u0[0], u0[1]), pk2(u1[0], u1[1])}), go + 2 * rq * 16384);
          ST64(((u32x2){pk2(u2[0], u2[1]), pk2(u3[0], u3[1])}), go + 2 * rq * 16384 + 256);
        }
      }
      PSTAMP(4);
      lds_ctl_init();
      WAITV(0); WAITL0();
      PSTAMP(5);
      BARRIER();
    }
  } else {
    rows_dma(w - 4);
    code_touch(w - 4);
    read_epoch();
    WAITV(1); BARRIER();
  }
  if (aj >= 15) {
    const int chn = min(32 * w + l31, 239);
    const char* wp0 = (const char*)p.wqkv + (size_t)(40 * aP + 20 * hh) * 3840 + chn * 16;
    u32x4 wv[20];
#pragma unroll
    for (int i = 0; i < 20; ++i) wv[i] = *(const u32x4*)(wp0 + (size_t)i * 3840);
    SBAR();
    f32x2 c2[4] = {{0.f, 0.f}, {0.f, 0.f}, {0.f, 0.f}, {0.f, 0.f}};
#pragma unroll
    for (int i = 0; i < 20; ++i)
#pragma unroll
      for (int e = 0; e < 4; ++e) c2[e] += (f32x2){bf2f(wv[i][e]), bfhi(wv[i][e])};
#pragma unroll
    for (int e = 0; e < 4; ++e) c2[e] = (f32x2){swap_add(c2[e][0]), swap_add(c2[e][1])};
    if (lane < 32 && 32 * w + l31 < 240) {
#pragma unroll
      for (int e = 0; e < 4; ++e) ST128(((u32x4){__float_as_uint(c2[e][0]), __float_as_uint(c2[e][1]), tag2[0], tag2[1]}), WS_CS1 + (unsigned)(((chn >> 4) * 16 + aP) * 64 + 4 * (chn & 15) + e) * 16);
    }
    lds_ctl_init(); WAITL0(); BARRIER();
  }
  const unsigned tag32 = tag2[0] | 1u;
  if (aj < 15 && tid == 0) ST64(tag2, WS_FLAGA + (aP * 16 + aj) * 128);
  PSTAMP(3);
  STAMP_PA();

  const int nt = w & 3, kh = w >> 2;
  const int tok = 64 * R + 16 * nt + l15;
  u32x2 xr[5], wrv[5];
  unsigned wr5[5];
  {
    const int t = w;
    *(LAS u32x2*)(L + L_V + 5 * PL + tid * 8) = (u32x2){(pp > 0 || tid >= 256) ? 0x00003f80u : 0u, 0u};
    if (pp == 0) {
#pragma unroll
      for (int i = 0; i < 3; ++i) { const int n = tid + 512 * i; if (n < 1280) *(LAS u32x4*)(L + (n >> 7) * PL + (n & 127) * 16) = (u32x4){0, 0, 0, 0}; }
    }
    if (kh == 0) {
#pragma unroll
      for (int ft = 0; ft < 5; ++ft) {
        xr[ft] = *(const u32x2*)(p.x + (size_t)tok * D + 80 * cc + 16 * ft + 4 * fg);
        wrv[ft] = *(const u32x2*)(p.wr + 80 * cc + 16 * ft + 4 * fg);
      }
    }
    if (bi == 31) {
#pragma unroll
      for (int i = 0; i < 5; ++i) wr5[i] = ((const unsigned*)p.wr)[lane + 64 * i];
    }
    f32x16 cdg, can;
    {
      auto diag_ok = [](int r) { unsigned long long m = 0; for (int l = 0; l < 64; ++l) if ((r & 3) + 8 * (r >> 2) + 4 * (l >> 5) <= (l & 31)) m |= 1ull << l; return m; };
#pragma unroll
      for (int r = 0; r < 16; ++r) { const bool ok = __builtin_amdgcn_inverse_ballot_w64(diag_ok(r)); cdg[r] = ok ? 0.f : -INFINITY; can[r] = ok ? -INFINITY : 0.f; }
      asm volatile("" : "+v"(cdg), "+v"(can));
    }
    unsigned wov[5];
#pragma unroll
    for (int j = 0; j < 5; ++j) { const int n = 64 * j + lane, rho = (n * 6554) >> 16, chn = n - 10 * rho; wov[j] = (unsigned)(8 * ((rho >> 2) & 3) + 4 * (rho >> 4) + (rho & 3)) * 1280u + chn * 16u; }
    asm volatile("" : "+v"(wov[0]), "+v"(wov[1]), "+v"(wov[2]), "+v"(wov[3]), "+v"(wov[4]));
    const char* wob = (const char*)p.wo + 160 * cc + (size_t)nt * 32 * 1280;
    const unsigned wol = lds0 + L_WO + nt * 5120;
    auto lds_wait = [&](int off, unsigned v) {
      unsigned sp = 0;
#pragma clang loop unroll(disable)
      while (__hip_atomic_load((LAS unsigned*)(L + off), __ATOMIC_RELAXED, __HIP_MEMORY_SCOPE_WORKGROUP) < v && ++sp < (SPIN_CAP << 4)) __builtin_amdgcn_s_sleep(0);
      FENCE();
    };
    WAITL0();
    if (kh == 0) {
      if (lane == 0) __hip_atomic_fetch_add((LAS unsigned*)(L + L_C1), 1u, __ATOMIC_RELAXED, __HIP_MEMORY_SCOPE_WORKGROUP);
      lds_wait(L_F1, 1);
    } else {
      if (w == 4) {
        const int sec = lane < 4 ? 0 : (lane < 12 ? 1 : 2);
        const int k = lane < 4 ? lane : (lane < 12 ? lane - 4 : lane - 12);
        const int col = sec * 640 + 20 * hd + ((k & 1) ? 19 : 0);
        const int Pn = sec == 0 ? 2 * pp + (k >> 1) : 2 * pp - 2 + (k >> 1);
        const bool need = lane < 20 && Pn >= 0;
        const unsigned fo = WS_FLAGA + (unsigned)((need ? Pn : 2 * pp) * 16 + (col >> 7)) * 128;
        unsigned spins = 0;
#pragma clang loop unroll(disable)
        for (;;) {
          FENCE();
          const u32x2 f = LD64(fo);
          if (__all(!need || (f[0] == tag2[0] && f[1] == tag2[1])) || ++spins > SPIN_CAP) break;
          __builtin_amdgcn_s_sleep(1);
        }
        if (lane == 0) __hip_atomic_store((LAS unsigned*)(L + L_F1), 1u, __ATOMIC_RELAXED, __HIP_MEMORY_SCOPE_WORKGROUP);
        STAMP(4);
      } else lds_wait(L_F1, 1);
      const char* qb = (const char*)p.ws + WS_QKV;
#pragma unroll
      for (int i = 0; i < 10; ++i) {
        const int pc = nt + 4 * i, pl = pc >> 2, qt = pc & 3;
        const int tok0 = 256 * pp - 256 + 128 * qt;
        if (tok0 >= 0) glds16c(lane * 16, qb + (size_t)((pl < 5 ? 160 + 5 * hd + pl : 320 + 5 * hd + pl - 5)) * 16384 + tok0 * 8, lds0 + pl * PL + qt * 1024);
      }
    }
    bf16x8 qf0; bf16x4 qf1;
    {
      const unsigned qo = WS_QKV + (unsigned)(5 * hd) * 16384u + (unsigned)(256 * pp + 32 * t + l31) * 8u;
      const u32x2 a = LD64(qo + (2 * hh) * 16384), c = LD64(qo + (2 * hh + 1) * 16384);
      u32x2 z = LD64(qo + 4 * 16384);
      asm volatile("s_waitcnt vmcnt(0)" : "+v"(z) :: "memory");
      if (hh) z = (u32x2){0, 0};
      qf0 = __builtin_bit_cast(bf16x8, (u32x4){a[0], a[1], c[0], c[1]});
      qf1 = __builtin_bit_cast(bf16x4, z);
    }
    if (kh == 1 && lane == 0) __hip_atomic_fetch_add((LAS unsigned*)(L + L_C1), 1u, __ATOMIC_RELAXED, __HIP_MEMORY_SCOPE_WORKGROUP);
    lds_wait(L_C1, 8);
    if (w == 4) STAMP(5);
#define WOP(i) GL_WO(wov[(i) % 5], wob + (size_t)((i) / 5) * 4 * 32 * 1280, wol + ((i) / 5) * 4 * 5120 + ((i) % 5) * 1024)
#define WOP3(i) do { if (kh == 0) { WOP(i); WOP((i) + 1); WOP((i) + 2); } SBAR(); } while (0)
    if (w == 0) STAMP(28);
    const LAS char* kb = L + L_K + l31 * 8 + (2 * hh) * PL;
    const LAS char* kb4 = L + (hh ? L_V + 5 * PL : L_K + 4 * PL) + l31 * 8;
    {
      const u32x2 k0 = *(const LAS u32x2*)(kb + (t + 8) * 256), k1 = *(const LAS u32x2*)(kb + (t + 8) * 256 + PL), k2 = *(const LAS u32x2*)(kb4 + (t + 8) * 256);
      const u32x4 q0 = __builtin_bit_cast(u32x4, qf0); u32x2 q1 = __builtin_bit_cast(u32x2, qf1);
      float d = dot2(q0[0], k0[0], 0.f); d = dot2(q0[1], k0[1], d); d = dot2(q0[2], k1[0], d); d = dot2(q0[3], k1[1], d);
      d = dot2(q1[0], k2[0], d); d = dot2(q1[1], k2[1], d);
      const unsigned nm = pk2(-swap_add(d), 0.f);
      if (hh) q1[0] = nm;
      qf1 = __builtin_bit_cast(bf16x4, q1);
    }
    const LAS char* vb = L + L_V + min(4 * ((lane >> 4) & 1) + (lane & 3), 5) * PL + (4 * hh + ((lane & 15) >> 2)) * 8;
    f32x16 o = (f32x16){};
    {
      f32x16 sa, sb;
      const f32x16 cz = {};
      bf16x4 ka, kc, kd;
      bf16x8 vq = {};
      u32x4 pq = {0, 0, 0, 0};
#define KRD(kt) do { ka = *(const LAS bf16x4*)(kb + (kt) * 256); kc = *(const LAS bf16x4*)(kb + (kt) * 256 + PL); kd = *(const LAS bf16x4*)(kb4 + (kt) * 256); } while (0)
#define EX2(S, i) pk2(__builtin_amdgcn_exp2f(S[i]), __builtin_amdgcn_exp2f(S[(i) + 1]))
#define GRP(nds, nv) do { __builtin_amdgcn_sched_group_barrier(0x8, 1, 0); if (nds) __builtin_amdgcn_sched_group_barrier(0x100, nds, 0); __builtin_amdgcn_sched_group_barrier(0x2, nv, 0); } while (0)
#define HALF(SC, SN, C, ktv, ktk) do { \
        u32x4 p0_; const LAS char* vp_ = vb + (ktv) * 256; \
        SN = MFMA32(cat8(ka, kc), qf0, C); \
        const bf16x8 v0_ = cat8(trd(vp_), trd(vp_ + 64)), v1_ = cat8(trd(vp_ + 128), trd(vp_ + 192)); \
        p0_[0] = EX2(SC, 0); p0_[1] = EX2(SC, 2); \
        GRP(4, 6); asm volatile("" : "+v"(SN), "+v"(p0_[0]), "+v"(p0_[1])); SBAR(); \
        o = MFMA32(vq, __builtin_bit_cast(bf16x8, pq), o); \
        p0_[2] = EX2(SC, 4); p0_[3] = EX2(SC, 6); \
        GRP(0, 6); asm volatile("" : "+v"(o), "+v"(p0_[2]), "+v"(p0_[3])); SBAR(); \
        SN = MFMA32K8(kd, qf1, SN); \
        KRD(ktk); \
        pq[0] = EX2(SC, 8); pq[1] = EX2(SC, 10); \
        GRP(3, 6); asm volatile("" : "+v"(SN), "+v"(pq[0]), "+v"(pq[1])); SBAR(); \
        o = MFMA32(v0_, __builtin_bit_cast(bf16x8, p0_), o); \
        pq[2] = EX2(SC, 12); pq[3] = EX2(SC, 14); vq = v1_; \
        GRP(0, 6); asm volatile("" : "+v"(o), "+v"(pq[2]), "+v"(pq[3])); SBAR(); } while (0)
      KRD(t + 8);
      sa = MFMA32(cat8(ka, kc), qf0, cdg); sa = MFMA32K8(kd, qf1, sa);
      KRD(t + 7);
      asm volatile("" : "+v"(sa)); SBAR();
      WOP3(0);
      HALF(sa, sb, cz, t + 8, t + 6);
      WOP3(3);
      HALF(sb, sa, cz, t + 7, t + 5);
      WOP3(6);
      HALF(sa, sb, cz, t + 6, t + 4);
      WOP3(9);
      HALF(sb, sa, cz, t + 5, t + 3);
      WOP3(12);
      HALF(sa, sb, cz, t + 4, t + 2);
      WOP3(15);
      HALF(sb, sa, cz, t + 3, t + 1);
      WOP3(18);
      HALF(sa, sb, cz, t + 2, t);
      if (kh == 0) { WOP(21); WOP(22); WOP(23); WOP(24); }
      SBAR();
      HALF(sb, sa, can, t + 1, t);
      {
        const LAS char* vp_ = vb + t * 256;
        const bf16x8 v0_ = cat8(trd(vp_), trd(vp_ + 64)), v1_ = cat8(trd(vp_ + 128), trd(vp_ + 192));
        o = MFMA32(vq, __builtin_bit_cast(bf16x8, pq), o);
        u32x4 p0_, p1_;
#pragma unroll
        for (int e = 0; e < 4; ++e) p0_[e] = EX2(sa, 2 * e);
        o = MFMA32(v0_, __builtin_bit_cast(bf16x8, p0_), o);
#pragma unroll
        for (int e = 0; e < 4; ++e) p1_[e] = EX2(sa, 8 + 2 * e);
        o = MFMA32(v1_, __builtin_bit_cast(bf16x8, p1_), o);
      }
    }
    float lsum;
    { auto r = __builtin_amdgcn_permlane32_swap(__float_as_uint(o[8]), __float_as_uint(o[8]), false, false); lsum = __uint_as_float(r[1]); }
    if (__builtin_expect(__any(!(lsum < 1.8446744e19f)), 0)) {
      const int lo = pp >= 1 ? t : 8;
      float mx = -INFINITY;
#pragma unroll 1
      for (int kt = t + 8; kt >= lo; --kt) {
        const bf16x4 ka = *(const LAS bf16x4*)(kb + kt * 256), kc = *(const LAS bf16x4*)(kb + kt * 256 + PL), kd = *(const LAS bf16x4*)(kb4 + kt * 256);
        f32x16 s = MFMA32(cat8(ka, kc), qf0, (f32x16){});
        s = MFMA32K8(kd, qf1, s);
#pragma unroll
        for (int r = 0; r < 16; ++r) { const int kr = (r & 3) + 8 * (r >> 2) + 4 * hh; const bool ok = kt == t + 8 ? kr <= l31 : (kt == t ? kr > l31 : true); mx = fmaxf(mx, ok ? s[r] : -INFINITY); }
      }
      { auto r = __builtin_amdgcn_permlane32_swap(__float_as_uint(mx), __float_as_uint(mx), false, false); mx = fmaxf(__uint_as_float(r[0]), __uint_as_float(r[1])); }
      o = (f32x16){};
#pragma unroll 1
      for (int kt = t + 8; kt >= lo; --kt) {
        const bf16x4 ka = *(const LAS bf16x4*)(kb + kt * 256), kc = *(const LAS bf16x4*)(kb + kt * 256 + PL), kd = *(const LAS bf16x4*)(kb4 + kt * 256);
        const LAS char* vp_ = vb + kt * 256;
        const bf16x8 v0_ = cat8(trd(vp_), trd(vp_ + 64)), v1_ = cat8(trd(vp_ + 128), trd(vp_ + 192));
        f32x16 s = MFMA32(cat8(ka, kc), qf0, (f32x16){});
        s = MFMA32K8(kd, qf1, s);
        u32x4 p0_, p1_;
#pragma unroll
        for (int r = 0; r < 16; ++r) { const int kr = (r & 3) + 8 * (r >> 2) + 4 * hh; const bool ok = kt == t + 8 ? kr <= l31 : (kt == t ? kr > l31 : true); s[r] = ok ? __builtin_amdgcn_exp2f(s[r] - mx) : 0.f; }
#pragma unroll
        for (int e = 0; e < 4; ++e) { p0_[e] = pk2(s[2 * e], s[2 * e + 1]); p1_[e] = pk2(s[8 + 2 * e], s[8 + 2 * e + 1]); }
        o = MFMA32(v0_, __builtin_bit_cast(bf16x8, p0_), o);
        o = MFMA32(v1_, __builtin_bit_cast(bf16x8, p1_), o);
      }
      auto r = __builtin_amdgcn_permlane32_swap(__float_as_uint(o[8]), __float_as_uint(o[8]), false, false); lsum = __uint_as_float(r[1]);
    }
    if (w == 0) STAMP(6);
    STAMP(32 + w);
    {
      const float inv = 1.f / lsum;
      unsigned A0 = pk2(o[0] * inv, o[1] * inv), A1 = pk2(o[2] * inv, o[3] * inv), B0 = pk2(o[4] * inv, o[5] * inv), B1 = pk2(o[6] * inv, o[7] * inv);
      unsigned C0 = pk2(o[8] * inv, o[9] * inv), C1 = pk2(o[10] * inv, o[11] * inv);
      const int tt = pp * 16 + 2 * t + (l31 >> 4);
      const unsigned base = WS_O + (unsigned)tt * 80 * 256 + (l31 & 15) * 16 + (unsigned)(5 * (hd >> 1)) * 256;
      if ((hd & 1) == 0) {
        auto s0 = __builtin_amdgcn_permlane32_swap(A0, B0, false, false), s1 = __builtin_amdgcn_permlane32_swap(A1, B1, false, false);
        ST128(((u32x4){s0[0], s1[0], s0[1], s1[1]}), base + hh * 256);
        if (hh == 0) ST64(((u32x2){C0, C1}), base + 2 * 256);
      } else {
        auto u0 = __builtin_amdgcn_permlane32_swap(A0, C0, false, false), u1 = __builtin_amdgcn_permlane32_swap(A1, C1, false, false);
        const u32x4 v = hh ? (u32x4){B0, B1, u0[0], u1[0]} : (u32x4){u0[1], u1[1], B0, B1};
        ST128(v, base + (3 + hh) * 256);
        if (hh == 0) ST64(((u32x2){A0, A1}), base + 2 * 256 + 8);
      }
      WAITV(0);
      if (lane == 0) ST64(tag2, WS_FLAGO + (((pp * 8 + t) * 2 + (hd >> 4)) * 16 + (hd & 15)) * 8);
    }
  }
  if (w == 0) STAMP(7);
  STAMP(40 + w);

  BARRIER();
  if (w == 0) STAMP(8);

  f32x4 acc[5];
#pragma unroll
  for (int ft = 0; ft < 5; ++ft) acc[ft] = (f32x4){0, 0, 0, 0};
  {
    const LAS char* wp = L + L_WO + (unsigned)(kh * 320 + 4 * fg + ((lane & 15) >> 2)) * 160 + (lane & 3) * 8;
    bf16x8 wa[3][5];
#pragma unroll
    for (int s = 0; s < 3; ++s)
#pragma unroll
      for (int ft = 0; ft < 5; ++ft) wa[s][ft] = cat8(trd(wp + s * 5120 + ft * 32), trd(wp + s * 5120 + 2560 + ft * 32));
    const int tg = 4 * R + nt;
    const unsigned fo = WS_FLAGO + ((((tg >> 4) * 8 + ((tg >> 1) & 7)) * 2 + kh) * 16 + l15) * 8;
    unsigned spins = 0;
#pragma clang loop unroll(disable)
    for (;;) {
      FENCE();
      const u32x2 f = LD64(fo);
      if (__all(f[0] == tag2[0] && f[1] == tag2[1]) || ++spins > SPIN_CAP) break;
      __builtin_amdgcn_s_sleep(1);
    }
    FENCE();
    if (w == 0) STAMP(9);
    STAMP(48 + w);
    u32x4 of[10];
#pragma unroll
    for (int s = 0; s < 10; ++s) of[s] = LD128(WS_O + (unsigned)(tg * 80 + 4 * (10 * kh + s)) * 256 + lane * 16);
    SBAR();
#pragma unroll
    for (int s = 0; s < 10; ++s) {
      const bf16x8 bfr = __builtin_bit_cast(bf16x8, of[s]);
#pragma unroll
      for (int ft = 0; ft < 5; ++ft) acc[ft] = MFMA16(s < 3 ? wa[s < 3 ? s : 0][ft] : cat8(trd(wp + s * 5120 + ft * 32), trd(wp + s * 5120 + 2560 + ft * 32)), bfr, acc[ft]);
    }
  }
  STAMP(56 + w);
  if (kh == 1) {
#pragma unroll
    for (int ft = 0; ft < 5; ++ft) *(LAS f32x4*)(L + L_RED + ((nt * 5 + ft) * 64 + lane) * 16) = acc[ft];
  }
  WAITL0(); BARRIER();
  if (w == 0) STAMP(10);
  if (kh == 0) {
    float s1 = 0.f, s2 = 0.f, s3 = 0.f;
#pragma unroll
    for (int ft = 0; ft < 5; ++ft) {
      const f32x4 o2 = *(const LAS f32x4*)(L + L_RED + ((nt * 5 + ft) * 64 + lane) * 16);
      const unsigned a01 = pk2(acc[ft][0] + o2[0], acc[ft][1] + o2[1]), a23 = pk2(acc[ft][2] + o2[2], acc[ft][3] + o2[3]);
      const unsigned h01 = pk2(bf2f(xr[ft][0]) + bf2f(a01), bfhi(xr[ft][0]) + bfhi(a01)), h23 = pk2(bf2f(xr[ft][1]) + bf2f(a23), bfhi(xr[ft][1]) + bfhi(a23));
      *(LAS u32x2*)(L + L_HST + nt * 2560 + l15 * 160 + ft * 32 + fg * 8) = (u32x2){h01, h23};
      s1 = dot2(h01, 0x3f803f80u, s1); s1 = dot2(h23, 0x3f803f80u, s1);
      s2 = dot2(h01, h01, s2); s2 = dot2(h23, h23, s2);
      s3 = dot2(h01, wrv[ft][0], s3); s3 = dot2(h23, wrv[ft][1], s3);
    }
    WAITL0();
#pragma unroll
    for (int i = 0; i < 3; ++i) {
      const int n = lane + 64 * i;
      if (n < 160) ST128(*(const LAS u32x4*)(L + L_HST + nt * 2560 + n * 16), WS_H + (unsigned)((R * 8 + cc) * 64 + 16 * nt) * 160 + n * 16);
    }
    s1 = swap_add(swap16_add(s1)); s2 = swap_add(swap16_add(s2)); s3 = swap_add(swap16_add(s3));
    WAITV(0);
    if (lane < 16) ST128(((u32x4){__float_as_uint(s1), __float_as_uint(s2), __float_as_uint(s3), tag32}), WS_STATS + ((R * 8 + cc) * 64 + 16 * nt + l15) * 16);
    if (w == 0) STAMP(11);
    BARRIER();
  } else {
    BARRIER();
    const int rl = lane >> 1, grow = 8 * ((rl >> 2) & 3) + 4 * (rl >> 4) + (rl & 3);
    if (is_gu) {
      const unsigned vo = (unsigned)grow * (F * 2) + (lane & 1) * 16;
#pragma unroll 1
      for (int pc = nt; pc < 40; pc += 4) { const int m = pc >= 20, s = pc - 20 * m; GL_FW(vo, (const char*)(m ? p.wu : p.wg) + (size_t)s * 32 * F * 2 + gs * 32, lds0 + L_FW + pc * 1024); }
    } else if (is_dn) {
      const unsigned vo = (unsigned)grow * (D * 2) + (lane & 1) * 16;
#pragma unroll 1
      for (int pc = nt; pc < 54; pc += 4) GL_FW(vo, (const char*)p.wd + (size_t)pc * 32 * D * 2 + dn * 32, lds0 + L_FW + pc * 1024);
    }
  }
  auto rank64 = [&](unsigned k) {
    const u32x4 oa = *(const LAS u32x4*)(L + L_KEY + w * 32), ob = *(const LAS u32x4*)(L + L_KEY + w * 32 + 16);
    unsigned c = 0;
#pragma unroll
    for (int e = 0; e < 4; ++e) c += (oa[e] > k) + (ob[e] > k);
    *(LAS unsigned char*)(L + L_PART + lane * 8 + w) = (unsigned char)c;
    WAITL0(); BARRIER();
    const u32x2 pr = *(const LAS u32x2*)(L + L_PART + lane * 8);
    return __builtin_amdgcn_sad_u8(pr[0], 0u, __builtin_amdgcn_sad_u8(pr[1], 0u, 0u));
  };
  if (bi == 31) {
    float swr = 0.f;
#pragma unroll
    for (int i = 0; i < 5; ++i) swr = dot2(wr5[i], 0x3f803f80u, swr);
    swr = wave_fsum(swr);
    const int tl = 32 * w + l31;
    const unsigned so = WS_STATS + (unsigned)(((4 * xcd + (tl >> 6)) * 8 + 4 * hh) * 64 + (tl & 63)) * 16;
    float t1, t2, t3;
    unsigned spins = 0;
#pragma clang loop unroll(disable)
    for (;;) {
      FENCE();
      const u32x4 g0 = LD128(so), g1 = LD128(so + 1024), g2 = LD128(so + 2048), g3 = LD128(so + 3072);
      t1 = (__uint_as_float(g0[0]) + __uint_as_float(g1[0])) + (__uint_as_float(g2[0]) + __uint_as_float(g3[0]));
      t2 = (__uint_as_float(g0[1]) + __uint_as_float(g1[1])) + (__uint_as_float(g2[1]) + __uint_as_float(g3[1]));
      t3 = (__uint_as_float(g0[2]) + __uint_as_float(g1[2])) + (__uint_as_float(g2[2]) + __uint_as_float(g3[2]));
      if (__all(g0[3] == tag32 && g1[3] == tag32 && g2[3] == tag32 && g3[3] == tag32) || ++spins > SPIN_CAP) break;
    }
    t1 = swap_add(t1); t2 = swap_add(t2); t3 = swap_add(t3);
    const float mean = t1 * (1.f / D);
    const float var = fmaxf(t2 * (1.f / D) - mean * mean, 0.f);
    const float mb = rbf(mean);
    const float sd = rbf(__builtin_amdgcn_sqrtf(rbf(rbf(var) + 1e-5f)));
    const unsigned rb = pk2((t3 - mb * swr) * __builtin_amdgcn_rcpf(sd), 0.f) & 0xffffu;
    const unsigned key = ((rb ^ ((rb & 0x8000u) ? 0xffffu : 0x8000u)) << 16) | (unsigned)(2047 - (256 * xcd + tl));
    const unsigned ms = (__float_as_uint(mb) & 0xffff0000u) | (__float_as_uint(sd) >> 16);
    if (w == 0) STAMP(12);
    if (lane < 32) *(LAS unsigned*)(L + L_K32 + tl * 4) = key;
    WAITL0();
    unsigned c = 0;
#pragma unroll
    for (int i = 0; i < 4; ++i) {
      const u32x4 o4 = *(const LAS u32x4*)(L + L_K32 + (32 * w + 16 * hh + 4 * i) * 4);
#pragma unroll
      for (int e = 0; e < 4; ++e) c += o4[e] > key;
    }
    { auto r = __builtin_amdgcn_permlane32_swap(c, c, false, false); c = r[0] + r[1]; }
    if (lane < 32 && c < 8) { *(LAS unsigned*)(L + L_KEY + (8 * w + c) * 4) = key; *(LAS unsigned*)(L + L_KEY + 256 + (8 * w + c) * 4) = ms; }
    WAITL0(); BARRIER();
    const unsigned k = *(const LAS unsigned*)(L + L_KEY + lane * 4), m2 = *(const LAS unsigned*)(L + L_KEY + 256 + lane * 4);
    const unsigned rk = rank64(k);
    if (w == 0) { if (rk < 8) ST128(((u32x4){k, tag2[0], m2, tag2[1]}), WS_CAND + xcd * 128 + rk * 16); STAMP(26); }
    BARRIER();
  }
  if (w == 0) {
    u32x4 a;
    unsigned spins = 0;
#pragma clang loop unroll(disable)
    for (;;) {
      FENCE();
      a = LD128(WS_CAND + lane * 16);
      if (__all(a[1] == tag2[0] && a[3] == tag2[1]) || ++spins > SPIN_CAP) break;
      if (is_gu || is_dn) __builtin_amdgcn_s_sleep(1); else __builtin_amdgcn_s_sleep(8);
    }
    STAMP(13);
    *(LAS unsigned*)(L + L_KEY + lane * 4) = a[0]; *(LAS unsigned*)(L + L_KEY + 256 + lane * 4) = a[2];
    if (b == 0 && lane == 0) ST64(tag2, WS_EPOCH);
  }
  const int hc0 = (lane * 6554) >> 16, hc1 = ((64 + l15) * 6554) >> 16;
  const unsigned ho0 = (unsigned)hc0 * (64 * 160) + (lane - 10 * hc0) * 16, ho1 = (unsigned)hc1 * (64 * 160) + (64 + l15 - 10 * hc1) * 16;
  if (kh == 1) WAITV(0);
  WAITL0(); BARRIER();
  const unsigned ck = *(const LAS unsigned*)(L + L_KEY + lane * 4), cm = *(const LAS unsigned*)(L + L_KEY + 256 + lane * 4);
  const unsigned crk = rank64(ck);
  if (w == 0) STAMP(14);

  auto h_row = [&](int row, u32x4& h0, u32x4& h1) {
    const unsigned rb = WS_H + (unsigned)((row >> 6) * 8 * 64 + (row & 63)) * 160;
    h0 = LD128(rb + ho0); h1 = LD128(rb + ho1);
  };
  auto ln8 = [&](const u32x4& h, float m, float iv) { u32x4 n;
#pragma unroll
    for (int e = 0; e < 4; ++e) { const unsigned d = pk2(bf2f(h[e]) - m, bfhi(h[e]) - m); n[e] = pk2(bf2f(d) * iv, bfhi(d) * iv); } return n; };
  auto out_row = [&]() {
    const int row = 64 * R + 8 * cc + w;
    if (!__any(crk < 8 && 2047 - (int)(ck & 0x7ff) == row)) {
      u32x4 h0, h1; h_row(row, h0, h1);
      const u32x4 g = LD128(WS_STATS + (unsigned)((R * 8 + (lane & 7)) * 64 + 8 * cc + w) * 16);
      float t1 = __uint_as_float(g[0]), t2 = __uint_as_float(g[1]);
      t1 = dppadd<0x141>(t1); t2 = dppadd<0x141>(t2);
      t1 = dppadd<0xb1>(t1); t2 = dppadd<0xb1>(t2);
      t1 = dppadd<0x4e>(t1); t2 = dppadd<0x4e>(t2);
      const float mean = t1 * (1.f / D);
      const float m = rbf(mean), iv = __builtin_amdgcn_rcpf(rbf(__builtin_amdgcn_sqrtf(rbf(rbf(fmaxf(t2 * (1.f / D) - mean * mean, 0.f)) + 1e-5f))));
      const u32x4 n0 = ln8(h0, m, iv), n1 = ln8(h1, m, iv);
      u32x4 r0, r1;
#pragma unroll
      for (int e = 0; e < 4; ++e) { r0[e] = pk2(bf2f(h0[e]) + bf2f(n0[e]), bfhi(h0[e]) + bfhi(n0[e])); r1[e] = pk2(bf2f(h1[e]) + bf2f(n1[e]), bfhi(h1[e]) + bfhi(n1[e])); }
      *(u32x4*)(p.out + (size_t)row * D + lane * 8) = r0;
      if (lane < 16) *(u32x4*)(p.out + (size_t)row * D + 512 + lane * 8) = r1;
    }
  };
  if (is_gu) {
    {
      const int sl = __builtin_ctzll(__ballot(crk == (unsigned)w));
      const u32x2 sk = (u32x2){(unsigned)__builtin_amdgcn_readlane((int)ck, sl), (unsigned)__builtin_amdgcn_readlane((int)cm, sl)};
      u32x4 h0, h1; h_row(2047 - (int)(sk[0] & 0x7ff), h0, h1);
      const float m = __uint_as_float(sk[1] & 0xffff0000u), iv = __builtin_amdgcn_rcpf(__uint_as_float(sk[1] << 16));
      *(LAS u32x4*)(L + L_XS + w * 1296 + lane * 16) = ln8(h0, m, iv);
      if (lane < 16) *(LAS u32x4*)(L + L_XS + w * 1296 + 1024 + lane * 16) = ln8(h1, m, iv);
    }
    WAITL0(); BARRIER();
    if (w == 0) STAMP(15);
    {
      const int m = w & 1, kq = w >> 1;
      f32x4 a4 = {0, 0, 0, 0}, b4 = {0, 0, 0, 0};
#pragma unroll
      for (int i = 0; i < 5; ++i) {
        const int s = 5 * kq + i;
        const LAS char* ap = L + L_FW + (m * 20 + s) * 1024 + lane * 8;
        const bf16x8 af = cat8(trd(ap), trd(ap + 512));
        const bf16x8 bfv = *(const LAS bf16x8*)(L + L_XS + (lane & 7) * 1296 + s * 64 + fg * 16);
        if (i & 1) b4 = MFMA16(af, bfv, b4); else a4 = MFMA16(af, bfv, a4);
      }
      *(LAS f32x4*)(L + L_FRED + (w * 64 + lane) * 16) = a4 + b4;
    }
    WAITL0(); BARRIER();
    if (w == 0) {
      f32x4 g4 = {0, 0, 0, 0}, u4 = {0, 0, 0, 0};
#pragma unroll
      for (int q = 0; q < 4; ++q) { g4 += *(const LAS f32x4*)(L + L_FRED + ((2 * q) * 64 + lane) * 16); u4 += *(const LAS f32x4*)(L + L_FRED + ((2 * q + 1) * 64 + lane) * 16); }
      float a[4];
#pragma unroll
      for (int e = 0; e < 4; ++e) { const float g = rbf(g4[e]), u = rbf(u4[e]); const float sg = rbf(g * __builtin_amdgcn_rcpf(1.f + __builtin_amdgcn_exp2f(g * -1.4426950408889634f))); a[e] = sg * u; }
      if (l15 < 8) ST128(((u32x4){pk2(a[0], a[1]), tag32, pk2(a[2], a[3]), tag32}), WS_ACT + (((gs >> 1) * 4 + 2 * (gs & 1) + (fg >> 1)) * 8 + l15) * 32 + (fg & 1) * 16);
      STAMP(16);
    }
    out_row();
  } else if (is_dn) {
    if (crk < 8) *(LAS u32x2*)(L + L_SEL + crk * 8) = (u32x2){ck, cm};
    WAITL0();
    const u32x2 sk = *(const LAS u32x2*)(L + L_SEL + (lane & 7) * 8);
    const int row = 2047 - (int)(sk[0] & 0x7ff);
    u32x2 hv = {0, 0}, nv = {0, 0};
    float sig = 0.f;
    if (w == 0) {
      const int col = 16 * dn + 4 * fg, c = col / 80;
      hv = LD64(WS_H + (unsigned)(((row >> 6) * 8 + c) * 64 + (row & 63)) * 160 + (col - 80 * c) * 2);
      const unsigned rk = sk[0] >> 16, rbits = (rk & 0x8000u) ? (rk ^ 0x8000u) : (rk ^ 0xffffu);
      sig = rbf(__builtin_amdgcn_rcpf(1.f + __builtin_amdgcn_exp2f(bf2f(rbits) * -1.4426950408889634f)));
      const float m = __uint_as_float(sk[1] & 0xffff0000u), iv = __builtin_amdgcn_rcpf(__uint_as_float(sk[1] << 16));
#pragma unroll
      for (int e = 0; e < 2; ++e) { const unsigned d2 = pk2(bf2f(hv[e]) - m, bfhi(hv[e]) - m); nv[e] = pk2(bf2f(d2) * iv, bfhi(d2) * iv); }
      asm volatile("" : "+v"(nv), "+v"(hv), "+v"(sig));
    } else out_row();
    f32x4 a4 = {0, 0, 0, 0}, b4 = {0, 0, 0, 0};
    {
      bf16x8 af[7];
#pragma unroll
      for (int i = 0; i < 7; ++i) { const LAS char* ap = L + L_FW + min(w + 8 * i, 53) * 1024 + lane * 8; af[i] = cat8(trd(ap), trd(ap + 512)); }
      u32x4 g0[7];
      unsigned spins = 0;
#pragma clang loop unroll(disable)
      for (;;) {
        FENCE();
        bool ok = true;
#pragma unroll
        for (int i = 0; i < 7; ++i) g0[i] = LD128(WS_ACT + ((min(w + 8 * i, 53) * 4 + fg) * 8 + (lane & 7)) * 32 + ((lane >> 3) & 1) * 16);
#pragma unroll
        for (int i = 0; i < 7; ++i) ok = ok && g0[i][1] == tag32 && g0[i][3] == tag32;
        if (__all(ok) || ++spins > SPIN_CAP) break;
      }
      if (w == 0) STAMP(17);
#pragma unroll
      for (int i = 0; i < 7; ++i) {
        const unsigned y0 = (unsigned)__builtin_amdgcn_update_dpp(0, (int)g0[i][0], 0x128, 0xf, 0xf, false), y1 = (unsigned)__builtin_amdgcn_update_dpp(0, (int)g0[i][2], 0x128, 0xf, 0xf, false);
        const bf16x8 bv = __builtin_bit_cast(bf16x8, (u32x4){g0[i][0], g0[i][2], y0, y1});
        if (i < 6 || w < 6) { if (i & 1) b4 = MFMA16(af[i], bv, b4); else a4 = MFMA16(af[i], bv, a4); }
      }
    }
    *(LAS f32x4*)(L + L_FRED + (w * 64 + lane) * 16) = a4 + b4;
    WAITL0(); BARRIER();
    if (w == 0) {
      f32x4 y = {0, 0, 0, 0};
#pragma unroll
      for (int q = 0; q < 8; ++q) y += *(const LAS f32x4*)(L + L_FRED + (q * 64 + lane) * 16);
      unsigned r2[2];
#pragma unroll
      for (int e = 0; e < 2; ++e) {
        const unsigned y2 = pk2(rbf(y[2 * e]) * sig, rbf(y[2 * e + 1]) * sig);
        const unsigned t2 = pk2(bf2f(nv[e]) + bf2f(y2), bfhi(nv[e]) + bfhi(y2));
        r2[e] = pk2(bf2f(hv[e]) + bf2f(t2), bfhi(hv[e]) + bfhi(t2));
      }
      if (l15 < 8) *(u32x2*)(p.out + (size_t)row * D + 16 * dn + 4 * fg) = (u32x2){r2[0], r2[1]};
      STAMP(18);
      out_row();
    }
  } else {
    out_row();
  }
  if (w == 0) STAMP(19);
  asm volatile("Lfk_code_end:");
  STAMP_FLUSH();
}

extern "C" void kernel_launch(void* const* d_in, const int* in_sizes, int n_in, void* d_out, int out_size, void* d_ws, size_t ws_size, hipStream_t stream) {
  Params p;
  p.x = (const u16*)d_in[0]; p.wqkv = (const u16*)d_in[1]; p.wo = (const u16*)d_in[2]; p.wr = (const u16*)d_in[4];
  p.wg = (const u16*)d_in[5]; p.wu = (const u16*)d_in[6]; p.wd = (const u16*)d_in[7]; p.out = (u16*)d_out; p.ws = (unsigned char*)d_ws + ((ws_size - (16u << 20)) & ~(size_t)4095);
  fused_kernel<<<256, 512, 0, stream>>>(p);
}
```

```cpp
#include <hip/hip_runtime.h>
#include <stdint.h>

typedef unsigned short u16;
typedef short bf16x8 __attribute__((ext_vector_type(8)));
typedef short bf16x4 __attribute__((ext_vector_type(4)));
typedef __bf16 bf2 __attribute__((ext_vector_type(2)));
typedef float f32x16 __attribute__((ext_vector_type(16)));
typedef float f32x4 __attribute__((ext_vector_type(4)));
typedef float f32x2 __attribute__((ext_vector_type(2)));
typedef unsigned u32x4 __attribute__((ext_vector_type(4)));
typedef unsigned u32x2 __attribute__((ext_vector_type(2)));
typedef unsigned long long u64;
#define LAS __attribute__((address_space(3)))
#define SBAR() __builtin_amdgcn_sched_barrier(0)
#define BARRIER() asm volatile("s_barrier" ::: "memory")
#define WAITL0() asm volatile("s_waitcnt lgkmcnt(0)" ::: "memory")
#define WAITV(n) asm volatile("s_waitcnt vmcnt(" #n ")" ::: "memory")
#define FENCE() asm volatile("" ::: "memory")

constexpr int S = 2048, D = 640, F = 1728;
constexpr int SC1 = 16;

constexpr unsigned WS_EPOCH = 0;
constexpr unsigned WS_FLAGA = 4096;
constexpr unsigned WS_FLAGO = WS_FLAGA + 32768;
constexpr unsigned WS_CAND = WS_FLAGO + 16384;
constexpr unsigned WS_HDONE = WS_CAND + 4096;
constexpr unsigned WS_ACT = WS_HDONE + 4096;
constexpr unsigned WS_ST1 = 131072;
constexpr unsigned WS_CS1 = WS_ST1 + 32768;
constexpr unsigned WS_STATS = 524288;
constexpr unsigned WS_QKV = 1048576;
constexpr unsigned WS_O = WS_QKV + 480 * 16384;
constexpr unsigned WS_H = WS_O + S * D * 2;
#define WS_TAIL 1
#define STAMP_OFF (15u << 20)
#define NSTAMP 64

constexpr int L_SLOT = 32768, L_W = 16384;
constexpr int PL = 4128;
constexpr int L_K = 0, L_V = 5 * PL;
constexpr int L_WO = 45568;
constexpr int L_ROWS = 131072;
constexpr int L_RED = 0;
constexpr int L_HST = 20480;
constexpr int L_FW = L_WO;
constexpr int L_XS = 0;
constexpr int L_FRED = 12288;
constexpr int L_MISC = L_WO + 640 * 160;
constexpr int L_RS = 0;
constexpr int L_CS = 1024;
constexpr int L_CNT = L_MISC + 3600;
constexpr int L_F1 = L_MISC + 3604, L_C1 = L_MISC + 3608;
constexpr int L_PART = L_MISC + 2048;
constexpr int L_K32 = L_MISC + 2560;
constexpr int L_HD = L_MISC + 3712;
constexpr int L_SEL = L_MISC + 3968;
constexpr int L_STAMP = L_MISC + 4096;
constexpr int L_KEY = L_MISC + 4608 + 256;
constexpr int LDS_BYTES = 163840;
static_assert(L_V + 6 * PL <= L_WO && L_KEY + 2048 <= LDS_BYTES, "lds map");

struct Params { const u16* x; const u16* wqkv; const u16* wo; const u16* wr; const u16* wg; const u16* wu; const u16* wd; u16* out; unsigned char* ws; };

__device__ __forceinline__ float bf2f(unsigned v) { return __uint_as_float(v << 16); }
__device__ __forceinline__ float bfhi(unsigned v) { return __uint_as_float(v & 0xffff0000u); }
__device__ __forceinline__ unsigned pk2(float a, float b) { f32x2 f = {a, b}; return __builtin_bit_cast(unsigned, __builtin_convertvector(f, bf2)); }
__device__ __forceinline__ float rbf(float f) { return __uint_as_float(pk2(f, 0.f) << 16); }
__device__ __forceinline__ float dot2(unsigned a, unsigned b, float c) { return __builtin_amdgcn_fdot2_f32_bf16(__builtin_bit_cast(bf2, a), __builtin_bit_cast(bf2, b), c, false); }
__device__ __forceinline__ float swap_add(float v) { auto r = __builtin_amdgcn_permlane32_swap(__float_as_uint(v), __float_as_uint(v), false, false); return __uint_as_float(r[0]) + __uint_as_float(r[1]); }
__device__ __forceinline__ float swap16_add(float v) { auto r = __builtin_amdgcn_permlane16_swap(__float_as_uint(v), __float_as_uint(v), false, false); return __uint_as_float(r[0]) + __uint_as_float(r[1]); }
template <int CTRL> __device__ __forceinline__ float dppadd(float v) { return v + __int_as_float(__builtin_amdgcn_update_dpp(0, __float_as_int(v), CTRL, 0xf, 0xf, false)); }
__device__ __forceinline__ float wave_fsum(float v) { v = dppadd<0x128>(v); v = dppadd<0x124>(v); v = dppadd<0x122>(v); v = dppadd<0x121>(v); return swap_add(swap16_add(v)); }
template <int CTRL> __device__ __forceinline__ unsigned dppmax(unsigned v) { return max(v, (unsigned)__builtin_amdgcn_update_dpp(0, (int)v, CTRL, 0xf, 0xf, false)); }
__device__ __forceinline__ unsigned wave_umax(unsigned v) {
  v = dppmax<0x128>(v); v = dppmax<0x124>(v); v = dppmax<0x122>(v); v = dppmax<0x121>(v);
  auto r = __builtin_amdgcn_permlane16_swap(v, v, false, false); v = max(r[0], r[1]);
  auto q = __builtin_amdgcn_permlane32_swap(v, v, false, false); return max(q[0], q[1]);
}
__device__ __forceinline__ void glds16(unsigned voff, const void* sbase, unsigned lds) {
  asm volatile("s_mov_b32 m0, %2\n\ts_nop 0\n\tglobal_load_lds_dwordx4 %0, %1" ::"v"(voff), "s"(sbase), "s"(lds) : "memory");
}
__device__ __forceinline__ void glds16c(unsigned voff, const void* sbase, unsigned lds) {
  asm volatile("s_mov_b32 m0, %2\n\ts_nop 0\n\tglobal_load_lds_dwordx4 %0, %1 sc1" ::"v"(voff), "s"(sbase), "s"(lds) : "memory");
}
__device__ __forceinline__ void touch64(unsigned voff, const void* sbase, unsigned lds) {
  asm volatile("s_mov_b32 m0, %2\n\ts_nop 0\n\tglobal_load_lds_dword %0, %1" ::"v"(voff), "s"(sbase), "s"(lds) : "memory");
}
__device__ __forceinline__ void glds16n(unsigned voff, const void* sbase, unsigned lds) {
  asm volatile("s_mov_b32 m0, %2\n\ts_nop 0\n\tglobal_load_lds_dwordx4 %0, %1 nt" ::"v"(voff), "s"(sbase), "s"(lds) : "memory");
}
#define GL_FW glds16
#define GL_WO glds16
#define GL_X glds16
#define GL_W glds16
__device__ __forceinline__ void touch_ptr(const void* g, unsigned lds) {
  asm volatile("s_mov_b32 m0, %1\n\ts_nop 0\n\tglobal_load_lds_dword %0, off" ::"v"(g), "s"(lds) : "memory");
}
__device__ __forceinline__ bf16x4 trd(const LAS char* p) { return __builtin_amdgcn_ds_read_tr16_b64_v4i16((LAS bf16x4*)p); }
__device__ __forceinline__ bf16x8 cat8(bf16x4 a, bf16x4 b) { return (bf16x8){a[0], a[1], a[2], a[3], b[0], b[1], b[2], b[3]}; }
#define MFMA32(a, b, c) __builtin_amdgcn_mfma_f32_32x32x16_bf16(a, b, c, 0, 0, 0)
#define MFMA32K8(a, b, c) __builtin_amdgcn_mfma_f32_32x32x8bf16_1k(a, b, c, 0, 0, 0)
#define MFMA16(a, b, c) __builtin_amdgcn_mfma_f32_16x16x32_bf16(a, b, c, 0, 0, 0)
#define LD128(off) __builtin_amdgcn_raw_buffer_load_b128(rws, (off), 0, SC1)
#define LD64(off) __builtin_amdgcn_raw_buffer_load_b64(rws, (off), 0, SC1)
#define ST128(v, off) __builtin_amdgcn_raw_buffer_store_b128((v), rws, (off), 0, SC1)
#define ST64(v, off) __builtin_amdgcn_raw_buffer_store_b64((v), rws, (off), 0, SC1)

#define STAMP(k) do {} while (0)
#define STAMP_INIT() do {} while (0)
#define PSTAMP(i) do {} while (0)
#define STAMP_ZERO() do {} while (0)
#define STAMP_PA() do {} while (0)
#define STAMP_FLUSH() do {} while (0)

#define ROT (((bi >> 3) * 5 / 2) % 10)
constexpr unsigned SPIN_CAP = 1u << 17;

__global__ __launch_bounds__(512, 2) void fused_kernel(Params p) {
  __shared__ __attribute__((aligned(16))) char smem[LDS_BYTES];
  LAS char* L = (LAS char*)smem;
  const unsigned lds0 = (unsigned)(uintptr_t)smem;
  const int tid = threadIdx.x, lane = tid & 63;
  const int w = __builtin_amdgcn_readfirstlane(tid >> 6);
  const int b = blockIdx.x, xcd = b & 7, bi = b >> 3;
  const int l31 = lane & 31, hh = lane >> 5, l15 = lane & 15, fg = lane >> 4;
  const __amdgpu_buffer_rsrc_t rws = __builtin_amdgcn_make_buffer_rsrc(p.ws, 0, 16u << 20, 0x00020000);
  const int aP = 4 * (xcd >> 1) + (bi >> 3), aj = 8 * (xcd & 1) + (bi & 7);
  const int hd = 4 * xcd + (bi >> 3), pp = bi & 7;
  const int R = 4 * xcd + (bi >> 3), cc = bi & 7;
  const int gs = xcd * 14 + bi;
  const bool is_gu = bi < 14 && gs < 108;
  const int dn = xcd * 5 + (bi - 14);
  const bool is_dn = bi >= 14 && bi < 19;
  asm volatile("Lfk_start:");
  STAMP_INIT();
  u32x2 tag2;
  auto read_epoch = [&]() {
    u64 e;
    asm volatile("s_load_dwordx2 %0, %1, 0x0\n\ts_waitcnt lgkmcnt(0)" : "=s"(e) : "s"(p.ws) : "memory");
    e += 1;
    tag2 = (u32x2){(unsigned)e, (unsigned)(e >> 32)};
  };

  auto rows_dma = [&](int lw) {
    const char* rb = (const char*)p.x + (size_t)(64 * R + 8 * cc + 2 * lw) * 1280;
    glds16(lane * 16, rb, lds0 + L_ROWS + lw * 2560);
    glds16(lane * 16, rb + 1024, lds0 + L_ROWS + lw * 2560 + 1024);
    if (lane < 32) glds16(lane * 16, rb + 2048, lds0 + L_ROWS + lw * 2560 + 2048);
  };
  auto slot_clears = [&]() {
    const unsigned so = WS_STATS + (R * 8 + cc) * 1024 + lane * 16, ao = WS_ACT + (((gs >> 1) * 4 + 2 * (gs & 1) + ((lane >> 4) & 1)) * 8 + (lane & 7)) * 32 + ((lane >> 3) & 1) * 16;
    const unsigned off = w == 0 ? so : ((w == 1 && is_gu && lane < 32) ? ao : 0x80000000u);
    ST128(((u32x4){0, 0, 0, 0}), off);
  };
  auto rows_finish = [&]() {
    float mean[2], rs[2];
#pragma unroll
    for (int r = 0; r < 2; ++r) {
      const u32x4 c0 = *(const LAS u32x4*)(L + L_ROWS + (2 * w + r) * 1280 + lane * 16), c1 = *(const LAS u32x4*)(L + L_ROWS + (2 * w + r) * 1280 + 1024 + l15 * 16);
      float s0 = 0.f, s1 = 0.f;
#pragma unroll
      for (int e = 0; e < 4; ++e) { s0 = dot2(c0[e], 0x3f803f80u, s0); s1 = dot2(c1[e], 0x3f803f80u, s1); }
      mean[r] = wave_fsum(s0 + (lane < 16 ? s1 : 0.f)) * (1.f / D);
      const f32x2 mm = {mean[r], mean[r]};
      f32x2 q0 = {0.f, 0.f}, q1 = {0.f, 0.f};
#pragma unroll
      for (int e = 0; e < 4; ++e) { const f32x2 d0 = (f32x2){bf2f(c0[e]), bfhi(c0[e])} - mm, d1 = (f32x2){bf2f(c1[e]), bfhi(c1[e])} - mm; q0 += d0 * d0; q1 += d1 * d1; }
      const float var = wave_fsum((q0[0] + q0[1]) + (lane < 16 ? q1[0] + q1[1] : 0.f)) * (1.f / D);
      rs[r] = __builtin_amdgcn_rcpf(rbf(__builtin_amdgcn_sqrtf(rbf(rbf(var) + 1e-5f))));
    }
    WAITV(0);
    ST128(((u32x4){__float_as_uint(rbf(lane ? mean[1] : mean[0])), __float_as_uint(lane ? rs[1] : rs[0]), tag2[0], tag2[1]}), lane < 2 ? WS_ST1 + (unsigned)(64 * R + 8 * cc + 2 * w + lane) * 16 : 0x80000000u);
  };

  auto code_touch = [&](int lw) {
    u64 pc; unsigned back, len;
    asm volatile("s_getpc_b64 %0\nLfk_here%=:\n\ts_mov_b32 %1, Lfk_here%=-Lfk_start\n\ts_mov_b32 %2, Lfk_code_end-Lfk_start" : "=s"(pc), "=s"(back), "=s"(len));
    const char* cb = (const char*)((pc - back) & ~(u64)127);
    touch_ptr(cb + min((unsigned)((bi * 4 + lw) * 3 + min(lane, 2)) * 128u, len), lds0 + L_MISC + 8192);
  };
  auto lds_ctl_init = [&]() {
    if (tid == 0) { *(LAS unsigned*)(L + L_F1) = 0; *(LAS unsigned*)(L + L_C1) = 0; }
    STAMP_ZERO();
  };
  if (aj < 15 && w >= 4) {
    const int lw = w - 4;
    const unsigned xsw = (unsigned)(4 * (lw & 1) + (lane >> 4));
    const unsigned voff_x = (unsigned)(lane >> 3) * 1280u + (((unsigned)(lane & 7) ^ xsw) << 4);
    const unsigned ch = (unsigned)(lane & 15) ^ (((unsigned)(lane >> 4) << 2) | (unsigned)lw);
    const unsigned voff_w = (unsigned)(lane >> 4) * 3840u + ch * 16u;
    const char* xb = (const char*)p.x + (size_t)(128 * aP + 8 * lw) * 1280;
    const char* wb = (const char*)p.wqkv + (size_t)(4 * lw) * 3840 + 256 * aj;
    const unsigned lbase = lds0 + lw * 1024;
    const int rot = ROT;
    auto issue = [&](int s) {
      const unsigned sl = lbase + (s & 3) * L_SLOT;
      int ss = s + rot; if (ss >= 10) ss -= 10;
#pragma unroll
      for (int i = 0; i < 4; ++i) {
        GL_X(voff_x, xb + (size_t)i * 32 * 1280 + ss * 128, sl + i * 4096);
        GL_W(voff_w, wb + ((size_t)ss * 64 + i * 16) * 3840, sl + L_W + i * 4096);
      }
    };
    rows_dma(lw);
    issue(0);
    code_touch(lw);
    read_epoch();
    WAITV(9); BARRIER();
    issue(1);
    WAITV(9); BARRIER();
    PSTAMP(1);
#pragma unroll 1
    for (int s = 0; s <= 6; ++s) { issue(s + 2); WAITV(8); BARRIER(); }
    issue(9);
    const unsigned so = WS_ST1 + (unsigned)(128 * aP + 32 * lw + l31) * 16, co = WS_CS1 + (unsigned)((aj * 16 + fg) * 64 + 16 * lw + l15) * 16;
    u32x4 gs1 = LD128(so), gc0 = LD128(co), gc1 = LD128(co + 4096), gc2 = LD128(co + 8192), gc3 = LD128(co + 12288);
    WAITV(13); BARRIER();
    WAITV(5); BARRIER();
    unsigned spins = 0;
#pragma clang loop unroll(disable)
    for (;;) {
      const bool ok = gs1[2] == tag2[0] && gs1[3] == tag2[1] && gc0[2] == tag2[0] && gc0[3] == tag2[1] && gc1[2] == tag2[0] && gc1[3] == tag2[1] && gc2[2] == tag2[0] && gc2[3] == tag2[1] && gc3[2] == tag2[0] && gc3[3] == tag2[1];
      if (__all(ok) || ++spins > SPIN_CAP) break;
      FENCE();
      gs1 = LD128(so); gc0 = LD128(co); gc1 = LD128(co + 4096); gc2 = LD128(co + 8192); gc3 = LD128(co + 12288);
    }
    if (lane < 32) *(LAS u32x2*)(L + L_RS + (32 * lw + l31) * 8) = (u32x2){gs1[0], gs1[1]};
    {
      const float c0 = swap_add(swap16_add((__uint_as_float(gc0[0]) + __uint_as_float(gc1[0])) + (__uint_as_float(gc2[0]) + __uint_as_float(gc3[0]))));
      const float c1 = swap_add(swap16_add((__uint_as_float(gc0[1]) + __uint_as_float(gc1[1])) + (__uint_as_float(gc2[1]) + __uint_as_float(gc3[1]))));
      if (lane < 16) *(LAS f32x2*)(L + L_CS + (32 * lw + 2 * lane) * 4) = (f32x2){c0, c1};
    }
    WAITL0(); BARRIER();
    BARRIER();
  } else if (w < 4) {
    slot_clears();
    read_epoch();
    BARRIER();
    rows_finish();
    if (aj < 15) {
      PSTAMP(7);
      const int wm = w >> 1, wn = w & 1;
      unsigned xa[4], wa[4];
#pragma unroll
      for (int ks = 0; ks < 4; ++ks) xa[ks] = (unsigned)(64 * wm + l31) * 128u + ((((unsigned)(2 * ks + hh)) ^ (((unsigned)l31 >> 1) & 7u)) << 4);
      {
        const unsigned blk = (lane >> 4) & 1, q = (lane & 15) >> 2, pq = lane & 3;
#pragma unroll
        for (int j = 0; j < 2; ++j)
#pragma unroll
          for (int t = 0; t < 2; ++t) {
            const unsigned row = 8 * hh + 4 * t + q, chn = 4 * (2 * wn + (j ^ wm)) + 2 * blk + (pq >> 1), m = (q << 2) | (unsigned)(2 * hh + t);
            wa[j * 2 + t] = L_W + 256u * row + 16u * (chn ^ m) + 8u * (pq & 1);
          }
      }
      f32x16 a00 = {}, a01 = {}, a10 = {}, a11 = {};
      bf16x8 xf0[2], wf0[2], xf1[2], wf1[2];

      unsigned xb[4], wb[4];
#define SETSLOT(so) do { _Pragma("unroll") for (int k_ = 0; k_ < 4; ++k_) { xb[k_] = xa[k_] + (so); wb[k_] = wa[k_] + (so); } } while (0)
#define RD(XF, WF, ks) do { \
        XF[0] = *(const LAS bf16x8*)(L + xb[ks]); WF[0] = cat8(trd(L + wb[0] + (ks) * 4096), trd(L + wb[1] + (ks) * 4096)); \
        WF[1] = cat8(trd(L + wb[2] + (ks) * 4096), trd(L + wb[3] + (ks) * 4096)); XF[1] = *(const LAS bf16x8*)(L + xb[ks] + 4096); } while (0)
#define MM(XF, WF) do { \
        a00 = MFMA32(WF[0], XF[0], a00); a01 = MFMA32(WF[1], XF[0], a01); \
        a10 = MFMA32(WF[0], XF[1], a10); a11 = MFMA32(WF[1], XF[1], a11); } while (0)
#define PINALL() asm volatile("" : "+v"(a00), "+v"(a01), "+v"(a10), "+v"(a11))
#define ILV() do { __builtin_amdgcn_sched_group_barrier(0x8, 1, 0); __builtin_amdgcn_sched_group_barrier(0x100, 3, 0); \
        __builtin_amdgcn_sched_group_barrier(0x8, 1, 0); __builtin_amdgcn_sched_group_barrier(0x100, 3, 0); __builtin_amdgcn_sched_group_barrier(0x8, 2, 0); } while (0)
#define STEP(XN, WN, ksn, XC, WC) do { RD(XN, WN, ksn); MM(XC, WC); ILV(); PINALL(); SBAR(); } while (0)
      BARRIER();
      SETSLOT(0);
      RD(xf0, wf0, 0);
      SBAR();
#pragma unroll 1
      for (int s = 0; s < 9; ++s) {
        STEP(xf1, wf1, 1, xf0, wf0);
        STEP(xf0, wf0, 2, xf1, wf1);
        STEP(xf1, wf1, 3, xf0, wf0);
        SETSLOT(((s + 1) & 3) * L_SLOT);
        WAITL0(); BARRIER();
        STEP(xf0, wf0, 0, xf1, wf1);
      }
      STEP(xf1, wf1, 1, xf0, wf0);
      STEP(xf0, wf0, 2, xf1, wf1);
      STEP(xf1, wf1, 3, xf0, wf0);
      MM(xf1, wf1); PINALL();
      PSTAMP(2);
      WAITL0(); BARRIER();
      PSTAMP(6);
      const float qs = aj < 5 ? 0.22360679774997896f * 1.4426950408889634f : 1.f;
      f32x2 rs0 = *(const LAS f32x2*)(L + L_RS + (64 * wm + l31) * 8), rs1 = *(const LAS f32x2*)(L + L_RS + (64 * wm + 32 + l31) * 8);
      const float sc0 = rs0[1] * qs, nm0 = -rs0[0] * sc0, sc1 = rs1[1] * qs, nm1 = -rs1[0] * sc1;
#pragma unroll
      for (int j = 0; j < 2; ++j) {
        const int fj = 64 * wn + 32 * (j ^ wm) + 4 * hh;
        const unsigned go = WS_QKV + (unsigned)(32 * aj + (fj >> 2)) * 16384u + (unsigned)(128 * aP + 64 * wm + l31) * 8u;
#pragma unroll
        for (int rq = 0; rq < 4; ++rq) {
          const f32x4 cs = *(const LAS f32x4*)(L + L_CS + (fj + 8 * rq) * 4);
          const f32x16& a0 = j == 0 ? a00 : a01; const f32x16& a1 = j == 0 ? a10 : a11;
          const f32x2 c01 = {cs[0], cs[1]}, c23 = {cs[2], cs[3]};
          const f32x2 u0 = (f32x2){a0[rq * 4], a0[rq * 4 + 1]} * sc0 + c01 * nm0, u1 = (f32x2){a0[rq * 4 + 2], a0[rq * 4 + 3]} * sc0 + c23 * nm0;
          const f32x2 u2 = (f32x2){a1[rq * 4], a1[rq * 4 + 1]} * sc1 + c01 * nm1, u3 = (f32x2){a1[rq * 4 + 2], a1[rq * 4 + 3]} * sc1 + c23 * nm1;
          ST64(((u32x2){pk2(u0[0], u0[1]), pk2(u1[0], u1[1])}), go + 2 * rq * 16384);
          ST64(((u32x2){pk2(u2[0], u2[1]), pk2(u3[0], u3[1])}), go + 2 * rq * 16384 + 256);
        }
      }
      PSTAMP(4);
      lds_ctl_init();
      WAITV(0); WAITL0();
      PSTAMP(5);
      BARRIER();
    }
  } else {
    rows_dma(w - 4);
    code_touch(w - 4);
    read_epoch();
    WAITV(1); BARRIER();
  }
  if (aj >= 15) {
    const int chn = min(32 * w + l31, 239);
    const char* wp0 = (const char*)p.wqkv + (size_t)(40 * aP + 20 * hh) * 3840 + chn * 16;
    u32x4 wv[20];
#pragma unroll
    for (int i = 0; i < 20; ++i) wv[i] = *(const u32x4*)(wp0 + (size_t)i * 3840);
    SBAR();
    f32x2 c2[4] = {{0.f, 0.f}, {0.f, 0.f}, {0.f, 0.f}, {0.f, 0.f}};
#pragma unroll
    for (int i = 0; i < 20; ++i)
#pragma unroll
      for (int e = 0; e < 4; ++e) c2[e] += (f32x2){bf2f(wv[i][e]), bfhi(wv[i][e])};
#pragma unroll
    for (int e = 0; e < 4; ++e) c2[e] = (f32x2){swap_add(c2[e][0]), swap_add(c2[e][1])};
    if (lane < 32 && 32 * w + l31 < 240) {
#pragma unroll
      for (int e = 0; e < 4; ++e) ST128(((u32x4){__float_as_uint(c2[e][0]), __float_as_uint(c2[e][1]), tag2[0], tag2[1]}), WS_CS1 + (unsigned)(((chn >> 4) * 16 + aP) * 64 + 4 * (chn & 15) + e) * 16);
    }
    lds_ctl_init(); WAITL0(); BARRIER();
  }
  const unsigned tag32 = tag2[0] | 1u;
  if (aj < 15 && tid == 0) ST64(tag2, WS_FLAGA + (aP * 16 + aj) * 128);
  PSTAMP(3);
  STAMP_PA();

  const int nt = w & 3, kh = w >> 2;
  const int tok = 64 * R + 16 * nt + l15;
  u32x2 xr[5], wrv[5];
  unsigned wr5[5];
  {
    const int t = w;
    *(LAS u32x2*)(L + L_V + 5 * PL + tid * 8) = (u32x2){(pp > 0 || tid >= 256) ? 0x00003f80u : 0u, 0u};
    if (pp == 0) {
#pragma unroll
      for (int i = 0; i < 3; ++i) { const int n = tid + 512 * i; if (n < 1280) *(LAS u32x4*)(L + (n >> 7) * PL + (n & 127) * 16) = (u32x4){0, 0, 0, 0}; }
    }
    if (kh == 0) {
#pragma unroll
      for (int ft = 0; ft < 5; ++ft) {
        xr[ft] = *(const u32x2*)(p.x + (size_t)tok * D + 80 * cc + 16 * ft + 4 * fg);
        wrv[ft] = *(const u32x2*)(p.wr + 80 * cc + 16 * ft + 4 * fg);
      }
    }
    if (bi == 31) {
#pragma unroll
      for (int i = 0; i < 5; ++i) wr5[i] = ((const unsigned*)p.wr)[lane + 64 * i];
    }
    f32x16 cdg, can;
    {
      auto diag_ok = [](int r) { unsigned long long m = 0; for (int l = 0; l < 64; ++l) if ((r & 3) + 8 * (r >> 2) + 4 * (l >> 5) <= (l & 31)) m |= 1ull << l; return m; };
#pragma unroll
      for (int r = 0; r < 16; ++r) { const bool ok = __builtin_amdgcn_inverse_ballot_w64(diag_ok(r)); cdg[r] = ok ? 0.f : -INFINITY; can[r] = ok ? -INFINITY : 0.f; }
      asm volatile("" : "+v"(cdg), "+v"(can));
    }
    unsigned wov[5];
#pragma unroll
    for (int j = 0; j < 5; ++j) { const int n = 64 * j + lane, rho = (n * 6554) >> 16, chn = n - 10 * rho; wov[j] = (unsigned)(8 * ((rho >> 2) & 3) + 4 * (rho >> 4) + (rho & 3)) * 1280u + chn * 16u; }
    asm volatile("" : "+v"(wov[0]), "+v"(wov[1]), "+v"(wov[2]), "+v"(wov[3]), "+v"(wov[4]));
    const char* wob = (const char*)p.wo + 160 * cc + (size_t)nt * 32 * 1280;
    const unsigned wol = lds0 + L_WO + nt * 5120;
    auto lds_wait = [&](int off, unsigned v) {
      unsigned sp = 0;
#pragma clang loop unroll(disable)
      while (__hip_atomic_load((LAS unsigned*)(L + off), __ATOMIC_RELAXED, __HIP_MEMORY_SCOPE_WORKGROUP) < v && ++sp < (SPIN_CAP << 4)) __builtin_amdgcn_s_sleep(0);
      FENCE();
    };
    WAITL0();
    if (kh == 0) {
      if (lane == 0) __hip_atomic_fetch_add((LAS unsigned*)(L + L_C1), 1u, __ATOMIC_RELAXED, __HIP_MEMORY_SCOPE_WORKGROUP);
      lds_wait(L_F1, 1);
    } else {
      if (w == 4) {
        const int sec = lane < 4 ? 0 : (lane < 12 ? 1 : 2);
        const int k = lane < 4 ? lane : (lane < 12 ? lane - 4 : lane - 12);
        const int col = sec * 640 + 20 * hd + ((k & 1) ? 19 : 0);
        const int Pn = sec == 0 ? 2 * pp + (k >> 1) : 2 * pp - 2 + (k >> 1);
        const bool need = lane < 20 && Pn >= 0;
        const unsigned fo = WS_FLAGA + (unsigned)((need ? Pn : 2 * pp) * 16 + (col >> 7)) * 128;
        unsigned spins = 0;
#pragma clang loop unroll(disable)
        for (;;) {
          FENCE();
          const u32x2 f = LD64(fo);
          if (__all(!need || (f[0] == tag2[0] && f[1] == tag2[1])) || ++spins > SPIN_CAP) break;
          __builtin_amdgcn_s_sleep(1);
        }
        if (lane == 0) __hip_atomic_store((LAS unsigned*)(L + L_F1), 1u, __ATOMIC_RELAXED, __HIP_MEMORY_SCOPE_WORKGROUP);
        STAMP(4);
      } else lds_wait(L_F1, 1);
      const char* qb = (const char*)p.ws + WS_QKV;
#pragma unroll
      for (int i = 0; i < 10; ++i) {
        const int pc = nt + 4 * i, pl = pc >> 2, qt = pc & 3;
        const int tok0 = 256 * pp - 256 + 128 * qt;
        if (tok0 >= 0) glds16c(lane * 16, qb + (size_t)((pl < 5 ? 160 + 5 * hd + pl : 320 + 5 * hd + pl - 5)) * 16384 + tok0 * 8, lds0 + pl * PL + qt * 1024);
      }
    }
    bf16x8 qf0; bf16x4 qf1;
    {
      const unsigned qo = WS_QKV + (unsigned)(5 * hd) * 16384u + (unsigned)(256 * pp + 32 * t + l31) * 8u;
      const u32x2 a = LD64(qo + (2 * hh) * 16384), c = LD64(qo + (2 * hh + 1) * 16384);
      u32x2 z = LD64(qo + 4 * 16384);
      asm volatile("s_waitcnt vmcnt(0)" : "+v"(z) :: "memory");
      if (hh) z = (u32x2){0, 0};
      qf0 = __builtin_bit_cast(bf16x8, (u32x4){a[0], a[1], c[0], c[1]});
      qf1 = __builtin_bit_cast(bf16x4, z);
    }
    if (kh == 1 && lane == 0) __hip_atomic_fetch_add((LAS unsigned*)(L + L_C1), 1u, __ATOMIC_RELAXED, __HIP_MEMORY_SCOPE_WORKGROUP);
    lds_wait(L_C1, 8);
    if (w == 4) STAMP(5);
#define WOP(i) GL_WO(wov[(i) % 5], wob + (size_t)((i) / 5) * 4 * 32 * 1280, wol + ((i) / 5) * 4 * 5120 + ((i) % 5) * 1024)
#define WOP3(i) do { if (kh == 0) { WOP(i); WOP((i) + 1); WOP((i) + 2); } SBAR(); } while (0)
    if (w == 0) STAMP(28);
    const LAS char* kb = L + L_K + l31 * 8 + (2 * hh) * PL;
    const LAS char* kb4 = L + (hh ? L_V + 5 * PL : L_K + 4 * PL) + l31 * 8;
    {
      const u32x2 k0 = *(const LAS u32x2*)(kb + (t + 8) * 256), k1 = *(const LAS u32x2*)(kb + (t + 8) * 256 + PL), k2 = *(const LAS u32x2*)(kb4 + (t + 8) * 256);
      const u32x4 q0 = __builtin_bit_cast(u32x4, qf0); u32x2 q1 = __builtin_bit_cast(u32x2, qf1);
      float d = dot2(q0[0], k0[0], 0.f); d = dot2(q0[1], k0[1], d); d = dot2(q0[2], k1[0], d); d = dot2(q0[3], k1[1], d);
      d = dot2(q1[0], k2[0], d); d = dot2(q1[1], k2[1], d);
      const unsigned nm = pk2(-swap_add(d), 0.f);
      if (hh) q1[0] = nm;
      qf1 = __builtin_bit_cast(bf16x4, q1);
    }
    const LAS char* vb = L + L_V + min(4 * ((lane >> 4) & 1) + (lane & 3), 5) * PL + (4 * hh + ((lane & 15) >> 2)) * 8;
    f32x16 o = (f32x16){};
    {
      f32x16 sa, sb;
      const f32x16 cz = {};
      bf16x4 ka, kc, kd;
      bf16x8 vq = {};
      u32x4 pq = {0, 0, 0, 0};
#define KRD(kt) do { ka = *(const LAS bf16x4*)(kb + (kt) * 256); kc = *(const LAS bf16x4*)(kb + (kt) * 256 + PL); kd = *(const LAS bf16x4*)(kb4 + (kt) * 256); } while (0)
#define EX2(S, i) pk2(__builtin_amdgcn_exp2f(S[i]), __builtin_amdgcn_exp2f(S[(i) + 1]))
#define GRP(nds, nv) do { __builtin_amdgcn_sched_group_barrier(0x8, 1, 0); if (nds) __builtin_amdgcn_sched_group_barrier(0x100, nds, 0); __builtin_amdgcn_sched_group_barrier(0x2, nv, 0); } while (0)
#define HALF(SC, SN, C, ktv, ktk) do { \
        u32x4 p0_; const LAS char* vp_ = vb + (ktv) * 256; \
        SN = MFMA32(cat8(ka, kc), qf0, C); \
        const bf16x8 v0_ = cat8(trd(vp_), trd(vp_ + 64)), v1_ = cat8(trd(vp_ + 128), trd(vp_ + 192)); \
        p0_[0] = EX2(SC, 0); p0_[1] = EX2(SC, 2); \
        GRP(4, 6); asm volatile("" : "+v"(SN), "+v"(p0_[0]), "+v"(p0_[1])); SBAR(); \
        o = MFMA32(vq, __builtin_bit_cast(bf16x8, pq), o); \
        p0_[2] = EX2(SC, 4); p0_[3] = EX2(SC, 6); \
        GRP(0, 6); asm volatile("" : "+v"(o), "+v"(p0_[2]), "+v"(p0_[3])); SBAR(); \
        SN = MFMA32K8(kd, qf1, SN); \
        KRD(ktk); \
        pq[0] = EX2(SC, 8); pq[1] = EX2(SC, 10); \
        GRP(3, 6); asm volatile("" : "+v"(SN), "+v"(pq[0]), "+v"(pq[1])); SBAR(); \
        o = MFMA32(v0_, __builtin_bit_cast(bf16x8, p0_), o); \
        pq[2] = EX2(SC, 12); pq[3] = EX2(SC, 14); vq = v1_; \
        GRP(0, 6); asm volatile("" : "+v"(o), "+v"(pq[2]), "+v"(pq[3])); SBAR(); } while (0)
      KRD(t + 8);
      sa = MFMA32(cat8(ka, kc), qf0, cdg); sa = MFMA32K8(kd, qf1, sa);
      KRD(t + 7);
      asm volatile("" : "+v"(sa)); SBAR();
      WOP3(0);
      HALF(sa, sb, cz, t + 8, t + 6);
      WOP3(3);
      HALF(sb, sa, cz, t + 7, t + 5);
      WOP3(6);
      HALF(sa, sb, cz, t + 6, t + 4);
      WOP3(9);
      HALF(sb, sa, cz, t + 5, t + 3);
      WOP3(12);
      HALF(sa, sb, cz, t + 4, t + 2);
      WOP3(15);
      HALF(sb, sa, cz, t + 3, t + 1);
      WOP3(18);
      HALF(sa, sb, cz, t + 2, t);
      if (kh == 0) { WOP(21); WOP(22); WOP(23); WOP(24); }
      SBAR();
      HALF(sb, sa, can, t + 1, t);
      {
        const LAS char* vp_ = vb + t * 256;
        const bf16x8 v0_ = cat8(trd(vp_), trd(vp_ + 64)), v1_ = cat8(trd(vp_ + 128), trd(vp_ + 192));
        o = MFMA32(vq, __builtin_bit_cast(bf16x8, pq), o);
        u32x4 p0_, p1_;
#pragma unroll
        for (int e = 0; e < 4; ++e) p0_[e] = EX2(sa, 2 * e);
        o = MFMA32(v0_, __builtin_bit_cast(bf16x8, p0_), o);
#pragma unroll
        for (int e = 0; e < 4; ++e) p1_[e] = EX2(sa, 8 + 2 * e);
        o = MFMA32(v1_, __builtin_bit_cast(bf16x8, p1_), o);
      }
    }
    float lsum;
    { auto r = __builtin_amdgcn_permlane32_swap(__float_as_uint(o[8]), __float_as_uint(o[8]), false, false); lsum = __uint_as_float(r[1]); }
    if (__builtin_expect(__any(!(lsum < 1.8446744e19f)), 0)) {
      const int lo = pp >= 1 ? t : 8;
      float mx = -INFINITY;
#pragma unroll 1
      for (int kt = t + 8; kt >= lo; --kt) {
        const bf16x4 ka = *(const LAS bf16x4*)(kb + kt * 256), kc = *(const LAS bf16x4*)(kb + kt * 256 + PL), kd = *(const LAS bf16x4*)(kb4 + kt * 256);
        f32x16 s = MFMA32(cat8(ka, kc), qf0, (f32x16){});
        s = MFMA32K8(kd, qf1, s);
#pragma unroll
        for (int r = 0; r < 16; ++r) { const int kr = (r & 3) + 8 * (r >> 2) + 4 * hh; const bool ok = kt == t + 8 ? kr <= l31 : (kt == t ? kr > l31 : true); mx = fmaxf(mx, ok ? s[r] : -INFINITY); }
      }
      { auto r = __builtin_amdgcn_permlane32_swap(__float_as_uint(mx), __float_as_uint(mx), false, false); mx = fmaxf(__uint_as_float(r[0]), __uint_as_float(r[1])); }
      o = (f32x16){};
#pragma unroll 1
      for (int kt = t + 8; kt >= lo; --kt) {
        const bf16x4 ka = *(const LAS bf16x4*)(kb + kt * 256), kc = *(const LAS bf16x4*)(kb + kt * 256 + PL), kd = *(const LAS bf16x4*)(kb4 + kt * 256);
        const LAS char* vp_ = vb + kt * 256;
        const bf16x8 v0_ = cat8(trd(vp_), trd(vp_ + 64)), v1_ = cat8(trd(vp_ + 128), trd(vp_ + 192));
        f32x16 s = MFMA32(cat8(ka, kc), qf0, (f32x16){});
        s = MFMA32K8(kd, qf1, s);
        u32x4 p0_, p1_;
#pragma unroll
        for (int r = 0; r < 16; ++r) { const int kr = (r & 3) + 8 * (r >> 2) + 4 * hh; const bool ok = kt == t + 8 ? kr <= l31 : (kt == t ? kr > l31 : true); s[r] = ok ? __builtin_amdgcn_exp2f(s[r] - mx) : 0.f; }
#pragma unroll
        for (int e = 0; e < 4; ++e) { p0_[e] = pk2(s[2 * e], s[2 * e + 1]); p1_[e] = pk2(s[8 + 2 * e], s[8 + 2 * e + 1]); }
        o = MFMA32(v0_, __builtin_bit_cast(bf16x8, p0_), o);
        o = MFMA32(v1_, __builtin_bit_cast(bf16x8, p1_), o);
      }
      auto r = __builtin_amdgcn_permlane32_swap(__float_as_uint(o[8]), __float_as_uint(o[8]), false, false); lsum = __uint_as_float(r[1]);
    }
    if (w == 0) STAMP(6);
    STAMP(32 + w);
    {
      const float inv = 1.f / lsum;
      unsigned A0 = pk2(o[0] * inv, o[1] * inv), A1 = pk2(o[2] * inv, o[3] * inv), B0 = pk2(o[4] * inv, o[5] * inv), B1 = pk2(o[6] * inv, o[7] * inv);
      unsigned C0 = pk2(o[8] * inv, o[9] * inv), C1 = pk2(o[10] * inv, o[11] * inv);
      const int tt = pp * 16 + 2 * t + (l31 >> 4);
      const unsigned base = WS_O + (unsigned)tt * 80 * 256 + (l31 & 15) * 16 + (unsigned)(5 * (hd >> 1)) * 256;
      if ((hd & 1) == 0) {
        auto s0 = __builtin_amdgcn_permlane32_swap(A0, B0, false, false), s1 = __builtin_amdgcn_permlane32_swap(A1, B1, false, false);
        ST128(((u32x4){s0[0], s1[0], s0[1], s1[1]}), base + hh * 256);
        if (hh == 0) ST64(((u32x2){C0, C1}), base + 2 * 256);
      } else {
        auto u0 = __builtin_amdgcn_permlane32_swap(A0, C0, false, false), u1 = __builtin_amdgcn_permlane32_swap(A1, C1, false, false);
        const u32x4 v = hh ? (u32x4){B0, B1, u0[0], u1[0]} : (u32x4){u0[1], u1[1], B0, B1};
        ST128(v, base + (3 + hh) * 256);
        if (hh == 0) ST64(((u32x2){A0, A1}), base + 2 * 256 + 8);
      }
      WAITV(0);
      if (lane == 0) ST64(tag2, WS_FLAGO + (((pp * 8 + t) * 2 + (hd >> 4)) * 16 + (hd & 15)) * 8);
    }
  }
  if (w == 0) STAMP(7);
  STAMP(40 + w);

  BARRIER();
  if (w == 0) STAMP(8);

  f32x4 acc[5];
#pragma unroll
  for (int ft = 0; ft < 5; ++ft) acc[ft] = (f32x4){0, 0, 0, 0};
  {
    const LAS char* wp = L + L_WO + (unsigned)(kh * 320 + 4 * fg + ((lane & 15) >> 2)) * 160 + (lane & 3) * 8;
    bf16x8 wa[3][5];
#pragma unroll
    for (int s = 0; s < 3; ++s)
#pragma unroll
      for (int ft = 0; ft < 5; ++ft) wa[s][ft] = cat8(trd(wp + s * 5120 + ft * 32), trd(wp + s * 5120 + 2560 + ft * 32));
    const int tg = 4 * R + nt;
    const unsigned fo = WS_FLAGO + ((((tg >> 4) * 8 + ((tg >> 1) & 7)) * 2 + kh) * 16 + l15) * 8;
    unsigned spins = 0;
#pragma clang loop unroll(disable)
    for (;;) {
      FENCE();
      const u32x2 f = LD64(fo);
      if (__all(f[0] == tag2[0] && f[1] == tag2[1]) || ++spins > SPIN_CAP) break;
      __builtin_amdgcn_s_sleep(1);
    }
    FENCE();
    if (w == 0) STAMP(9);
    STAMP(48 + w);
    u32x4 of[10];
#pragma unroll
    for (int s = 0; s < 10; ++s) of[s] = LD128(WS_O + (unsigned)(tg * 80 + 4 * (10 * kh + s)) * 256 + lane * 16);
    SBAR();
#pragma unroll
    for (int s = 0; s < 10; ++s) {
      const bf16x8 bfr = __builtin_bit_cast(bf16x8, of[s]);
#pragma unroll
      for (int ft = 0; ft < 5; ++ft) acc[ft] = MFMA16(s < 3 ? wa[s < 3 ? s : 0][ft] : cat8(trd(wp + s * 5120 + ft * 32), trd(wp + s * 5120 + 2560 + ft * 32)), bfr, acc[ft]);
    }
  }
  STAMP(56 + w);
  if (kh == 1) {
#pragma unroll
    for (int ft = 0; ft < 5; ++ft) *(LAS f32x4*)(L + L_RED + ((nt * 5 + ft) * 64 + lane) * 16) = acc[ft];
  }
  WAITL0(); BARRIER();
  if (w == 0) STAMP(10);
  if (kh == 0) {
    float s1 = 0.f, s2 = 0.f, s3 = 0.f;
#pragma unroll
    for (int ft = 0; ft < 5; ++ft) {
      const f32x4 o2 = *(const LAS f32x4*)(L + L_RED + ((nt * 5 + ft) * 64 + lane) * 16);
      const unsigned a01 = pk2(acc[ft][0] + o2[0], acc[ft][1] + o2[1]), a23 = pk2(acc[ft][2] + o2[2], acc[ft][3] + o2[3]);
      const unsigned h01 = pk2(bf2f(xr[ft][0]) + bf2f(a01), bfhi(xr[ft][0]) + bfhi(a01)), h23 = pk2(bf2f(xr[ft][1]) + bf2f(a23), bfhi(xr[ft][1]) + bfhi(a23));
      *(LAS u32x2*)(L + L_HST + nt * 2560 + l15 * 160 + ft * 32 + fg * 8) = (u32x2){h01, h23};
      s1 = dot2(h01, 0x3f803f80u, s1); s1 = dot2(h23, 0x3f803f80u, s1);
      s2 = dot2(h01, h01, s2); s2 = dot2(h23, h23, s2);
      s3 = dot2(h01, wrv[ft][0], s3); s3 = dot2(h23, wrv[ft][1], s3);
    }
    WAITL0();
#pragma unroll
    for (int i = 0; i < 3; ++i) {
      const int n = lane + 64 * i;
      if (n < 160) ST128(*(const LAS u32x4*)(L + L_HST + nt * 2560 + n * 16), WS_H + (unsigned)((R * 8 + cc) * 64 + 16 * nt) * 160 + n * 16);
    }
    s1 = swap_add(swap16_add(s1)); s2 = swap_add(swap16_add(s2)); s3 = swap_add(swap16_add(s3));
    WAITV(0);
    if (lane < 16) ST128(((u32x4){__float_as_uint(s1), __float_as_uint(s2), __float_as_uint(s3), tag32}), WS_STATS + ((R * 8 + cc) * 64 + 16 * nt + l15) * 16);
    if (w == 0) STAMP(11);
    BARRIER();
  } else {
    BARRIER();
    const int rl = lane >> 1, grow = 8 * ((rl >> 2) & 3) + 4 * (rl >> 4) + (rl & 3);
    if (is_gu) {
      const unsigned vo = (unsigned)grow * (F * 2) + (lane & 1) * 16;
#pragma unroll 1
      for (int pc = nt; pc < 40; pc += 4) { const int m = pc >= 20, s = pc - 20 * m; GL_FW(vo, (const char*)(m ? p.wu : p.wg) + (size_t)s * 32 * F * 2 + gs * 32, lds0 + L_FW + pc * 1024); }
    } else if (is_dn) {
      const unsigned vo = (unsigned)grow * (D * 2) + (lane & 1) * 16;
#pragma unroll 1
      for (int pc = nt; pc < 54; pc += 4) GL_FW(vo, (const char*)p.wd + (size_t)pc * 32 * D * 2 + dn * 32, lds0 + L_FW + pc * 1024);
    }
  }
  auto rank64 = [&](unsigned k) {
    const u32x4 oa = *(const LAS u32x4*)(L + L_KEY + w * 32), ob = *(const LAS u32x4*)(L + L_KEY + w * 32 + 16);
    unsigned c = 0;
#pragma unroll
    for (int e = 0; e < 4; ++e) c += (oa[e] > k) + (ob[e] > k);
    *(LAS unsigned char*)(L + L_PART + lane * 8 + w) = (unsigned char)c;
    WAITL0(); BARRIER();
    const u32x2 pr = *(const LAS u32x2*)(L + L_PART + lane * 8);
    return __builtin_amdgcn_sad_u8(pr[0], 0u, __builtin_amdgcn_sad_u8(pr[1], 0u, 0u));
  };
  if (bi == 31) {
    float swr = 0.f;
#pragma unroll
    for (int i = 0; i < 5; ++i) swr = dot2(wr5[i], 0x3f803f80u, swr);
    swr = wave_fsum(swr);
    const int tl = 32 * w + l31;
    const unsigned so = WS_STATS + (unsigned)(((4 * xcd + (tl >> 6)) * 8 + 4 * hh) * 64 + (tl & 63)) * 16;
    float t1, t2, t3;
    unsigned spins = 0;
#pragma clang loop unroll(disable)
    for (;;) {
      FENCE();
      const u32x4 g0 = LD128(so), g1 = LD128(so + 1024), g2 = LD128(so + 2048), g3 = LD128(so + 3072);
      t1 = (__uint_as_float(g0[0]) + __uint_as_float(g1[0])) + (__uint_as_float(g2[0]) + __uint_as_float(g3[0]));
      t2 = (__uint_as_float(g0[1]) + __uint_as_float(g1[1])) + (__uint_as_float(g2[1]) + __uint_as_float(g3[1]));
      t3 = (__uint_as_float(g0[2]) + __uint_as_float(g1[2])) + (__uint_as_float(g2[2]) + __uint_as_float(g3[2]));
      if (__all(g0[3] == tag32 && g1[3] == tag32 && g2[3] == tag32 && g3[3] == tag32) || ++spins > SPIN_CAP) break;
    }
    t1 = swap_add(t1); t2 = swap_add(t2); t3 = swap_add(t3);
    const float mean = t1 * (1.f / D);
    const float var = fmaxf(t2 * (1.f / D) - mean * mean, 0.f);
    const float mb = rbf(mean);
    const float sd = rbf(__builtin_amdgcn_sqrtf(rbf(rbf(var) + 1e-5f)));
    const unsigned rb = pk2((t3 - mb * swr) * __builtin_amdgcn_rcpf(sd), 0.f) & 0xffffu;
    const unsigned key = ((rb ^ ((rb & 0x8000u) ? 0xffffu : 0x8000u)) << 16) | (unsigned)(2047 - (256 * xcd + tl));
    const unsigned ms = (__float_as_uint(mb) & 0xffff0000u) | (__float_as_uint(sd) >> 16);
    if (w == 0) STAMP(12);
    if (lane < 32) *(LAS unsigned*)(L + L_K32 + tl * 4) = key;
    WAITL0();
    unsigned c = 0;
#pragma unroll
    for (int i = 0; i < 4; ++i) {
      const u32x4 o4 = *(const LAS u32x4*)(L + L_K32 + (32 * w + 16 * hh + 4 * i) * 4);
#pragma unroll
      for (int e = 0; e < 4; ++e) c += o4[e] > key;
    }
    { auto r = __builtin_amdgcn_permlane32_swap(c, c, false, false); c = r[0] + r[1]; }
    if (lane < 32 && c < 8) { *(LAS unsigned*)(L + L_KEY + (8 * w + c) * 4) = key; *(LAS unsigned*)(L + L_KEY + 256 + (8 * w + c) * 4) = ms; }
    WAITL0(); BARRIER();
    const unsigned k = *(const LAS unsigned*)(L + L_KEY + lane * 4), m2 = *(const LAS unsigned*)(L + L_KEY + 256 + lane * 4);
    const unsigned rk = rank64(k);
    if (w == 0) { if (rk < 8) ST128(((u32x4){k, tag2[0], m2, tag2[1]}), WS_CAND + xcd * 128 + rk * 16); STAMP(26); }
    BARRIER();
  }
  if (w == 0) {
    u32x4 a;
    unsigned spins = 0;
#pragma clang loop unroll(disable)
    for (;;) {
      FENCE();
      a = LD128(WS_CAND + lane * 16);
      if (__all(a[1] == tag2[0] && a[3] == tag2[1]) || ++spins > SPIN_CAP) break;
      if (is_gu || is_dn) __builtin_amdgcn_s_sleep(1); else __builtin_amdgcn_s_sleep(8);
    }
    STAMP(13);
    *(LAS unsigned*)(L + L_KEY + lane * 4) = a[0]; *(LAS unsigned*)(L + L_KEY + 256 + lane * 4) = a[2];
    if (b == 0 && lane == 0) ST64(tag2, WS_EPOCH);
  }
  const int hc0 = (lane * 6554) >> 16, hc1 = ((64 + l15) * 6554) >> 16;
  const unsigned ho0 = (unsigned)hc0 * (64 * 160) + (lane - 10 * hc0) * 16, ho1 = (unsigned)hc1 * (64 * 160) + (64 + l15 - 10 * hc1) * 16;
  if (kh == 1) WAITV(0);
  WAITL0(); BARRIER();
  const unsigned ck = *(const LAS unsigned*)(L + L_KEY + lane * 4), cm = *(const LAS unsigned*)(L + L_KEY + 256 + lane * 4);
  const unsigned crk = rank64(ck);
  if (w == 0) STAMP(14);

  auto h_row = [&](int row, u32x4& h0, u32x4& h1) {
    const unsigned rb = WS_H + (unsigned)((row >> 6) * 8 * 64 + (row & 63)) * 160;
    h0 = LD128(rb + ho0); h1 = LD128(rb + ho1);
  };
  auto ln8 = [&](const u32x4& h, float m, float iv) { u32x4 n;
#pragma unroll
    for (int e = 0; e < 4; ++e) { const unsigned d = pk2(bf2f(h[e]) - m, bfhi(h[e]) - m); n[e] = pk2(bf2f(d) * iv, bfhi(d) * iv); } return n; };
  auto out_row = [&]() {
    const int row = 64 * R + 8 * cc + w;
    if (!__any(crk < 8 && 2047 - (int)(ck & 0x7ff) == row)) {
      u32x4 h0, h1; h_row(row, h0, h1);
      const u32x4 g = LD128(WS_STATS + (unsigned)((R * 8 + (lane & 7)) * 64 + 8 * cc + w) * 16);
      float t1 = __uint_as_float(g[0]), t2 = __uint_as_float(g[1]);
      t1 = dppadd<0x141>(t1); t2 = dppadd<0x141>(t2);
      t1 = dppadd<0xb1>(t1); t2 = dppadd<0xb1>(t2);
      t1 = dppadd<0x4e>(t1); t2 = dppadd<0x4e>(t2);
      const float mean = t1 * (1.f / D);
      const float m = rbf(mean), iv = __builtin_amdgcn_rcpf(rbf(__builtin_amdgcn_sqrtf(rbf(rbf(fmaxf(t2 * (1.f / D) - mean * mean, 0.f)) + 1e-5f))));
      const u32x4 n0 = ln8(h0, m, iv), n1 = ln8(h1, m, iv);
      u32x4 r0, r1;
#pragma unroll
      for (int e = 0; e < 4; ++e) { r0[e] = pk2(bf2f(h0[e]) + bf2f(n0[e]), bfhi(h0[e]) + bfhi(n0[e])); r1[e] = pk2(bf2f(h1[e]) + bf2f(n1[e]), bfhi(h1[e]) + bfhi(n1[e])); }
      *(u32x4*)(p.out + (size_t)row * D + lane * 8) = r0;
      if (lane < 16) *(u32x4*)(p.out + (size_t)row * D + 512 + lane * 8) = r1;
    }
  };
  if (is_gu) {
    {
      const int sl = __builtin_ctzll(__ballot(crk == (unsigned)w));
      const u32x2 sk = (u32x2){(unsigned)__builtin_amdgcn_readlane((int)ck, sl), (unsigned)__builtin_amdgcn_readlane((int)cm, sl)};
      u32x4 h0, h1; h_row(2047 - (int)(sk[0] & 0x7ff), h0, h1);
      const float m = __uint_as_float(sk[1] & 0xffff0000u), iv = __builtin_amdgcn_rcpf(__uint_as_float(sk[1] << 16));
      *(LAS u32x4*)(L + L_XS + w * 1296 + lane * 16) = ln8(h0, m, iv);
      if (lane < 16) *(LAS u32x4*)(L + L_XS + w * 1296 + 1024 + lane * 16) = ln8(h1, m, iv);
    }
    WAITL0(); BARRIER();
    if (w == 0) STAMP(15);
    {
      const int m = w & 1, kq = w >> 1;
      f32x4 a4 = {0, 0, 0, 0}, b4 = {0, 0, 0, 0};
#pragma unroll
      for (int i = 0; i < 5; ++i) {
        const int s = 5 * kq + i;
        const LAS char* ap = L + L_FW + (m * 20 + s) * 1024 + lane * 8;
        const bf16x8 af = cat8(trd(ap), trd(ap + 512));
        const bf16x8 bfv = *(const LAS bf16x8*)(L + L_XS + (lane & 7) * 1296 + s * 64 + fg * 16);
        if (i & 1) b4 = MFMA16(af, bfv, b4); else a4 = MFMA16(af, bfv, a4);
      }
      *(LAS f32x4*)(L + L_FRED + (w * 64 + lane) * 16) = a4 + b4;
    }
    WAITL0(); BARRIER();
    if (w == 0) {
      f32x4 g4 = {0, 0, 0, 0}, u4 = {0, 0, 0, 0};
#pragma unroll
      for (int q = 0; q < 4; ++q) { g4 += *(const LAS f32x4*)(L + L_FRED + ((2 * q) * 64 + lane) * 16); u4 += *(const LAS f32x4*)(L + L_FRED + ((2 * q + 1) * 64 + lane) * 16); }
      float a[4];
#pragma unroll
      for (int e = 0; e < 4; ++e) { const float g = rbf(g4[e]), u = rbf(u4[e]); const float sg = rbf(g * __builtin_amdgcn_rcpf(1.f + __builtin_amdgcn_exp2f(g * -1.4426950408889634f))); a[e] = sg * u; }
      if (l15 < 8) ST128(((u32x4){pk2(a[0], a[1]), tag32, pk2(a[2], a[3]), tag32}), WS_ACT + (((gs >> 1) * 4 + 2 * (gs & 1) + (fg >> 1)) * 8 + l15) * 32 + (fg & 1) * 16);
      STAMP(16);
    }
    out_row();
  } else if (is_dn) {
    if (crk < 8) *(LAS u32x2*)(L + L_SEL + crk * 8) = (u32x2){ck, cm};
    WAITL0();
    const u32x2 sk = *(const LAS u32x2*)(L + L_SEL + (lane & 7) * 8);
    const int row = 2047 - (int)(sk[0] & 0x7ff);
    u32x2 hv = {0, 0}, nv = {0, 0};
    float sig = 0.f;
    if (w == 0) {
      const int col = 16 * dn + 4 * fg, c = col / 80;
      hv = LD64(WS_H + (unsigned)(((row >> 6) * 8 + c) * 64 + (row & 63)) * 160 + (col - 80 * c) * 2);
      const unsigned rk = sk[0] >> 16, rbits = (rk & 0x8000u) ? (rk ^ 0x8000u) : (rk ^ 0xffffu);
      sig = rbf(__builtin_amdgcn_rcpf(1.f + __builtin_amdgcn_exp2f(bf2f(rbits) * -1.4426950408889634f)));
      const float m = __uint_as_float(sk[1] & 0xffff0000u), iv = __builtin_amdgcn_rcpf(__uint_as_float(sk[1] << 16));
#pragma unroll
      for (int e = 0; e < 2; ++e) { const unsigned d2 = pk2(bf2f(hv[e]) - m, bfhi(hv[e]) - m); nv[e] = pk2(bf2f(d2) * iv, bfhi(d2) * iv); }
      asm volatile("" : "+v"(nv), "+v"(hv), "+v"(sig));
    } else out_row();
    f32x4 a4 = {0, 0, 0, 0}, b4 = {0, 0, 0, 0};
    {
      bf16x8 af[7];
#pragma unroll
      for (int i = 0; i < 7; ++i) { const LAS char* ap = L + L_FW + min(w + 8 * i, 53) * 1024 + lane * 8; af[i] = cat8(trd(ap), trd(ap + 512)); }
      u32x4 g0[7];
      unsigned spins = 0;
#pragma clang loop unroll(disable)
      for (;;) {
        FENCE();
        bool ok = true;
#pragma unroll
        for (int i = 0; i < 7; ++i) g0[i] = LD128(WS_ACT + ((min(w + 8 * i, 53) * 4 + fg) * 8 + (lane & 7)) * 32 + ((lane >> 3) & 1) * 16);
#pragma unroll
        for (int i = 0; i < 7; ++i) ok = ok && g0[i][1] == tag32 && g0[i][3] == tag32;
        if (__all(ok) || ++spins > SPIN_CAP) break;
      }
      if (w == 0) STAMP(17);
#pragma unroll
      for (int i = 0; i < 7; ++i) {
        const unsigned y0 = (unsigned)__builtin_amdgcn_update_dpp(0, (int)g0[i][0], 0x128, 0xf, 0xf, false), y1 = (unsigned)__builtin_amdgcn_update_dpp(0, (int)g0[i][2], 0x128, 0xf, 0xf, false);
        const bf16x8 bv = __builtin_bit_cast(bf16x8, (u32x4){g0[i][0], g0[i][2], y0, y1});
        if (i < 6 || w < 6) { if (i & 1) b4 = MFMA16(af[i], bv, b4); else a4 = MFMA16(af[i], bv, a4); }
      }
    }
    *(LAS f32x4*)(L + L_FRED + (w * 64 + lane) * 16) = a4 + b4;
    WAITL0(); BARRIER();
    if (w == 0) {
      f32x4 y = {0, 0, 0, 0};
#pragma unroll
      for (int q = 0; q < 8; ++q) y += *(const LAS f32x4*)(L + L_FRED + (q * 64 + lane) * 16);
      unsigned r2[2];
#pragma unroll
      for (int e = 0; e < 2; ++e) {
        const unsigned y2 = pk2(rbf(y[2 * e]) * sig, rbf(y[2 * e + 1]) * sig);
        const unsigned t2 = pk2(bf2f(nv[e]) + bf2f(y2), bfhi(nv[e]) + bfhi(y2));
        r2[e] = pk2(bf2f(hv[e]) + bf2f(t2), bfhi(hv[e]) + bfhi(t2));
      }
      if (l15 < 8) *(u32x2*)(p.out + (size_t)row * D + 16 * dn + 4 * fg) = (u32x2){r2[0], r2[1]};
      STAMP(18);
      out_row();
    }
  } else {
    out_row();
  }
  if (w == 0) STAMP(19);
  asm volatile("Lfk_code_end:");
  STAMP_FLUSH();
}

extern "C" void kernel_launch(void* const* d_in, const int* in_sizes, int n_in, void* d_out, int out_size, void* d_ws, size_t ws_size, hipStream_t stream) {
  Params p;
  p.x = (const u16*)d_in[0]; p.wqkv = (const u16*)d_in[1]; p.wo = (const u16*)d_in[2]; p.wr = (const u16*)d_in[4];
  p.wg = (const u16*)d_in[5]; p.wu = (const u16*)d_in[6]; p.wd = (const u16*)d_in[7]; p.out = (u16*)d_out; p.ws = (unsigned char*)d_ws + ((ws_size - (16u << 20)) & ~(size_t)4095);
  fused_kernel<<<256, 512, 0, stream>>>(p);
}
```
